```python
import torch

def _softmax(x, axis=-1):
    return torch.softmax(x, dim=axis)
def _gelu(x):
    return torch.nn.functional.gelu(x, approximate="tanh")
def _silu(x):
    return torch.nn.functional.silu(x)
def _sigmoid(x):
    return torch.sigmoid(x)
def _layernorm(x, axis=-1, eps=1e-5):
    m = x.mean(dim=axis, keepdim=True)
    v = x.var(dim=axis, keepdim=True, unbiased=False)
    return (x - m) / torch.sqrt(v + eps)
def _scatter_add(tgt, idx, src, axis=0):
    return tgt.clone().index_add_(axis, idx, src)
def _segment_sum(src, seg, n):
    out = torch.zeros((n,) + src.shape[1:], dtype=src.dtype)
    return out.index_add_(0, seg, src)
def _pad(x, pw):
    flat = tuple(v for pair in reversed(pw) for v in pair)
    return torch.nn.functional.pad(x, flat)

kernel_name = 'gated_mix__linear_kernel_hedgehog__grouped_kv_mqa__prompt'

def setup_inputs():

    _w = (lambda a: (lambda t: t.to(torch.bfloat16) if t.is_floating_point() else t)(torch.as_tensor(a)))
    def _A_setup():
        B, S, D, H = 1, 2048, 640, 32
        Dh = D // H
        return {
            "x": _w((lambda *s: torch.randn(*s, dtype=torch.bfloat16) * 0.05)(B, S, D)),
            "wqkv": _w((lambda *s: torch.randn(*s, dtype=torch.bfloat16) * 1.6)(D, 3 * D)),
            "w_phi": _w((lambda *s: torch.randn(*s, dtype=torch.bfloat16) * 1.6)(H, Dh, Dh) * (Dh ** -0.5)),
            "wo": _w((lambda *s: torch.randn(*s, dtype=torch.bfloat16) * 0.05)(D, D)),
        }
    _A_in = _A_setup()

    def _B_setup():
        B, S, D, H = 1, 2048, 640, 32
        Dh = D // H
        return {
            "x": _w((lambda *s: torch.randn(*s, dtype=torch.bfloat16) * 0.05)(B, S, D)),
            "wq": _w((lambda *s: torch.randn(*s, dtype=torch.bfloat16) * 1.6)(D, H * Dh)),
            "wk": _w((lambda *s: torch.randn(*s, dtype=torch.bfloat16) * 1.6)(D, Dh)),
            "wv": _w((lambda *s: torch.randn(*s, dtype=torch.bfloat16) * 0.05)(D, Dh)),
            "wo": _w((lambda *s: torch.randn(*s, dtype=torch.bfloat16) * 0.05)(H * Dh, D)),
        }
    _B_in = _B_setup()

    return {
        "A_x": _A_in["x"],
        "A_wqkv": _A_in["wqkv"],
        "A_w_phi": _A_in["w_phi"],
        "A_wo": _A_in["wo"],
        "B_x": _B_in["x"],
        "B_wq": _B_in["wq"],
        "B_wk": _B_in["wk"],
        "B_wv": _B_in["wv"],
        "B_wo": _B_in["wo"],
        "gate_w": _w((lambda *s: torch.randn(*s, dtype=torch.bfloat16) * 0.05)(640) * 16.0),
    }

def reference(A_x, A_wqkv, A_w_phi, A_wo, B_x, B_wq, B_wk, B_wv, B_wo, gate_w):
    def _A_ref(x, wqkv, w_phi, wo):
        B, S, D = x.shape
        H = 32; Dh = D // H
        qkv = (lambda x, *d: x.permute(*d))((x @ wqkv).reshape(B, S, 3, H, Dh), 2, 0, 3, 1, 4)
        q, k, v = qkv[0], qkv[1], qkv[2]
        pq = torch.einsum("bhsd,hdf->bhsf", q, w_phi)
        pk = torch.einsum("bhsd,hdf->bhsf", k, w_phi)
        phi_q = torch.concatenate([_softmax(pq, axis=-1), _softmax(-pq, axis=-1)], axis=-1)
        phi_k = torch.concatenate([_softmax(pk, axis=-1), _softmax(-pk, axis=-1)], axis=-1)
        kv = torch.einsum("bhsf,bhsd->bhfd", phi_k, v)
        z  = torch.einsum("bhsf->bhf", phi_k)
        num = torch.einsum("bhqf,bhfd->bhqd", phi_q, kv)
        den = torch.einsum("bhqf,bhf->bhq", phi_q, z)[..., None] + 1e-6
        o = num / den
        return (lambda x, *d: x.permute(*d))(o, 0, 2, 1, 3).reshape(B, S, D) @ wo

    def _B_ref(x, wq, wk, wv, wo):
        B, S, D = x.shape
        H = 32
        Dh = D // H
        q = (lambda x, *d: x.permute(*d))((x @ wq).reshape(B, S, H, Dh), 0, 2, 1, 3)
        k = x @ wk
        v = x @ wv
        scores = torch.einsum("bhqd,bkd->bhqk", q, k) * (Dh ** -0.5)
        p = _softmax(scores, axis=-1)
        o = torch.einsum("bhqk,bkd->bhqd", p, v)
        return (lambda x, *d: x.permute(*d))(o, 0, 2, 1, 3).reshape(B, S, H * Dh) @ wo

    x = A_x
    g = _sigmoid((x * gate_w).sum(-1, keepdims=True))
    a = _A_ref(x, A_wqkv, A_w_phi, A_wo)
    b = _B_ref(x, B_wq, B_wk, B_wv, B_wo)
    return g * a + (1 - g) * b
```

```cpp
#include <hip/hip_runtime.h>
#include <hip/hip_bf16.h>
#include <stdint.h>

namespace gm {
typedef float f32x16 __attribute__((ext_vector_type(16)));
typedef float f32x4 __attribute__((ext_vector_type(4)));
typedef float f32x2 __attribute__((ext_vector_type(2)));
typedef short s16x4 __attribute__((ext_vector_type(4)));
typedef __bf16 b16x2 __attribute__((ext_vector_type(2)));
typedef __bf16 b16x8 __attribute__((ext_vector_type(8)));
typedef unsigned u32x4 __attribute__((ext_vector_type(4)));
typedef unsigned u32x2 __attribute__((ext_vector_type(2)));
typedef unsigned long long u64;
typedef unsigned short u16;
#define DI __device__ __forceinline__
#define LAS __attribute__((address_space(3)))
#define GAS __attribute__((address_space(1)))
typedef GAS u64 gu64;

constexpr int SEQ = 2048, DM = 640, DH = 20;
constexpr unsigned WS_EPOCH = 0, WS_FKV = 1024, WS_FPART = 4096, WS_FOA = 8192, WS_FOB = 12288, WS_G = 16384, WS_KV = 32768;
constexpr unsigned WS_PART = 262144, PART_B = 6720, WS_OA = 2097152, WS_OB = 5242880, WS_END = 8388608;
constexpr int VB = 352, TILE_B = 1280 + 4 * VB, CH_T = 8, CH_B = TILE_B * CH_T, STEP_B = 2 * CH_B;
constexpr int STAGE_B = 36864, ST_W1 = 10240, ST_X = 20480;
constexpr int L_PHK = 43008, L_VIM = 63488, L_QB = 75776, L_PHQ = 129024, L_RED = 152960, L_WPHI = 161152, L_ZERO = 162816, L_TOTAL = 163840;
constexpr int L_KVT = L_RED, L_WO_HI = 129024, L_XCH = 86016;
constexpr float LOG2E = 1.4426950408889634f;
constexpr float QSCALE = 0.22360679774997896f * LOG2E;
constexpr unsigned SPIN_CAP = 20000;

DI unsigned pk(float a, float b) { f32x2 v = {a, b}; b16x2 r = __builtin_convertvector(v, b16x2); return __builtin_bit_cast(unsigned, r); }
DI f32x16 mfma32(u32x4 a, u32x4 b, f32x16 c) { return __builtin_amdgcn_mfma_f32_32x32x16_bf16(__builtin_bit_cast(b16x8, a), __builtin_bit_cast(b16x8, b), c, 0, 0, 0); }
DI f32x16 mfma32k8(u32x2 a, u32x2 b, f32x16 c) { return __builtin_amdgcn_mfma_f32_32x32x8bf16_1k(__builtin_bit_cast(s16x4, a), __builtin_bit_cast(s16x4, b), c, 0, 0, 0); }
DI f32x4 mfma16(u32x4 a, u32x4 b, f32x4 c) { return __builtin_amdgcn_mfma_f32_16x16x32_bf16(__builtin_bit_cast(b16x8, a), __builtin_bit_cast(b16x8, b), c, 0, 0, 0); }
DI u32x2 trd(const LAS char* p) { return __builtin_bit_cast(u32x2, __builtin_amdgcn_ds_read_tr16_b64_v4i16((LAS s16x4*)p)); }
DI u32x4 trd2(const LAS char* p0, const LAS char* p1) { u32x2 a = trd(p0), b = trd(p1); return (u32x4){a.x, a.y, b.x, b.y}; }
DI u32x4 ldsr16(const LAS char* p) { return *(const LAS u32x4*)p; }
DI u32x2 ldsr8(const LAS char* p) { return *(const LAS u32x2*)p; }
DI float ex2(float x) { return __builtin_amdgcn_exp2f(x); }
DI void dma16(const void* g, unsigned lds_base) {
  unsigned sv; asm volatile("s_mov_b32 %0, m0\n\ts_mov_b32 m0, %2\n\ts_nop 0\n\tglobal_load_lds_dwordx4 %1, off\n\ts_mov_b32 m0, %0" : "=&s"(sv) : "v"(g), "s"(lds_base) : "memory"); }
DI void dma16s(const void* base, unsigned voff, unsigned lds_base) {
  unsigned sv; asm volatile("s_mov_b32 %0, m0\n\ts_mov_b32 m0, %3\n\ts_nop 0\n\tglobal_load_lds_dwordx4 %1, %2\n\ts_mov_b32 m0, %0" : "=&s"(sv) : "v"(voff), "s"(base), "s"(lds_base) : "memory"); }
DI void dma16s_sc1(const void* base, unsigned voff, unsigned lds_base) {
  unsigned sv; asm volatile("s_mov_b32 %0, m0\n\ts_mov_b32 m0, %3\n\ts_nop 0\n\tglobal_load_lds_dwordx4 %1, %2 sc1\n\ts_mov_b32 m0, %0" : "=&s"(sv) : "v"(voff), "s"(base), "s"(lds_base) : "memory"); }
DI u32x4 ld16_async(const void* p) { u32x4 r; asm volatile("global_load_dwordx4 %0, %1, off" : "=v"(r) : "v"(p) : "memory"); return r; }
DI u32x2 ld8_sc1_async(const void* p) { u32x2 r; asm volatile("global_load_dwordx2 %0, %1, off sc1" : "=v"(r) : "v"(p) : "memory"); return r; }
#define BAR_L() asm volatile("s_waitcnt lgkmcnt(0)\n\ts_barrier" ::: "memory")
#define WAIT_BAR(N) asm volatile("s_waitcnt vmcnt(" #N ") lgkmcnt(0)\n\ts_barrier" ::: "memory")
#define WAIT_VM(N) asm volatile("s_waitcnt vmcnt(" #N ")" ::: "memory")
#define SBAR() __builtin_amdgcn_sched_barrier(0)
#define RLX_AGENT __ATOMIC_RELAXED, __HIP_MEMORY_SCOPE_AGENT
DI float half_swap_max(float m) { auto rr = __builtin_amdgcn_permlane32_swap(__float_as_uint(m), __float_as_uint(m), false, false); return fmaxf(__uint_as_float(rr[0]), __uint_as_float(rr[1])); }
DI float from_upper(float v) { auto rr = __builtin_amdgcn_permlane32_swap(__float_as_uint(v), __float_as_uint(v), false, false); return __uint_as_float(rr[1]); }

template <int NF>
DI void wait_flags(const char* ws, unsigned off, u64 tag, int lane) {
  gu64* f = (gu64*)(ws + off);
#pragma unroll 1
  for (unsigned spin = 0; spin < SPIN_CAP; ++spin) {
    bool ok;
    if (NF == 256) {
      u64 a = __hip_atomic_load(f + lane, RLX_AGENT), b = __hip_atomic_load(f + 64 + lane, RLX_AGENT), c = __hip_atomic_load(f + 128 + lane, RLX_AGENT), d = __hip_atomic_load(f + 192 + lane, RLX_AGENT);
      ok = (a == tag) & (b == tag) & (c == tag) & (d == tag);
    } else {
      ok = __hip_atomic_load(f + (lane & (NF - 1)), RLX_AGENT) == tag;
    }
    if (__all(ok)) break;
    __builtin_amdgcn_s_sleep(2);
  }
  asm volatile("" ::: "memory");
}

#define STAMP(i) do {} while (0)

__global__ __launch_bounds__(512, 2) void fused(const u16* __restrict__ x, const u16* __restrict__ wqkv, const u16* __restrict__ wphi, const u16* __restrict__ woA,
                                                const u16* __restrict__ wq, const u16* __restrict__ wk, const u16* __restrict__ wv, const u16* __restrict__ woB,
                                                const u16* __restrict__ gatew, u16* __restrict__ out, char* ws) {
  __shared__ __attribute__((aligned(16))) char lds[L_TOTAL];
  const int tid = threadIdx.x, lane = tid & 63;
  const int w = __builtin_amdgcn_readfirstlane(tid >> 6) & 7;
  const int l15 = lane & 15, g = lane >> 4, q4 = l15 >> 2, p4 = lane & 3, r = lane & 31, h = lane >> 5;
  const int bx = blockIdx.x & 7, bi = blockIdx.x >> 3;
  const int rt = (bx & 1) * 8 + (bi & 7), hp = (bx >> 1) * 4 + (bi >> 3);
  const int w3 = w & 3, w2 = w >> 2;
  LAS char* lp = (LAS char*)lds;
  const unsigned lds0 = (unsigned)(uintptr_t)lds;
  const __amdgpu_buffer_rsrc_t wsr = __builtin_amdgcn_make_buffer_rsrc(ws, 0, WS_END, 0x00020000);
  STAMP(0);

  const int T0 = rt * 128 + (hp >> 1) * 16;
  const bool isV = hp & 1;
  u32x2 ep = ld8_sc1_async(ws + WS_EPOCH);
  u32x4 xf0, xf1, xf2, xf3, xf4, gf0, gf1, gf2, gf3, gf4, wpc;
  { const u16* xp = x + (size_t)(T0 + l15) * DM + w3 * 160 + g * 8;
    xf0 = ld16_async(xp); xf1 = ld16_async(xp + 32); xf2 = ld16_async(xp + 64); xf3 = ld16_async(xp + 96); xf4 = ld16_async(xp + 128);
    const u16* gp = gatew + w3 * 160 + g * 8;
    gf0 = ld16_async(gp); gf1 = ld16_async(gp + 32); gf2 = ld16_async(gp + 64); gf3 = ld16_async(gp + 96); gf4 = ld16_async(gp + 128);
    wpc = ld16_async(wphi + hp * 800 + (tid < 100 ? tid : 99) * 8); }
  { const u16* wsrc = isV ? wv : wk;
#pragma unroll
    for (int j = 0; j < 4; j++) { const int p = w + 8 * j; if (p < 25) dma16s(wsrc, p * 1024 + lane * 16, lds0 + 3 * STAGE_B + p * 1024); } }
  const char* sp[3]; unsigned sinc[3];
#pragma unroll
  for (int j = 0; j < 3; j++) {
    const int p = j < 2 ? w + 8 * j : 16 + w3;
    const int c = p * 64 + lane, fhh = c >= 640, rem = c - fhh * 640, rho = (rem * 6554) >> 16, cc = rem - rho * 10;
    const int k = (rho & ~12) | ((rho & 4) << 1) | ((rho & 8) >> 1);
    const bool hi = cc >= 5, isq = fhh && hi; const int c5 = cc - (hi ? 5 : 0);
    const unsigned rs = isq ? 1280u : 3840u;
    const unsigned off = k * rs + (isq ? 0 : (fhh ? 2560 : (hi ? 1280 : 0))) + hp * 80 + c5 * 16 + (j == 2 ? w2 * 64 * rs : 0);
    sp[j] = (isq ? (const char*)wq : (const char*)wqkv) + off; sinc[j] = (j == 2 ? 128 : 64) * rs;
  }
  unsigned xoff[2];
#pragma unroll
  for (int j = 0; j < 2; j++) { const int row = (w + 8 * j) * 8 + (lane >> 3); xoff[j] = (rt * 128 + row) * 1280 + (((lane & 7) ^ ((row >> 1) & 7)) * 16); }
  auto stage = [&](int st) {
    const unsigned lb = lds0 + (st & 3) * STAGE_B;
    dma16(sp[0], lb + w * 1024); sp[0] += sinc[0];
    dma16(sp[1], lb + (8 + w) * 1024); sp[1] += sinc[1];
    if ((st & 1) == w2) { dma16(sp[2], lb + (16 + w3) * 1024); sp[2] += sinc[2]; }
    dma16s(x + st * 64, xoff[0], lb + (20 + w) * 1024);
    dma16s(x + st * 64, xoff[1], lb + (28 + w) * 1024);
  };
  stage(0); stage(1); stage(2);
  if (tid < 64) *(LAS u32x4*)(lp + L_ZERO + tid * 16) = (u32x4){0u, 0u, 0u, 0u};
  asm volatile("s_waitcnt vmcnt(13)" : "+v"(xf0), "+v"(xf1), "+v"(xf2), "+v"(xf3), "+v"(xf4), "+v"(gf0), "+v"(gf1), "+v"(gf2), "+v"(gf3), "+v"(gf4), "+v"(wpc), "+v"(ep) :: "memory");
  const u64 tag = (((u64)(unsigned)__builtin_amdgcn_readfirstlane(ep.y) << 32) | (unsigned)__builtin_amdgcn_readfirstlane(ep.x)) + 1ull;
  if (tid < 100) *(LAS u32x4*)(lp + L_WPHI + tid * 16) = wpc;
  BAR_L();
  STAMP(1);
  { const LAS char* wb = lp + 3 * STAGE_B + (w3 * 160 + g * 8 + q4) * 40 + (w2 ? 32 : p4 * 8);
    const bool gl = w2 && l15 == 4;
    f32x4 acc = {};
#define P0STEP(s, XF, GF) { u32x4 a = trd2(wb + (s) * 1280, wb + (s) * 1280 + 160); if (gl) a = GF; acc = mfma16(a, XF, acc); }
    P0STEP(0, xf0, gf0) P0STEP(1, xf1, gf1) P0STEP(2, xf2, gf2) P0STEP(3, xf3, gf3) P0STEP(4, xf4, gf4)
#undef P0STEP
    *(LAS f32x4*)(lp + L_RED + tid * 16) = acc; }
  BAR_L();
  if (w == 0) {
    const unsigned tb = WS_KV + (T0 >> 5) * TILE_B; const int s16 = (T0 >> 4) & 1;
    if (!isV) {
      const int f0 = lane >> 5, ga = f0 ? (g & 1) : 2 * g;
      const LAS f32x4* rp = (const LAS f32x4*)(lp + L_RED) + f0 * 256 + ga * 16 + l15;
      f32x4 a = (rp[0] + rp[64]) + (rp[128] + rp[192]), b = (rp[16] + rp[80]) + (rp[144] + rp[208]);
      u32x4 v = {pk(a[0], a[1]), pk(a[2], a[3]), pk(b[0], b[1]), pk(b[2], b[3])};
      if (lane < 32) __builtin_amdgcn_raw_buffer_store_b128(v, wsr, tb + (g * 32 + s16 * 16 + l15) * 16, 0, 16);
      else if (lane < 48) __builtin_amdgcn_raw_buffer_store_b64((u32x2){v.x, v.y}, wsr, tb + 1024 + (s16 * 16 + l15) * 8, 0, 16);
      else { float gv = 1.0f / (1.0f + __expf(-a[0])); __builtin_amdgcn_raw_buffer_store_b32(__float_as_uint(gv), wsr, WS_G + (T0 + l15) * 4, 0, 16); }
    } else if (lane < 44) {
      const int hh = lane >= 22, d = lane - hh * 22, dd = d < 20 ? d : 0;
      const LAS float* rp = (const LAS float*)(lp + L_RED) + ((dd >> 4) * 256 + ((dd & 15) >> 2) * 16 + 4 * hh) * 4 + (dd & 3);
      float e[8];
#pragma unroll
      for (int j = 0; j < 8; j++) { const int tk = (8 * (j >> 2) + (j & 3)) * 4; e[j] = (rp[tk] + rp[tk + 256]) + (rp[tk + 512] + rp[tk + 768]); }
      u32x4 v = {pk(e[0], e[1]), pk(e[2], e[3]), pk(e[4], e[5]), pk(e[6], e[7])};
      if (d == 20) v = (u32x4){0x3F803F80u, 0x3F803F80u, 0x3F803F80u, 0x3F803F80u};
      if (d == 21) v = (u32x4){0u, 0u, 0u, 0u};
      __builtin_amdgcn_raw_buffer_store_b128(v, wsr, tb + 1280 + (s16 * 2 + hh) * VB + d * 16, 0, 16);
    }
  }
  STAMP(2);
  f32x4 acc[2][5];
#pragma unroll
  for (int a = 0; a < 2; a++)
#pragma unroll
    for (int b = 0; b < 5; b++) acc[a][b] = (f32x4){0.f, 0.f, 0.f, 0.f};
  const int wlane = ((g >> 1) * 16 + (g & 1) * 4 + q4) * 160 + p4 * 40;
  const int xlane = (w3 * 32 + l15) * 128;
  const int xo0 = ST_X + xlane + ((g ^ (l15 >> 1)) * 16), xo1 = ST_X + xlane + (((g ^ (l15 >> 1)) ^ 4) * 16);
  auto chunk = [&](int buf) {
    const LAS char* sb = lp + buf * STAGE_B;
    const LAS char* wb = sb + w2 * ST_W1 + wlane;
#pragma unroll
    for (int s = 0; s < 2; s++) {
      const LAS char* xb = sb + (s ? xo1 : xo0);
      const u32x4 b0 = ldsr16(xb), b1 = ldsr16(xb + 2048);
#pragma unroll
      for (int t = 0; t < 5; t++) {
        const u32x4 a = trd2(wb + s * 5120 + t * 8, wb + s * 5120 + 1280 + t * 8);
        acc[0][t] = mfma16(a, b0, acc[0][t]); acc[1][t] = mfma16(a, b1, acc[1][t]);
      }
    }
  };
#pragma unroll 1
  for (int i = 0; i < 7; i++) {
    WAIT_BAR(9);
    if (i == 2 && tid == 0) __hip_atomic_store((gu64*)(ws + WS_FKV) + blockIdx.x, tag, RLX_AGENT);
    stage(i + 3);
    chunk(i & 3);
  }
  WAIT_BAR(9);
  chunk(3);
  WAIT_BAR(4);
  chunk(0);
  WAIT_BAR(0);
  STAMP(3);
  wait_flags<256>(ws, WS_FKV, tag, lane);
  auto kv_step = [&](int s, int slot) {
#pragma unroll
    for (int j = 0; j < 6; j++) { int p = w + 8 * j; p = p > 41 ? 41 : p; dma16s_sc1(ws + WS_KV + s * STEP_B, p * 1024 + lane * 16, lds0 + slot * STEP_B + p * 1024); }
  };
  kv_step(0, 2);
  if (blockIdx.x == 0 && tid == 0) __hip_atomic_store((gu64*)(ws + WS_EPOCH), tag, RLX_AGENT);
  chunk(1);
  BAR_L();
  STAMP(4);
  kv_step(1, 0);
  if (w2 == 0) {
    const LAS char* pb = (p4 == g) ? lp + L_WPHI + (g & 1) * 800 + q4 * 40 : lp + L_ZERO;
    u32x4 wf[3][5];
#pragma unroll
    for (int t = 0; t < 5; t++) {
      wf[0][t] = trd2(pb + t * 8, pb + 160 + t * 8); wf[1][t] = trd2(pb + 320 + t * 8, pb + 480 + t * 8);
      const u32x2 c = trd(pb + 640 + t * 8); wf[2][t] = (u32x4){c.x, c.y, 0u, 0u};
    }
#pragma unroll
    for (int tt = 0; tt < 2; tt++) {
      const u32x4 b0 = {pk(acc[tt][0][0], acc[tt][0][1]), pk(acc[tt][0][2], acc[tt][0][3]), pk(acc[tt][1][0], acc[tt][1][1]), pk(acc[tt][1][2], acc[tt][1][3])};
      const u32x4 b1 = {pk(acc[tt][2][0], acc[tt][2][1]), pk(acc[tt][2][2], acc[tt][2][3]), pk(acc[tt][3][0], acc[tt][3][1]), pk(acc[tt][3][2], acc[tt][3][3])};
      const u32x4 b2 = {pk(acc[tt][4][0], acc[tt][4][1]), pk(acc[tt][4][2], acc[tt][4][3]), 0u, 0u};
      float p[20];
#pragma unroll
      for (int t = 0; t < 5; t++) {
        f32x4 c = {}; c = mfma16(wf[0][t], b0, c); c = mfma16(wf[1][t], b1, c); c = mfma16(wf[2][t], b2, c);
        p[4 * t] = c[0]; p[4 * t + 1] = c[1]; p[4 * t + 2] = c[2]; p[4 * t + 3] = c[3];
      }
      float mx = p[0], mn = p[0];
#pragma unroll
      for (int f = 1; f < 20; f++) { mx = fmaxf(mx, p[f]); mn = fminf(mn, p[f]); }
      const float ma = -mx * LOG2E, mb = mn * LOG2E;
      float ea[20], eb[20], sa = 0.f, sb = 0.f;
#pragma unroll
      for (int f = 0; f < 20; f++) { ea[f] = ex2(fmaf(p[f], LOG2E, ma)); eb[f] = ex2(fmaf(p[f], -LOG2E, mb)); sa += ea[f]; sb += eb[f]; }
      const float ia = __builtin_amdgcn_rcpf(sa), ib = __builtin_amdgcn_rcpf(sb);
      unsigned o[20];
#pragma unroll
      for (int f = 0; f < 10; f++) { o[f] = pk(ea[2 * f] * ia, ea[2 * f + 1] * ia); o[10 + f] = pk(eb[2 * f] * ib, eb[2 * f + 1] * ib); }
      LAS char* dst = lp + ((g >> 1) ? L_PHK : L_PHQ) + ((g & 1) * 128 + w3 * 32 + tt * 16 + l15) * 80;
#pragma unroll
      for (int c = 0; c < 5; c++) *(LAS u32x4*)(dst + c * 16) = (u32x4){o[4 * c], o[4 * c + 1], o[4 * c + 2], o[4 * c + 3]};
    }
  } else {
    const float sc = (g >> 1) ? QSCALE : 1.0f;
#pragma unroll
    for (int tt = 0; tt < 2; tt++) {
      const int row = (g & 1) * 128 + w3 * 32 + tt * 16 + l15;
      LAS char* dst = (g >> 1) ? lp + L_QB + row * 40 : lp + L_VIM + row * 48;
#pragma unroll
      for (int t = 0; t < 5; t++) *(LAS u32x2*)(dst + t * 8) = (u32x2){pk(acc[tt][t][0] * sc, acc[tt][t][1] * sc), pk(acc[tt][t][2] * sc, acc[tt][t][3] * sc)};
      if (g < 2) *(LAS u32x2*)(dst + 40) = (u32x2){0x00003F80u, 0u};
    }
  }
  BAR_L();
  STAMP(5);
  if (w < 6) {
    const int ft = w >> 1, dt = w & 1;
    const LAS char* ab = lp + L_PHK + (g * 8 + q4) * 80 + ft * 32 + p4 * 8;
    const LAS char* bb = lp + L_VIM + (g * 8 + q4) * 48 + dt * 32 + p4 * 8;
#pragma unroll
    for (int hd = 0; hd < 2; hd++) {
      f32x4 c = {};
#pragma unroll
      for (int ks = 0; ks < 4; ks++) {
        const int tk = hd * 128 + ks * 32;
        c = mfma16(trd2(ab + tk * 80, ab + (tk + 4) * 80), trd2(bb + tk * 48, bb + (tk + 4) * 48), c);
      }
      const int d = dt * 16 + l15, f = ft * 16 + g * 4;
      if (d < 21 && f < 40) __builtin_amdgcn_raw_buffer_store_b128(__builtin_bit_cast(u32x4, c), wsr, WS_PART + (hp * 16 + rt) * PART_B + ((hd * 21 + d) * 40 + f) * 4, 0, 16);
    }
  }
  u32x4 q1A, q1B; u32x2 q2A, q2B;
  { const LAS char* qa = lp + L_QB + (w3 * 32 + r) * 40; const LAS char* qb = qa + 128 * 40;
    u32x2 a = ldsr8(qa + 16 * h), b = ldsr8(qa + 16 * h + 8); q1A = (u32x4){a.x, a.y, b.x, b.y};
    a = ldsr8(qb + 16 * h); b = ldsr8(qb + 16 * h + 8); q1B = (u32x4){a.x, a.y, b.x, b.y};
    a = ldsr8(qa + 32); b = ldsr8(qb + 32);
    q2A = h ? (u32x2){0u, 0u} : a; q2B = h ? (u32x2){0u, 0u} : b; }
  WAIT_BAR(0);
  if (tid == 0) __hip_atomic_store((gu64*)(ws + WS_FPART) + hp * 16 + rt, tag, RLX_AGENT);
  kv_step(2, 1);
  STAMP(6);
  const int vr = r <= 21 ? r : 21;
  const int ok1 = lane * 16, ok2 = h ? (1280 + 20 * 16) : (1024 + r * 8), ov = 1280 + h * VB + vr * 16;
  const f32x16 Z = {};
  { const LAS char* t0 = lp + 2 * STEP_B;
    const u32x4 k1 = ldsr16(t0 + ok1); const u32x2 k2 = ldsr8(t0 + ok2);
    f32x16 sa = mfma32(k1, q1A, Z); sa = mfma32k8(k2, q2A, sa);
    f32x16 sb = mfma32(k1, q1B, Z); sb = mfma32k8(k2, q2B, sb);
    float ma = sa[0], mb = sb[0];
#pragma unroll
    for (int i = 1; i < 16; i++) { ma = fmaxf(ma, sa[i]); mb = fmaxf(mb, sb[i]); }
    ma = half_swap_max(ma); mb = half_swap_max(mb);
    if (h) { q2A.x = pk(-ma, 0.f); q2B.x = pk(-mb, 0.f); } }
  f32x16 OA = {}, OB = {}, SA, SB;
  u32x4 k1, v1, v2, pA1, pA2, pB1, pB2; u32x2 k2;
  float e0, e1, e2, e3;
  const LAS char* cb = lp + 2 * STEP_B + w2 * CH_B;
  k1 = ldsr16(cb + ok1); k2 = ldsr8(cb + ok2);
  SA = mfma32(k1, q1A, Z); SA = mfma32k8(k2, q2A, SA);
  v1 = ldsr16(cb + ov); v2 = v1;
  e0 = e1 = e2 = e3 = 0.f; pB2 = (u32x4){0u, 0u, 0u, 0u};
#define G(MF, LD, C0, C1, X, S_, i) MF; LD; SBAR(); C0; C1; SBAR(); X##0 = ex2(S_[i]); X##1 = ex2(S_[i + 1]); X##2 = ex2(S_[i + 2]); X##3 = ex2(S_[i + 3]); SBAR();
#define TILE(TB, TN) { float a0, a1, a2, a3; \
    G(SB = mfma32(k1, q1B, Z),    k1 = ldsr16((TN) + ok1),          pB2[2] = pk(e0, e1), pB2[3] = pk(e2, e3), a, SA, 0) \
    G(OB = mfma32(v2, pB2, OB),   v2 = ldsr16((TB) + ov + 2 * VB),  pA1[0] = pk(a0, a1), pA1[1] = pk(a2, a3), e, SA, 4) \
    G(SB = mfma32k8(k2, q2B, SB), k2 = ldsr8((TN) + ok2),           pA1[2] = pk(e0, e1), pA1[3] = pk(e2, e3), a, SA, 8) \
    G(OA = mfma32(v1, pA1, OA),   (void)0,                          pA2[0] = pk(a0, a1), pA2[1] = pk(a2, a3), e, SA, 12) \
    G(SA = mfma32(k1, q1A, Z),    (void)0,                          pA2[2] = pk(e0, e1), pA2[3] = pk(e2, e3), a, SB, 0) \
    G(OA = mfma32(v2, pA2, OA),   (void)0,                          pB1[0] = pk(a0, a1), pB1[1] = pk(a2, a3), e, SB, 4) \
    G(SA = mfma32k8(k2, q2A, SA), (void)0,                          pB1[2] = pk(e0, e1), pB1[3] = pk(e2, e3), a, SB, 8) \
    G(OB = mfma32(v1, pB1, OB),   v1 = ldsr16((TN) + ov),           pB2[0] = pk(a0, a1), pB2[1] = pk(a2, a3), e, SB, 12) }
  const int C0 = (hp >> 1) * 80;
  unsigned woff[5];
#pragma unroll
  for (int j = 0; j < 5; j++) { const int c = j * 64 + lane, rho = c / 10, cc = c - rho * 10; const int k = (rho & ~12) | ((rho & 4) << 1) | ((rho & 8) >> 1); woff[j] = (k * 640 + C0 + cc * 8) * 2; }
  auto wo_stage = [&](const u16* wo) {
#pragma unroll
    for (int jj = 0; jj < 3; jj++) { const int gr = w + 8 * jj; if (gr < 20) {
#pragma unroll
        for (int j = 0; j < 5; j++) dma16s(wo + gr * 32 * 640, woff[j], lds0 + (gr < 16 ? gr * 5120 : L_WO_HI + (gr - 16) * 5120) + j * 1024); } }
  };
  const int tokA = rt * 128 + w3 * 32 + r;
  f32x4 part[16];
#pragma unroll 1
  for (int s = 0; s < 4; s++) {
    const int sn = s == 0 ? 0 : (s == 1 ? 1 : 2);
    const LAS char* cn = lp + sn * STEP_B + w2 * CH_B;
#pragma unroll
    for (int t = 0; t < CH_T - 1; t++) TILE(cb + t * TILE_B, cb + (t + 1) * TILE_B);
    if (s == 0) {
      wait_flags<16>(ws, WS_FPART + hp * 128, tag, lane);
      const int idx = tid < 420 ? tid : 419;
#pragma unroll
      for (int j = 0; j < 16; j++) part[j] = __builtin_bit_cast(f32x4, __builtin_amdgcn_raw_buffer_load_b128(wsr, WS_PART + (hp * 16 + j) * PART_B + idx * 16, 0, 16));
    } else if (s == 1) {
      f32x4 a = ((part[0] + part[1]) + (part[2] + part[3])) + ((part[4] + part[5]) + (part[6] + part[7]));
      a += ((part[8] + part[9]) + (part[10] + part[11])) + ((part[12] + part[13]) + (part[14] + part[15]));
      if (tid < 420) { const int row = tid / 10, fq = tid - row * 10, hd = row >= 21, d = row - hd * 21;
        *(LAS u32x2*)(lp + L_KVT + hd * 3072 + d * 96 + fq * 8) = (u32x2){pk(a[0], a[1]), pk(a[2], a[3])}; }
      else if (tid < 484) { const int row = tid - 420; *(LAS u32x4*)(lp + L_KVT + row * 96 + 80) = (u32x4){0u, 0u, 0u, 0u}; }
      WAIT_BAR(0);
      kv_step(3, 2);
      const LAS char* ka = lp + L_KVT + w2 * 3072 + r * 96 + h * 16;
      const LAS char* qa = lp + L_PHQ + (w2 * 128 + w3 * 32 + r) * 80;
      f32x16 o = mfma32(ldsr16(ka), ldsr16(qa + h * 16), Z);
      o = mfma32(ldsr16(ka + 32), ldsr16(qa + 32 + h * 16), o);
      o = mfma32(ldsr16(ka + 64), ldsr16(qa + 64), o);
      const float inv = 1.0f / (from_upper(o[8]) + 1e-6f);
      const unsigned ob = WS_OA + (tokA * DM + (hp * 2 + w2) * DH + 4 * h) * 2;
#pragma unroll
      for (int c = 0; c < 3; c++) if (c < 2 || !h) __builtin_amdgcn_raw_buffer_store_b64((u32x2){pk(o[4 * c] * inv, o[4 * c + 1] * inv), pk(o[4 * c + 2] * inv, o[4 * c + 3] * inv)}, wsr, ob + c * 16, 0, 16);
    } else if (s == 2) {
      WAIT_BAR(0);
      if (tid == 0) __hip_atomic_store((gu64*)(ws + WS_FOA) + rt * 16 + hp, tag, RLX_AGENT);
    }
    TILE(cb + 7 * TILE_B, cn);
    cb = cn;
    if (s == 2) { BAR_L(); wo_stage(woA); }
  }
  pB2[2] = pk(e0, e1); pB2[3] = pk(e2, e3);
  OB = mfma32(v2, pB2, OB);
#undef TILE
#undef G
  STAMP(7);
  const int R0 = rt * 128 + (hp & 1) * 64 + w3 * 16 + l15;
  const unsigned ofr = (R0 * DM + w2 * 320 + g * 8) * 2;
  wait_flags<16>(ws, WS_FOA + rt * 128, tag, lane);
  u32x4 of[10];
#pragma unroll
  for (int ks = 0; ks < 10; ks++) of[ks] = __builtin_amdgcn_raw_buffer_load_b128(wsr, WS_OA + ofr + ks * 64, 0, 16);
  const float gt = __uint_as_float(__builtin_amdgcn_raw_buffer_load_b32(wsr, WS_G + R0 * 4, 0, 16));
  BAR_L();
  { LAS f32x4* xb = (LAS f32x4*)(lp + L_XCH) + w3 * (64 * 6);
    const f32x16 snd = w2 ? OA : OB;
#pragma unroll
    for (int c = 0; c < 3; c++) xb[(w2 * 3 + c) * 64 + lane] = (f32x4){snd[4 * c], snd[4 * c + 1], snd[4 * c + 2], snd[4 * c + 3]};
    BAR_L();
    f32x16 O = w2 ? OB : OA;
#pragma unroll
    for (int c = 0; c < 3; c++) { const f32x4 t = xb[((w2 ^ 1) * 3 + c) * 64 + lane]; O[4 * c] += t[0]; O[4 * c + 1] += t[1]; O[4 * c + 2] += t[2]; O[4 * c + 3] += t[3]; }
    const float inv = 1.0f / from_upper(O[8]);
    const unsigned ob = WS_OB + (tokA * DM + (hp * 2 + w2) * DH + 4 * h) * 2;
#pragma unroll
    for (int c = 0; c < 3; c++) if (c < 2 || !h) __builtin_amdgcn_raw_buffer_store_b64((u32x2){pk(O[4 * c] * inv, O[4 * c + 1] * inv), pk(O[4 * c + 2] * inv, O[4 * c + 3] * inv)}, wsr, ob + c * 16, 0, 16);
  }
  WAIT_BAR(0);
  if (tid == 0) __hip_atomic_store((gu64*)(ws + WS_FOB) + rt * 16 + hp, tag, RLX_AGENT);
  STAMP(8);
  auto outproj = [&](f32x4 (&c)[5]) {
#pragma unroll
    for (int t = 0; t < 5; t++) c[t] = (f32x4){0.f, 0.f, 0.f, 0.f};
#pragma unroll
    for (int ks = 0; ks < 10; ks++) {
      const int gr = w2 * 10 + ks;
      const LAS char* wb = lp + (gr < 16 ? gr * 5120 : L_WO_HI + (gr - 16) * 5120) + wlane;
#pragma unroll
      for (int t = 0; t < 5; t++) c[t] = mfma16(trd2(wb + t * 8, wb + 1280 + t * 8), of[ks], c[t]);
    }
  };
  f32x4 ca[5], cbb[5];
  outproj(ca);
  BAR_L();
  wo_stage(woB);
  STAMP(9);
  wait_flags<16>(ws, WS_FOB + rt * 128, tag, lane);
#pragma unroll
  for (int ks = 0; ks < 10; ks++) of[ks] = __builtin_amdgcn_raw_buffer_load_b128(wsr, WS_OB + ofr + ks * 64, 0, 16);
  WAIT_BAR(0);
  STAMP(10);
  outproj(cbb);
  { LAS f32x4* xb = (LAS f32x4*)(lp + L_XCH) + w3 * (64 * 5);
    f32x4 v[5];
#pragma unroll
    for (int t = 0; t < 5; t++) v[t] = ca[t] * gt + cbb[t] * (1.0f - gt);
#pragma unroll
    for (int t = 0; t < 5; t++) if ((t < 3) == (w2 == 1)) xb[t * 64 + lane] = v[t];
    BAR_L();
    u16* op = out + (size_t)R0 * DM + C0 + g * 20;
#pragma unroll
    for (int t = 0; t < 5; t++) if ((t < 3) == (w2 == 0)) { const f32x4 s = v[t] + xb[t * 64 + lane]; *(u32x2*)(op + t * 4) = (u32x2){pk(s[0], s[1]), pk(s[2], s[3])}; }
  }
  STAMP(11);
}
}

extern "C" void kernel_launch(void* const* d_in, const int* in_sizes, int n_in, void* d_out, int out_size, void* d_ws, size_t ws_size, hipStream_t stream) {
  using namespace gm;
  fused<<<256, 512, 0, stream>>>((const u16*)d_in[0], (const u16*)d_in[1], (const u16*)d_in[2], (const u16*)d_in[3], (const u16*)d_in[5], (const u16*)d_in[6],
                                 (const u16*)d_in[7], (const u16*)d_in[8], (const u16*)d_in[9], (u16*)d_out, (char*)d_ws);
}
```

```cpp
#include <hip/hip_runtime.h>
#include <hip/hip_bf16.h>
#include <stdint.h>

namespace gm {
typedef float f32x16 __attribute__((ext_vector_type(16)));
typedef float f32x4 __attribute__((ext_vector_type(4)));
typedef float f32x2 __attribute__((ext_vector_type(2)));
typedef short s16x4 __attribute__((ext_vector_type(4)));
typedef __bf16 b16x2 __attribute__((ext_vector_type(2)));
typedef __bf16 b16x8 __attribute__((ext_vector_type(8)));
typedef unsigned u32x4 __attribute__((ext_vector_type(4)));
typedef unsigned u32x2 __attribute__((ext_vector_type(2)));
typedef unsigned long long u64;
typedef unsigned short u16;
#define DI __device__ __forceinline__
#define LAS __attribute__((address_space(3)))
#define GAS __attribute__((address_space(1)))
typedef GAS u64 gu64;

constexpr int DM = 640, DH = 20;
constexpr unsigned WS_EPOCH = 0, WS_FKV = 2048, WS_FKVR = 4096, WS_FQ = 8192, WS_FPART = 16384, WS_FOA = 24576, WS_FOB = 40960, WS_G = 57344;
constexpr unsigned WS_KVR = 65536, KVR_B = 8192, WS_KV = 131072, WS_QX = 327680, QX_B = 10240, WS_PART = 1703936, PART_B = 13440;
constexpr unsigned WS_OA = 3670016, WS_OB = 6291456, WS_END = 8912896, OT_B = 20480;
constexpr int VB = 352, TILE_B = 1280 + 4 * VB, CH_B = TILE_B * 8, STEP_B = 2 * CH_B;
constexpr int STAGE_B = 36864, ST_W1 = 10240, ST_X = 20480;
constexpr int L_P0W = 36864, L_P0X0 = 62464, L_P0X1 = 147456, L_RED = 157696;
constexpr int L_HO = 86016, L_PHK = 96256, L_QL = 96256, L_VIM = 137216, L_KVT = 147456, L_WPHI = 158592, L_ZERO = 161792, L_TOTAL = 163840;
constexpr int L_WO = 43008, L_XCH = 0;
constexpr float LOG2E = 1.4426950408889634f;
constexpr float QSCALE = 0.22360679774997896f * LOG2E;
constexpr unsigned SPIN_CAP = 20000;

DI unsigned pk(float a, float b) { f32x2 v = {a, b}; b16x2 r = __builtin_convertvector(v, b16x2); return __builtin_bit_cast(unsigned, r); }
DI f32x16 mfma32(u32x4 a, u32x4 b, f32x16 c) { return __builtin_amdgcn_mfma_f32_32x32x16_bf16(__builtin_bit_cast(b16x8, a), __builtin_bit_cast(b16x8, b), c, 0, 0, 0); }
DI f32x16 mfma32k8(u32x2 a, u32x2 b, f32x16 c) { return __builtin_amdgcn_mfma_f32_32x32x8bf16_1k(__builtin_bit_cast(s16x4, a), __builtin_bit_cast(s16x4, b), c, 0, 0, 0); }
DI f32x4 mfma16(u32x4 a, u32x4 b, f32x4 c) { return __builtin_amdgcn_mfma_f32_16x16x32_bf16(__builtin_bit_cast(b16x8, a), __builtin_bit_cast(b16x8, b), c, 0, 0, 0); }
DI u32x2 trd(const LAS char* p) { return __builtin_bit_cast(u32x2, __builtin_amdgcn_ds_read_tr16_b64_v4i16((LAS s16x4*)p)); }
DI u32x4 trd2(const LAS char* p0, const LAS char* p1) { u32x2 a = trd(p0), b = trd(p1); return (u32x4){a.x, a.y, b.x, b.y}; }
DI u32x4 ldsr16(const LAS char* p) { return *(const LAS u32x4*)p; }
DI u32x2 ldsr8(const LAS char* p) { return *(const LAS u32x2*)p; }
DI float ex2(float x) { return __builtin_amdgcn_exp2f(x); }
DI void dma16s(const void* base, unsigned voff, unsigned lds_base) {
  asm volatile("s_mov_b32 m0, %2\n\ts_nop 0\n\tglobal_load_lds_dwordx4 %0, %1" :: "v"(voff), "s"(base), "s"(lds_base) : "memory"); }
DI void dma16s_sc1(const void* base, unsigned voff, unsigned lds_base) {
  asm volatile("s_mov_b32 m0, %2\n\ts_nop 0\n\tglobal_load_lds_dwordx4 %0, %1 sc1" :: "v"(voff), "s"(base), "s"(lds_base) : "memory"); }
DI void dma4s(const void* base, unsigned voff, unsigned lds_base) {
  asm volatile("s_mov_b32 m0, %2\n\ts_nop 0\n\tglobal_load_lds_dword %0, %1" :: "v"(voff), "s"(base), "s"(lds_base) : "memory"); }
DI u32x4 ld16_async(const void* p) { u32x4 r; asm volatile("global_load_dwordx4 %0, %1, off" : "=v"(r) : "v"(p) : "memory"); return r; }
DI u32x2 ld8_sc1_async(const void* p) { u32x2 r; asm volatile("global_load_dwordx2 %0, %1, off sc1" : "=v"(r) : "v"(p) : "memory"); return r; }
#define BAR_L() asm volatile("s_waitcnt lgkmcnt(0)\n\ts_barrier" ::: "memory")
#define WAIT_BAR(N) asm volatile("s_waitcnt vmcnt(" #N ") lgkmcnt(0)\n\ts_barrier" ::: "memory")
#define WAIT_VM(N) asm volatile("s_waitcnt vmcnt(" #N ")" ::: "memory")
#define SBAR() __builtin_amdgcn_sched_barrier(0)
#define RLX_AGENT __ATOMIC_RELAXED, __HIP_MEMORY_SCOPE_AGENT
DI float half_swap_max(float m) { auto rr = __builtin_amdgcn_permlane32_swap(__float_as_uint(m), __float_as_uint(m), false, false); return fmaxf(__uint_as_float(rr[0]), __uint_as_float(rr[1])); }
DI float from_upper(float v) { auto rr = __builtin_amdgcn_permlane32_swap(__float_as_uint(v), __float_as_uint(v), false, false); return __uint_as_float(rr[1]); }

template <int NF>
DI bool flags_set(const char* ws, unsigned off, u64 tag, int lane) {
  gu64* f = (gu64*)(ws + off);
  bool ok = __hip_atomic_load(f + (NF < 64 ? (lane & (NF - 1)) : lane), RLX_AGENT) == tag;
  if (NF >= 128) ok &= __hip_atomic_load(f + 64 + lane, RLX_AGENT) == tag;
  if (NF >= 256) { ok &= __hip_atomic_load(f + 128 + lane, RLX_AGENT) == tag; ok &= __hip_atomic_load(f + 192 + lane, RLX_AGENT) == tag; }
  return ok;
}
template <int NF>
DI void wait_flags(const char* ws, unsigned off, u64 tag, int lane) {
#pragma unroll 1
  for (unsigned spin = 0; spin < SPIN_CAP; ++spin) {
    if (__all(flags_set<NF>(ws, off, tag, lane))) break;
    __builtin_amdgcn_s_sleep(2);
  }
  asm volatile("" ::: "memory");
}
DI void set_flag(char* ws, unsigned off, u64 tag, int lane) { if (lane == 0) __hip_atomic_store((gu64*)(ws + off), tag, RLX_AGENT); }
DI unsigned nz(unsigned v) { return v < 0xFFFFFFFEu ? v : 0xFFFFFFFEu; }
DI unsigned umax3(unsigned a, unsigned b, unsigned c) { unsigned m = a > b ? a : b; return m > c ? m : c; }
DI bool missing(u32x4 v) { return __any(umax3(umax3(v.x, v.y, v.z), v.w, 0u) == 0xFFFFFFFFu); }
#define ONES ((u32x4){0xFFFFFFFFu, 0xFFFFFFFFu, 0xFFFFFFFFu, 0xFFFFFFFFu})
#define ST16(v, off) __builtin_amdgcn_raw_buffer_store_b128((v), wsr, (off), 0, 16)
#define ST8(v, off) __builtin_amdgcn_raw_buffer_store_b64((v), wsr, (off), 0, 16)
#define LD16(off) __builtin_amdgcn_raw_buffer_load_b128(wsr, (off), 0, 16)
#define LD8(off) __builtin_amdgcn_raw_buffer_load_b64(wsr, (off), 0, 16)

#define STAMP(i) do {} while (0)

__global__ __launch_bounds__(512, 2) void fused(const u16* __restrict__ x, const u16* __restrict__ wqkv, const u16* __restrict__ wphi, const u16* __restrict__ woA,
                                                const u16* __restrict__ wq, const u16* __restrict__ wk, const u16* __restrict__ wv, const u16* __restrict__ woB,
                                                const u16* __restrict__ gatew, u16* __restrict__ out, char* ws) {
  __shared__ __attribute__((aligned(16))) char lds[L_TOTAL];
  const int tid = threadIdx.x, lane = tid & 63;
  const int w = __builtin_amdgcn_readfirstlane(tid >> 6) & 7;
  const int l15 = lane & 15, g = lane >> 4, q4 = l15 >> 2, p4 = lane & 3, r = lane & 31, h = lane >> 5;
  const int bx = blockIdx.x & 7, bi = blockIdx.x >> 3;
  const int rt = (bx & 1) * 8 + (bi & 7), hp = (bx >> 1) * 4 + ((bi >> 3) & 3), hq = hp >> 1, role = hp & 1;
  const int w3 = w & 3, w2 = w >> 2;
  LAS char* lp = (LAS char*)lds;
  const unsigned lds0 = (unsigned)(uintptr_t)lds;
  const __amdgpu_buffer_rsrc_t wsr = __builtin_amdgcn_make_buffer_rsrc(ws, 0, WS_END, 0x00020000);
  STAMP(0);

  const int T0 = rt * 128 + hq * 16;
  const int C0 = hq * 80;
  const int wlane = ((g >> 1) * 16 + (g & 1) * 4 + q4) * 160 + p4 * 40;
  u64 tag;
  unsigned o0[10], o1[10];
#define KVP(s, slot, p) dma16s_sc1(ws + WS_KV + (s) * STEP_B + (p) * 1024, lane * 16, lds0 + (slot) * STEP_B + (p) * 1024)
#define KVL(s, slot) do { _Pragma("unroll") for (int j = 0; j < 11; j++) if (j < 10 || w3 < 2) KVP(s, slot, w3 + 4 * j); } while (0)
  const int qr = w2 * 16 + l15;
  if (w2 == 0) {
    u32x4 gf0, gf1, gf2, gf3, gf4, wpc, tch;
    u32x2 ep = ld8_sc1_async(ws + WS_EPOCH);
    const int lx = w3 < 2 ? L_P0X0 + w3 * 5120 : L_P0X1 + (w3 - 2) * 5120;
    { const u16* wsrc = (role ? wv : wk) + w3 * 3200;
#pragma unroll
      for (int j = 0; j < 5; j++) { const int n = j * 64 + lane, row = (n * 3277) >> 16, cc = n - row * 20; dma16s(x + T0 * DM + w3 * 160, row * 1280 + cc * 16, lds0 + lx + j * 1024); }
#pragma unroll
      for (int j = 0; j < 6; j++) dma16s(wsrc + j * 512, lane * 16, lds0 + L_P0W + w3 * 6400 + j * 1024);
      if (lane < 16) dma16s(wsrc + 6 * 512, lane * 16, lds0 + L_P0W + w3 * 6400 + 6 * 1024);
      const u16* gp = gatew + w3 * 160 + g * 8;
      gf0 = ld16_async(gp); gf1 = ld16_async(gp + 32); gf2 = ld16_async(gp + 64); gf3 = ld16_async(gp + 96); gf4 = ld16_async(gp + 128);
      wpc = ld16_async(wphi + hq * 1600 + (tid < 200 ? tid : 199) * 8);
      const char* tp = ws + (lane & 7) * 1114112;
      if (w3 == 1) tp = (const char*)(woA + C0 + (lane & 7) * 51200);
      if (w3 == 2) tp = (const char*)(woB + C0 + (lane & 7) * 51200);
      if (w3 == 3) tp = (const char*)(out + (rt * 128 + role * 64) * DM + C0);
      tch = ld16_async(tp);
    }
    asm volatile("s_waitcnt vmcnt(1)" : "+v"(gf0), "+v"(gf1), "+v"(gf2), "+v"(gf3), "+v"(gf4), "+v"(wpc), "+v"(ep) :: "memory");
    tag = (((u64)(unsigned)__builtin_amdgcn_readfirstlane(ep.y) << 32) | (unsigned)__builtin_amdgcn_readfirstlane(ep.x)) + 1ull;
    STAMP(1);
    { const LAS char* wb = lp + L_P0W + (w3 * 160 + g * 8 + q4) * 40;
      const LAS char* xb = lp + lx + l15 * 320 + g * 16;
      const bool gl = l15 == 4;
      f32x4 c0 = {}, c1 = {};
#define P0STEP(s, GF) { const u32x4 b = ldsr16(xb + (s) * 64); c0 = mfma16(trd2(wb + p4 * 8 + (s) * 1280, wb + p4 * 8 + (s) * 1280 + 160), b, c0); \
        u32x4 a = trd2(wb + 32 + (s) * 1280, wb + 32 + (s) * 1280 + 160); if (gl) a = GF; c1 = mfma16(a, b, c1); }
      P0STEP(0, gf0) P0STEP(1, gf1) P0STEP(2, gf2) P0STEP(3, gf3) P0STEP(4, gf4)
#undef P0STEP
      *(LAS f32x4*)(lp + L_RED + tid * 16) = c0;
      if (lane < 32) *(LAS f32x4*)(lp + L_RED + 4096 + (w3 * 32 + lane) * 16) = c1; }
    STAMP(2);
    f32x4 acc[2][2][5];
#pragma unroll
    for (int a = 0; a < 4; a++)
#pragma unroll
      for (int b = 0; b < 5; b++) acc[a >> 1][a & 1][b] = (f32x4){0.f, 0.f, 0.f, 0.f};
    const int xo0 = ST_X + (w3 * 32 + l15) * 128 + ((g ^ (l15 >> 1)) * 16), xo1 = xo0 ^ 64;
    u32x4 fa[10], fb[10], xa0, xa1, xb0, xb1;
#pragma unroll
    for (int j = 0; j < 10; j++) fb[j] = (u32x4){0u, 0u, 0u, 0u};
    xb0 = xb1 = (u32x4){0u, 0u, 0u, 0u};
#define FRD(F, j, s) F[j] = trd2(wb + ((j) / 5) * ST_W1 + (s) * 5120 + ((j) % 5) * 8, wb + ((j) / 5) * ST_W1 + (s) * 5120 + 1280 + ((j) % 5) * 8)
#define FMM(F, j, X0, X1) acc[0][(j) / 5][(j) % 5] = mfma16(F[j], X0, acc[0][(j) / 5][(j) % 5]); acc[1][(j) / 5][(j) % 5] = mfma16(F[j], X1, acc[1][(j) / 5][(j) % 5])
#pragma unroll 1
    for (int i = 0; i < 10; i++) {
      BAR_L();
      if (i == 1 && role) {
        const unsigned qb = WS_QX + (rt * 8 + hq) * QX_B + w3 * 1280 + lane * 16;
        ST16(ONES, qb); ST16(ONES, qb + 5120); if (lane < 32) ST16(ONES, qb + 1024 + (lane >> 4) * (5120 - 256)); }
      if (i == 4) { asm volatile("s_waitcnt vmcnt(0)" : "+v"(tch) :: "memory"); if (role) set_flag(ws, WS_FQ + ((rt * 8 + hq) * 4 + w3) * 8, tag, lane);
                    if (tid < 200) *(LAS u32x4*)(lp + L_WPHI + tid * 16) = wpc; }
      const LAS char* sb = lp + ((i + 2) & 3) * STAGE_B;
      const LAS char* wb = sb + wlane;
      xa0 = ldsr16(sb + xo0); xa1 = ldsr16(sb + xo0 + 2048); SBAR();
#pragma unroll
      for (int j = 0; j < 10; j++) { FRD(fa, j, 0); FMM(fb, j, xb0, xb1); SBAR(); }
      xb0 = ldsr16(sb + xo1); xb1 = ldsr16(sb + xo1 + 2048); SBAR();
#pragma unroll
      for (int j = 0; j < 10; j++) { if (j < 4) { FRD(fb, 2 * j, 1); FRD(fb, 2 * j + 1, 1); } else if (j < 6) { FRD(fb, 4 + j, 1); } FMM(fa, j, xa0, xa1); SBAR(); }
    }
#pragma unroll
    for (int j = 0; j < 10; j++) { FMM(fb, j, xb0, xb1); }
#undef FRD
#undef FMM
    STAMP(3);
    const float sc = role ? QSCALE : 1.0f;
    unsigned u0[10], u1[10];
#pragma unroll
    for (int t = 0; t < 5; t++) {
      o1[2 * t] = nz(pk(acc[0][1][t][0] * sc, acc[0][1][t][1] * sc)); o1[2 * t + 1] = nz(pk(acc[0][1][t][2] * sc, acc[0][1][t][3] * sc));
      u1[2 * t] = nz(pk(acc[1][1][t][0] * sc, acc[1][1][t][1] * sc)); u1[2 * t + 1] = nz(pk(acc[1][1][t][2] * sc, acc[1][1][t][3] * sc)); }
    const int qfo = ((g & 1) * 4 + w3) * 1280;
    if (role && g < 2) { const unsigned qb = WS_QX + (rt * 8 + hq) * QX_B + qfo;
      ST16(((u32x4){o1[0], o1[1], o1[2], o1[3]}), qb + l15 * 16); ST16(((u32x4){u1[0], u1[1], u1[2], u1[3]}), qb + 256 + l15 * 16);
      ST16(((u32x4){o1[4], o1[5], o1[6], o1[7]}), qb + 512 + l15 * 16); ST16(((u32x4){u1[4], u1[5], u1[6], u1[7]}), qb + 768 + l15 * 16);
      ST8(((u32x2){o1[8], o1[9]}), qb + 1024 + l15 * 8); ST8(((u32x2){u1[8], u1[9]}), qb + 1152 + l15 * 8); }
#pragma unroll
    for (int t = 0; t < 5; t++) {
      u0[2 * t] = pk(acc[1][0][t][0], acc[1][0][t][1]); u0[2 * t + 1] = pk(acc[1][0][t][2], acc[1][0][t][3]);
      o0[2 * t] = pk(acc[0][0][t][0], acc[0][0][t][1]); o0[2 * t + 1] = pk(acc[0][0][t][2], acc[0][0][t][3]); }
    *(LAS u32x4*)(lp + L_HO + tid * 16) = (u32x4){u0[0], u0[1], u0[2], u0[3]}; *(LAS u32x4*)(lp + L_HO + 4096 + tid * 16) = (u32x4){u0[4], u0[5], u0[6], u0[7]};
    *(LAS u32x2*)(lp + L_HO + 8192 + tid * 8) = (u32x2){u0[8], u0[9]};
    BAR_L();
    if (role) {
      if (g >= 2) { LAS char* d = lp + L_QL + qfo;
        *(LAS u32x4*)(d + l15 * 16) = (u32x4){o1[0], o1[1], o1[2], o1[3]}; *(LAS u32x4*)(d + 256 + l15 * 16) = (u32x4){u1[0], u1[1], u1[2], u1[3]};
        *(LAS u32x4*)(d + 512 + l15 * 16) = (u32x4){o1[4], o1[5], o1[6], o1[7]}; *(LAS u32x4*)(d + 768 + l15 * 16) = (u32x4){u1[4], u1[5], u1[6], u1[7]};
        *(LAS u32x2*)(d + 1024 + l15 * 8) = (u32x2){o1[8], o1[9]}; *(LAS u32x2*)(d + 1152 + l15 * 8) = (u32x2){u1[8], u1[9]}; }
    } else { LAS char* d = lp + L_VIM + (g * 128 + w3 * 32 + l15) * 40;
#pragma unroll
      for (int t = 0; t < 5; t++) { *(LAS u32x2*)(d + t * 8) = (u32x2){o1[2 * t], o1[2 * t + 1]}; *(LAS u32x2*)(d + 640 + t * 8) = (u32x2){u1[2 * t], u1[2 * t + 1]}; } }
  } else {
    u32x2 ep = ld8_sc1_async(ws + WS_EPOCH);
    const bool second = w3 >> 1;
    const unsigned rs = (second && role) ? 1280u : 3840u;
    const char* wsrc = (second ? (role ? (const char*)(wq + C0) : (const char*)(wqkv + 1280 + C0)) : (const char*)(wqkv + (role ? 0 : 640) + C0)) + (w3 & 1) * 32 * rs;
    const char* xsrc = (const char*)(x + (rt * 128 + w3 * 32) * DM);
    int rot = (bi & 7) + 3 * ((bi >> 3) & 3); rot = rot >= 10 ? rot - 10 : rot;
    unsigned so[5], xoff[2];
#pragma unroll
    for (int j = 0; j < 5; j++) { const int c = j * 64 + lane, rho = (c * 6554) >> 16, cc = c - rho * 10; const int k = (rho & ~12) | ((rho & 4) << 1) | ((rho & 8) >> 1); so[j] = k * rs + cc * 16; }
#pragma unroll
    for (int j = 0; j < 2; j++) { const int row = j * 8 + (lane >> 3); xoff[j] = row * 1280 + (((lane & 7) ^ ((row >> 1) & 7)) * 16); }
    auto stage = [&](int st) {
      const unsigned lb = lds0 + ((st + 2) & 3) * STAGE_B;
      int kc = st + rot; kc = kc >= 10 ? kc - 10 : kc;
      const char* bw = wsrc + kc * 64 * rs; const char* bxp = xsrc + kc * 128;
#pragma unroll
      for (int j = 0; j < 5; j++) { dma16s(bw, so[j], lb + (w3 * 5 + j) * 1024); if (j < 4) dma16s(bxp + (j >> 1) * 16 * 1280, xoff[j & 1], lb + ST_X + (w3 * 4 + j) * 1024); }
    };
    stage(0); stage(1); stage(2);
    asm volatile("s_waitcnt vmcnt(18)" : "+v"(ep) :: "memory");
    tag = (((u64)(unsigned)__builtin_amdgcn_readfirstlane(ep.y) << 32) | (unsigned)__builtin_amdgcn_readfirstlane(ep.x)) + 1ull;
    STAMP(1); STAMP(2);
#pragma unroll 1
    for (int i = 0; i < 7; i++) {
      WAIT_BAR(18);
      if (i == 0 && w3 == 0) {
        const unsigned tb = WS_KV + (T0 >> 5) * TILE_B; const int s16 = hq & 1;
        if (!role) {
          const LAS f32x4* rp = (const LAS f32x4*)(lp + L_RED) + (h ? 256 + (g & 1) * 16 : g * 32) + l15;
          const int st = h ? 32 : 64, o2 = h ? 0 : 16;
          const f32x4 a = (rp[0] + rp[st]) + (rp[2 * st] + rp[3 * st]), b = (rp[o2] + rp[st + o2]) + (rp[2 * st + o2] + rp[3 * st + o2]);
          const u32x4 v = {pk(a[0], a[1]), pk(a[2], a[3]), pk(b[0], b[1]), pk(b[2], b[3])};
          if (lane < 32) ST16(v, tb + (g * 32 + s16 * 16 + l15) * 16);
          else if (lane < 48) ST8(((u32x2){v.x, v.y}), tb + 1024 + (s16 * 16 + l15) * 8);
          else { const float gv = 1.0f / (1.0f + __expf(-a[0])); __builtin_amdgcn_raw_buffer_store_b32(__float_as_uint(gv), wsr, WS_G + (T0 + l15) * 4, 0, 16); }
        } else if (lane < 44) {
          const int hh = lane >= 22, d = lane - hh * 22, dd = d < 20 ? d : 0, st = dd >= 16 ? 128 : 256;
          const LAS float* rp = (const LAS float*)(lp + L_RED) + ((dd >> 4) * 256 + ((dd & 15) >> 2) * 16 + 4 * hh) * 4 + (dd & 3);
          float e[8];
#pragma unroll
          for (int j = 0; j < 8; j++) { const int tk = (8 * (j >> 2) + (j & 3)) * 4; e[j] = (rp[tk] + rp[tk + st]) + (rp[tk + 2 * st] + rp[tk + 3 * st]); }
          u32x4 v = {pk(e[0], e[1]), pk(e[2], e[3]), pk(e[4], e[5]), pk(e[6], e[7])};
          if (d == 20) v = (u32x4){0x3F803F80u, 0x3F803F80u, 0x3F803F80u, 0x3F803F80u};
          if (d == 21) v = (u32x4){0u, 0u, 0u, 0u};
          ST16(v, tb + 1280 + (s16 * 2 + hh) * VB + d * 16);
        }
      }
      if (i == 2) { if (w3 == 0) set_flag(ws, WS_FKV + blockIdx.x * 8, tag, lane);
                    if (w3 >= 2) *(LAS u32x4*)(lp + L_ZERO + (tid - 384) * 16) = (u32x4){0u, 0u, 0u, 0u}; }
      stage(i + 3);
    }
    WAIT_BAR(18);
    u64 fa, fb, fc, fd, fe;
    { gu64* f = (gu64*)(ws + WS_FKV) + lane;
      fa = __hip_atomic_load(f, RLX_AGENT); fb = __hip_atomic_load(f + 64, RLX_AGENT); fc = __hip_atomic_load(f + 128, RLX_AGENT); fd = __hip_atomic_load(f + 192, RLX_AGENT);
      fe = __hip_atomic_load((gu64*)(ws + WS_FQ) + (rt * 8 + hq) * 4 + p4, RLX_AGENT); }
    WAIT_BAR(14);
    asm volatile("" : "+v"(fa), "+v"(fb), "+v"(fc), "+v"(fd), "+v"(fe));
    const bool kvok = __all((fa == tag) & (fb == tag) & (fc == tag) & (fd == tag) & (fe == tag));
    asm volatile("" ::: "memory");
    if (kvok) KVL(0, 0);
    BAR_L();
    if (!kvok) { wait_flags<256>(ws, WS_FKV, tag, lane); wait_flags<4>(ws, WS_FQ + (rt * 8 + hq) * 32, tag, lane); KVL(0, 0); }
    KVL(1, 1);
    if (blockIdx.x == 0 && w3 == 0) set_flag(ws, WS_EPOCH, tag, lane);
    STAMP(3);
    BAR_L();
    const int ht = tid - 256;
    const u32x4 a = ldsr16(lp + L_HO + ht * 16), b = ldsr16(lp + L_HO + 4096 + ht * 16); const u32x2 c = ldsr8(lp + L_HO + 8192 + ht * 8);
    o0[0] = a.x; o0[1] = a.y; o0[2] = a.z; o0[3] = a.w; o0[4] = b.x; o0[5] = b.y; o0[6] = b.z; o0[7] = b.w; o0[8] = c.x; o0[9] = c.y;
  }
  STAMP(4);
  u32x4 ph[5];
  { const LAS char* pb = (p4 == g) ? lp + L_WPHI + g * 800 + q4 * 40 : lp + L_ZERO;
    const u32x4 b0 = {o0[0], o0[1], o0[2], o0[3]}, b1 = {o0[4], o0[5], o0[6], o0[7]}, b2 = {o0[8], o0[9], 0u, 0u};
    float p[20];
#pragma unroll
    for (int t = 0; t < 5; t++) {
      const u32x2 c2 = trd(pb + 640 + t * 8);
      f32x4 c = {}; c = mfma16(trd2(pb + t * 8, pb + 160 + t * 8), b0, c); c = mfma16(trd2(pb + 320 + t * 8, pb + 480 + t * 8), b1, c); c = mfma16((u32x4){c2.x, c2.y, 0u, 0u}, b2, c);
      p[4 * t] = c[0]; p[4 * t + 1] = c[1]; p[4 * t + 2] = c[2]; p[4 * t + 3] = c[3];
    }
    float mx = p[0], mn = p[0];
#pragma unroll
    for (int f = 1; f < 20; f++) { mx = fmaxf(mx, p[f]); mn = fminf(mn, p[f]); }
    const float ma = -mx * LOG2E, mb = mn * LOG2E;
    float ea[20], eb[20], sa = 0.f, sb = 0.f;
#pragma unroll
    for (int f = 0; f < 20; f++) { ea[f] = ex2(fmaf(p[f], LOG2E, ma)); sa += ea[f]; eb[f] = ex2(fmaf(p[f], -LOG2E, mb)); sb += eb[f]; }
    const float ia = __builtin_amdgcn_rcpf(sa), ib = __builtin_amdgcn_rcpf(sb);
    unsigned o[20];
#pragma unroll
    for (int f = 0; f < 10; f++) { o[f] = pk(ea[2 * f] * ia, ea[2 * f + 1] * ia); o[10 + f] = pk(eb[2 * f] * ib, eb[2 * f + 1] * ib); }
#pragma unroll
    for (int c = 0; c < 5; c++) ph[c] = (u32x4){o[4 * c], o[4 * c + 1], o[4 * c + 2], o[4 * c + 3]};
  }
  u32x4 q1A, q1B; u32x2 q2A, q2B;
  if (role) {
    WAIT_BAR(0);
    STAMP(5);
    const LAS char* qa = lp + L_QL + w3 * 1280;
    q1A = ldsr16(qa + lane * 16); q1B = ldsr16(qa + 5120 + lane * 16); q2A = ldsr8(qa + 1024 + r * 8); q2B = ldsr8(qa + 6144 + r * 8);
    BAR_L();
  } else {
    const unsigned qb = WS_QX + (rt * 8 + hq) * QX_B + w3 * 1280;
    q1A = LD16(qb + lane * 16); q1B = LD16(qb + 5120 + lane * 16); q2A = LD8(qb + 1024 + r * 8); q2B = LD8(qb + 6144 + r * 8);
    { LAS char* d = lp + L_PHK + (g * 128 + w3 * 32 + qr) * 80;
#pragma unroll
      for (int c = 0; c < 5; c++) *(LAS u32x4*)(d + c * 16) = ph[c]; }
    BAR_L();
    STAMP(5);
    { const int hd = w >> 1, dt = w & 1;
      const LAS char* ab = lp + L_PHK + (hd * 128 + g * 8 + q4) * 80 + p4 * 8;
      const LAS char* bb = lp + L_VIM + (hd * 128 + g * 8 + q4) * 40 + dt * 32 + p4 * 8;
      const bool ones = dt && l15 == 4;
      f32x4 c[3] = {};
#pragma unroll
      for (int ks = 0; ks < 4; ks++) {
        u32x4 b = trd2(bb + ks * 32 * 40, bb + (ks * 32 + 4) * 40);
        if (ones) b = (u32x4){0x3F803F80u, 0x3F803F80u, 0x3F803F80u, 0x3F803F80u};
#pragma unroll
        for (int ft = 0; ft < 3; ft++) c[ft] = mfma16(trd2(ab + ks * 32 * 80 + ft * 32, ab + (ks * 32 + 4) * 80 + ft * 32), b, c[ft]);
      }
      asm volatile("" : "+v"(c[0]), "+v"(c[1]), "+v"(c[2]), "+v"(q1A), "+v"(q1B), "+v"(q2A), "+v"(q2B) :: "memory");
#pragma unroll 1
      for (unsigned spin = 0; spin < SPIN_CAP; ++spin) {
        if (!(missing(q1A) | missing(q1B) | missing((u32x4){q2A.x, q2A.y, q2B.x, q2B.y}))) break;
        __builtin_amdgcn_s_sleep(1);
        asm volatile("" ::: "memory");
        q1A = LD16(qb + lane * 16); q1B = LD16(qb + 5120 + lane * 16); q2A = LD8(qb + 1024 + r * 8); q2B = LD8(qb + 6144 + r * 8);
      }
      asm volatile("" : "+v"(q1A), "+v"(q1B), "+v"(q2A), "+v"(q2B) :: "memory");
      const int d = dt * 16 + l15;
#pragma unroll
      for (int ft = 0; ft < 3; ft++) if (d < 21 && ft * 4 + g < 10) ST16(__builtin_bit_cast(u32x4, c[ft]), WS_PART + (hq * 16 + rt) * PART_B + ((hd * 10 + ft * 4 + g) * 21 + d) * 16);
    }
    WAIT_BAR(3);
  }
  STAMP(6);
  const int vr = r <= 21 ? r : 21;
  const int ok1 = lane * 16, ok2 = h ? (1280 + 20 * 16) : (1024 + r * 8), ov = 1280 + h * VB + vr * 16;
  const f32x16 Z = {};
  if (h) { q2A = (u32x2){0u, 0u}; q2B = (u32x2){0u, 0u}; }
  { const LAS char* t0 = lp;
    const u32x4 k1 = ldsr16(t0 + ok1); const u32x2 k2 = ldsr8(t0 + ok2);
    f32x16 sa = mfma32(k1, q1A, Z); sa = mfma32k8(k2, q2A, sa);
    f32x16 sb = mfma32(k1, q1B, Z); sb = mfma32k8(k2, q2B, sb);
    float ma = sa[0], mb = sb[0];
#pragma unroll
    for (int i = 1; i < 16; i++) { ma = fmaxf(ma, sa[i]); mb = fmaxf(mb, sb[i]); }
    ma = half_swap_max(ma); mb = half_swap_max(mb);
    const float mah = __uint_as_float(__float_as_uint(ma) & 0xFFFF0000u), mbh = __uint_as_float(__float_as_uint(mb) & 0xFFFF0000u);
    if (h) { q2A.x = pk(-mah, mah - ma); q2B.x = pk(-mbh, mbh - mb); } }
  f32x16 OA = {}, OB = {}, SA, SB;
  u32x4 k1, v1, v2, v2n, pA1, pA2, pB1, pB2; u32x2 k2;
  float e0, e1, e2, e3;
  const LAS char* c0p = lp + w2 * CH_B + ok1; const LAS char* c1p = lp + w2 * CH_B + ok2; const LAS char* c2p = lp + w2 * CH_B + ov;
  k1 = ldsr16(c0p); k2 = ldsr8(c1p);
  SA = mfma32(k1, q1A, Z); SA = mfma32k8(k2, q2A, SA);
  v1 = ldsr16(c2p); v2 = v1;
  e0 = e1 = e2 = e3 = 0.f; pB2 = (u32x4){0u, 0u, 0u, 0u};
#define G(MF, LD, C0_, C1_, X, S_, i) MF; LD; SBAR(); C0_; C1_; SBAR(); X##0 = ex2(S_[i]); X##1 = ex2(S_[i + 1]); X##2 = ex2(S_[i + 2]); X##3 = ex2(S_[i + 3]); SBAR();
#define TILE(TB, TN, V2, H0, H1, H2) { float a0, a1, a2, a3; \
    G(SB = mfma32(k1, q1B, Z),    k1 = ldsr16(c0p + (TN)), pB2[2] = pk(e0, e1), pB2[3] = pk(e2, e3), a, SA, 0) \
    G(OB = mfma32(v2, pB2, OB),   V2,                      pA1[0] = pk(a0, a1), pA1[1] = pk(a2, a3), e, SA, 4) \
    G(SB = mfma32k8(k2, q2B, SB), k2 = ldsr8(c1p + (TN)),  pA1[2] = pk(e0, e1), pA1[3] = pk(e2, e3), a, SA, 8) \
    G(OA = mfma32(v1, pA1, OA),   H0,                      pA2[0] = pk(a0, a1), pA2[1] = pk(a2, a3), e, SA, 12) \
    G(SA = mfma32(k1, q1A, Z),    H1,                      pA2[2] = pk(e0, e1), pA2[3] = pk(e2, e3), a, SB, 0) \
    G(OA = mfma32(v2, pA2, OA),   H2,                      pB1[0] = pk(a0, a1), pB1[1] = pk(a2, a3), e, SB, 4) \
    G(SA = mfma32k8(k2, q2A, SA), (void)0,                 pB1[2] = pk(e0, e1), pB1[3] = pk(e2, e3), a, SB, 8) \
    G(OB = mfma32(v1, pB1, OB),   v1 = ldsr16(c2p + (TN)), pB2[0] = pk(a0, a1), pB2[1] = pk(a2, a3), e, SB, 12) }
#define NOP (void)0
#define TL(sl, t, H0, H1, H2) TILE((sl) * STEP_B + (t) * TILE_B, (sl) * STEP_B + ((t) + 1) * TILE_B, v2 = ldsr16(c2p + (sl) * STEP_B + (t) * TILE_B + 2 * VB), H0, H1, H2)
#define TL6(sl, H0, H1) TL(sl, 6, H0, H1, v2n = ldsr16(c2p + (sl) * STEP_B + 7 * TILE_B + 2 * VB))
#define TL7(sl, sn, H0, H1, H2) TILE((sl) * STEP_B + 7 * TILE_B, (sn) * STEP_B, v2 = v2n, H0, H1, H2)
  unsigned woff[5];
#pragma unroll
  for (int j = 0; j < 5; j++) { const int c = j * 64 + lane, rho = (c * 6554) >> 16, cc = c - rho * 10; const int k = (rho & ~12) | ((rho & 4) << 1) | ((rho & 8) >> 1); woff[j] = k * 1280 + cc * 16; }
#define WOP(wo, gr, j, dst) dma16s((wo) + C0 + (gr) * 32 * 640, woff[j], lds0 + (dst) + (j) * 1024)
#define WA(gr, j) WOP(woA, gr, j, L_WO + (gr) * 5120)
#define WX(j) do { if (w < 4) WA(16 + w3, j); else if (w < 7) WOP(woB, 17 + w3, j, 145408 + w3 * 5120); } while (0)
#define KW(s, slot, j) KVP(s, slot, w * 5 + (j))
#define KX(s, slot) do { if (w < 2) KVP(s, slot, 40 + w); } while (0)
#define PRIO(n) __builtin_amdgcn_s_setprio(n)
#define FILL(j) do { if (role) { const int n_ = w * 3 + (j); ST16(ONES, WS_OB + (rt * 8 + hq) * OT_B + (n_ < 20 ? n_ : 19) * 1024 + lane * 16); } } while (0)
  const bool red = !role && w == 0;
  const int slot = rt * 53 + lane;
  f32x4 part[16];
  auto oa_part = [&]() {
    const LAS char* kb = (q4 == g) ? lp + L_KVT + g * 1920 + p4 * 80 : lp + L_ZERO;
    f32x4 o[6]; u32x4 ka[6], kc[6];
#pragma unroll
    for (int t = 0; t < 6; t++) { o[t] = (f32x4){0.f, 0.f, 0.f, 0.f}; ka[t] = ldsr16(kb + t * 320); }
    SBAR();
#pragma unroll
    for (int t = 0; t < 6; t++) kc[t] = ldsr16(kb + t * 320 + 16);
    SBAR();
#pragma unroll
    for (int t = 0; t < 6; t++) { o[t] = mfma16(ka[t], ph[0], o[t]); ka[t] = ldsr16(kb + t * 320 + 32); SBAR(); }
#pragma unroll
    for (int t = 0; t < 6; t++) { o[t] = mfma16(kc[t], ph[1], o[t]); kc[t] = ldsr16(kb + t * 320 + 48); SBAR(); }
#pragma unroll
    for (int t = 0; t < 6; t++) { o[t] = mfma16(ka[t], ph[2], o[t]); ka[t] = ldsr16(kb + t * 320 + 64); SBAR(); }
#pragma unroll
    for (int t = 0; t < 6; t++) o[t] = mfma16(kc[t], ph[3], o[t]);
#pragma unroll
    for (int t = 0; t < 6; t++) o[5 - t] = mfma16(ka[5 - t], ph[4], o[5 - t]);
    const float inv = __builtin_amdgcn_rcpf(o[5][0] + 1e-6f);
    LAS char* tb = lp + L_WO + w * 5120;
#pragma unroll
    for (int t = 0; t < 5; t++) { const int k0 = g * 20 + 4 * t;
      *(LAS u32x2*)(tb + (k0 >> 3) * 256 + (k0 & 4) * 2 + l15 * 16) = (u32x2){pk(o[t][0] * inv, o[t][1] * inv), pk(o[t][2] * inv, o[t][3] * inv)}; }
    const unsigned oa = WS_OA + (rt * 8 + w3 * 2 + w2) * OT_B + hq * 2560 + lane * 16;
    const u32x4 z0 = ldsr16(tb + lane * 16), z1 = ldsr16(tb + 1024 + lane * 16), z2 = ldsr16(tb + 2048 + r * 16);
    ST16(z0, oa); ST16(z1, oa + 1024); if (!h) ST16(z2, oa + 2048);
    SBAR();
  };
  PRIO(3);
  TL(0, 0, NOP, NOP, NOP)
  TL(0, 1, if (!role) { WAIT_VM(0); set_flag(ws, WS_FPART + ((hq * 16 + rt) * 8 + w) * 8, tag, lane); }, NOP, KX(2, 2))
  TL(0, 2, KW(2, 2, 0), NOP, KW(2, 2, 1)) TL(0, 3, KW(2, 2, 2), NOP, KW(2, 2, 3)) TL(0, 4, KW(2, 2, 4), NOP, FILL(0))
  TL(0, 5, FILL(1), NOP, FILL(2)) TL6(0, NOP, NOP)
  TL7(0, 1, if (red) { wait_flags<128>(ws, WS_FPART + hq * 1024, tag, lane); const int sc_ = slot < 840 ? slot : 839;
                      _Pragma("unroll") for (int j = 0; j < 16; j++) part[j] = __builtin_bit_cast(f32x4, LD16(WS_PART + (hq * 16 + j) * PART_B + sc_ * 16)); }, NOP, NOP)
  PRIO(2);
  TL(1, 0, NOP, NOP, NOP)
  TL(1, 1, if (red) { f32x4 a = ((part[0] + part[1]) + (part[2] + part[3])) + ((part[4] + part[5]) + (part[6] + part[7]));
                      a += ((part[8] + part[9]) + (part[10] + part[11])) + ((part[12] + part[13]) + (part[14] + part[15]));
                      const int hf = (slot * 3121) >> 16; const int d = slot - hf * 21; const int hd = (hf * 26) >> 8; const int fq = hf - hd * 10;
                      if (lane < 53 && slot < 840) ST8(((u32x2){pk(a[0], a[1]), pk(a[2], a[3])}), WS_KVR + hq * KVR_B + hd * 1920 + d * 80 + fq * 8);
                      WAIT_VM(0); set_flag(ws, WS_FKVR + (hq * 16 + rt) * 8, tag, lane); }, NOP, NOP)
  TL(1, 2, if (w == 5 || w == 6) { const int row = (w - 5) * 32 + (lane >> 1);
                      dma4s(woB + C0 + ((rt & 7) * 2 + role) * 40 * 640, (row < 40 ? row : 39) * 1280 + (lane & 1) * 156, lds0 + 145408); }, NOP, NOP)
  TL(1, 3, NOP, NOP, NOP)
  PRIO(1);
  TL(1, 4, if (role && w == 1) { wait_flags<16>(ws, WS_FKVR + hq * 128, tag, lane);
                      _Pragma("unroll") for (int p = 0; p < 8; p++) dma16s_sc1(ws + WS_KVR + hq * KVR_B + p * 1024, lane * 16, lds0 + L_KVT + p * 1024); }, NOP, NOP)
  TL(1, 5, NOP, NOP, NOP)
  PRIO(0);
  TL6(1, NOP, NOP)
  WAIT_BAR(0);
  STAMP(12);
  PRIO(3);
  TL7(1, 2, NOP, NOP, NOP)
  if (role && w2) oa_part();
  TL(2, 0, KX(3, 0), NOP, KW(3, 0, 0)) TL(2, 1, KW(3, 0, 1), NOP, KW(3, 0, 2)) TL(2, 2, KW(3, 0, 3), NOP, KW(3, 0, 4))
  PRIO(2);
  if (role && !w2) oa_part();
  TL(2, 3, NOP, NOP, WA(w, 0)) TL(2, 4, WA(w, 1), NOP, WA(w, 2))
  PRIO(1);
  TL(2, 5, WA(w, 3), NOP, WA(w, 4))
  PRIO(0);
  TL6(2, NOP, NOP)
  WAIT_BAR(5);
  STAMP(13);
  PRIO(3);
  if (role) set_flag(ws, WS_FOA + ((rt * 8 + hq) * 8 + w) * 8, tag, lane);
  TL7(2, 0, NOP, NOP, NOP)
  TL(0, 0, WA(8 + w, 0), NOP, WA(8 + w, 1)) TL(0, 1, WA(8 + w, 2), NOP, WA(8 + w, 3)) TL(0, 2, WA(8 + w, 4), NOP, WX(0))
  TL(0, 3, WX(1), NOP, WX(2))
  PRIO(2);
  TL(0, 4, WX(3), NOP, WX(4))
  u64 foa;
  TL(0, 5, foa = __hip_atomic_load((gu64*)(ws + WS_FOA) + rt * 64 + lane, RLX_AGENT), NOP, NOP)
  PRIO(1);
  TL6(0, NOP, NOP)
  PRIO(0);
  const int tp = w & 1, kq = w >> 1;
  const unsigned ofr = (rt * 8 + role * 4 + tp * 2) * OT_B + kq * 5120 + lane * 16;
  const int R0 = rt * 128 + role * 64 + tp * 32 + l15;
  u32x4 of[2][5]; float gt0, gt1;
  asm volatile("" : "+v"(foa));
  if (!__all(foa == tag)) wait_flags<64>(ws, WS_FOA + rt * 512, tag, lane);
  asm volatile("" ::: "memory");
#define OFL(u) do { _Pragma("unroll") for (int i = 0; i < 5; i++) of[u][i] = LD16(WS_OA + ofr + (u) * OT_B + i * 1024); } while (0)
  TL7(0, 0, OFL(0), (gt0 = __uint_as_float(__builtin_amdgcn_raw_buffer_load_b32(wsr, WS_G + R0 * 4, 0, 16)), gt1 = __uint_as_float(__builtin_amdgcn_raw_buffer_load_b32(wsr, WS_G + R0 * 4 + 64, 0, 16))), OFL(1))
  pB2[2] = pk(e0, e1); pB2[3] = pk(e2, e3);
  OB = mfma32(v2, pB2, OB);
#undef TL
#undef TL6
#undef TL7
#undef TILE
#undef G
  STAMP(7);
  { LAS f32x4* xb = (LAS f32x4*)(lp + L_XCH) + w3 * (64 * 6);
#pragma unroll
    for (int i = 0; i < 12; i++) { auto rr = __builtin_amdgcn_permlane16_swap(__float_as_uint(OA[i]), __float_as_uint(OB[i]), false, false); OA[i] = __uint_as_float(rr[0]); OB[i] = __uint_as_float(rr[1]); }
    const f32x16 snd = w2 ? OA : OB;
    BAR_L();
#pragma unroll
    for (int c = 0; c < 3; c++) xb[(w2 * 3 + c) * 64 + lane] = (f32x4){snd[4 * c], snd[4 * c + 1], snd[4 * c + 2], snd[4 * c + 3]};
    asm volatile("s_waitcnt vmcnt(12) lgkmcnt(0)\n\ts_barrier" ::: "memory");
    f32x16 O = w2 ? OB : OA;
#pragma unroll
    for (int c = 0; c < 3; c++) { const f32x4 t = xb[((w2 ^ 1) * 3 + c) * 64 + lane]; O[4 * c] += t[0]; O[4 * c + 1] += t[1]; O[4 * c + 2] += t[2]; O[4 * c + 3] += t[3]; }
    float l = from_upper(O[8]);
    float amax = 0.f;
#pragma unroll
    for (int i = 0; i < 12; i++) amax += fabsf(O[i]);
    const bool good = (l >= 0x1p-30f) && (l <= 0x1p100f) && (amax <= 0x1p126f);
    if (__builtin_expect(!__all(good), 0)) {
      u32x4 q1; u32x2 q2;
#pragma unroll
      for (int i = 0; i < 4; i++) { auto rr = __builtin_amdgcn_permlane16_swap(q1A[i], q1B[i], false, false); q1[i] = w2 ? rr[1] : rr[0]; }
#pragma unroll
      for (int i = 0; i < 2; i++) { auto rr = __builtin_amdgcn_permlane16_swap(q2A[i], q2B[i], false, false); q2[i] = h ? 0u : (w2 ? rr[1] : rr[0]); }
      float m = -INFINITY;
#pragma unroll 1
      for (int t = 0; t < 64; t++) {
        f32x16 s = mfma32(LD16(WS_KV + t * TILE_B + ok1), q1, Z); s = mfma32k8(LD8(WS_KV + t * TILE_B + ok2), q2, s);
#pragma unroll
        for (int i = 0; i < 16; i++) m = fmaxf(m, s[i]);
      }
      m = half_swap_max(m);
      O = Z;
#pragma unroll 1
      for (int t = 0; t < 64; t++) {
        f32x16 s = mfma32(LD16(WS_KV + t * TILE_B + ok1), q1, Z); s = mfma32k8(LD8(WS_KV + t * TILE_B + ok2), q2, s);
#pragma unroll
        for (int i = 0; i < 16; i++) s[i] = ex2(s[i] - m);
        O = mfma32(LD16(WS_KV + t * TILE_B + ov), (u32x4){pk(s[0], s[1]), pk(s[2], s[3]), pk(s[4], s[5]), pk(s[6], s[7])}, O);
        O = mfma32(LD16(WS_KV + t * TILE_B + ov + 2 * VB), (u32x4){pk(s[8], s[9]), pk(s[10], s[11]), pk(s[12], s[13]), pk(s[14], s[15])}, O);
      }
      l = from_upper(O[8]);
    }
    const float inv = __builtin_amdgcn_rcpf(l);
    LAS char* tb = lp + 24576 + w * 1536;
#pragma unroll
    for (int c = 0; c < 3; c++) { const int k0 = (g & 1) * 20 + 8 * c + 4 * h;
      *(LAS u32x2*)(tb + ((c == 2 && h) ? 1280 + (g & 1) * 8 : (k0 >> 3) * 256 + (k0 & 4) * 2) + l15 * 16) = (u32x2){nz(pk(O[4 * c] * inv, O[4 * c + 1] * inv)), nz(pk(O[4 * c + 2] * inv, O[4 * c + 3] * inv))}; }
    const unsigned ob = WS_OB + (rt * 8 + w3 * 2 + w2) * OT_B + hp * 1280 + lane * 16;
    const u32x4 z0 = ldsr16(tb + lane * 16), z1 = ldsr16(tb + 1024 + l15 * 16);
    ST16(z0, ob); if (lane < 16) ST16(z1, ob + 1024);
  }
  STAMP(8);
  u32x4 wf[5][5];
#pragma unroll
  for (int i = 0; i < 5; i++) { const LAS char* wb = lp + L_WO + (kq * 5 + i) * 5120 + wlane;
#pragma unroll
    for (int t = 0; t < 5; t++) wf[i][t] = trd2(wb + t * 8, wb + 1280 + t * 8); }
  BAR_L();
  f32x4 ca[2][5], cb[2][5];
#pragma unroll
  for (int t = 0; t < 5; t++) { ca[0][t] = ca[1][t] = cb[0][t] = cb[1][t] = (f32x4){0.f, 0.f, 0.f, 0.f}; }
#pragma unroll
  for (int i = 0; i < 5; i++) {
    WOP(woB, w, i, L_WO + w * 5120); SBAR();
#pragma unroll
    for (int t = 0; t < 5; t++) ca[0][t] = mfma16(wf[i][t], of[0][i], ca[0][t]);
    SBAR(); WOP(woB, 8 + w, i, L_WO + (8 + w) * 5120); if (w == i) WOP(woB, 16, i, L_WO + 16 * 5120); SBAR();
#pragma unroll
    for (int t = 0; t < 5; t++) ca[1][t] = mfma16(wf[i][t], of[1][i], ca[1][t]);
    SBAR();
  }
  STAMP(9);
#pragma unroll
  for (int u = 0; u < 2; u++)
#pragma unroll
    for (int i = 0; i < 5; i++) of[u][i] = LD16(WS_OB + ofr + u * OT_B + i * 1024);
  WAIT_BAR(10);
#pragma unroll
  for (int i = 0; i < 5; i++) { const int gr = kq * 5 + i; const LAS char* wb = lp + (gr < 17 ? L_WO + gr * 5120 : 145408 + (gr - 17) * 5120) + wlane;
#pragma unroll
    for (int t = 0; t < 5; t++) wf[i][t] = trd2(wb + t * 8, wb + 1280 + t * 8); }
#pragma unroll
  for (int t = 0; t < 5; t++) { ca[0][t] *= gt0; ca[1][t] *= gt1; }
  BAR_L();
  SBAR();
  STAMP(10);
#pragma unroll
  for (int u = 0; u < 2; u++)
#pragma unroll
    for (int i = 0; i < 5; i++) {
      if (__builtin_expect(missing(of[u][i]), 0)) {
#pragma unroll 1
        for (unsigned spin = 0; spin < SPIN_CAP; ++spin) {
          asm volatile("" ::: "memory");
#pragma unroll
          for (int j = u * 5 + i; j < 10; j++) of[j / 5][j % 5] = LD16(WS_OB + ofr + (j / 5) * OT_B + (j % 5) * 1024);
          if (!missing(of[u][i])) break;
          __builtin_amdgcn_s_sleep(1);
        }
      }
#pragma unroll
      for (int t = 0; t < 5; t++) cb[u][t] = mfma16(wf[i][t], of[u][i], cb[u][t]);
      SBAR();
    }
  {
    LAS f32x4* xb = (LAS f32x4*)(lp + (tp ? L_WO : 0)) + lane;
    f32x4 v[10];
#pragma unroll
    for (int it = 0; it < 10; it++) { const float gg = 1.0f - (it < 5 ? gt0 : gt1); v[it] = cb[it / 5][it % 5] * gg + ca[it / 5][it % 5]; }
#pragma unroll
    for (int it = 0; it < 10; it++) if (((it * 13) >> 5) != kq) xb[(it * 4 + kq) * 64] = v[it];
    BAR_L();
    u16* op = out + (size_t)R0 * DM + C0 + g * 20;
#pragma unroll
    for (int it = 0; it < 10; it++) if (((it * 13) >> 5) == kq) { const int o_ = (it * 13) >> 5;
      const f32x4 z = (v[it] + xb[(it * 4 + ((o_ + 1) & 3)) * 64]) + (xb[(it * 4 + ((o_ + 2) & 3)) * 64] + xb[(it * 4 + ((o_ + 3) & 3)) * 64]);
      *(u32x2*)(op + (it / 5) * 16 * DM + (it % 5) * 4) = (u32x2){pk(z[0], z[1]), pk(z[2], z[3])}; }
  }
  STAMP(11);
}
}

extern "C" void kernel_launch(void* const* d_in, const int* in_sizes, int n_in, void* d_out, int out_size, void* d_ws, size_t ws_size, hipStream_t stream) {
  using namespace gm;
  fused<<<256, 512, 0, stream>>>((const u16*)d_in[0], (const u16*)d_in[1], (const u16*)d_in[2], (const u16*)d_in[3], (const u16*)d_in[5], (const u16*)d_in[6],
                                 (const u16*)d_in[7], (const u16*)d_in[8], (const u16*)d_in[9], (u16*)d_out, (char*)d_ws);
}
```

```cpp
#include <hip/hip_runtime.h>
#include <hip/hip_bf16.h>
#include <stdint.h>

namespace gm {
typedef float f32x16 __attribute__((ext_vector_type(16)));
typedef float f32x4 __attribute__((ext_vector_type(4)));
typedef float f32x2 __attribute__((ext_vector_type(2)));
typedef short s16x4 __attribute__((ext_vector_type(4)));
typedef __bf16 b16x2 __attribute__((ext_vector_type(2)));
typedef __bf16 b16x8 __attribute__((ext_vector_type(8)));
typedef unsigned u32x4 __attribute__((ext_vector_type(4)));
typedef unsigned u32x2 __attribute__((ext_vector_type(2)));
typedef unsigned long long u64;
typedef unsigned short u16;
#define DI __device__ __forceinline__
#define LAS __attribute__((address_space(3)))
#define GAS __attribute__((address_space(1)))
typedef GAS u64 gu64;

constexpr int DM = 640, DH = 20;
constexpr unsigned WS_EPOCH = 0, WS_FKV = 2048, WS_FKVR = 4096, WS_FQ = 8192, WS_FPART = 16384, WS_FOA = 24576, WS_FOB = 40960, WS_G = 57344;
constexpr unsigned WS_KVR = 65536, KVR_B = 8192, WS_KV = 131072, WS_QX = 327680, QX_B = 10240, WS_PART = 1703936, PART_B = 13440;
constexpr unsigned WS_OA = 3670016, WS_OB = 6291456, WS_END = 8912896, OT_B = 20480;
constexpr int VB = 352, TILE_B = 1280 + 4 * VB, CH_B = TILE_B * 8, STEP_B = 2 * CH_B;
constexpr int STAGE_B = 36864, ST_W1 = 10240, ST_X = 20480;
constexpr int L_BUF = 43008, L_P0W = 0, L_P0X0 = 25600, L_P0X1 = 153600, L_RED = 158720, L_RED1 = 40960;
constexpr int L_HO = 86016, L_PHK = 96256, L_QL = 96256, L_VIM = 137216, L_KVT = 147456, L_WPHI = 158592, L_ZERO = 161792, L_TOTAL = 163840;
constexpr int L_WO = 43008, L_XCH = 0;
constexpr float LOG2E = 1.4426950408889634f;
constexpr float QSCALE = 0.22360679774997896f * LOG2E;
constexpr unsigned SPIN_CAP = 20000;

DI unsigned pk(float a, float b) { f32x2 v = {a, b}; b16x2 r = __builtin_convertvector(v, b16x2); return __builtin_bit_cast(unsigned, r); }
DI f32x16 mfma32(u32x4 a, u32x4 b, f32x16 c) { return __builtin_amdgcn_mfma_f32_32x32x16_bf16(__builtin_bit_cast(b16x8, a), __builtin_bit_cast(b16x8, b), c, 0, 0, 0); }
DI f32x16 mfma32k8(u32x2 a, u32x2 b, f32x16 c) { return __builtin_amdgcn_mfma_f32_32x32x8bf16_1k(__builtin_bit_cast(s16x4, a), __builtin_bit_cast(s16x4, b), c, 0, 0, 0); }
DI f32x4 mfma16(u32x4 a, u32x4 b, f32x4 c) { return __builtin_amdgcn_mfma_f32_16x16x32_bf16(__builtin_bit_cast(b16x8, a), __builtin_bit_cast(b16x8, b), c, 0, 0, 0); }
DI u32x2 trd(const LAS char* p) { return __builtin_bit_cast(u32x2, __builtin_amdgcn_ds_read_tr16_b64_v4i16((LAS s16x4*)p)); }
DI u32x4 trd2(const LAS char* p0, const LAS char* p1) { u32x2 a = trd(p0), b = trd(p1); return (u32x4){a.x, a.y, b.x, b.y}; }
DI u32x4 ldsr16(const LAS char* p) { return *(const LAS u32x4*)p; }
DI u32x2 ldsr8(const LAS char* p) { return *(const LAS u32x2*)p; }
DI float ex2(float x) { return __builtin_amdgcn_exp2f(x); }
DI void dma16s(const void* base, unsigned voff, unsigned lds_base) {
  asm volatile("s_mov_b32 m0, %2\n\ts_nop 0\n\tglobal_load_lds_dwordx4 %0, %1" :: "v"(voff), "s"(base), "s"(lds_base) : "memory"); }
DI void dma16s_sc1(const void* base, unsigned voff, unsigned lds_base) {
  asm volatile("s_mov_b32 m0, %2\n\ts_nop 0\n\tglobal_load_lds_dwordx4 %0, %1 sc1" :: "v"(voff), "s"(base), "s"(lds_base) : "memory"); }
DI void dma4s(const void* base, unsigned voff, unsigned lds_base) {
  asm volatile("s_mov_b32 m0, %2\n\ts_nop 0\n\tglobal_load_lds_dword %0, %1" :: "v"(voff), "s"(base), "s"(lds_base) : "memory"); }
DI u32x4 ld16_async(const void* p) { u32x4 r; asm volatile("global_load_dwordx4 %0, %1, off" : "=v"(r) : "v"(p) : "memory"); return r; }
DI u32x2 ld8_sc1_async(const void* p) { u32x2 r; asm volatile("global_load_dwordx2 %0, %1, off sc1" : "=v"(r) : "v"(p) : "memory"); return r; }
#define BAR_L() asm volatile("s_waitcnt lgkmcnt(0)\n\ts_barrier" ::: "memory")
#define WAIT_BAR(N) asm volatile("s_waitcnt vmcnt(" #N ") lgkmcnt(0)\n\ts_barrier" ::: "memory")
#define WAIT_VM(N) asm volatile("s_waitcnt vmcnt(" #N ")" ::: "memory")
#define SBAR() __builtin_amdgcn_sched_barrier(0)
#define RLX_AGENT __ATOMIC_RELAXED, __HIP_MEMORY_SCOPE_AGENT
DI float half_swap_max(float m) { auto rr = __builtin_amdgcn_permlane32_swap(__float_as_uint(m), __float_as_uint(m), false, false); return fmaxf(__uint_as_float(rr[0]), __uint_as_float(rr[1])); }
DI float from_upper(float v) { auto rr = __builtin_amdgcn_permlane32_swap(__float_as_uint(v), __float_as_uint(v), false, false); return __uint_as_float(rr[1]); }

template <int NF>
DI bool flags_set(const char* ws, unsigned off, u64 tag, int lane) {
  gu64* f = (gu64*)(ws + off);
  bool ok = __hip_atomic_load(f + (NF < 64 ? (lane & (NF - 1)) : lane), RLX_AGENT) == tag;
  if (NF >= 128) ok &= __hip_atomic_load(f + 64 + lane, RLX_AGENT) == tag;
  if (NF >= 256) { ok &= __hip_atomic_load(f + 128 + lane, RLX_AGENT) == tag; ok &= __hip_atomic_load(f + 192 + lane, RLX_AGENT) == tag; }
  return ok;
}
template <int NF>
DI void wait_flags(const char* ws, unsigned off, u64 tag, int lane) {
#pragma unroll 1
  for (unsigned spin = 0; spin < SPIN_CAP; ++spin) {
    if (__all(flags_set<NF>(ws, off, tag, lane))) break;
    __builtin_amdgcn_s_sleep(2);
  }
  asm volatile("" ::: "memory");
}
DI void set_flag(char* ws, unsigned off, u64 tag, int lane) { if (lane == 0) __hip_atomic_store((gu64*)(ws + off), tag, RLX_AGENT); }
DI unsigned nz(unsigned v) { return v < 0xFFFFFFFEu ? v : 0xFFFFFFFEu; }
DI unsigned umax3(unsigned a, unsigned b, unsigned c) { unsigned m = a > b ? a : b; return m > c ? m : c; }
DI bool missing(u32x4 v) { return __any(umax3(umax3(v.x, v.y, v.z), v.w, 0u) == 0xFFFFFFFFu); }
#define ONES ((u32x4){0xFFFFFFFFu, 0xFFFFFFFFu, 0xFFFFFFFFu, 0xFFFFFFFFu})
#define ST16(v, off) __builtin_amdgcn_raw_buffer_store_b128((v), wsr, (off), 0, 16)
#define ST8(v, off) __builtin_amdgcn_raw_buffer_store_b64((v), wsr, (off), 0, 16)
#define LD16(off) __builtin_amdgcn_raw_buffer_load_b128(wsr, (off), 0, 16)
#define LD8(off) __builtin_amdgcn_raw_buffer_load_b64(wsr, (off), 0, 16)

#define STAMP(i) do {} while (0)

__global__ __launch_bounds__(512, 2) void fused(const u16* __restrict__ x, const u16* __restrict__ wqkv, const u16* __restrict__ wphi, const u16* __restrict__ woA,
                                                const u16* __restrict__ wq, const u16* __restrict__ wk, const u16* __restrict__ wv, const u16* __restrict__ woB,
                                                const u16* __restrict__ gatew, u16* __restrict__ out, char* ws) {
  __shared__ __attribute__((aligned(16))) char lds[L_TOTAL];
  const int tid = threadIdx.x, lane = tid & 63;
  const int w = __builtin_amdgcn_readfirstlane(tid >> 6) & 7;
  const int l15 = lane & 15, g = lane >> 4, q4 = l15 >> 2, p4 = lane & 3, r = lane & 31, h = lane >> 5;
  const int bx = blockIdx.x & 7, bi = blockIdx.x >> 3;
  const int rt = (bx & 1) * 8 + (bi & 7), hp = (bx >> 1) * 4 + ((bi >> 3) & 3), hq = hp >> 1, role = hp & 1;
  const int w3 = w & 3, w2 = w >> 2;
  LAS char* lp = (LAS char*)lds;
  const unsigned lds0 = (unsigned)(uintptr_t)lds;
  const __amdgpu_buffer_rsrc_t wsr = __builtin_amdgcn_make_buffer_rsrc(ws, 0, WS_END, 0x00020000);
  STAMP(0);

  const int T0 = rt * 128 + hq * 16;
  const int C0 = hq * 80;
  const int wlane = ((g >> 1) * 16 + (g & 1) * 4 + q4) * 160 + p4 * 40;
  u64 tag;
  unsigned o0[10], o1[10];
#define KVP(s, slot, p) dma16s_sc1(ws + WS_KV + (s) * STEP_B + (p) * 1024, lane * 16, lds0 + (slot) * STEP_B + (p) * 1024)
#define KVL(s, slot) do { _Pragma("unroll") for (int j = 0; j < 11; j++) if (j < 10 || w3 < 2) KVP(s, slot, w3 + 4 * j); } while (0)
#define KW(s, slot, j) KVP(s, slot, w * 5 + (j))
#define KX(s, slot) do { if (w < 2) KVP(s, slot, 40 + w); } while (0)
  const int qr = w2 * 16 + l15;
  if (w2 == 0) {
    u32x4 gf0, gf1, gf2, gf3, gf4, wpc, tch;
    u32x2 ep = ld8_sc1_async(ws + WS_EPOCH);
    const int lx = w3 < 3 ? L_P0X0 + w3 * 5120 : L_P0X1;
    { const u16* wsrc = (role ? wv : wk) + w3 * 3200;
#pragma unroll
      for (int j = 0; j < 5; j++) { const int n = j * 64 + lane, row = (n * 3277) >> 16, cc = n - row * 20; dma16s(x + T0 * DM + w3 * 160, row * 1280 + cc * 16, lds0 + lx + j * 1024); }
#pragma unroll
      for (int j = 0; j < 6; j++) dma16s(wsrc + j * 512, lane * 16, lds0 + L_P0W + w3 * 6400 + j * 1024);
      if (lane < 16) dma16s(wsrc + 6 * 512, lane * 16, lds0 + L_P0W + w3 * 6400 + 6 * 1024);
      const u16* gp = gatew + w3 * 160 + g * 8;
      gf0 = ld16_async(gp); gf1 = ld16_async(gp + 32); gf2 = ld16_async(gp + 64); gf3 = ld16_async(gp + 96); gf4 = ld16_async(gp + 128);
      wpc = ld16_async(wphi + hq * 1600 + (tid < 200 ? tid : 199) * 8);
      const char* tp = ws + (lane & 7) * 1114112;
      if (w3 == 1) tp = (const char*)(woA + C0 + (lane & 7) * 51200);
      if (w3 == 2) tp = (const char*)(woB + C0 + (lane & 7) * 51200);
      if (w3 == 3) tp = (const char*)(out + (rt * 128 + role * 64) * DM + C0);
      tch = ld16_async(tp);
    }
    asm volatile("s_waitcnt vmcnt(1)" : "+v"(gf0), "+v"(gf1), "+v"(gf2), "+v"(gf3), "+v"(gf4), "+v"(wpc), "+v"(ep) :: "memory");
    tag = (((u64)(unsigned)__builtin_amdgcn_readfirstlane(ep.y) << 32) | (unsigned)__builtin_amdgcn_readfirstlane(ep.x)) + 1ull;
    STAMP(1);
    { const LAS char* wb = lp + L_P0W + (w3 * 160 + g * 8 + q4) * 40;
      const LAS char* xb = lp + lx + l15 * 320 + g * 16;
      const bool gl = l15 == 4;
      f32x4 c0 = {}, c1 = {};
#define P0STEP(s, GF) { const u32x4 b = ldsr16(xb + (s) * 64); c0 = mfma16(trd2(wb + p4 * 8 + (s) * 1280, wb + p4 * 8 + (s) * 1280 + 160), b, c0); \
        u32x4 a = trd2(wb + 32 + (s) * 1280, wb + 32 + (s) * 1280 + 160); if (gl) a = GF; c1 = mfma16(a, b, c1); }
      P0STEP(0, gf0) P0STEP(1, gf1) P0STEP(2, gf2) P0STEP(3, gf3) P0STEP(4, gf4)
#undef P0STEP
      *(LAS f32x4*)(lp + L_RED + tid * 16) = c0;
      if (lane < 32) *(LAS f32x4*)(lp + L_RED1 + (w3 * 32 + lane) * 16) = c1; }
    STAMP(2);
    f32x16 acc[5];
#pragma unroll
    for (int b = 0; b < 5; b++) acc[b] = (f32x16){};
    const int wm = g & 1, whd = 2 * (p4 & 1) + (p4 >> 1);
    const int wo0 = wm * ST_W1 + (8 * h + 4 * wm + q4) * 160 + whd * 40, wo1 = wm * ST_W1 + (8 * h + 4 * (wm ^ 1) + q4) * 160 + whd * 40;
    const int xo = ST_X + (w3 * 32 + r) * 128 + ((h ^ ((r >> 1) & 7)) * 16);
    u32x4 fa[5], fb[5], xa, xb;
#pragma unroll
    for (int j = 0; j < 5; j++) fb[j] = (u32x4){0u, 0u, 0u, 0u};
    xb = (u32x4){0u, 0u, 0u, 0u};
#define KSTEP(FN, XN, s, FC, XC) { XN = ldsr16(sb + (xo ^ ((s) * 32))); \
      _Pragma("unroll") for (int j = 0; j < 5; j++) { acc[j] = mfma32(FC[j], XC, acc[j]); FN[j] = trd2(sb + wo0 + (s) * 2560 + j * 8, sb + wo1 + (s) * 2560 + j * 8); SBAR(); } }
    int bsel = 2;
#pragma unroll 1
    for (int i = 0; i < 10; i++) {
      BAR_L();
      if (i == 1 && role) {
        const unsigned qb = WS_QX + (rt * 8 + hq) * QX_B + w3 * 1280 + lane * 16;
        ST16(ONES, qb); ST16(ONES, qb + 5120); if (lane < 32) ST16(ONES, qb + 1024 + (lane >> 4) * (5120 - 256)); }
      if (i == 4) { asm volatile("s_waitcnt vmcnt(0)" : "+v"(tch) :: "memory"); if (role) set_flag(ws, WS_FQ + ((rt * 8 + hq) * 4 + w3) * 8, tag, lane);
                    if (tid < 200) *(LAS u32x4*)(lp + L_WPHI + tid * 16) = wpc; }
      const LAS char* sb = lp + L_BUF + bsel * STAGE_B;
      bsel = bsel == 2 ? 0 : bsel + 1;
      KSTEP(fa, xa, 0, fb, xb) KSTEP(fb, xb, 1, fa, xa) KSTEP(fa, xa, 2, fb, xb)
      { xb = ldsr16(sb + (xo ^ 96));
        acc[0] = mfma32(fa[0], xa, acc[0]); fb[0] = trd2(sb + wo0 + 7680, sb + wo1 + 7680); fb[1] = trd2(sb + wo0 + 7688, sb + wo1 + 7688); SBAR();
        acc[1] = mfma32(fa[1], xa, acc[1]); fb[2] = trd2(sb + wo0 + 7696, sb + wo1 + 7696); fb[3] = trd2(sb + wo0 + 7704, sb + wo1 + 7704); SBAR();
        acc[2] = mfma32(fa[2], xa, acc[2]); fb[4] = trd2(sb + wo0 + 7712, sb + wo1 + 7712); SBAR();
        acc[3] = mfma32(fa[3], xa, acc[3]); acc[4] = mfma32(fa[4], xa, acc[4]); SBAR(); }
    }
#pragma unroll
    for (int j = 0; j < 5; j++) acc[j] = mfma32(fb[j], xb, acc[j]);
#undef KSTEP
    STAMP(3);
    const float sc = role ? QSCALE : 1.0f;
    unsigned u0[10], u1[10];
#pragma unroll
    for (int t = 0; t < 5; t++)
#pragma unroll
      for (int e = 0; e < 2; e++) {
        auto r1 = __builtin_amdgcn_permlane16_swap(nz(pk(acc[t][8 + 2 * e] * sc, acc[t][9 + 2 * e] * sc)), nz(pk(acc[t][12 + 2 * e] * sc, acc[t][13 + 2 * e] * sc)), false, false);
        o1[2 * t + e] = r1[0]; u1[2 * t + e] = r1[1]; }
    const int qfo = ((g & 1) * 4 + w3) * 1280;
    if (role && g < 2) { const unsigned qb = WS_QX + (rt * 8 + hq) * QX_B + qfo;
      ST16(((u32x4){o1[0], o1[1], o1[2], o1[3]}), qb + l15 * 16); ST16(((u32x4){u1[0], u1[1], u1[2], u1[3]}), qb + 256 + l15 * 16);
      ST16(((u32x4){o1[4], o1[5], o1[6], o1[7]}), qb + 512 + l15 * 16); ST16(((u32x4){u1[4], u1[5], u1[6], u1[7]}), qb + 768 + l15 * 16);
      ST8(((u32x2){o1[8], o1[9]}), qb + 1024 + l15 * 8); ST8(((u32x2){u1[8], u1[9]}), qb + 1152 + l15 * 8); }
#pragma unroll
    for (int t = 0; t < 5; t++)
#pragma unroll
      for (int e = 0; e < 2; e++) {
        auto r0 = __builtin_amdgcn_permlane16_swap(pk(acc[t][2 * e], acc[t][1 + 2 * e]), pk(acc[t][4 + 2 * e], acc[t][5 + 2 * e]), false, false);
        o0[2 * t + e] = r0[0]; u0[2 * t + e] = r0[1]; }
    *(LAS u32x4*)(lp + L_HO + tid * 16) = (u32x4){u0[0], u0[1], u0[2], u0[3]}; *(LAS u32x4*)(lp + L_HO + 4096 + tid * 16) = (u32x4){u0[4], u0[5], u0[6], u0[7]};
    *(LAS u32x2*)(lp + L_HO + 8192 + tid * 8) = (u32x2){u0[8], u0[9]};
    BAR_L();
    if (role) {
      if (g >= 2) { LAS char* d = lp + L_QL + qfo;
        *(LAS u32x4*)(d + l15 * 16) = (u32x4){o1[0], o1[1], o1[2], o1[3]}; *(LAS u32x4*)(d + 256 + l15 * 16) = (u32x4){u1[0], u1[1], u1[2], u1[3]};
        *(LAS u32x4*)(d + 512 + l15 * 16) = (u32x4){o1[4], o1[5], o1[6], o1[7]}; *(LAS u32x4*)(d + 768 + l15 * 16) = (u32x4){u1[4], u1[5], u1[6], u1[7]};
        *(LAS u32x2*)(d + 1024 + l15 * 8) = (u32x2){o1[8], o1[9]}; *(LAS u32x2*)(d + 1152 + l15 * 8) = (u32x2){u1[8], u1[9]}; }
    } else { LAS char* d = lp + L_VIM + (g * 128 + w3 * 32 + l15) * 40;
#pragma unroll
      for (int t = 0; t < 5; t++) { *(LAS u32x2*)(d + t * 8) = (u32x2){o1[2 * t], o1[2 * t + 1]}; *(LAS u32x2*)(d + 640 + t * 8) = (u32x2){u1[2 * t], u1[2 * t + 1]}; } }
  } else {
    u32x2 ep = ld8_sc1_async(ws + WS_EPOCH);
    const bool second = w3 >> 1;
    const unsigned rs = (second && role) ? 1280u : 3840u;
    const char* wsrc = (second ? (role ? (const char*)(wq + C0) : (const char*)(wqkv + 1280 + C0)) : (const char*)(wqkv + (role ? 0 : 640) + C0)) + (w3 & 1) * 32 * rs;
    const char* xsrc = (const char*)(x + (rt * 128 + w3 * 32) * DM);
    int rot = (bi & 7) + 3 * ((bi >> 3) & 3); rot = rot >= 10 ? rot - 10 : rot;
    unsigned so[5], xoff[2];
#pragma unroll
    for (int j = 0; j < 5; j++) { const int c = j * 64 + lane, rho = (c * 6554) >> 16, cc = c - rho * 10; so[j] = (rho ^ (second ? 4 : 0)) * rs + cc * 16; }
#pragma unroll
    for (int j = 0; j < 2; j++) { const int row = j * 8 + (lane >> 3); xoff[j] = row * 1280 + (((lane & 7) ^ ((row >> 1) & 7)) * 16); }
    int bsel = 2, kc = rot;
    auto stage = [&]() {
      const unsigned lb = lds0 + L_BUF + bsel * STAGE_B;
      const char* bw = wsrc + kc * 64 * rs; const char* bxp = xsrc + kc * 128;
#pragma unroll
      for (int j = 0; j < 5; j++) { dma16s(bw, so[j], lb + (w3 * 5 + j) * 1024); if (j < 4) dma16s(bxp + (j >> 1) * 16 * 1280, xoff[j & 1], lb + ST_X + (w3 * 4 + j) * 1024); }
      bsel = bsel == 2 ? 0 : bsel + 1; kc = kc == 9 ? 0 : kc + 1;
    };
    stage(); stage();
    asm volatile("s_waitcnt vmcnt(9)" : "+v"(ep) :: "memory");
    tag = (((u64)(unsigned)__builtin_amdgcn_readfirstlane(ep.y) << 32) | (unsigned)__builtin_amdgcn_readfirstlane(ep.x)) + 1ull;
    STAMP(1); STAMP(2);
#pragma unroll 1
    for (int i = 0; i < 8; i++) {
      WAIT_BAR(9);
      stage();
      if (i == 0 && w3 == 0) {
        const unsigned tb = WS_KV + (T0 >> 5) * TILE_B; const int s16 = hq & 1;
        if (!role) {
          const LAS f32x4* rp = (const LAS f32x4*)(lp + (h ? L_RED1 : L_RED)) + (h ? (g & 1) * 16 : g * 32) + l15;
          const int st = h ? 32 : 64, o2 = h ? 0 : 16;
          const f32x4 a = (rp[0] + rp[st]) + (rp[2 * st] + rp[3 * st]), b = (rp[o2] + rp[st + o2]) + (rp[2 * st + o2] + rp[3 * st + o2]);
          const u32x4 v = {pk(a[0], a[1]), pk(a[2], a[3]), pk(b[0], b[1]), pk(b[2], b[3])};
          if (lane < 32) ST16(v, tb + (g * 32 + s16 * 16 + l15) * 16);
          else if (lane < 48) ST8(((u32x2){v.x, v.y}), tb + 1024 + (s16 * 16 + l15) * 8);
          else { const float gv = 1.0f / (1.0f + __expf(-a[0])); __builtin_amdgcn_raw_buffer_store_b32(__float_as_uint(gv), wsr, WS_G + (T0 + l15) * 4, 0, 16); }
        } else if (lane < 44) {
          const int hh = lane >= 22, d = lane - hh * 22, dd = d < 20 ? d : 0, st = dd >= 16 ? 128 : 256;
          const LAS float* rp = (const LAS float*)(lp + (dd >= 16 ? L_RED1 : L_RED)) + (((dd & 15) >> 2) * 16 + 4 * hh) * 4 + (dd & 3);
          float e[8];
#pragma unroll
          for (int j = 0; j < 8; j++) { const int tk = (8 * (j >> 2) + (j & 3)) * 4; e[j] = (rp[tk] + rp[tk + st]) + (rp[tk + 2 * st] + rp[tk + 3 * st]); }
          u32x4 v = {pk(e[0], e[1]), pk(e[2], e[3]), pk(e[4], e[5]), pk(e[6], e[7])};
          if (d == 20) v = (u32x4){0x3F803F80u, 0x3F803F80u, 0x3F803F80u, 0x3F803F80u};
          if (d == 21) v = (u32x4){0u, 0u, 0u, 0u};
          ST16(v, tb + 1280 + (s16 * 2 + hh) * VB + d * 16);
        }
      }
      if (i == 2) { if (w3 == 0) set_flag(ws, WS_FKV + blockIdx.x * 8, tag, lane);
                    if (w3 >= 2) *(LAS u32x4*)(lp + L_ZERO + (tid - 384) * 16) = (u32x4){0u, 0u, 0u, 0u}; }
    }
    u64 fa, fb, fc, fd, fe;
    { gu64* f = (gu64*)(ws + WS_FKV) + lane;
      fa = __hip_atomic_load(f, RLX_AGENT); fb = __hip_atomic_load(f + 64, RLX_AGENT); fc = __hip_atomic_load(f + 128, RLX_AGENT); fd = __hip_atomic_load(f + 192, RLX_AGENT);
      fe = __hip_atomic_load((gu64*)(ws + WS_FQ) + (rt * 8 + hq) * 4 + p4, RLX_AGENT); }
    WAIT_BAR(14);
    asm volatile("" : "+v"(fa), "+v"(fb), "+v"(fc), "+v"(fd), "+v"(fe));
    const bool kvok = __all((fa == tag) & (fb == tag) & (fc == tag) & (fd == tag) & (fe == tag));
    asm volatile("" ::: "memory");
    BAR_L();
    if (!kvok) { wait_flags<256>(ws, WS_FKV, tag, lane); wait_flags<4>(ws, WS_FQ + (rt * 8 + hq) * 32, tag, lane); }
    KVL(0, 0);
    if (blockIdx.x == 0 && w3 == 0) set_flag(ws, WS_EPOCH, tag, lane);
    STAMP(3);
    BAR_L();
    const int ht = tid - 256;
    const u32x4 a = ldsr16(lp + L_HO + ht * 16), b = ldsr16(lp + L_HO + 4096 + ht * 16); const u32x2 c = ldsr8(lp + L_HO + 8192 + ht * 8);
    o0[0] = a.x; o0[1] = a.y; o0[2] = a.z; o0[3] = a.w; o0[4] = b.x; o0[5] = b.y; o0[6] = b.z; o0[7] = b.w; o0[8] = c.x; o0[9] = c.y;
  }
  STAMP(4);
  u32x4 ph[5];
  { const LAS char* pb = (p4 == g) ? lp + L_WPHI + g * 800 + q4 * 40 : lp + L_ZERO;
    const u32x4 b0 = {o0[0], o0[1], o0[2], o0[3]}, b1 = {o0[4], o0[5], o0[6], o0[7]}, b2 = {o0[8], o0[9], 0u, 0u};
    float p[20];
#pragma unroll
    for (int t = 0; t < 5; t++) {
      const u32x2 c2 = trd(pb + 640 + t * 8);
      f32x4 c = {}; c = mfma16(trd2(pb + t * 8, pb + 160 + t * 8), b0, c); c = mfma16(trd2(pb + 320 + t * 8, pb + 480 + t * 8), b1, c); c = mfma16((u32x4){c2.x, c2.y, 0u, 0u}, b2, c);
      p[4 * t] = c[0]; p[4 * t + 1] = c[1]; p[4 * t + 2] = c[2]; p[4 * t + 3] = c[3];
    }
    KX(1, 1); KW(1, 1, 0); SBAR();
    float mx = p[0], mn = p[0];
#pragma unroll
    for (int f = 1; f < 20; f++) { mx = fmaxf(mx, p[f]); mn = fminf(mn, p[f]); }
    const float ma = -mx * LOG2E, mb = mn * LOG2E;
    float ea[20], eb[20], sa = 0.f, sb = 0.f;
#pragma unroll
    for (int f = 0; f < 20; f++) { if (f % 4 == 0 && f < 16) { SBAR(); KW(1, 1, 1 + f / 4); SBAR(); }
      ea[f] = ex2(fmaf(p[f], LOG2E, ma)); sa += ea[f]; eb[f] = ex2(fmaf(p[f], -LOG2E, mb)); sb += eb[f]; }
    const float ia = __builtin_amdgcn_rcpf(sa), ib = __builtin_amdgcn_rcpf(sb);
    unsigned o[20];
#pragma unroll
    for (int f = 0; f < 10; f++) { o[f] = pk(ea[2 * f] * ia, ea[2 * f + 1] * ia); o[10 + f] = pk(eb[2 * f] * ib, eb[2 * f + 1] * ib); }
#pragma unroll
    for (int c = 0; c < 5; c++) ph[c] = (u32x4){o[4 * c], o[4 * c + 1], o[4 * c + 2], o[4 * c + 3]};
  }
  u32x4 q1A, q1B; u32x2 q2A, q2B;
  if (role) {
    WAIT_BAR(0);
    STAMP(5);
    const LAS char* qa = lp + L_QL + w3 * 1280;
    q1A = ldsr16(qa + lane * 16); q1B = ldsr16(qa + 5120 + lane * 16); q2A = ldsr8(qa + 1024 + r * 8); q2B = ldsr8(qa + 6144 + r * 8);
    BAR_L();
  } else {
    const unsigned qb = WS_QX + (rt * 8 + hq) * QX_B + w3 * 1280;
    q1A = LD16(qb + lane * 16); q1B = LD16(qb + 5120 + lane * 16); q2A = LD8(qb + 1024 + r * 8); q2B = LD8(qb + 6144 + r * 8);
    { LAS char* d = lp + L_PHK + (g * 128 + w3 * 32 + qr) * 80;
#pragma unroll
      for (int c = 0; c < 5; c++) *(LAS u32x4*)(d + c * 16) = ph[c]; }
    BAR_L();
    STAMP(5);
    { const int hd = w >> 1, dt = w & 1;
      const LAS char* ab = lp + L_PHK + (hd * 128 + g * 8 + q4) * 80 + p4 * 8;
      const LAS char* bb = lp + L_VIM + (hd * 128 + g * 8 + q4) * 40 + dt * 32 + p4 * 8;
      const bool ones = dt && l15 == 4;
      f32x4 c[3] = {};
#pragma unroll
      for (int ks = 0; ks < 4; ks++) {
        u32x4 b = trd2(bb + ks * 32 * 40, bb + (ks * 32 + 4) * 40);
        if (ones) b = (u32x4){0x3F803F80u, 0x3F803F80u, 0x3F803F80u, 0x3F803F80u};
#pragma unroll
        for (int ft = 0; ft < 3; ft++) c[ft] = mfma16(trd2(ab + ks * 32 * 80 + ft * 32, ab + (ks * 32 + 4) * 80 + ft * 32), b, c[ft]);
      }
      asm volatile("" : "+v"(c[0]), "+v"(c[1]), "+v"(c[2]), "+v"(q1A), "+v"(q1B), "+v"(q2A), "+v"(q2B) :: "memory");
#pragma unroll 1
      for (unsigned spin = 0; spin < SPIN_CAP; ++spin) {
        if (!(missing(q1A) | missing(q1B) | missing((u32x4){q2A.x, q2A.y, q2B.x, q2B.y}))) break;
        __builtin_amdgcn_s_sleep(1);
        asm volatile("" ::: "memory");
        q1A = LD16(qb + lane * 16); q1B = LD16(qb + 5120 + lane * 16); q2A = LD8(qb + 1024 + r * 8); q2B = LD8(qb + 6144 + r * 8);
      }
      asm volatile("" : "+v"(q1A), "+v"(q1B), "+v"(q2A), "+v"(q2B) :: "memory");
      const int d = dt * 16 + l15;
#pragma unroll
      for (int ft = 0; ft < 3; ft++) if (d < 21 && ft * 4 + g < 10) ST16(__builtin_bit_cast(u32x4, c[ft]), WS_PART + (hq * 16 + rt) * PART_B + ((hd * 10 + ft * 4 + g) * 21 + d) * 16);
    }
    WAIT_BAR(3);
  }
  STAMP(6);
  const int vr = r <= 21 ? r : 21;
  const int ok1 = lane * 16, ok2 = h ? (1280 + 20 * 16) : (1024 + r * 8), ov = 1280 + h * VB + vr * 16;
  const f32x16 Z = {};
  if (h) { q2A = (u32x2){0u, 0u}; q2B = (u32x2){0u, 0u}; }
  { const LAS char* t0 = lp;
    const u32x4 k1 = ldsr16(t0 + ok1); const u32x2 k2 = ldsr8(t0 + ok2);
    f32x16 sa = mfma32(k1, q1A, Z); sa = mfma32k8(k2, q2A, sa);
    f32x16 sb = mfma32(k1, q1B, Z); sb = mfma32k8(k2, q2B, sb);
    float ma = sa[0], mb = sb[0];
#pragma unroll
    for (int i = 1; i < 16; i++) { ma = fmaxf(ma, sa[i]); mb = fmaxf(mb, sb[i]); }
    ma = half_swap_max(ma); mb = half_swap_max(mb);
    const float mah = __uint_as_float(__float_as_uint(ma) & 0xFFFF0000u), mbh = __uint_as_float(__float_as_uint(mb) & 0xFFFF0000u);
    if (h) { q2A.x = pk(-mah, mah - ma); q2B.x = pk(-mbh, mbh - mb); } }
  f32x16 OA = {}, OB = {}, SA, SB;
  u32x4 k1, v1, v2, v2n, pA1, pA2, pB1, pB2; u32x2 k2;
  float e0, e1, e2, e3;
  const LAS char* c0p = lp + w2 * CH_B + ok1; const LAS char* c1p = lp + w2 * CH_B + ok2; const LAS char* c2p = lp + w2 * CH_B + ov;
  k1 = ldsr16(c0p); k2 = ldsr8(c1p);
  SA = mfma32(k1, q1A, Z); SA = mfma32k8(k2, q2A, SA);
  v1 = ldsr16(c2p); v2 = v1;
  e0 = e1 = e2 = e3 = 0.f; pB2 = (u32x4){0u, 0u, 0u, 0u};
#define G(MF, LD, C0_, C1_, X, S_, i) MF; LD; SBAR(); C0_; C1_; SBAR(); X##0 = ex2(S_[i]); X##1 = ex2(S_[i + 1]); X##2 = ex2(S_[i + 2]); X##3 = ex2(S_[i + 3]); SBAR();
#define TILE(TB, TN, V2, H0, H1, H2) { float a0, a1, a2, a3; \
    G(SB = mfma32(k1, q1B, Z),    k1 = ldsr16(c0p + (TN)), pB2[2] = pk(e0, e1), pB2[3] = pk(e2, e3), a, SA, 0) \
    G(OB = mfma32(v2, pB2, OB),   V2,                      pA1[0] = pk(a0, a1), pA1[1] = pk(a2, a3), e, SA, 4) \
    G(SB = mfma32k8(k2, q2B, SB), k2 = ldsr8(c1p + (TN)),  pA1[2] = pk(e0, e1), pA1[3] = pk(e2, e3), a, SA, 8) \
    G(OA = mfma32(v1, pA1, OA),   H0,                      pA2[0] = pk(a0, a1), pA2[1] = pk(a2, a3), e, SA, 12) \
    G(SA = mfma32(k1, q1A, Z),    H1,                      pA2[2] = pk(e0, e1), pA2[3] = pk(e2, e3), a, SB, 0) \
    G(OA = mfma32(v2, pA2, OA),   H2,                      pB1[0] = pk(a0, a1), pB1[1] = pk(a2, a3), e, SB, 4) \
    G(SA = mfma32k8(k2, q2A, SA), (void)0,                 pB1[2] = pk(e0, e1), pB1[3] = pk(e2, e3), a, SB, 8) \
    G(OB = mfma32(v1, pB1, OB),   v1 = ldsr16(c2p + (TN)), pB2[0] = pk(a0, a1), pB2[1] = pk(a2, a3), e, SB, 12) }
#define NOP (void)0
#define TL(sl, t, H0, H1, H2) TILE((sl) * STEP_B + (t) * TILE_B, (sl) * STEP_B + ((t) + 1) * TILE_B, v2 = ldsr16(c2p + (sl) * STEP_B + (t) * TILE_B + 2 * VB), H0, H1, H2)
#define TL6(sl, H0, H1) TL(sl, 6, H0, H1, v2n = ldsr16(c2p + (sl) * STEP_B + 7 * TILE_B + 2 * VB))
#define TL7(sl, sn, H0, H1, H2) TILE((sl) * STEP_B + 7 * TILE_B, (sn) * STEP_B, v2 = v2n, H0, H1, H2)
  unsigned woff[5];
#pragma unroll
  for (int j = 0; j < 5; j++) { const int c = j * 64 + lane, rho = (c * 6554) >> 16, cc = c - rho * 10; const int k = (rho & ~12) | ((rho & 4) << 1) | ((rho & 8) >> 1); woff[j] = k * 1280 + cc * 16; }
#define WOP(wo, gr, j, dst) dma16s((wo) + C0 + (gr) * 32 * 640, woff[j], lds0 + (dst) + (j) * 1024)
#define WA(gr, j) WOP(woA, gr, j, L_WO + (gr) * 5120)
#define WX(j) do { if (w < 4) WA(16 + w3, j); else if (w < 7) WOP(woB, 17 + w3, j, 145408 + w3 * 5120); } while (0)
#define PRIO(n) __builtin_amdgcn_s_setprio(n)
#define FILL(j) do { if (role) { const int n_ = w * 3 + (j); ST16(ONES, WS_OB + (rt * 8 + hq) * OT_B + (n_ < 20 ? n_ : 19) * 1024 + lane * 16); } } while (0)
  const bool red = !role && w == 0;
  const int slot = rt * 53 + lane;
  f32x4 part[16];
  auto oa_part = [&]() {
    const LAS char* kb = (q4 == g) ? lp + L_KVT + g * 1920 + p4 * 80 : lp + L_ZERO;
    f32x4 o[6]; u32x4 ka[6], kc[6];
#pragma unroll
    for (int t = 0; t < 6; t++) { o[t] = (f32x4){0.f, 0.f, 0.f, 0.f}; ka[t] = ldsr16(kb + t * 320); }
    SBAR();
#pragma unroll
    for (int t = 0; t < 6; t++) kc[t] = ldsr16(kb + t * 320 + 16);
    SBAR();
#pragma unroll
    for (int t = 0; t < 6; t++) { o[t] = mfma16(ka[t], ph[0], o[t]); ka[t] = ldsr16(kb + t * 320 + 32); SBAR(); }
#pragma unroll
    for (int t = 0; t < 6; t++) { o[t] = mfma16(kc[t], ph[1], o[t]); kc[t] = ldsr16(kb + t * 320 + 48); SBAR(); }
#pragma unroll
    for (int t = 0; t < 6; t++) { o[t] = mfma16(ka[t], ph[2], o[t]); ka[t] = ldsr16(kb + t * 320 + 64); SBAR(); }
#pragma unroll
    for (int t = 0; t < 6; t++) o[t] = mfma16(kc[t], ph[3], o[t]);
#pragma unroll
    for (int t = 0; t < 6; t++) o[5 - t] = mfma16(ka[5 - t], ph[4], o[5 - t]);
    const float inv = __builtin_amdgcn_rcpf(o[5][0] + 1e-6f);
    LAS char* tb = lp + L_WO + w * 5120;
#pragma unroll
    for (int t = 0; t < 5; t++) { const int k0 = g * 20 + 4 * t;
      *(LAS u32x2*)(tb + (k0 >> 3) * 256 + (k0 & 4) * 2 + l15 * 16) = (u32x2){pk(o[t][0] * inv, o[t][1] * inv), pk(o[t][2] * inv, o[t][3] * inv)}; }
    const unsigned oa = WS_OA + (rt * 8 + w3 * 2 + w2) * OT_B + hq * 2560 + lane * 16;
    const u32x4 z0 = ldsr16(tb + lane * 16), z1 = ldsr16(tb + 1024 + lane * 16), z2 = ldsr16(tb + 2048 + r * 16);
    ST16(z0, oa); ST16(z1, oa + 1024); if (!h) ST16(z2, oa + 2048);
    SBAR();
  };
  PRIO(3);
  TL(0, 0, NOP, NOP, NOP)
  TL(0, 1, if (!role) { WAIT_VM(0); set_flag(ws, WS_FPART + ((hq * 16 + rt) * 8 + w) * 8, tag, lane); }, NOP, KX(2, 2))
  PRIO(2);
  TL(0, 2, KW(2, 2, 0), NOP, KW(2, 2, 1)) TL(0, 3, KW(2, 2, 2), NOP, KW(2, 2, 3)) TL(0, 4, KW(2, 2, 4), NOP, FILL(0))
  TL(0, 5, FILL(1), NOP, FILL(2))
  TL6(0, if (red) { wait_flags<128>(ws, WS_FPART + hq * 1024, tag, lane); const int sc_ = slot < 840 ? slot : 839;
                      _Pragma("unroll") for (int j = 0; j < 16; j++) part[j] = __builtin_bit_cast(f32x4, LD16(WS_PART + (hq * 16 + j) * PART_B + sc_ * 16)); }, NOP)
  TL7(0, 1, NOP, NOP, NOP)
  TL(1, 0, if (red) { f32x4 a = ((part[0] + part[1]) + (part[2] + part[3])) + ((part[4] + part[5]) + (part[6] + part[7]));
                      a += ((part[8] + part[9]) + (part[10] + part[11])) + ((part[12] + part[13]) + (part[14] + part[15]));
                      const int hf = (slot * 3121) >> 16; const int d = slot - hf * 21; const int hd = (hf * 26) >> 8; const int fq = hf - hd * 10;
                      if (lane < 53 && slot < 840) ST8(((u32x2){pk(a[0], a[1]), pk(a[2], a[3])}), WS_KVR + hq * KVR_B + hd * 1920 + d * 80 + fq * 8);
                      WAIT_VM(0); set_flag(ws, WS_FKVR + (hq * 16 + rt) * 8, tag, lane); }, NOP, NOP)
  TL(1, 1, NOP, NOP, NOP)
  TL(1, 2, if (w == 5 || w == 6) { const int row = (w - 5) * 32 + (lane >> 1);
                      dma4s(woB + C0 + ((rt & 7) * 2 + role) * 40 * 640, (row < 40 ? row : 39) * 1280 + (lane & 1) * 156, lds0 + 145408); }, NOP, NOP)
  TL(1, 3, NOP, NOP, NOP)
  PRIO(1);
  TL(1, 4, if (role && w == 1) { wait_flags<16>(ws, WS_FKVR + hq * 128, tag, lane);
                      _Pragma("unroll") for (int p = 0; p < 8; p++) dma16s_sc1(ws + WS_KVR + hq * KVR_B + p * 1024, lane * 16, lds0 + L_KVT + p * 1024); }, NOP, NOP)
  TL(1, 5, NOP, NOP, NOP)
  PRIO(0);
  TL6(1, NOP, NOP)
  WAIT_BAR(0);
  STAMP(12);
  PRIO(3);
  TL7(1, 2, NOP, NOP, NOP)
  if (role && w2) oa_part();
  TL(2, 0, KX(3, 0), NOP, KW(3, 0, 0)) TL(2, 1, KW(3, 0, 1), NOP, KW(3, 0, 2)) TL(2, 2, KW(3, 0, 3), NOP, KW(3, 0, 4))
  PRIO(2);
  if (role && !w2) oa_part();
  TL(2, 3, NOP, NOP, WA(w, 0)) TL(2, 4, WA(w, 1), NOP, WA(w, 2))
  PRIO(1);
  TL(2, 5, WA(w, 3), NOP, WA(w, 4))
  PRIO(0);
  TL6(2, NOP, NOP)
  WAIT_BAR(5);
  STAMP(13);
  PRIO(3);
  if (role) set_flag(ws, WS_FOA + ((rt * 8 + hq) * 8 + w) * 8, tag, lane);
  TL7(2, 0, NOP, NOP, NOP)
  TL(0, 0, WA(8 + w, 0), NOP, WA(8 + w, 1)) TL(0, 1, WA(8 + w, 2), NOP, WA(8 + w, 3)) TL(0, 2, WA(8 + w, 4), NOP, WX(0))
  TL(0, 3, WX(1), NOP, WX(2))
  PRIO(2);
  TL(0, 4, WX(3), NOP, WX(4))
  u64 foa;
  TL(0, 5, foa = __hip_atomic_load((gu64*)(ws + WS_FOA) + rt * 64 + lane, RLX_AGENT), NOP, NOP)
  PRIO(1);
  TL6(0, NOP, NOP)
  PRIO(0);
  const int tp = w & 1, kq = w >> 1;
  const unsigned ofr = (rt * 8 + role * 4 + tp * 2) * OT_B + kq * 5120 + lane * 16;
  const int R0 = rt * 128 + role * 64 + tp * 32 + l15;
  u32x4 of[2][5]; float gt0, gt1;
  asm volatile("" : "+v"(foa));
  if (!__all(foa == tag)) wait_flags<64>(ws, WS_FOA + rt * 512, tag, lane);
  asm volatile("" ::: "memory");
#define OFL(u) do { _Pragma("unroll") for (int i = 0; i < 5; i++) of[u][i] = LD16(WS_OA + ofr + (u) * OT_B + i * 1024); } while (0)
  TL7(0, 0, OFL(0), (gt0 = __uint_as_float(__builtin_amdgcn_raw_buffer_load_b32(wsr, WS_G + R0 * 4, 0, 16)), gt1 = __uint_as_float(__builtin_amdgcn_raw_buffer_load_b32(wsr, WS_G + R0 * 4 + 64, 0, 16))), OFL(1))
  pB2[2] = pk(e0, e1); pB2[3] = pk(e2, e3);
  OB = mfma32(v2, pB2, OB);
#undef TL
#undef TL6
#undef TL7
#undef TILE
#undef G
  STAMP(7);
  { LAS f32x4* xb = (LAS f32x4*)(lp + L_XCH) + w3 * (64 * 6);
#pragma unroll
    for (int i = 0; i < 12; i++) { auto rr = __builtin_amdgcn_permlane16_swap(__float_as_uint(OA[i]), __float_as_uint(OB[i]), false, false); OA[i] = __uint_as_float(rr[0]); OB[i] = __uint_as_float(rr[1]); }
    const f32x16 snd = w2 ? OA : OB;
    BAR_L();
#pragma unroll
    for (int c = 0; c < 3; c++) xb[(w2 * 3 + c) * 64 + lane] = (f32x4){snd[4 * c], snd[4 * c + 1], snd[4 * c + 2], snd[4 * c + 3]};
    asm volatile("s_waitcnt vmcnt(12) lgkmcnt(0)\n\ts_barrier" ::: "memory");
    f32x16 O = w2 ? OB : OA;
#pragma unroll
    for (int c = 0; c < 3; c++) { const f32x4 t = xb[((w2 ^ 1) * 3 + c) * 64 + lane]; O[4 * c] += t[0]; O[4 * c + 1] += t[1]; O[4 * c + 2] += t[2]; O[4 * c + 3] += t[3]; }
    float l = from_upper(O[8]);
    float amax = 0.f;
#pragma unroll
    for (int i = 0; i < 12; i++) amax += fabsf(O[i]);
    const bool good = (l >= 0x1p-30f) && (l <= 0x1p100f) && (amax <= 0x1p126f);
    if (__builtin_expect(!__all(good), 0)) {
      u32x4 q1; u32x2 q2;
#pragma unroll
      for (int i = 0; i < 4; i++) { auto rr = __builtin_amdgcn_permlane16_swap(q1A[i], q1B[i], false, false); q1[i] = w2 ? rr[1] : rr[0]; }
#pragma unroll
      for (int i = 0; i < 2; i++) { auto rr = __builtin_amdgcn_permlane16_swap(q2A[i], q2B[i], false, false); q2[i] = h ? 0u : (w2 ? rr[1] : rr[0]); }
      float m = -INFINITY;
#pragma unroll 1
      for (int t = 0; t < 64; t++) {
        f32x16 s = mfma32(LD16(WS_KV + t * TILE_B + ok1), q1, Z); s = mfma32k8(LD8(WS_KV + t * TILE_B + ok2), q2, s);
#pragma unroll
        for (int i = 0; i < 16; i++) m = fmaxf(m, s[i]);
      }
      m = half_swap_max(m);
      O = Z;
#pragma unroll 1
      for (int t = 0; t < 64; t++) {
        f32x16 s = mfma32(LD16(WS_KV + t * TILE_B + ok1), q1, Z); s = mfma32k8(LD8(WS_KV + t * TILE_B + ok2), q2, s);
#pragma unroll
        for (int i = 0; i < 16; i++) s[i] = ex2(s[i] - m);
        O = mfma32(LD16(WS_KV + t * TILE_B + ov), (u32x4){pk(s[0], s[1]), pk(s[2], s[3]), pk(s[4], s[5]), pk(s[6], s[7])}, O);
        O = mfma32(LD16(WS_KV + t * TILE_B + ov + 2 * VB), (u32x4){pk(s[8], s[9]), pk(s[10], s[11]), pk(s[12], s[13]), pk(s[14], s[15])}, O);
      }
      l = from_upper(O[8]);
    }
    const float inv = __builtin_amdgcn_rcpf(l);
    LAS char* tb = lp + 24576 + w * 1536;
#pragma unroll
    for (int c = 0; c < 3; c++) { const int k0 = (g & 1) * 20 + 8 * c + 4 * h;
      *(LAS u32x2*)(tb + ((c == 2 && h) ? 1280 + (g & 1) * 8 : (k0 >> 3) * 256 + (k0 & 4) * 2) + l15 * 16) = (u32x2){nz(pk(O[4 * c] * inv, O[4 * c + 1] * inv)), nz(pk(O[4 * c + 2] * inv, O[4 * c + 3] * inv))}; }
    const unsigned ob = WS_OB + (rt * 8 + w3 * 2 + w2) * OT_B + hp * 1280 + lane * 16;
    const u32x4 z0 = ldsr16(tb + lane * 16), z1 = ldsr16(tb + 1024 + l15 * 16);
    ST16(z0, ob); if (lane < 16) ST16(z1, ob + 1024);
  }
  STAMP(8);
  u32x4 wf[5][5];
#pragma unroll
  for (int i = 0; i < 5; i++) { const LAS char* wb = lp + L_WO + (kq * 5 + i) * 5120 + wlane;
#pragma unroll
    for (int t = 0; t < 5; t++) wf[i][t] = trd2(wb + t * 8, wb + 1280 + t * 8); }
  BAR_L();
  f32x4 ca[2][5], cb[2][5];
#pragma unroll
  for (int t = 0; t < 5; t++) { ca[0][t] = ca[1][t] = cb[0][t] = cb[1][t] = (f32x4){0.f, 0.f, 0.f, 0.f}; }
#pragma unroll
  for (int i = 0; i < 5; i++) {
    WOP(woB, w, i, L_WO + w * 5120); SBAR();
#pragma unroll
    for (int t = 0; t < 5; t++) ca[0][t] = mfma16(wf[i][t], of[0][i], ca[0][t]);
    SBAR(); WOP(woB, 8 + w, i, L_WO + (8 + w) * 5120); if (w == i) WOP(woB, 16, i, L_WO + 16 * 5120); SBAR();
#pragma unroll
    for (int t = 0; t < 5; t++) ca[1][t] = mfma16(wf[i][t], of[1][i], ca[1][t]);
    SBAR();
  }
  STAMP(9);
#pragma unroll
  for (int u = 0; u < 2; u++)
#pragma unroll
    for (int i = 0; i < 5; i++) of[u][i] = LD16(WS_OB + ofr + u * OT_B + i * 1024);
  WAIT_BAR(10);
#pragma unroll
  for (int i = 0; i < 5; i++) { const int gr = kq * 5 + i; const LAS char* wb = lp + (gr < 17 ? L_WO + gr * 5120 : 145408 + (gr - 17) * 5120) + wlane;
#pragma unroll
    for (int t = 0; t < 5; t++) wf[i][t] = trd2(wb + t * 8, wb + 1280 + t * 8); }
#pragma unroll
  for (int t = 0; t < 5; t++) { ca[0][t] *= gt0; ca[1][t] *= gt1; }
  BAR_L();
  SBAR();
  STAMP(10);
#pragma unroll
  for (int u = 0; u < 2; u++)
#pragma unroll
    for (int i = 0; i < 5; i++) {
      if (__builtin_expect(missing(of[u][i]), 0)) {
#pragma unroll 1
        for (unsigned spin = 0; spin < SPIN_CAP; ++spin) {
          asm volatile("" ::: "memory");
#pragma unroll
          for (int j = u * 5 + i; j < 10; j++) of[j / 5][j % 5] = LD16(WS_OB + ofr + (j / 5) * OT_B + (j % 5) * 1024);
          if (!missing(of[u][i])) break;
          __builtin_amdgcn_s_sleep(1);
        }
      }
#pragma unroll
      for (int t = 0; t < 5; t++) cb[u][t] = mfma16(wf[i][t], of[u][i], cb[u][t]);
      SBAR();
    }
  {
    LAS f32x4* xb = (LAS f32x4*)(lp + (tp ? L_WO : 0)) + lane;
    f32x4 v[10];
#pragma unroll
    for (int it = 0; it < 10; it++) { const float gg = 1.0f - (it < 5 ? gt0 : gt1); v[it] = cb[it / 5][it % 5] * gg + ca[it / 5][it % 5]; }
#pragma unroll
    for (int it = 0; it < 10; it++) if (((it * 13) >> 5) != kq) xb[(it * 4 + kq) * 64] = v[it];
    BAR_L();
    u16* op = out + (size_t)R0 * DM + C0 + g * 20;
#pragma unroll
    for (int it = 0; it < 10; it++) if (((it * 13) >> 5) == kq) { const int o_ = (it * 13) >> 5;
      const f32x4 z = (v[it] + xb[(it * 4 + ((o_ + 1) & 3)) * 64]) + (xb[(it * 4 + ((o_ + 2) & 3)) * 64] + xb[(it * 4 + ((o_ + 3) & 3)) * 64]);
      __hip_atomic_store((gu64*)(op + (it / 5) * 16 * DM + (it % 5) * 4), ((u64)pk(z[2], z[3]) << 32) | pk(z[0], z[1]), __ATOMIC_RELAXED, __HIP_MEMORY_SCOPE_SYSTEM); }
  }
  STAMP(11);
}
}

extern "C" void kernel_launch(void* const* d_in, const int* in_sizes, int n_in, void* d_out, int out_size, void* d_ws, size_t ws_size, hipStream_t stream) {
  using namespace gm;
  fused<<<256, 512, 0, stream>>>((const u16*)d_in[0], (const u16*)d_in[1], (const u16*)d_in[2], (const u16*)d_in[3], (const u16*)d_in[5], (const u16*)d_in[6],
                                 (const u16*)d_in[7], (const u16*)d_in[8], (const u16*)d_in[9], (u16*)d_out, (char*)d_ws);
}
```

```cpp
#include <hip/hip_runtime.h>
#include <hip/hip_bf16.h>
#include <stdint.h>

namespace gm {
typedef float f32x16 __attribute__((ext_vector_type(16)));
typedef float f32x4 __attribute__((ext_vector_type(4)));
typedef float f32x2 __attribute__((ext_vector_type(2)));
typedef short s16x4 __attribute__((ext_vector_type(4)));
typedef __bf16 b16x2 __attribute__((ext_vector_type(2)));
typedef __bf16 b16x8 __attribute__((ext_vector_type(8)));
typedef unsigned u32x4 __attribute__((ext_vector_type(4)));
typedef unsigned u32x2 __attribute__((ext_vector_type(2)));
typedef unsigned long long u64;
typedef unsigned short u16;
#define DI __device__ __forceinline__
#define LAS __attribute__((address_space(3)))
#define GAS __attribute__((address_space(1)))
typedef GAS u64 gu64;

constexpr int DM = 640, DH = 20;
constexpr unsigned WS_EPOCH = 0, WS_FKV = 2048, WS_FKVR = 4096, WS_FQ = 8192, WS_FPART = 16384, WS_FOA = 24576, WS_FOB = 40960, WS_G = 57344;
constexpr unsigned WS_KVR = 65536, KVR_B = 8192, WS_KV = 131072, WS_QX = 327680, QX_B = 10240, WS_PART = 1703936, PART_B = 13440;
constexpr unsigned WS_OA = 3670016, WS_OB = 6291456, WS_END = 8912896, OT_B = 20480;
constexpr int VB = 352, TILE_B = 1280 + 4 * VB, CH_B = TILE_B * 8, STEP_B = 2 * CH_B;
constexpr int STAGE_B = 36864, ST_W1 = 10240, ST_X = 20480;
constexpr int L_BUF = 43008, L_P0W = 0, L_P0X0 = 25600, L_P0X1 = 153600, L_RED = 158720, L_RED1 = 40960;
constexpr int L_HO = 86016, L_PHK = 96256, L_QL = 96256, L_VIM = 137216, L_KVT = 147456, L_WPHI = 158592, L_ZERO = 161792, L_TOTAL = 163840;
constexpr int L_WO = 43008, L_XCH = 0;
constexpr float LOG2E = 1.4426950408889634f;
constexpr float QSCALE = 0.22360679774997896f * LOG2E;
constexpr unsigned SPIN_CAP = 20000;

DI unsigned pk(float a, float b) { f32x2 v = {a, b}; b16x2 r = __builtin_convertvector(v, b16x2); return __builtin_bit_cast(unsigned, r); }
DI f32x16 mfma32(u32x4 a, u32x4 b, f32x16 c) { return __builtin_amdgcn_mfma_f32_32x32x16_bf16(__builtin_bit_cast(b16x8, a), __builtin_bit_cast(b16x8, b), c, 0, 0, 0); }
DI f32x16 mfma32k8(u32x2 a, u32x2 b, f32x16 c) { return __builtin_amdgcn_mfma_f32_32x32x8bf16_1k(__builtin_bit_cast(s16x4, a), __builtin_bit_cast(s16x4, b), c, 0, 0, 0); }
DI f32x4 mfma16(u32x4 a, u32x4 b, f32x4 c) { return __builtin_amdgcn_mfma_f32_16x16x32_bf16(__builtin_bit_cast(b16x8, a), __builtin_bit_cast(b16x8, b), c, 0, 0, 0); }
DI u32x2 trd(const LAS char* p) { return __builtin_bit_cast(u32x2, __builtin_amdgcn_ds_read_tr16_b64_v4i16((LAS s16x4*)p)); }
DI u32x4 trd2(const LAS char* p0, const LAS char* p1) { u32x2 a = trd(p0), b = trd(p1); return (u32x4){a.x, a.y, b.x, b.y}; }
DI u32x4 ldsr16(const LAS char* p) { return *(const LAS u32x4*)p; }
DI u32x2 ldsr8(const LAS char* p) { return *(const LAS u32x2*)p; }
DI float ex2(float x) { return __builtin_amdgcn_exp2f(x); }
DI void dma16s(const void* base, unsigned voff, unsigned lds_base) {
  asm volatile("s_mov_b32 m0, %2\n\ts_nop 0\n\tglobal_load_lds_dwordx4 %0, %1" :: "v"(voff), "s"(base), "s"(lds_base) : "memory"); }
DI void dma16s_sc1(const void* base, unsigned voff, unsigned lds_base) {
  asm volatile("s_mov_b32 m0, %2\n\ts_nop 0\n\tglobal_load_lds_dwordx4 %0, %1 sc1" :: "v"(voff), "s"(base), "s"(lds_base) : "memory"); }
DI void dma4s(const void* base, unsigned voff, unsigned lds_base) {
  asm volatile("s_mov_b32 m0, %2\n\ts_nop 0\n\tglobal_load_lds_dword %0, %1" :: "v"(voff), "s"(base), "s"(lds_base) : "memory"); }
DI u32x4 ld16_async(const void* p) { u32x4 r; asm volatile("global_load_dwordx4 %0, %1, off" : "=v"(r) : "v"(p) : "memory"); return r; }
DI u32x2 ld8_sc1_async(const void* p) { u32x2 r; asm volatile("global_load_dwordx2 %0, %1, off sc1" : "=v"(r) : "v"(p) : "memory"); return r; }
#define BAR_L() asm volatile("s_waitcnt lgkmcnt(0)\n\ts_barrier" ::: "memory")
#define WAIT_BAR(N) asm volatile("s_waitcnt vmcnt(" #N ") lgkmcnt(0)\n\ts_barrier" ::: "memory")
#define WAIT_VM(N) asm volatile("s_waitcnt vmcnt(" #N ")" ::: "memory")
#define SBAR() __builtin_amdgcn_sched_barrier(0)
#define RLX_AGENT __ATOMIC_RELAXED, __HIP_MEMORY_SCOPE_AGENT
DI float half_swap_max(float m) { auto rr = __builtin_amdgcn_permlane32_swap(__float_as_uint(m), __float_as_uint(m), false, false); return fmaxf(__uint_as_float(rr[0]), __uint_as_float(rr[1])); }
DI float from_upper(float v) { auto rr = __builtin_amdgcn_permlane32_swap(__float_as_uint(v), __float_as_uint(v), false, false); return __uint_as_float(rr[1]); }

template <int NF>
DI bool flags_set(const char* ws, unsigned off, u64 tag, int lane) {
  gu64* f = (gu64*)(ws + off);
  bool ok = __hip_atomic_load(f + (NF < 64 ? (lane & (NF - 1)) : lane), RLX_AGENT) == tag;
  if (NF >= 128) ok &= __hip_atomic_load(f + 64 + lane, RLX_AGENT) == tag;
  if (NF >= 256) { ok &= __hip_atomic_load(f + 128 + lane, RLX_AGENT) == tag; ok &= __hip_atomic_load(f + 192 + lane, RLX_AGENT) == tag; }
  return ok;
}
template <int NF>
DI void wait_flags(const char* ws, unsigned off, u64 tag, int lane) {
#pragma unroll 1
  for (unsigned spin = 0; spin < SPIN_CAP; ++spin) {
    if (__all(flags_set<NF>(ws, off, tag, lane))) break;
    __builtin_amdgcn_s_sleep(2);
  }
  asm volatile("" ::: "memory");
}
DI void set_flag(char* ws, unsigned off, u64 tag, int lane) { if (lane == 0) __hip_atomic_store((gu64*)(ws + off), tag, RLX_AGENT); }
DI unsigned nz(unsigned v) { return v < 0xFFFFFFFEu ? v : 0xFFFFFFFEu; }
DI unsigned umax3(unsigned a, unsigned b, unsigned c) { unsigned m = a > b ? a : b; return m > c ? m : c; }
DI bool missing(u32x4 v) { return __any(umax3(umax3(v.x, v.y, v.z), v.w, 0u) == 0xFFFFFFFFu); }
#define ONES ((u32x4){0xFFFFFFFFu, 0xFFFFFFFFu, 0xFFFFFFFFu, 0xFFFFFFFFu})
#define ST16(v, off) __builtin_amdgcn_raw_buffer_store_b128((v), wsr, (off), 0, 16)
#define ST8(v, off) __builtin_amdgcn_raw_buffer_store_b64((v), wsr, (off), 0, 16)
#define LD16(off) __builtin_amdgcn_raw_buffer_load_b128(wsr, (off), 0, 16)
#define LD8(off) __builtin_amdgcn_raw_buffer_load_b64(wsr, (off), 0, 16)

#define STAMP(i) do { if ((i) == 11) { gu64* sp_ = (gu64*)(ws + 12582912) + lane; \
   u64 a_ = __hip_atomic_load(sp_, RLX_AGENT), b_ = __hip_atomic_load(sp_ + 64, RLX_AGENT), c_ = __hip_atomic_load(sp_ + 128, RLX_AGENT), d_ = __hip_atomic_load(sp_ + 192, RLX_AGENT); \
   u64 lo_ = tstart - 4096, hi_ = tstart + 4096; \
   while (lo_ < hi_) { u64 mid_ = (lo_ + hi_) >> 1; int n_ = __popcll(__ballot(a_ <= mid_)) + __popcll(__ballot(b_ <= mid_)) + __popcll(__ballot(c_ <= mid_)) + __popcll(__ballot(d_ <= mid_)); if (n_ >= 1) hi_ = mid_; else lo_ = mid_ + 1; } \
   while ((long long)(__builtin_amdgcn_s_memrealtime() - lo_) < 6000ll) __builtin_amdgcn_s_sleep(2); } } while (0)

__global__ __launch_bounds__(512, 2) void fused(const u16* __restrict__ x, const u16* __restrict__ wqkv, const u16* __restrict__ wphi, const u16* __restrict__ woA,
                                                const u16* __restrict__ wq, const u16* __restrict__ wk, const u16* __restrict__ wv, const u16* __restrict__ woB,
                                                const u16* __restrict__ gatew, u16* __restrict__ out, char* ws) {
  __shared__ __attribute__((aligned(16))) char lds[L_TOTAL];
  const int tid = threadIdx.x, lane = tid & 63;
  const int w = __builtin_amdgcn_readfirstlane(tid >> 6) & 7;
  const int l15 = lane & 15, g = lane >> 4, q4 = l15 >> 2, p4 = lane & 3, r = lane & 31, h = lane >> 5;
  const int bx = blockIdx.x & 7, bi = blockIdx.x >> 3;
  const int rt = (bx & 1) * 8 + (bi & 7), hp = (bx >> 1) * 4 + ((bi >> 3) & 3), hq = hp >> 1, role = hp & 1;
  const int w3 = w & 3, w2 = w >> 2;
  LAS char* lp = (LAS char*)lds;
  const unsigned lds0 = (unsigned)(uintptr_t)lds;
  const __amdgpu_buffer_rsrc_t wsr = __builtin_amdgcn_make_buffer_rsrc(ws, 0, WS_END, 0x00020000);
  const u64 tstart = __builtin_amdgcn_s_memrealtime();

  const int T0 = rt * 128 + hq * 16;
  const int C0 = hq * 80;
  const int wlane = ((g >> 1) * 16 + (g & 1) * 4 + q4) * 160 + p4 * 40;
  u64 tag;
  unsigned o0[10], o1[10];
#define KVP(s, slot, p) dma16s_sc1(ws + WS_KV + (s) * STEP_B + (p) * 1024, lane * 16, lds0 + (slot) * STEP_B + (p) * 1024)
#define KVL(s, slot) do { _Pragma("unroll") for (int j = 0; j < 11; j++) if (j < 10 || w3 < 2) KVP(s, slot, w3 + 4 * j); } while (0)
#define KW(s, slot, j) KVP(s, slot, w * 5 + (j))
#define KX(s, slot) do { if (w < 2) KVP(s, slot, 40 + w); } while (0)
  const int qr = w2 * 16 + l15;
  if (w2 == 0) {
    u32x4 gf0, gf1, gf2, gf3, gf4, wpc, tch;
    u32x2 ep = ld8_sc1_async(ws + WS_EPOCH);
    const int lx = w3 < 3 ? L_P0X0 + w3 * 5120 : L_P0X1;
    { const u16* wsrc = (role ? wv : wk) + w3 * 3200;
#pragma unroll
      for (int j = 0; j < 5; j++) { const int n = j * 64 + lane, row = (n * 3277) >> 16, cc = n - row * 20; dma16s(x + T0 * DM + w3 * 160, row * 1280 + cc * 16, lds0 + lx + j * 1024); }
#pragma unroll
      for (int j = 0; j < 6; j++) dma16s(wsrc + j * 512, lane * 16, lds0 + L_P0W + w3 * 6400 + j * 1024);
      if (lane < 16) dma16s(wsrc + 6 * 512, lane * 16, lds0 + L_P0W + w3 * 6400 + 6 * 1024);
      const u16* gp = gatew + w3 * 160 + g * 8;
      gf0 = ld16_async(gp); gf1 = ld16_async(gp + 32); gf2 = ld16_async(gp + 64); gf3 = ld16_async(gp + 96); gf4 = ld16_async(gp + 128);
      wpc = ld16_async(wphi + hq * 1600 + (tid < 200 ? tid : 199) * 8);
      const char* tp = ws + (lane & 7) * 1114112;
      if (w3 == 1) tp = (const char*)(woA + C0 + (lane & 7) * 51200);
      if (w3 == 2) tp = (const char*)(woB + C0 + (lane & 7) * 51200);
      if (w3 == 3) tp = (const char*)(out + (rt * 128 + role * 64) * DM + C0);
      tch = ld16_async(tp);
    }
    asm volatile("s_waitcnt vmcnt(1)" : "+v"(gf0), "+v"(gf1), "+v"(gf2), "+v"(gf3), "+v"(gf4), "+v"(wpc), "+v"(ep) :: "memory");
    tag = (((u64)(unsigned)__builtin_amdgcn_readfirstlane(ep.y) << 32) | (unsigned)__builtin_amdgcn_readfirstlane(ep.x)) + 1ull;
    STAMP(1);
    { const LAS char* wb = lp + L_P0W + (w3 * 160 + g * 8 + q4) * 40;
      const LAS char* xb = lp + lx + l15 * 320 + g * 16;
      const bool gl = l15 == 4;
      f32x4 c0 = {}, c1 = {};
#define P0STEP(s, GF) { const u32x4 b = ldsr16(xb + (s) * 64); c0 = mfma16(trd2(wb + p4 * 8 + (s) * 1280, wb + p4 * 8 + (s) * 1280 + 160), b, c0); \
        u32x4 a = trd2(wb + 32 + (s) * 1280, wb + 32 + (s) * 1280 + 160); if (gl) a = GF; c1 = mfma16(a, b, c1); }
      P0STEP(0, gf0) P0STEP(1, gf1) P0STEP(2, gf2) P0STEP(3, gf3) P0STEP(4, gf4)
#undef P0STEP
      *(LAS f32x4*)(lp + L_RED + tid * 16) = c0;
      if (lane < 32) *(LAS f32x4*)(lp + L_RED1 + (w3 * 32 + lane) * 16) = c1; }
    STAMP(2);
    f32x16 acc[5];
#pragma unroll
    for (int b = 0; b < 5; b++) acc[b] = (f32x16){};
    const int wm = g & 1, whd = 2 * (p4 & 1) + (p4 >> 1);
    const int wo0 = wm * ST_W1 + (8 * h + 4 * wm + q4) * 160 + whd * 40, wo1 = wm * ST_W1 + (8 * h + 4 * (wm ^ 1) + q4) * 160 + whd * 40;
    const int xo = ST_X + (w3 * 32 + r) * 128 + ((h ^ ((r >> 1) & 7)) * 16);
    u32x4 fa[5], fb[5], xa, xb;
#pragma unroll
    for (int j = 0; j < 5; j++) fb[j] = (u32x4){0u, 0u, 0u, 0u};
    xb = (u32x4){0u, 0u, 0u, 0u};
#define KSTEP(FN, XN, s, FC, XC) { XN = ldsr16(sb + (xo ^ ((s) * 32))); \
      _Pragma("unroll") for (int j = 0; j < 5; j++) { acc[j] = mfma32(FC[j], XC, acc[j]); FN[j] = trd2(sb + wo0 + (s) * 2560 + j * 8, sb + wo1 + (s) * 2560 + j * 8); SBAR(); } }
    int bsel = 2;
#pragma unroll 1
    for (int i = 0; i < 10; i++) {
      BAR_L();
      if (i == 1 && role) {
        const unsigned qb = WS_QX + (rt * 8 + hq) * QX_B + w3 * 1280 + lane * 16;
        ST16(ONES, qb); ST16(ONES, qb + 5120); if (lane < 32) ST16(ONES, qb + 1024 + (lane >> 4) * (5120 - 256)); }
      if (i == 4) { asm volatile("s_waitcnt vmcnt(0)" : "+v"(tch) :: "memory"); if (role) set_flag(ws, WS_FQ + ((rt * 8 + hq) * 4 + w3) * 8, tag, lane);
                    if (tid < 200) *(LAS u32x4*)(lp + L_WPHI + tid * 16) = wpc; }
      const LAS char* sb = lp + L_BUF + bsel * STAGE_B;
      bsel = bsel == 2 ? 0 : bsel + 1;
      KSTEP(fa, xa, 0, fb, xb) KSTEP(fb, xb, 1, fa, xa) KSTEP(fa, xa, 2, fb, xb)
      { xb = ldsr16(sb + (xo ^ 96));
        acc[0] = mfma32(fa[0], xa, acc[0]); fb[0] = trd2(sb + wo0 + 7680, sb + wo1 + 7680); fb[1] = trd2(sb + wo0 + 7688, sb + wo1 + 7688); SBAR();
        acc[1] = mfma32(fa[1], xa, acc[1]); fb[2] = trd2(sb + wo0 + 7696, sb + wo1 + 7696); fb[3] = trd2(sb + wo0 + 7704, sb + wo1 + 7704); SBAR();
        acc[2] = mfma32(fa[2], xa, acc[2]); fb[4] = trd2(sb + wo0 + 7712, sb + wo1 + 7712); SBAR();
        acc[3] = mfma32(fa[3], xa, acc[3]); acc[4] = mfma32(fa[4], xa, acc[4]); SBAR(); }
    }
#pragma unroll
    for (int j = 0; j < 5; j++) acc[j] = mfma32(fb[j], xb, acc[j]);
#undef KSTEP
    STAMP(3);
    const float sc = role ? QSCALE : 1.0f;
    unsigned u0[10], u1[10];
#pragma unroll
    for (int t = 0; t < 5; t++)
#pragma unroll
      for (int e = 0; e < 2; e++) {
        auto r1 = __builtin_amdgcn_permlane16_swap(nz(pk(acc[t][8 + 2 * e] * sc, acc[t][9 + 2 * e] * sc)), nz(pk(acc[t][12 + 2 * e] * sc, acc[t][13 + 2 * e] * sc)), false, false);
        o1[2 * t + e] = r1[0]; u1[2 * t + e] = r1[1]; }
    const int qfo = ((g & 1) * 4 + w3) * 1280;
    if (role && g < 2) { const unsigned qb = WS_QX + (rt * 8 + hq) * QX_B + qfo;
      ST16(((u32x4){o1[0], o1[1], o1[2], o1[3]}), qb + l15 * 16); ST16(((u32x4){u1[0], u1[1], u1[2], u1[3]}), qb + 256 + l15 * 16);
      ST16(((u32x4){o1[4], o1[5], o1[6], o1[7]}), qb + 512 + l15 * 16); ST16(((u32x4){u1[4], u1[5], u1[6], u1[7]}), qb + 768 + l15 * 16);
      ST8(((u32x2){o1[8], o1[9]}), qb + 1024 + l15 * 8); ST8(((u32x2){u1[8], u1[9]}), qb + 1152 + l15 * 8); }
#pragma unroll
    for (int t = 0; t < 5; t++)
#pragma unroll
      for (int e = 0; e < 2; e++) {
        auto r0 = __builtin_amdgcn_permlane16_swap(pk(acc[t][2 * e], acc[t][1 + 2 * e]), pk(acc[t][4 + 2 * e], acc[t][5 + 2 * e]), false, false);
        o0[2 * t + e] = r0[0]; u0[2 * t + e] = r0[1]; }
    *(LAS u32x4*)(lp + L_HO + tid * 16) = (u32x4){u0[0], u0[1], u0[2], u0[3]}; *(LAS u32x4*)(lp + L_HO + 4096 + tid * 16) = (u32x4){u0[4], u0[5], u0[6], u0[7]};
    *(LAS u32x2*)(lp + L_HO + 8192 + tid * 8) = (u32x2){u0[8], u0[9]};
    BAR_L();
    if (role) {
      if (g >= 2) { LAS char* d = lp + L_QL + qfo;
        *(LAS u32x4*)(d + l15 * 16) = (u32x4){o1[0], o1[1], o1[2], o1[3]}; *(LAS u32x4*)(d + 256 + l15 * 16) = (u32x4){u1[0], u1[1], u1[2], u1[3]};
        *(LAS u32x4*)(d + 512 + l15 * 16) = (u32x4){o1[4], o1[5], o1[6], o1[7]}; *(LAS u32x4*)(d + 768 + l15 * 16) = (u32x4){u1[4], u1[5], u1[6], u1[7]};
        *(LAS u32x2*)(d + 1024 + l15 * 8) = (u32x2){o1[8], o1[9]}; *(LAS u32x2*)(d + 1152 + l15 * 8) = (u32x2){u1[8], u1[9]}; }
    } else { LAS char* d = lp + L_VIM + (g * 128 + w3 * 32 + l15) * 40;
#pragma unroll
      for (int t = 0; t < 5; t++) { *(LAS u32x2*)(d + t * 8) = (u32x2){o1[2 * t], o1[2 * t + 1]}; *(LAS u32x2*)(d + 640 + t * 8) = (u32x2){u1[2 * t], u1[2 * t + 1]}; } }
  } else {
    u32x2 ep = ld8_sc1_async(ws + WS_EPOCH);
    const bool second = w3 >> 1;
    const unsigned rs = (second && role) ? 1280u : 3840u;
    const char* wsrc = (second ? (role ? (const char*)(wq + C0) : (const char*)(wqkv + 1280 + C0)) : (const char*)(wqkv + (role ? 0 : 640) + C0)) + (w3 & 1) * 32 * rs;
    const char* xsrc = (const char*)(x + (rt * 128 + w3 * 32) * DM);
    int rot = (bi & 7) + 3 * ((bi >> 3) & 3); rot = rot >= 10 ? rot - 10 : rot;
    unsigned so[5], xoff[2];
#pragma unroll
    for (int j = 0; j < 5; j++) { const int c = j * 64 + lane, rho = (c * 6554) >> 16, cc = c - rho * 10; so[j] = (rho ^ (second ? 4 : 0)) * rs + cc * 16; }
#pragma unroll
    for (int j = 0; j < 2; j++) { const int row = j * 8 + (lane >> 3); xoff[j] = row * 1280 + (((lane & 7) ^ ((row >> 1) & 7)) * 16); }
    int bsel = 2, kc = rot;
    auto stage = [&]() {
      const unsigned lb = lds0 + L_BUF + bsel * STAGE_B;
      const char* bw = wsrc + kc * 64 * rs; const char* bxp = xsrc + kc * 128;
#pragma unroll
      for (int j = 0; j < 5; j++) { dma16s(bw, so[j], lb + (w3 * 5 + j) * 1024); if (j < 4) dma16s(bxp + (j >> 1) * 16 * 1280, xoff[j & 1], lb + ST_X + (w3 * 4 + j) * 1024); }
      bsel = bsel == 2 ? 0 : bsel + 1; kc = kc == 9 ? 0 : kc + 1;
    };
    stage(); stage();
    asm volatile("s_waitcnt vmcnt(9)" : "+v"(ep) :: "memory");
    tag = (((u64)(unsigned)__builtin_amdgcn_readfirstlane(ep.y) << 32) | (unsigned)__builtin_amdgcn_readfirstlane(ep.x)) + 1ull;
    STAMP(1); STAMP(2);
#pragma unroll 1
    for (int i = 0; i < 8; i++) {
      WAIT_BAR(9);
      stage();
      if (i == 0 && w3 == 0) {
        if (lane == 0) __hip_atomic_store((gu64*)(ws + 12582912) + blockIdx.x, tstart, RLX_AGENT);
        const unsigned tb = WS_KV + (T0 >> 5) * TILE_B; const int s16 = hq & 1;
        if (!role) {
          const LAS f32x4* rp = (const LAS f32x4*)(lp + (h ? L_RED1 : L_RED)) + (h ? (g & 1) * 16 : g * 32) + l15;
          const int st = h ? 32 : 64, o2 = h ? 0 : 16;
          const f32x4 a = (rp[0] + rp[st]) + (rp[2 * st] + rp[3 * st]), b = (rp[o2] + rp[st + o2]) + (rp[2 * st + o2] + rp[3 * st + o2]);
          const u32x4 v = {pk(a[0], a[1]), pk(a[2], a[3]), pk(b[0], b[1]), pk(b[2], b[3])};
          if (lane < 32) ST16(v, tb + (g * 32 + s16 * 16 + l15) * 16);
          else if (lane < 48) ST8(((u32x2){v.x, v.y}), tb + 1024 + (s16 * 16 + l15) * 8);
          else { const float gv = 1.0f / (1.0f + __expf(-a[0])); __builtin_amdgcn_raw_buffer_store_b32(__float_as_uint(gv), wsr, WS_G + (T0 + l15) * 4, 0, 16); }
        } else if (lane < 44) {
          const int hh = lane >= 22, d = lane - hh * 22, dd = d < 20 ? d : 0, st = dd >= 16 ? 128 : 256;
          const LAS float* rp = (const LAS float*)(lp + (dd >= 16 ? L_RED1 : L_RED)) + (((dd & 15) >> 2) * 16 + 4 * hh) * 4 + (dd & 3);
          float e[8];
#pragma unroll
          for (int j = 0; j < 8; j++) { const int tk = (8 * (j >> 2) + (j & 3)) * 4; e[j] = (rp[tk] + rp[tk + st]) + (rp[tk + 2 * st] + rp[tk + 3 * st]); }
          u32x4 v = {pk(e[0], e[1]), pk(e[2], e[3]), pk(e[4], e[5]), pk(e[6], e[7])};
          if (d == 20) v = (u32x4){0x3F803F80u, 0x3F803F80u, 0x3F803F80u, 0x3F803F80u};
          if (d == 21) v = (u32x4){0u, 0u, 0u, 0u};
          ST16(v, tb + 1280 + (s16 * 2 + hh) * VB + d * 16);
        }
      }
      if (i == 2) { if (w3 == 0) set_flag(ws, WS_FKV + blockIdx.x * 8, tag, lane);
                    if (w3 >= 2) *(LAS u32x4*)(lp + L_ZERO + (tid - 384) * 16) = (u32x4){0u, 0u, 0u, 0u}; }
    }
    u64 fa, fb, fc, fd, fe;
    { gu64* f = (gu64*)(ws + WS_FKV) + lane;
      fa = __hip_atomic_load(f, RLX_AGENT); fb = __hip_atomic_load(f + 64, RLX_AGENT); fc = __hip_atomic_load(f + 128, RLX_AGENT); fd = __hip_atomic_load(f + 192, RLX_AGENT);
      fe = __hip_atomic_load((gu64*)(ws + WS_FQ) + (rt * 8 + hq) * 4 + p4, RLX_AGENT); }
    WAIT_BAR(14);
    asm volatile("" : "+v"(fa), "+v"(fb), "+v"(fc), "+v"(fd), "+v"(fe));
    const bool kvok = __all((fa == tag) & (fb == tag) & (fc == tag) & (fd == tag) & (fe == tag));
    asm volatile("" ::: "memory");
    BAR_L();
    if (!kvok) { wait_flags<256>(ws, WS_FKV, tag, lane); wait_flags<4>(ws, WS_FQ + (rt * 8 + hq) * 32, tag, lane); }
    KVL(0, 0);
    if (blockIdx.x == 0 && w3 == 0) set_flag(ws, WS_EPOCH, tag, lane);
    STAMP(3);
    BAR_L();
    const int ht = tid - 256;
    const u32x4 a = ldsr16(lp + L_HO + ht * 16), b = ldsr16(lp + L_HO + 4096 + ht * 16); const u32x2 c = ldsr8(lp + L_HO + 8192 + ht * 8);
    o0[0] = a.x; o0[1] = a.y; o0[2] = a.z; o0[3] = a.w; o0[4] = b.x; o0[5] = b.y; o0[6] = b.z; o0[7] = b.w; o0[8] = c.x; o0[9] = c.y;
  }
  STAMP(4);
  u32x4 ph[5];
  { const LAS char* pb = (p4 == g) ? lp + L_WPHI + g * 800 + q4 * 40 : lp + L_ZERO;
    const u32x4 b0 = {o0[0], o0[1], o0[2], o0[3]}, b1 = {o0[4], o0[5], o0[6], o0[7]}, b2 = {o0[8], o0[9], 0u, 0u};
    float p[20];
#pragma unroll
    for (int t = 0; t < 5; t++) {
      const u32x2 c2 = trd(pb + 640 + t * 8);
      f32x4 c = {}; c = mfma16(trd2(pb + t * 8, pb + 160 + t * 8), b0, c); c = mfma16(trd2(pb + 320 + t * 8, pb + 480 + t * 8), b1, c); c = mfma16((u32x4){c2.x, c2.y, 0u, 0u}, b2, c);
      p[4 * t] = c[0]; p[4 * t + 1] = c[1]; p[4 * t + 2] = c[2]; p[4 * t + 3] = c[3];
    }
    KX(1, 1); KW(1, 1, 0); SBAR();
    float mx = p[0], mn = p[0];
#pragma unroll
    for (int f = 1; f < 20; f++) { mx = fmaxf(mx, p[f]); mn = fminf(mn, p[f]); }
    const float ma = -mx * LOG2E, mb = mn * LOG2E;
    float ea[20], eb[20], sa = 0.f, sb = 0.f;
#pragma unroll
    for (int f = 0; f < 20; f++) { if (f % 4 == 0 && f < 16) { SBAR(); KW(1, 1, 1 + f / 4); SBAR(); }
      ea[f] = ex2(fmaf(p[f], LOG2E, ma)); sa += ea[f]; eb[f] = ex2(fmaf(p[f], -LOG2E, mb)); sb += eb[f]; }
    const float ia = __builtin_amdgcn_rcpf(sa), ib = __builtin_amdgcn_rcpf(sb);
    unsigned o[20];
#pragma unroll
    for (int f = 0; f < 10; f++) { o[f] = pk(ea[2 * f] * ia, ea[2 * f + 1] * ia); o[10 + f] = pk(eb[2 * f] * ib, eb[2 * f + 1] * ib); }
#pragma unroll
    for (int c = 0; c < 5; c++) ph[c] = (u32x4){o[4 * c], o[4 * c + 1], o[4 * c + 2], o[4 * c + 3]};
  }
  u32x4 q1A, q1B; u32x2 q2A, q2B;
  if (role) {
    WAIT_BAR(0);
    STAMP(5);
    const LAS char* qa = lp + L_QL + w3 * 1280;
    q1A = ldsr16(qa + lane * 16); q1B = ldsr16(qa + 5120 + lane * 16); q2A = ldsr8(qa + 1024 + r * 8); q2B = ldsr8(qa + 6144 + r * 8);
    BAR_L();
  } else {
    const unsigned qb = WS_QX + (rt * 8 + hq) * QX_B + w3 * 1280;
    q1A = LD16(qb + lane * 16); q1B = LD16(qb + 5120 + lane * 16); q2A = LD8(qb + 1024 + r * 8); q2B = LD8(qb + 6144 + r * 8);
    { LAS char* d = lp + L_PHK + (g * 128 + w3 * 32 + qr) * 80;
#pragma unroll
      for (int c = 0; c < 5; c++) *(LAS u32x4*)(d + c * 16) = ph[c]; }
    BAR_L();
    STAMP(5);
    { const int hd = w >> 1, dt = w & 1;
      const LAS char* ab = lp + L_PHK + (hd * 128 + g * 8 + q4) * 80 + p4 * 8;
      const LAS char* bb = lp + L_VIM + (hd * 128 + g * 8 + q4) * 40 + dt * 32 + p4 * 8;
      const bool ones = dt && l15 == 4;
      f32x4 c[3] = {};
#pragma unroll
      for (int ks = 0; ks < 4; ks++) {
        u32x4 b = trd2(bb + ks * 32 * 40, bb + (ks * 32 + 4) * 40);
        if (ones) b = (u32x4){0x3F803F80u, 0x3F803F80u, 0x3F803F80u, 0x3F803F80u};
#pragma unroll
        for (int ft = 0; ft < 3; ft++) c[ft] = mfma16(trd2(ab + ks * 32 * 80 + ft * 32, ab + (ks * 32 + 4) * 80 + ft * 32), b, c[ft]);
      }
      asm volatile("" : "+v"(c[0]), "+v"(c[1]), "+v"(c[2]), "+v"(q1A), "+v"(q1B), "+v"(q2A), "+v"(q2B) :: "memory");
#pragma unroll 1
      for (unsigned spin = 0; spin < SPIN_CAP; ++spin) {
        if (!(missing(q1A) | missing(q1B) | missing((u32x4){q2A.x, q2A.y, q2B.x, q2B.y}))) break;
        __builtin_amdgcn_s_sleep(1);
        asm volatile("" ::: "memory");
        q1A = LD16(qb + lane * 16); q1B = LD16(qb + 5120 + lane * 16); q2A = LD8(qb + 1024 + r * 8); q2B = LD8(qb + 6144 + r * 8);
      }
      asm volatile("" : "+v"(q1A), "+v"(q1B), "+v"(q2A), "+v"(q2B) :: "memory");
      const int d = dt * 16 + l15;
#pragma unroll
      for (int ft = 0; ft < 3; ft++) if (d < 21 && ft * 4 + g < 10) ST16(__builtin_bit_cast(u32x4, c[ft]), WS_PART + (hq * 16 + rt) * PART_B + ((hd * 10 + ft * 4 + g) * 21 + d) * 16);
    }
    WAIT_BAR(3);
  }
  STAMP(6);
  const int vr = r <= 21 ? r : 21;
  const int ok1 = lane * 16, ok2 = h ? (1280 + 20 * 16) : (1024 + r * 8), ov = 1280 + h * VB + vr * 16;
  const f32x16 Z = {};
  if (h) { q2A = (u32x2){0u, 0u}; q2B = (u32x2){0u, 0u}; }
  { const LAS char* t0 = lp;
    const u32x4 k1 = ldsr16(t0 + ok1); const u32x2 k2 = ldsr8(t0 + ok2);
    f32x16 sa = mfma32(k1, q1A, Z); sa = mfma32k8(k2, q2A, sa);
    f32x16 sb = mfma32(k1, q1B, Z); sb = mfma32k8(k2, q2B, sb);
    float ma = sa[0], mb = sb[0];
#pragma unroll
    for (int i = 1; i < 16; i++) { ma = fmaxf(ma, sa[i]); mb = fmaxf(mb, sb[i]); }
    ma = half_swap_max(ma); mb = half_swap_max(mb);
    const float mah = __uint_as_float(__float_as_uint(ma) & 0xFFFF0000u), mbh = __uint_as_float(__float_as_uint(mb) & 0xFFFF0000u);
    if (h) { q2A.x = pk(-mah, mah - ma); q2B.x = pk(-mbh, mbh - mb); } }
  f32x16 OA = {}, OB = {}, SA, SB;
  u32x4 k1, v1, v2, v2n, pA1, pA2, pB1, pB2; u32x2 k2;
  float e0, e1, e2, e3;
  const LAS char* c0p = lp + w2 * CH_B + ok1; const LAS char* c1p = lp + w2 * CH_B + ok2; const LAS char* c2p = lp + w2 * CH_B + ov;
  k1 = ldsr16(c0p); k2 = ldsr8(c1p);
  SA = mfma32(k1, q1A, Z); SA = mfma32k8(k2, q2A, SA);
  v1 = ldsr16(c2p); v2 = v1;
  e0 = e1 = e2 = e3 = 0.f; pB2 = (u32x4){0u, 0u, 0u, 0u};
#define G(MF, LD, C0_, C1_, X, S_, i) MF; LD; SBAR(); C0_; C1_; SBAR(); X##0 = ex2(S_[i]); X##1 = ex2(S_[i + 1]); X##2 = ex2(S_[i + 2]); X##3 = ex2(S_[i + 3]); SBAR();
#define TILE(TB, TN, V2, H0, H1, H2) { float a0, a1, a2, a3; \
    G(SB = mfma32(k1, q1B, Z),    k1 = ldsr16(c0p + (TN)), pB2[2] = pk(e0, e1), pB2[3] = pk(e2, e3), a, SA, 0) \
    G(OB = mfma32(v2, pB2, OB),   V2,                      pA1[0] = pk(a0, a1), pA1[1] = pk(a2, a3), e, SA, 4) \
    G(SB = mfma32k8(k2, q2B, SB), k2 = ldsr8(c1p + (TN)),  pA1[2] = pk(e0, e1), pA1[3] = pk(e2, e3), a, SA, 8) \
    G(OA = mfma32(v1, pA1, OA),   H0,                      pA2[0] = pk(a0, a1), pA2[1] = pk(a2, a3), e, SA, 12) \
    G(SA = mfma32(k1, q1A, Z),    H1,                      pA2[2] = pk(e0, e1), pA2[3] = pk(e2, e3), a, SB, 0) \
    G(OA = mfma32(v2, pA2, OA),   H2,                      pB1[0] = pk(a0, a1), pB1[1] = pk(a2, a3), e, SB, 4) \
    G(SA = mfma32k8(k2, q2A, SA), (void)0,                 pB1[2] = pk(e0, e1), pB1[3] = pk(e2, e3), a, SB, 8) \
    G(OB = mfma32(v1, pB1, OB),   v1 = ldsr16(c2p + (TN)), pB2[0] = pk(a0, a1), pB2[1] = pk(a2, a3), e, SB, 12) }
#define NOP (void)0
#define TL(sl, t, H0, H1, H2) TILE((sl) * STEP_B + (t) * TILE_B, (sl) * STEP_B + ((t) + 1) * TILE_B, v2 = ldsr16(c2p + (sl) * STEP_B + (t) * TILE_B + 2 * VB), H0, H1, H2)
#define TL6(sl, H0, H1) TL(sl, 6, H0, H1, v2n = ldsr16(c2p + (sl) * STEP_B + 7 * TILE_B + 2 * VB))
#define TL7(sl, sn, H0, H1, H2) TILE((sl) * STEP_B + 7 * TILE_B, (sn) * STEP_B, v2 = v2n, H0, H1, H2)
  unsigned woff[5];
#pragma unroll
  for (int j = 0; j < 5; j++) { const int c = j * 64 + lane, rho = (c * 6554) >> 16, cc = c - rho * 10; const int k = (rho & ~12) | ((rho & 4) << 1) | ((rho & 8) >> 1); woff[j] = k * 1280 + cc * 16; }
#define WOP(wo, gr, j, dst) dma16s((wo) + C0 + (gr) * 32 * 640, woff[j], lds0 + (dst) + (j) * 1024)
#define WA(gr, j) WOP(woA, gr, j, L_WO + (gr) * 5120)
#define WX(j) do { if (w < 4) WA(16 + w3, j); else if (w < 7) WOP(woB, 17 + w3, j, 145408 + w3 * 5120); } while (0)
#define PRIO(n) __builtin_amdgcn_s_setprio(n)
#define FILL(j) do { if (role) { const int n_ = w * 3 + (j); ST16(ONES, WS_OB + (rt * 8 + hq) * OT_B + (n_ < 20 ? n_ : 19) * 1024 + lane * 16); } } while (0)
  const bool red = !role && w == 0;
  const int slot = rt * 53 + lane;
  f32x4 part[16];
  auto oa_part = [&]() {
    const LAS char* kb = (q4 == g) ? lp + L_KVT + g * 1920 + p4 * 80 : lp + L_ZERO;
    f32x4 o[6]; u32x4 ka[6], kc[6];
#pragma unroll
    for (int t = 0; t < 6; t++) { o[t] = (f32x4){0.f, 0.f, 0.f, 0.f}; ka[t] = ldsr16(kb + t * 320); }
    SBAR();
#pragma unroll
    for (int t = 0; t < 6; t++) kc[t] = ldsr16(kb + t * 320 + 16);
    SBAR();
#pragma unroll
    for (int t = 0; t < 6; t++) { o[t] = mfma16(ka[t], ph[0], o[t]); ka[t] = ldsr16(kb + t * 320 + 32); SBAR(); }
#pragma unroll
    for (int t = 0; t < 6; t++) { o[t] = mfma16(kc[t], ph[1], o[t]); kc[t] = ldsr16(kb + t * 320 + 48); SBAR(); }
#pragma unroll
    for (int t = 0; t < 6; t++) { o[t] = mfma16(ka[t], ph[2], o[t]); ka[t] = ldsr16(kb + t * 320 + 64); SBAR(); }
#pragma unroll
    for (int t = 0; t < 6; t++) o[t] = mfma16(kc[t], ph[3], o[t]);
#pragma unroll
    for (int t = 0; t < 6; t++) o[5 - t] = mfma16(ka[5 - t], ph[4], o[5 - t]);
    const float inv = __builtin_amdgcn_rcpf(o[5][0] + 1e-6f);
    LAS char* tb = lp + L_WO + w * 5120;
#pragma unroll
    for (int t = 0; t < 5; t++) { const int k0 = g * 20 + 4 * t;
      *(LAS u32x2*)(tb + (k0 >> 3) * 256 + (k0 & 4) * 2 + l15 * 16) = (u32x2){pk(o[t][0] * inv, o[t][1] * inv), pk(o[t][2] * inv, o[t][3] * inv)}; }
    const unsigned oa = WS_OA + (rt * 8 + w3 * 2 + w2) * OT_B + hq * 2560 + lane * 16;
    const u32x4 z0 = ldsr16(tb + lane * 16), z1 = ldsr16(tb + 1024 + lane * 16), z2 = ldsr16(tb + 2048 + r * 16);
    ST16(z0, oa); ST16(z1, oa + 1024); if (!h) ST16(z2, oa + 2048);
    SBAR();
  };
  PRIO(3);
  TL(0, 0, NOP, NOP, NOP)
  TL(0, 1, if (!role) { WAIT_VM(0); set_flag(ws, WS_FPART + ((hq * 16 + rt) * 8 + w) * 8, tag, lane); }, NOP, KX(2, 2))
  PRIO(2);
  TL(0, 2, KW(2, 2, 0), NOP, KW(2, 2, 1)) TL(0, 3, KW(2, 2, 2), NOP, KW(2, 2, 3)) TL(0, 4, KW(2, 2, 4), NOP, FILL(0))
  TL(0, 5, FILL(1), NOP, FILL(2))
  TL6(0, if (red) { wait_flags<128>(ws, WS_FPART + hq * 1024, tag, lane); const int sc_ = slot < 840 ? slot : 839;
                      _Pragma("unroll") for (int j = 0; j < 16; j++) part[j] = __builtin_bit_cast(f32x4, LD16(WS_PART + (hq * 16 + j) * PART_B + sc_ * 16)); }, NOP)
  TL7(0, 1, NOP, NOP, NOP)
  TL(1, 0, if (red) { f32x4 a = ((part[0] + part[1]) + (part[2] + part[3])) + ((part[4] + part[5]) + (part[6] + part[7]));
                      a += ((part[8] + part[9]) + (part[10] + part[11])) + ((part[12] + part[13]) + (part[14] + part[15]));
                      const int hf = (slot * 3121) >> 16; const int d = slot - hf * 21; const int hd = (hf * 26) >> 8; const int fq = hf - hd * 10;
                      if (lane < 53 && slot < 840) ST8(((u32x2){pk(a[0], a[1]), pk(a[2], a[3])}), WS_KVR + hq * KVR_B + hd * 1920 + d * 80 + fq * 8);
                      WAIT_VM(0); set_flag(ws, WS_FKVR + (hq * 16 + rt) * 8, tag, lane); }, NOP, NOP)
  TL(1, 1, NOP, NOP, NOP)
  TL(1, 2, if (w == 5 || w == 6) { const int row = (w - 5) * 32 + (lane >> 1);
                      dma4s(woB + C0 + ((rt & 7) * 2 + role) * 40 * 640, (row < 40 ? row : 39) * 1280 + (lane & 1) * 156, lds0 + 145408); }, NOP, NOP)
  TL(1, 3, NOP, NOP, NOP)
  PRIO(1);
  TL(1, 4, if (role && w == 1) { wait_flags<16>(ws, WS_FKVR + hq * 128, tag, lane);
                      _Pragma("unroll") for (int p = 0; p < 8; p++) dma16s_sc1(ws + WS_KVR + hq * KVR_B + p * 1024, lane * 16, lds0 + L_KVT + p * 1024); }, NOP, NOP)
  TL(1, 5, NOP, NOP, NOP)
  PRIO(0);
  TL6(1, NOP, NOP)
  WAIT_BAR(0);
  STAMP(12);
  PRIO(3);
  TL7(1, 2, NOP, NOP, NOP)
  if (role && w2) oa_part();
  TL(2, 0, KX(3, 0), NOP, KW(3, 0, 0)) TL(2, 1, KW(3, 0, 1), NOP, KW(3, 0, 2)) TL(2, 2, KW(3, 0, 3), NOP, KW(3, 0, 4))
  PRIO(2);
  if (role && !w2) oa_part();
  TL(2, 3, NOP, NOP, WA(w, 0)) TL(2, 4, WA(w, 1), NOP, WA(w, 2))
  PRIO(1);
  TL(2, 5, WA(w, 3), NOP, WA(w, 4))
  PRIO(0);
  TL6(2, NOP, NOP)
  WAIT_BAR(5);
  STAMP(13);
  PRIO(3);
  if (role) set_flag(ws, WS_FOA + ((rt * 8 + hq) * 8 + w) * 8, tag, lane);
  TL7(2, 0, NOP, NOP, NOP)
  TL(0, 0, WA(8 + w, 0), NOP, WA(8 + w, 1)) TL(0, 1, WA(8 + w, 2), NOP, WA(8 + w, 3)) TL(0, 2, WA(8 + w, 4), NOP, WX(0))
  TL(0, 3, WX(1), NOP, WX(2))
  PRIO(2);
  TL(0, 4, WX(3), NOP, WX(4))
  u64 foa;
  TL(0, 5, foa = __hip_atomic_load((gu64*)(ws + WS_FOA) + rt * 64 + lane, RLX_AGENT), NOP, NOP)
  PRIO(1);
  TL6(0, NOP, NOP)
  PRIO(0);
  const int tp = w & 1, kq = w >> 1;
  const unsigned ofr = (rt * 8 + role * 4 + tp * 2) * OT_B + kq * 5120 + lane * 16;
  const int R0 = rt * 128 + role * 64 + tp * 32 + l15;
  u32x4 of[2][5]; float gt0, gt1;
  asm volatile("" : "+v"(foa));
  if (!__all(foa == tag)) wait_flags<64>(ws, WS_FOA + rt * 512, tag, lane);
  asm volatile("" ::: "memory");
#define OFL(u) do { _Pragma("unroll") for (int i = 0; i < 5; i++) of[u][i] = LD16(WS_OA + ofr + (u) * OT_B + i * 1024); } while (0)
  TL7(0, 0, OFL(0), (gt0 = __uint_as_float(__builtin_amdgcn_raw_buffer_load_b32(wsr, WS_G + R0 * 4, 0, 16)), gt1 = __uint_as_float(__builtin_amdgcn_raw_buffer_load_b32(wsr, WS_G + R0 * 4 + 64, 0, 16))), OFL(1))
  pB2[2] = pk(e0, e1); pB2[3] = pk(e2, e3);
  OB = mfma32(v2, pB2, OB);
#undef TL
#undef TL6
#undef TL7
#undef TILE
#undef G
  STAMP(7);
  { LAS f32x4* xb = (LAS f32x4*)(lp + L_XCH) + w3 * (64 * 6);
#pragma unroll
    for (int i = 0; i < 12; i++) { auto rr = __builtin_amdgcn_permlane16_swap(__float_as_uint(OA[i]), __float_as_uint(OB[i]), false, false); OA[i] = __uint_as_float(rr[0]); OB[i] = __uint_as_float(rr[1]); }
    const f32x16 snd = w2 ? OA : OB;
    BAR_L();
#pragma unroll
    for (int c = 0; c < 3; c++) xb[(w2 * 3 + c) * 64 + lane] = (f32x4){snd[4 * c], snd[4 * c + 1], snd[4 * c + 2], snd[4 * c + 3]};
    asm volatile("s_waitcnt vmcnt(12) lgkmcnt(0)\n\ts_barrier" ::: "memory");
    f32x16 O = w2 ? OB : OA;
#pragma unroll
    for (int c = 0; c < 3; c++) { const f32x4 t = xb[((w2 ^ 1) * 3 + c) * 64 + lane]; O[4 * c] += t[0]; O[4 * c + 1] += t[1]; O[4 * c + 2] += t[2]; O[4 * c + 3] += t[3]; }
    float l = from_upper(O[8]);
    float amax = 0.f;
#pragma unroll
    for (int i = 0; i < 12; i++) amax += fabsf(O[i]);
    const bool good = (l >= 0x1p-30f) && (l <= 0x1p100f) && (amax <= 0x1p126f);
    if (__builtin_expect(!__all(good), 0)) {
      u32x4 q1; u32x2 q2;
#pragma unroll
      for (int i = 0; i < 4; i++) { auto rr = __builtin_amdgcn_permlane16_swap(q1A[i], q1B[i], false, false); q1[i] = w2 ? rr[1] : rr[0]; }
#pragma unroll
      for (int i = 0; i < 2; i++) { auto rr = __builtin_amdgcn_permlane16_swap(q2A[i], q2B[i], false, false); q2[i] = h ? 0u : (w2 ? rr[1] : rr[0]); }
      float m = -INFINITY;
#pragma unroll 1
      for (int t = 0; t < 64; t++) {
        f32x16 s = mfma32(LD16(WS_KV + t * TILE_B + ok1), q1, Z); s = mfma32k8(LD8(WS_KV + t * TILE_B + ok2), q2, s);
#pragma unroll
        for (int i = 0; i < 16; i++) m = fmaxf(m, s[i]);
      }
      m = half_swap_max(m);
      O = Z;
#pragma unroll 1
      for (int t = 0; t < 64; t++) {
        f32x16 s = mfma32(LD16(WS_KV + t * TILE_B + ok1), q1, Z); s = mfma32k8(LD8(WS_KV + t * TILE_B + ok2), q2, s);
#pragma unroll
        for (int i = 0; i < 16; i++) s[i] = ex2(s[i] - m);
        O = mfma32(LD16(WS_KV + t * TILE_B + ov), (u32x4){pk(s[0], s[1]), pk(s[2], s[3]), pk(s[4], s[5]), pk(s[6], s[7])}, O);
        O = mfma32(LD16(WS_KV + t * TILE_B + ov + 2 * VB), (u32x4){pk(s[8], s[9]), pk(s[10], s[11]), pk(s[12], s[13]), pk(s[14], s[15])}, O);
      }
      l = from_upper(O[8]);
    }
    const float inv = __builtin_amdgcn_rcpf(l);
    LAS char* tb = lp + 24576 + w * 1536;
#pragma unroll
    for (int c = 0; c < 3; c++) { const int k0 = (g & 1) * 20 + 8 * c + 4 * h;
      *(LAS u32x2*)(tb + ((c == 2 && h) ? 1280 + (g & 1) * 8 : (k0 >> 3) * 256 + (k0 & 4) * 2) + l15 * 16) = (u32x2){nz(pk(O[4 * c] * inv, O[4 * c + 1] * inv)), nz(pk(O[4 * c + 2] * inv, O[4 * c + 3] * inv))}; }
    const unsigned ob = WS_OB + (rt * 8 + w3 * 2 + w2) * OT_B + hp * 1280 + lane * 16;
    const u32x4 z0 = ldsr16(tb + lane * 16), z1 = ldsr16(tb + 1024 + l15 * 16);
    ST16(z0, ob); if (lane < 16) ST16(z1, ob + 1024);
  }
  STAMP(8);
  u32x4 wf[5][5];
#pragma unroll
  for (int i = 0; i < 5; i++) { const LAS char* wb = lp + L_WO + (kq * 5 + i) * 5120 + wlane;
#pragma unroll
    for (int t = 0; t < 5; t++) wf[i][t] = trd2(wb + t * 8, wb + 1280 + t * 8); }
  BAR_L();
  f32x4 ca[2][5], cb[2][5];
#pragma unroll
  for (int t = 0; t < 5; t++) { ca[0][t] = ca[1][t] = cb[0][t] = cb[1][t] = (f32x4){0.f, 0.f, 0.f, 0.f}; }
#pragma unroll
  for (int i = 0; i < 5; i++) {
    WOP(woB, w, i, L_WO + w * 5120); SBAR();
#pragma unroll
    for (int t = 0; t < 5; t++) ca[0][t] = mfma16(wf[i][t], of[0][i], ca[0][t]);
    SBAR(); WOP(woB, 8 + w, i, L_WO + (8 + w) * 5120); if (w == i) WOP(woB, 16, i, L_WO + 16 * 5120); SBAR();
#pragma unroll
    for (int t = 0; t < 5; t++) ca[1][t] = mfma16(wf[i][t], of[1][i], ca[1][t]);
    SBAR();
  }
  STAMP(9);
#pragma unroll
  for (int u = 0; u < 2; u++)
#pragma unroll
    for (int i = 0; i < 5; i++) of[u][i] = LD16(WS_OB + ofr + u * OT_B + i * 1024);
  WAIT_BAR(10);
#pragma unroll
  for (int i = 0; i < 5; i++) { const int gr = kq * 5 + i; const LAS char* wb = lp + (gr < 17 ? L_WO + gr * 5120 : 145408 + (gr - 17) * 5120) + wlane;
#pragma unroll
    for (int t = 0; t < 5; t++) wf[i][t] = trd2(wb + t * 8, wb + 1280 + t * 8); }
#pragma unroll
  for (int t = 0; t < 5; t++) { ca[0][t] *= gt0; ca[1][t] *= gt1; }
  BAR_L();
  SBAR();
  STAMP(10);
#pragma unroll
  for (int u = 0; u < 2; u++)
#pragma unroll
    for (int i = 0; i < 5; i++) {
      if (__builtin_expect(missing(of[u][i]), 0)) {
#pragma unroll 1
        for (unsigned spin = 0; spin < SPIN_CAP; ++spin) {
          asm volatile("" ::: "memory");
#pragma unroll
          for (int j = u * 5 + i; j < 10; j++) of[j / 5][j % 5] = LD16(WS_OB + ofr + (j / 5) * OT_B + (j % 5) * 1024);
          if (!missing(of[u][i])) break;
          __builtin_amdgcn_s_sleep(1);
        }
      }
#pragma unroll
      for (int t = 0; t < 5; t++) cb[u][t] = mfma16(wf[i][t], of[u][i], cb[u][t]);
      SBAR();
    }
  {
    LAS f32x4* xb = (LAS f32x4*)(lp + (tp ? L_WO : 0)) + lane;
    f32x4 v[10];
#pragma unroll
    for (int it = 0; it < 10; it++) { const float gg = 1.0f - (it < 5 ? gt0 : gt1); v[it] = cb[it / 5][it % 5] * gg + ca[it / 5][it % 5]; }
#pragma unroll
    for (int it = 0; it < 10; it++) if (((it * 13) >> 5) != kq) xb[(it * 4 + kq) * 64] = v[it];
    BAR_L();
    u16* op = out + (size_t)R0 * DM + C0 + g * 20;
#pragma unroll
    for (int it = 0; it < 10; it++) if (((it * 13) >> 5) == kq) { const int o_ = (it * 13) >> 5;
      const f32x4 z = (v[it] + xb[(it * 4 + ((o_ + 1) & 3)) * 64]) + (xb[(it * 4 + ((o_ + 2) & 3)) * 64] + xb[(it * 4 + ((o_ + 3) & 3)) * 64]);
      *(u32x2*)(op + (it / 5) * 16 * DM + (it % 5) * 4) = (u32x2){pk(z[0], z[1]), pk(z[2], z[3])}; }
  }
  STAMP(11);
}
}

extern "C" void kernel_launch(void* const* d_in, const int* in_sizes, int n_in, void* d_out, int out_size, void* d_ws, size_t ws_size, hipStream_t stream) {
  using namespace gm;
  fused<<<256, 512, 0, stream>>>((const u16*)d_in[0], (const u16*)d_in[1], (const u16*)d_in[2], (const u16*)d_in[3], (const u16*)d_in[5], (const u16*)d_in[6],
                                 (const u16*)d_in[7], (const u16*)d_in[8], (const u16*)d_in[9], (u16*)d_out, (char*)d_ws);
}
```

```cpp
#include <hip/hip_runtime.h>
#include <hip/hip_bf16.h>
#include <stdint.h>

namespace gm {
typedef float f32x16 __attribute__((ext_vector_type(16)));
typedef float f32x4 __attribute__((ext_vector_type(4)));
typedef float f32x2 __attribute__((ext_vector_type(2)));
typedef short s16x4 __attribute__((ext_vector_type(4)));
typedef __bf16 b16x2 __attribute__((ext_vector_type(2)));
typedef __bf16 b16x8 __attribute__((ext_vector_type(8)));
typedef unsigned u32x4 __attribute__((ext_vector_type(4)));
typedef unsigned u32x2 __attribute__((ext_vector_type(2)));
typedef unsigned long long u64;
typedef unsigned short u16;
#define DI __device__ __forceinline__
#define LAS __attribute__((address_space(3)))
#define GAS __attribute__((address_space(1)))
typedef GAS u64 gu64;

constexpr int DM = 640;
constexpr unsigned WS_EPOCH = 0, WS_FKV = 2048, WS_FKVR = 4096, WS_FQ = 8192, WS_FPART = 16384, WS_FOA = 24576, WS_G = 57344;
constexpr unsigned WS_KVR = 8912896, KVR_B = 10240, WS_KV = 131072, WS_QX = 327680, QX_B = 10240, WS_PART = 1703936, PART_B = 15360;
constexpr unsigned WS_OA = 3670016, WS_OB = 6291456, WS_END = 8994816, OT_B = 20480;
constexpr int VB = 352, TILE_B = 1280 + 4 * VB, CH_B = TILE_B * 8, STEP_B = 2 * CH_B;
constexpr int STAGE_B = 36864, ST_W1 = 10240, ST_X = 20480;
constexpr int L_BUF = 43008, L_P0W = 0, L_P0X0 = 25600, L_P0X1 = 153600, L_RED = 158720, L_RED1 = 40960;
constexpr int L_HO = 86016, L_QL = 96256, L_VIM = 96256, L_PHK = 116736, L_WPHI = 158592, L_ZERO = 161792, L_CNT = 163712, L_TOTAL = 163840;
constexpr int L_WO = 43008;
constexpr int CODE_LINES = 0;
constexpr float LOG2E = 1.4426950408889634f;
constexpr float QSCALE = 0.22360679774997896f * LOG2E;
constexpr unsigned SPIN_CAP = 20000, LSPIN_CAP = 400000;

DI unsigned pk(float a, float b) { f32x2 v = {a, b}; b16x2 r = __builtin_convertvector(v, b16x2); return __builtin_bit_cast(unsigned, r); }
DI f32x16 mfma32(u32x4 a, u32x4 b, f32x16 c) { return __builtin_amdgcn_mfma_f32_32x32x16_bf16(__builtin_bit_cast(b16x8, a), __builtin_bit_cast(b16x8, b), c, 0, 0, 0); }
DI f32x16 mfma32k8(u32x2 a, u32x2 b, f32x16 c) { return __builtin_amdgcn_mfma_f32_32x32x8bf16_1k(__builtin_bit_cast(s16x4, a), __builtin_bit_cast(s16x4, b), c, 0, 0, 0); }
DI f32x4 mfma16(u32x4 a, u32x4 b, f32x4 c) { return __builtin_amdgcn_mfma_f32_16x16x32_bf16(__builtin_bit_cast(b16x8, a), __builtin_bit_cast(b16x8, b), c, 0, 0, 0); }
DI u32x2 trd(const LAS char* p) { return __builtin_bit_cast(u32x2, __builtin_amdgcn_ds_read_tr16_b64_v4i16((LAS s16x4*)p)); }
DI u32x4 trd2(const LAS char* p0, const LAS char* p1) { u32x2 a = trd(p0), b = trd(p1); return (u32x4){a.x, a.y, b.x, b.y}; }
DI u32x4 ldsr16(const LAS char* p) { return *(const LAS u32x4*)p; }
DI u32x2 ldsr8(const LAS char* p) { return *(const LAS u32x2*)p; }
DI float ex2(float x) { return __builtin_amdgcn_exp2f(x); }
DI void dma16s(const void* base, unsigned voff, unsigned lds_base) {
  asm volatile("s_mov_b32 m0, %2\n\ts_nop 0\n\tglobal_load_lds_dwordx4 %0, %1" :: "v"(voff), "s"(base), "s"(lds_base) : "memory"); }
DI void dma16s_sc1(const void* base, unsigned voff, unsigned lds_base) {
  asm volatile("s_mov_b32 m0, %2\n\ts_nop 0\n\tglobal_load_lds_dwordx4 %0, %1 sc1" :: "v"(voff), "s"(base), "s"(lds_base) : "memory"); }
DI void dma4s(const void* base, unsigned voff, unsigned lds_base) {
  asm volatile("s_mov_b32 m0, %2\n\ts_nop 0\n\tglobal_load_lds_dword %0, %1" :: "v"(voff), "s"(base), "s"(lds_base) : "memory"); }
DI u32x4 ld16_async(const void* p) { u32x4 r; asm volatile("global_load_dwordx4 %0, %1, off" : "=v"(r) : "v"(p) : "memory"); return r; }
DI u32x2 ld8_sc1_async(const void* p) { u32x2 r; asm volatile("global_load_dwordx2 %0, %1, off sc1" : "=v"(r) : "v"(p) : "memory"); return r; }
#define BAR_L() asm volatile("s_waitcnt lgkmcnt(0)\n\ts_barrier" ::: "memory")
#define WAIT_BAR(N) asm volatile("s_waitcnt vmcnt(" #N ") lgkmcnt(0)\n\ts_barrier" ::: "memory")
#define WAIT_VM(N) asm volatile("s_waitcnt vmcnt(" #N ")" ::: "memory")
#define SBAR() __builtin_amdgcn_sched_barrier(0)
#define RLX_AGENT __ATOMIC_RELAXED, __HIP_MEMORY_SCOPE_AGENT
DI float half_swap_max(float m) { auto rr = __builtin_amdgcn_permlane32_swap(__float_as_uint(m), __float_as_uint(m), false, false); return fmaxf(__uint_as_float(rr[0]), __uint_as_float(rr[1])); }
DI float from_upper(float v) { auto rr = __builtin_amdgcn_permlane32_swap(__float_as_uint(v), __float_as_uint(v), false, false); return __uint_as_float(rr[1]); }

template <int NF>
DI bool flags_set(const char* ws, unsigned off, u64 tag, int lane) {
  gu64* f = (gu64*)(ws + off);
  bool ok = __hip_atomic_load(f + (NF < 64 ? (lane & (NF - 1)) : lane), RLX_AGENT) == tag;
  if (NF >= 128) ok &= __hip_atomic_load(f + 64 + lane, RLX_AGENT) == tag;
  if (NF >= 256) { ok &= __hip_atomic_load(f + 128 + lane, RLX_AGENT) == tag; ok &= __hip_atomic_load(f + 192 + lane, RLX_AGENT) == tag; }
  return ok;
}
template <int NF>
DI void wait_flags(const char* ws, unsigned off, u64 tag, int lane) {
#pragma unroll 1
  for (unsigned spin = 0; spin < SPIN_CAP; ++spin) {
    if (__all(flags_set<NF>(ws, off, tag, lane))) break;
    __builtin_amdgcn_s_sleep(2);
  }
  asm volatile("" ::: "memory");
}
DI void set_flag(char* ws, unsigned off, u64 tag, int lane) { if (lane == 0) __hip_atomic_store((gu64*)(ws + off), tag, RLX_AGENT); }
DI unsigned nz(unsigned v) { return v < 0xFFFFFFFEu ? v : 0xFFFFFFFEu; }
DI unsigned umax3(unsigned a, unsigned b, unsigned c) { unsigned m = a > b ? a : b; return m > c ? m : c; }
DI bool missing(u32x4 v) { return __any(umax3(umax3(v.x, v.y, v.z), v.w, 0u) == 0xFFFFFFFFu); }
#define ONES ((u32x4){0xFFFFFFFFu, 0xFFFFFFFFu, 0xFFFFFFFFu, 0xFFFFFFFFu})
#define ST16(v, off) __builtin_amdgcn_raw_buffer_store_b128((v), wsr, (off), 0, 16)
#define ST8(v, off) __builtin_amdgcn_raw_buffer_store_b64((v), wsr, (off), 0, 16)
#define LD16(off) __builtin_amdgcn_raw_buffer_load_b128(wsr, (off), 0, 16)
#define LD8(off) __builtin_amdgcn_raw_buffer_load_b64(wsr, (off), 0, 16)

#define STAMP(i) do {} while (0)

__global__ __launch_bounds__(512, 2) void fused(const u16* __restrict__ x, const u16* __restrict__ wqkv, const u16* __restrict__ wphi, const u16* __restrict__ woA,
                                                const u16* __restrict__ wq, const u16* __restrict__ wk, const u16* __restrict__ wv, const u16* __restrict__ woB,
                                                const u16* __restrict__ gatew, u16* __restrict__ out, char* ws) {
  __shared__ __attribute__((aligned(16))) char lds[L_TOTAL];
  const int tid = threadIdx.x, lane = tid & 63;
  const int w = __builtin_amdgcn_readfirstlane(tid >> 6) & 7;
  const int l15 = lane & 15, g = lane >> 4, q4 = l15 >> 2, p4 = lane & 3, r = lane & 31, h = lane >> 5;
  const int bx = blockIdx.x & 7, bi = blockIdx.x >> 3;
  const int rt = (bx & 1) * 8 + (bi & 7), hp = (bx >> 1) * 4 + ((bi >> 3) & 3), hq = hp >> 1, role = hp & 1;
  const int w3 = w & 3, w2 = w >> 2;
  LAS char* lp = (LAS char*)lds;
  const unsigned lds0 = (unsigned)(uintptr_t)lds;
  const __amdgpu_buffer_rsrc_t wsr = __builtin_amdgcn_make_buffer_rsrc(ws, 0, WS_END, 0x00020000);
  LAS unsigned* cnt = (LAS unsigned*)(lp + L_CNT);
#define ARRIVE(c) do { asm volatile("s_waitcnt lgkmcnt(0)" ::: "memory"); if (lane == 0) __hip_atomic_fetch_add(cnt + (c), 1u, __ATOMIC_RELAXED, __HIP_MEMORY_SCOPE_WORKGROUP); asm volatile("" ::: "memory"); } while (0)
#define PEEK(c) ((unsigned)__builtin_amdgcn_readfirstlane(__hip_atomic_load(cnt + (c), __ATOMIC_RELAXED, __HIP_MEMORY_SCOPE_WORKGROUP)))
#define AWAIT(c, n) do { _Pragma("unroll 1") for (unsigned sp_ = 0; sp_ < LSPIN_CAP; ++sp_) { if (PEEK(c) >= (unsigned)(n)) break; __builtin_amdgcn_s_sleep(1); } asm volatile("" ::: "memory"); } while (0)
  STAMP(0);
  u64 pcr; asm volatile("s_getpc_b64 %0" : "=s"(pcr));
  const char* pc0 = (const char*)(pcr & ~127ull);

  const int T0 = rt * 128 + hq * 16;
  const int C0 = hq * 80;
  const int wlane = ((g >> 1) * 16 + (g & 1) * 4 + q4) * 160 + p4 * 40;
  u64 tag;
  unsigned o0[10];
#define KVP(s, slot, p) dma16s_sc1(ws + WS_KV + (s) * STEP_B + (p) * 1024, lane * 16, lds0 + (slot) * STEP_B + (p) * 1024)
#define KVL(s, slot) do { _Pragma("unroll") for (int j = 0; j < 11; j++) if (j < 10 || w3 < 2) KVP(s, slot, w3 + 4 * j); } while (0)
#define KW(s, slot, j) KVP(s, slot, w * 5 + (j))
#define KX(s, slot) do { if (w < 2) KVP(s, slot, 40 + w); } while (0)
  if (w2 == 0) {
    u32x4 gf0, gf1, gf2, gf3, gf4, wpc; u32x2 tch;
    u32x2 ep = ld8_sc1_async(ws + WS_EPOCH);
    const int lx = w3 < 3 ? L_P0X0 + w3 * 5120 : L_P0X1;
    { const u16* wsrc = (role ? wv : wk) + w3 * 3200;
#pragma unroll
      for (int j = 0; j < 5; j++) { const int n = j * 64 + lane, row = (n * 3277) >> 16, cc = n - row * 20; dma16s(x + T0 * DM + w3 * 160, row * 1280 + cc * 16, lds0 + lx + j * 1024); }
#pragma unroll
      for (int j = 0; j < 6; j++) dma16s(wsrc + j * 512, lane * 16, lds0 + L_P0W + w3 * 6400 + j * 1024);
      if (lane < 16) dma16s(wsrc + 6 * 512, lane * 16, lds0 + L_P0W + w3 * 6400 + 6 * 1024);
      const u16* gp = gatew + w3 * 160 + g * 8;
      gf0 = ld16_async(gp); gf1 = ld16_async(gp + 32); gf2 = ld16_async(gp + 64); gf3 = ld16_async(gp + 96); gf4 = ld16_async(gp + 128);
      wpc = ld16_async(wphi + hq * 1600 + (tid < 200 ? tid : 199) * 8);
      const char* tp = ws + (lane & 7) * 1114112;
      if (lane >= 8) tp = ws + (lane < 16 ? WS_FKV + blockIdx.x * 128 : WS_KVR + blockIdx.x * 320 + (lane & 3) * 96);
      if (w3 == 1) tp = (const char*)(woA + C0 + (lane & 7) * 51200);
      if (w3 == 2) tp = (const char*)(woB + C0 + (lane & 7) * 51200);
      if (w3 == 3) { tp = (const char*)(out + (rt * 128 + role * 64) * DM + C0);
        if (lane < CODE_LINES) tp = pc0 + ((lane * 32 + (bi & 31)) * 128); }
      tch = ld8_sc1_async(tp);
    }
    asm volatile("s_waitcnt vmcnt(1)" : "+v"(gf0), "+v"(gf1), "+v"(gf2), "+v"(gf3), "+v"(gf4), "+v"(wpc), "+v"(ep) :: "memory");
    tag = (((u64)(unsigned)__builtin_amdgcn_readfirstlane(ep.y) << 32) | (unsigned)__builtin_amdgcn_readfirstlane(ep.x)) + 1ull;
    STAMP(1);
    { const LAS char* wb = lp + L_P0W + (w3 * 160 + g * 8 + q4) * 40;
      const LAS char* xb = lp + lx + l15 * 320 + g * 16;
      const bool gl = l15 == 4;
      f32x4 c0 = {}, c1 = {};
#define P0STEP(s, GF) { const u32x4 b = ldsr16(xb + (s) * 64); c0 = mfma16(trd2(wb + p4 * 8 + (s) * 1280, wb + p4 * 8 + (s) * 1280 + 160), b, c0); \
        u32x4 a = trd2(wb + 32 + (s) * 1280, wb + 32 + (s) * 1280 + 160); if (gl) a = GF; c1 = mfma16(a, b, c1); }
      P0STEP(0, gf0) P0STEP(1, gf1) P0STEP(2, gf2) P0STEP(3, gf3) P0STEP(4, gf4)
#undef P0STEP
      *(LAS f32x4*)(lp + L_RED + tid * 16) = c0;
      if (lane < 32) *(LAS f32x4*)(lp + L_RED1 + (w3 * 32 + lane) * 16) = c1; }
    STAMP(2);
    f32x16 acc[5];
#pragma unroll
    for (int b = 0; b < 5; b++) acc[b] = (f32x16){};
    const int wm = g & 1, whd = p4;
    const int wo0 = wm * ST_W1 + (8 * h + 4 * wm + q4) * 160 + whd * 40, wo1 = wm * ST_W1 + (8 * h + 4 * (wm ^ 1) + q4) * 160 + whd * 40;
    const int xo = ST_X + (w3 * 32 + r) * 128 + ((h ^ ((r >> 1) & 7)) * 16);
    u32x4 fa[5], fb[5], xa, xb;
#pragma unroll
    for (int j = 0; j < 5; j++) fb[j] = (u32x4){0u, 0u, 0u, 0u};
    xb = (u32x4){0u, 0u, 0u, 0u};
#define KSTEP(FN, XN, s, FC, XC) { XN = ldsr16(sb + (xo ^ ((s) * 32))); \
      _Pragma("unroll") for (int j = 0; j < 5; j++) { acc[j] = mfma32(FC[j], XC, acc[j]); FN[j] = trd2(sb + wo0 + (s) * 2560 + j * 8, sb + wo1 + (s) * 2560 + j * 8); SBAR(); } }
    int bsel = 2;
#pragma unroll 1
    for (int i = 0; i < 10; i++) {
      BAR_L();
      if (i == 1 && role) {
        const unsigned qb = WS_QX + (rt * 8 + hq) * QX_B + w3 * 1280 + lane * 16;
        ST16(ONES, qb); ST16(ONES, qb + 5120); if (lane < 32) ST16(ONES, qb + 1024 + (lane >> 4) * (5120 - 256)); }
      if (i == 4) { asm volatile("s_waitcnt vmcnt(0)" : "+v"(tch) :: "memory"); if (role) set_flag(ws, WS_FQ + ((rt * 8 + hq) * 4 + w3) * 8, tag, lane);
                    if (tid < 200) *(LAS u32x4*)(lp + L_WPHI + tid * 16) = wpc; }
      const LAS char* sb = lp + L_BUF + bsel * STAGE_B;
      bsel = bsel == 2 ? 0 : bsel + 1;
      KSTEP(fa, xa, 0, fb, xb) KSTEP(fb, xb, 1, fa, xa) KSTEP(fa, xa, 2, fb, xb)
      { xb = ldsr16(sb + (xo ^ 96));
        acc[0] = mfma32(fa[0], xa, acc[0]); fb[0] = trd2(sb + wo0 + 7680, sb + wo1 + 7680); fb[1] = trd2(sb + wo0 + 7688, sb + wo1 + 7688); SBAR();
        acc[1] = mfma32(fa[1], xa, acc[1]); fb[2] = trd2(sb + wo0 + 7696, sb + wo1 + 7696); fb[3] = trd2(sb + wo0 + 7704, sb + wo1 + 7704); SBAR();
        acc[2] = mfma32(fa[2], xa, acc[2]); fb[4] = trd2(sb + wo0 + 7712, sb + wo1 + 7712); SBAR();
        acc[3] = mfma32(fa[3], xa, acc[3]); acc[4] = mfma32(fa[4], xa, acc[4]); SBAR(); }
    }
#pragma unroll
    for (int j = 0; j < 5; j++) acc[j] = mfma32(fb[j], xb, acc[j]);
#undef KSTEP
    STAMP(3);
#define PK2(t, i, sc) pk(acc[t][i] * (sc), acc[t][(i) + 1] * (sc))
#define ROW(i, sc) const u32x4 ra = {PK2(0, i, sc), PK2(0, (i) + 2, sc), PK2(1, i, sc), PK2(1, (i) + 2, sc)}, rb = {PK2(2, i, sc), PK2(2, (i) + 2, sc), PK2(3, i, sc), PK2(3, (i) + 2, sc)}; const u32x2 rc = {PK2(4, i, sc), PK2(4, (i) + 2, sc)}
    { ROW(4, 1.0f);
      *(LAS u32x4*)(lp + L_HO + tid * 16) = ra; *(LAS u32x4*)(lp + L_HO + 4096 + tid * 16) = rb; *(LAS u32x2*)(lp + L_HO + 8192 + tid * 8) = rc; }
    BAR_L();
    const int qfo = (h * 4 + w3) * 1280;
    if (role) {
      { ROW(8, QSCALE); const unsigned qb = WS_QX + (rt * 8 + hq) * QX_B + qfo;
        ST16(((u32x4){nz(ra.x), nz(ra.y), nz(ra.z), nz(ra.w)}), qb + r * 16); ST16(((u32x4){nz(rb.x), nz(rb.y), nz(rb.z), nz(rb.w)}), qb + 512 + r * 16); ST8(((u32x2){nz(rc.x), nz(rc.y)}), qb + 1024 + r * 8); }
      { ROW(12, QSCALE); LAS char* d = lp + L_QL + qfo;
        *(LAS u32x4*)(d + r * 16) = ra; *(LAS u32x4*)(d + 512 + r * 16) = rb; *(LAS u32x2*)(d + 1024 + r * 8) = rc; }
    } else { LAS char* d = lp + L_VIM + (h * 128 + w3 * 32 + r) * 40;
#pragma unroll
      for (int t = 0; t < 5; t++) { *(LAS u32x2*)(d + t * 8) = (u32x2){PK2(t, 8, 1.0f), PK2(t, 10, 1.0f)}; *(LAS u32x2*)(d + 10240 + t * 8) = (u32x2){PK2(t, 12, 1.0f), PK2(t, 14, 1.0f)}; } }
#pragma unroll
    for (int t = 0; t < 5; t++) { o0[2 * t] = PK2(t, 0, 1.0f); o0[2 * t + 1] = PK2(t, 2, 1.0f); }
#undef ROW
#undef PK2
  } else {
    u32x2 ep = ld8_sc1_async(ws + WS_EPOCH);
    const bool second = w3 >> 1;
    const unsigned rs = (second && role) ? 1280u : 3840u;
    const char* wsrc = (second ? (role ? (const char*)(wq + C0) : (const char*)(wqkv + 1280 + C0)) : (const char*)(wqkv + (role ? 0 : 640) + C0)) + (w3 & 1) * 32 * rs;
    const char* xsrc = (const char*)(x + (rt * 128 + w3 * 32) * DM);
    int rot = (bi & 7) + 3 * ((bi >> 3) & 3); rot = rot >= 10 ? rot - 10 : rot;
    unsigned so[5], xoff[2];
#pragma unroll
    for (int j = 0; j < 5; j++) { const int c = j * 64 + lane, rho = (c * 6554) >> 16, cc = c - rho * 10; so[j] = (rho ^ (second ? 4 : 0)) * rs + cc * 16; }
#pragma unroll
    for (int j = 0; j < 2; j++) { const int row = j * 8 + (lane >> 3); xoff[j] = row * 1280 + (((lane & 7) ^ ((row >> 1) & 7)) * 16); }
    int bsel = 2, kc = rot;
    auto stage = [&]() {
      const unsigned lb = lds0 + L_BUF + bsel * STAGE_B;
      const char* bw = wsrc + kc * 64 * rs; const char* bxp = xsrc + kc * 128;
#pragma unroll
      for (int j = 0; j < 5; j++) { dma16s(bw, so[j], lb + (w3 * 5 + j) * 1024); if (j < 4) dma16s(bxp + (j >> 1) * 16 * 1280, xoff[j & 1], lb + ST_X + (w3 * 4 + j) * 1024); }
      bsel = bsel == 2 ? 0 : bsel + 1; kc = kc == 9 ? 0 : kc + 1;
    };
    stage(); stage();
    asm volatile("s_waitcnt vmcnt(9)" : "+v"(ep) :: "memory");
    tag = (((u64)(unsigned)__builtin_amdgcn_readfirstlane(ep.y) << 32) | (unsigned)__builtin_amdgcn_readfirstlane(ep.x)) + 1ull;
    STAMP(1); STAMP(2);
#pragma unroll 1
    for (int i = 0; i < 8; i++) {
      WAIT_BAR(9);
      stage();
      if (i == 0 && w3 == 0) {
        const unsigned tb = WS_KV + (T0 >> 5) * TILE_B; const int s16 = hq & 1;
        if (!role) {
          const LAS f32x4* rp = (const LAS f32x4*)(lp + (h ? L_RED1 : L_RED)) + (h ? (g & 1) * 16 : g * 32) + l15;
          const int st = h ? 32 : 64, o2 = h ? 0 : 16;
          const f32x4 a = (rp[0] + rp[st]) + (rp[2 * st] + rp[3 * st]), b = (rp[o2] + rp[st + o2]) + (rp[2 * st + o2] + rp[3 * st + o2]);
          const u32x4 v = {pk(a[0], a[1]), pk(a[2], a[3]), pk(b[0], b[1]), pk(b[2], b[3])};
          if (lane < 32) ST16(v, tb + (g * 32 + s16 * 16 + l15) * 16);
          else if (lane < 48) ST8(((u32x2){v.x, v.y}), tb + 1024 + (s16 * 16 + l15) * 8);
          else { const float gv = 1.0f / (1.0f + __expf(-a[0])); __builtin_amdgcn_raw_buffer_store_b32(__float_as_uint(gv), wsr, WS_G + (T0 + l15) * 4, 0, 16); }
        } else if (lane < 44) {
          const int hh = lane >= 22, d = lane - hh * 22, dd = d < 20 ? d : 0, st = dd >= 16 ? 128 : 256;
          const LAS float* rp = (const LAS float*)(lp + (dd >= 16 ? L_RED1 : L_RED)) + (((dd & 15) >> 2) * 16 + 4 * hh) * 4 + (dd & 3);
          float e[8];
#pragma unroll
          for (int j = 0; j < 8; j++) { const int tk = (8 * (j >> 2) + (j & 3)) * 4; e[j] = (rp[tk] + rp[tk + st]) + (rp[tk + 2 * st] + rp[tk + 3 * st]); }
          u32x4 v = {pk(e[0], e[1]), pk(e[2], e[3]), pk(e[4], e[5]), pk(e[6], e[7])};
          if (d == 20) v = (u32x4){0x3F803F80u, 0x3F803F80u, 0x3F803F80u, 0x3F803F80u};
          if (d == 21) v = (u32x4){0u, 0u, 0u, 0u};
          ST16(v, tb + 1280 + (s16 * 2 + hh) * VB + d * 16);
        }
      }
      if (i == 2) { if (w3 == 0) set_flag(ws, WS_FKV + blockIdx.x * 8, tag, lane);
                    if (w3 >= 2) *(LAS u32x4*)(lp + L_ZERO + (tid - 384) * 16) = (u32x4){0u, 0u, 0u, 0u}; }
    }
    u64 fa, fb, fc, fd, fe;
    { gu64* f = (gu64*)(ws + WS_FKV) + lane;
      fa = __hip_atomic_load(f, RLX_AGENT); fb = __hip_atomic_load(f + 64, RLX_AGENT); fc = __hip_atomic_load(f + 128, RLX_AGENT); fd = __hip_atomic_load(f + 192, RLX_AGENT);
      fe = __hip_atomic_load((gu64*)(ws + WS_FQ) + (rt * 8 + hq) * 4 + p4, RLX_AGENT); }
    WAIT_BAR(14);
    asm volatile("" : "+v"(fa), "+v"(fb), "+v"(fc), "+v"(fd), "+v"(fe));
    const bool kvok = __all((fa == tag) & (fb == tag) & (fc == tag) & (fd == tag) & (fe == tag));
    asm volatile("" ::: "memory");
    BAR_L();
    if (!kvok) { wait_flags<256>(ws, WS_FKV, tag, lane); wait_flags<4>(ws, WS_FQ + (rt * 8 + hq) * 32, tag, lane); }
    KVL(0, 0);
    if (blockIdx.x == 0 && w3 == 0) set_flag(ws, WS_EPOCH, tag, lane);
    STAMP(3);
    BAR_L();
    const int ht = tid - 256;
    const u32x4 a = ldsr16(lp + L_HO + ht * 16), b = ldsr16(lp + L_HO + 4096 + ht * 16); const u32x2 c = ldsr8(lp + L_HO + 8192 + ht * 8);
    o0[0] = a.x; o0[1] = a.y; o0[2] = a.z; o0[3] = a.w; o0[4] = b.x; o0[5] = b.y; o0[6] = b.z; o0[7] = b.w; o0[8] = c.x; o0[9] = c.y;
  }
  STAMP(4);
  u32x4 ph[5];
  { const LAS char* pb = ((p4 & 1) == h) ? lp + L_WPHI + (2 * w2 + h) * 800 + q4 * 40 + (2 * (g & 1) + (p4 >> 1)) * 8 : lp + L_ZERO;
    const u32x4 b0 = {o0[0], o0[1], o0[2], o0[3]}, b1 = {o0[4], o0[5], o0[6], o0[7]}, b2 = {o0[8], o0[9], 0u, 0u};
    const u32x2 c0 = trd(pb + 640), c1 = trd(pb + 672);
    f32x16 P0 = {}, P1 = {};
    P0 = mfma32(trd2(pb, pb + 160), b0, P0); P1 = mfma32(trd2(pb + 32, pb + 192), b0, P1);
    P0 = mfma32(trd2(pb + 320, pb + 480), b1, P0); P1 = mfma32(trd2(pb + 352, pb + 512), b1, P1);
    P0 = mfma32((u32x4){c0.x, c0.y, 0u, 0u}, b2, P0); P1 = mfma32((u32x4){c1.x, c1.y, 0u, 0u}, b2, P1);
    KX(1, 1); KW(1, 1, 0); SBAR();
    f32x2 p[10];
#pragma unroll
    for (int f = 0; f < 10; f++) p[f] = f < 8 ? (f32x2){P0[(2 * f) & 15], P0[(2 * f + 1) & 15]} : (f32x2){P1[(2 * f) & 3], P1[(2 * f + 1) & 3]};
    float mx = fmaxf(p[0][0], p[0][1]), mn = fminf(p[0][0], p[0][1]);
#pragma unroll
    for (int f = 1; f < 10; f++) { mx = fmaxf(fmaxf(mx, p[f][0]), p[f][1]); mn = fminf(fminf(mn, p[f][0]), p[f][1]); }
    const f32x2 ma = {-mx * LOG2E, -mx * LOG2E}, mb = {mn * LOG2E, mn * LOG2E}, ca_ = {LOG2E, LOG2E}, cb_ = {-LOG2E, -LOG2E};
    f32x2 ea[10], eb[10], sa = {0.f, 0.f}, sb = {0.f, 0.f};
#pragma unroll
    for (int f = 0; f < 10; f++) { if (f % 2 == 0 && f < 8) { SBAR(); KW(1, 1, 1 + f / 2); SBAR(); }
      const f32x2 xa = __builtin_elementwise_fma(p[f], ca_, ma), xb = __builtin_elementwise_fma(p[f], cb_, mb);
      ea[f] = (f32x2){ex2(xa[0]), ex2(xa[1])}; eb[f] = (f32x2){ex2(xb[0]), ex2(xb[1])}; sa += ea[f]; sb += eb[f]; }
    const float ia = __builtin_amdgcn_rcpf(sa[0] + sa[1]), ib = __builtin_amdgcn_rcpf(sb[0] + sb[1]);
    unsigned o[20];
#pragma unroll
    for (int f = 0; f < 10; f++) { const f32x2 ya = ea[f] * ia, yb = eb[f] * ib; o[f] = pk(ya[0], ya[1]); o[10 + f] = pk(yb[0], yb[1]); }
#pragma unroll
    for (int c = 0; c < 5; c++) ph[c] = (u32x4){o[4 * c], o[4 * c + 1], o[4 * c + 2], o[4 * c + 3]};
    asm volatile("" : "+v"(ph[0]), "+v"(ph[1]), "+v"(ph[2]), "+v"(ph[3]), "+v"(ph[4]));
  }
  u32x4 q1A, q1B; u32x2 q2A, q2B;
  auto kv_job = [&]() {
    const int hd = w >> 1, dt = w & 1;
    const LAS char* ab = lp + L_PHK + (hd * 128 + g * 8 + q4) * 80 + p4 * 8;
    const LAS char* bb = lp + L_VIM + (hd * 128 + g * 8 + q4) * 40 + dt * 32 + p4 * 8;
    const bool ones = dt && l15 == 4;
    f32x4 c[3] = {};
#pragma unroll
    for (int ks = 0; ks < 4; ks++) {
      u32x4 b = trd2(bb + ks * 32 * 40, bb + (ks * 32 + 4) * 40);
      if (ones) b = (u32x4){0x3F803F80u, 0x3F803F80u, 0x3F803F80u, 0x3F803F80u};
#pragma unroll
      for (int ft = 0; ft < 3; ft++) c[ft] = mfma16(trd2(ab + ks * 32 * 80 + ft * 32, ab + (ks * 32 + 4) * 80 + ft * 32), b, c[ft]);
    }
    ARRIVE(0);
    const unsigned pb = WS_PART + (hq * 16 + rt) * PART_B + hd * 3840 + (dt ? 2560 + g * 128 : g * 256) + l15 * 16;
#pragma unroll
    for (int ft = 0; ft < 3; ft++) if ((!dt || l15 < 8) && ft * 4 + g < 10) ST16(__builtin_bit_cast(u32x4, c[ft]), pb + ft * (dt ? 512 : 1024));
    SBAR();
  };
  if (role) {
    WAIT_BAR(0);
    STAMP(5);
    const LAS char* qa = lp + L_QL + w3 * 1280;
    q1A = ldsr16(qa + lane * 16); q1B = ldsr16(qa + 5120 + lane * 16); q2A = ldsr8(qa + 1024 + r * 8); q2B = ldsr8(qa + 6144 + r * 8);
    ARRIVE(0);
  } else {
    const unsigned qb = WS_QX + (rt * 8 + hq) * QX_B + w3 * 1280;
    q1A = LD16(qb + lane * 16); q1B = LD16(qb + 5120 + lane * 16); q2A = LD8(qb + 1024 + r * 8); q2B = LD8(qb + 6144 + r * 8);
    { LAS char* d = lp + L_PHK + ((2 * w2 + h) * 128 + w3 * 32 + r) * 80;
#pragma unroll
      for (int c = 0; c < 5; c++) *(LAS u32x4*)(d + c * 16) = ph[c]; }
    WAIT_BAR(4);
    STAMP(5);
#pragma unroll 1
    for (unsigned spin = 0; spin < SPIN_CAP; ++spin) {
      if (!(missing(q1A) | missing(q1B) | missing((u32x4){q2A.x, q2A.y, q2B.x, q2B.y}))) break;
      __builtin_amdgcn_s_sleep(1);
      asm volatile("" ::: "memory");
      q1A = LD16(qb + lane * 16); q1B = LD16(qb + 5120 + lane * 16); q2A = LD8(qb + 1024 + r * 8); q2B = LD8(qb + 6144 + r * 8);
    }
    asm volatile("" : "+v"(q1A), "+v"(q1B), "+v"(q2A), "+v"(q2B) :: "memory");
    if (w2) kv_job();
  }
  STAMP(6);
  const int vr = r <= 21 ? r : 21;
  const int ok1 = lane * 16, ok2 = h ? (1280 + 20 * 16) : (1024 + r * 8), ov = 1280 + h * VB + vr * 16;
  const f32x16 Z = {};
  if (h) { q2A = (u32x2){0u, 0u}; q2B = (u32x2){0u, 0u}; }
  { const LAS char* t0 = lp;
    const u32x4 k1 = ldsr16(t0 + ok1); const u32x2 k2 = ldsr8(t0 + ok2);
    f32x16 sa = mfma32(k1, q1A, Z); sa = mfma32k8(k2, q2A, sa);
    f32x16 sb = mfma32(k1, q1B, Z); sb = mfma32k8(k2, q2B, sb);
    float ma = sa[0], mb = sb[0];
#pragma unroll
    for (int i = 1; i < 16; i++) { ma = fmaxf(ma, sa[i]); mb = fmaxf(mb, sb[i]); }
    ma = half_swap_max(ma); mb = half_swap_max(mb);
    const float mah = __uint_as_float(__float_as_uint(ma) & 0xFFFF0000u), mbh = __uint_as_float(__float_as_uint(mb) & 0xFFFF0000u);
    if (h) { q2A.x = pk(-mah, mah - ma); q2B.x = pk(-mbh, mbh - mb); } }
  f32x16 OA = {}, OB = {}, SA, SB;
  u32x4 k1, v1, v2, v2n, pA1, pA2, pB1, pB2; u32x2 k2;
  float e0, e1, e2, e3;
  const LAS char* c0p = lp + w2 * CH_B + ok1; const LAS char* c1p = lp + w2 * CH_B + ok2; const LAS char* c2p = lp + w2 * CH_B + ov;
  k1 = ldsr16(c0p); k2 = ldsr8(c1p);
  SA = mfma32(k1, q1A, Z); SA = mfma32k8(k2, q2A, SA);
  v1 = ldsr16(c2p); v2 = v1;
  e0 = e1 = e2 = e3 = 0.f; pB2 = (u32x4){0u, 0u, 0u, 0u};
#define G(MF, LD, C0_, C1_, X, S_, i) MF; LD; SBAR(); C0_; C1_; SBAR(); X##0 = ex2(S_[i]); X##1 = ex2(S_[i + 1]); X##2 = ex2(S_[i + 2]); X##3 = ex2(S_[i + 3]); SBAR();
#define TILE(TB, TN, V2, H0, H1, H2) { float a0, a1, a2, a3; \
    G(SB = mfma32(k1, q1B, Z),    k1 = ldsr16(c0p + (TN)), pB2[2] = pk(e0, e1), pB2[3] = pk(e2, e3), a, SA, 0) \
    G(OB = mfma32(v2, pB2, OB),   V2,                      pA1[0] = pk(a0, a1), pA1[1] = pk(a2, a3), e, SA, 4) \
    G(SB = mfma32k8(k2, q2B, SB), k2 = ldsr8(c1p + (TN)),  pA1[2] = pk(e0, e1), pA1[3] = pk(e2, e3), a, SA, 8) \
    G(OA = mfma32(v1, pA1, OA),   H0,                      pA2[0] = pk(a0, a1), pA2[1] = pk(a2, a3), e, SA, 12) \
    G(SA = mfma32(k1, q1A, Z),    H1,                      pA2[2] = pk(e0, e1), pA2[3] = pk(e2, e3), a, SB, 0) \
    G(OA = mfma32(v2, pA2, OA),   H2,                      pB1[0] = pk(a0, a1), pB1[1] = pk(a2, a3), e, SB, 4) \
    G(SA = mfma32k8(k2, q2A, SA), (void)0,                 pB1[2] = pk(e0, e1), pB1[3] = pk(e2, e3), a, SB, 8) \
    G(OB = mfma32(v1, pB1, OB),   v1 = ldsr16(c2p + (TN)), pB2[0] = pk(a0, a1), pB2[1] = pk(a2, a3), e, SB, 12) }
#define NOP (void)0
#define TL(sl, t, H0, H1, H2) TILE((sl) * STEP_B + (t) * TILE_B, (sl) * STEP_B + ((t) + 1) * TILE_B, v2 = ldsr16(c2p + (sl) * STEP_B + (t) * TILE_B + 2 * VB), H0, H1, H2)
#define TL6(sl, H0, H1) TL(sl, 6, H0, H1, v2n = ldsr16(c2p + (sl) * STEP_B + 7 * TILE_B + 2 * VB))
#define TL7(sl, sn, H0, H1, H2) TILE((sl) * STEP_B + 7 * TILE_B, (sn) * STEP_B, v2 = v2n, H0, H1, H2)
  unsigned woff[5];
#pragma unroll
  for (int j = 0; j < 5; j++) { const int c = j * 64 + lane, rho = (c * 6554) >> 16, cc = c - rho * 10; const int k = (rho & ~12) | ((rho & 4) << 1) | ((rho & 8) >> 1); woff[j] = k * 1280 + cc * 16; }
#define WOP(wo, gr, j, dst) dma16s((wo) + C0 + (gr) * 32 * 640, woff[j], lds0 + (dst) + (j) * 1024)
#define WA(gr, j) WOP(woA, gr, j, L_WO + (gr) * 5120)
#define WX(j) do { if (w < 4) WA(16 + w3, j); else if (w < 7) WOP(woB, 17 + w3, j, 145408 + w3 * 5120); } while (0)
#define PRIO(n) __builtin_amdgcn_s_setprio(n)
#define FILL(j) do { if (role) { const int n_ = w * 3 + (j); ST16(ONES, WS_OB + (rt * 8 + hq) * OT_B + (n_ < 20 ? n_ : 19) * 1024 + lane * 16); } } while (0)
  const bool red = !role && w == 0;
  f32x4 part[16]; u64 fp0, fp1;
  auto red_load = [&]() {
    asm volatile("" : "+v"(fp0), "+v"(fp1));
    if (!__all((fp0 == tag) & (fp1 == tag))) wait_flags<128>(ws, WS_FPART + hq * 1024, tag, lane);
    asm volatile("" ::: "memory");
#pragma unroll
    for (int j = 0; j < 16; j++) part[j] = __builtin_bit_cast(f32x4, LD16(WS_PART + (hq * 16 + j) * PART_B + (rt * 60 + (lane < 60 ? lane : 59)) * 16));
  };
  auto red_store = [&]() {
    f32x4 a = ((part[0] + part[1]) + (part[2] + part[3])) + ((part[4] + part[5]) + (part[6] + part[7]));
    a += ((part[8] + part[9]) + (part[10] + part[11])) + ((part[12] + part[13]) + (part[14] + part[15]));
    const int hd = rt >> 2, u = (rt & 3) * 60 + lane, hi = u >= 160, fq = hi ? (u - 160) >> 3 : u >> 4, di = hi ? u & 7 : u & 15;
    if (lane < 60) ST8(((u32x2){pk(a[0], a[1]), pk(a[2], a[3])}), WS_KVR + hq * KVR_B + (((hd >> 1) * 2 + hi) * 5 + (fq >> 1)) * 512 + ((hd & 1) * 16 + di) * 16 + (fq & 1) * 8);
  };
  u32x4 ka[10]; u64 fkr;
  auto oa_load = [&]() {
    asm volatile("" : "+v"(fkr));
    if (!__all(fkr == tag)) wait_flags<16>(ws, WS_FKVR + hq * 128, tag, lane);
    asm volatile("" ::: "memory");
    const unsigned kb = (((r >> 2) & 1) == h) ? WS_KVR + hq * KVR_B + w2 * 5120 + (h * 16 + (r & 3) + 4 * (r >> 3)) * 16 : 0x7FFF0000u;
#pragma unroll
    for (int j = 0; j < 10; j++) ka[j] = LD16(kb + j * 512);
  };
  auto oa_part = [&]() {
    f32x16 a0 = Z, a1 = Z;
#pragma unroll
    for (int s = 0; s < 5; s++) { a0 = mfma32(ka[s], ph[s], a0); a1 = mfma32(ka[5 + s], ph[s], a1); }
    const float inv = __builtin_amdgcn_rcpf(a1[4] + 1e-6f);
    LAS char* tb = lp + L_WO + w * 5120;
#pragma unroll
    for (int t = 0; t < 5; t++) { const int k0 = h * 20 + 4 * t;
      const u32x2 v = t < 4 ? (u32x2){pk(a0[(4 * t) & 15] * inv, a0[(4 * t + 1) & 15] * inv), pk(a0[(4 * t + 2) & 15] * inv, a0[(4 * t + 3) & 15] * inv)} : (u32x2){pk(a1[0] * inv, a1[1] * inv), pk(a1[2] * inv, a1[3] * inv)};
      *(LAS u32x2*)(tb + (r >> 4) * 1280 + (k0 >> 3) * 256 + (k0 & 4) * 2 + l15 * 16) = v; }
    const unsigned oa = WS_OA + (rt * 8 + w3 * 2) * OT_B + (hq * 2 + w2) * 1280;
    const u32x4 z0 = ldsr16(tb + lane * 16), z1 = ldsr16(tb + 1280 + lane * 16), z2 = ldsr16(tb + (r >> 4) * 1280 + 1024 + l15 * 16);
    ST16(z0, oa + lane * 16); ST16(z1, oa + OT_B + lane * 16); if (!h) ST16(z2, oa + (r >> 4) * OT_B + 1024 + l15 * 16);
    SBAR();
  };
  PRIO(3);
  TL(0, 0, NOP, NOP, NOP)
  if (!role && !w2) kv_job();
  TL(0, 1, FILL(0), FILL(1), FILL(2))
  TL(0, 2, NOP, NOP, if (!role) { WAIT_VM(0); set_flag(ws, WS_FPART + ((hq * 16 + rt) * 8 + w) * 8, tag, lane); })
  PRIO(2);
  TL(0, 3, NOP, NOP, NOP)
  STAMP(14);
  AWAIT(0, 8);
  TL(0, 4, KX(2, 2), KW(2, 2, 0), KW(2, 2, 1)) TL(0, 5, KW(2, 2, 2), KW(2, 2, 3), KW(2, 2, 4))
  TL6(0, NOP, if (red) { fp0 = __hip_atomic_load((gu64*)(ws + WS_FPART) + hq * 128 + lane, RLX_AGENT); fp1 = __hip_atomic_load((gu64*)(ws + WS_FPART) + hq * 128 + 64 + lane, RLX_AGENT); })
  TL7(0, 1, NOP, NOP, NOP)
  STAMP(15);
  TL(1, 0, NOP, NOP, NOP)
  if (red) red_load();
  TL(1, 1, NOP, NOP, NOP)
  TL(1, 2, if (w == 5 || w == 6) { const int row = (w - 5) * 32 + (lane >> 1);
                      dma4s(woB + C0 + ((rt & 7) * 2 + role) * 40 * 640, (row < 40 ? row : 39) * 1280 + (lane & 1) * 156, lds0 + 157696); }, NOP, NOP)
  if (red) red_store();
  TL(1, 3, NOP, NOP, NOP)
  PRIO(1);
  TL(1, 4, NOP, NOP, if (red) { WAIT_VM(0); set_flag(ws, WS_FKVR + (hq * 16 + rt) * 8, tag, lane); }) TL(1, 5, NOP, NOP, NOP)
  PRIO(0);
  TL6(1, NOP, NOP)
  WAIT_BAR(0);
  STAMP(12);
  PRIO(3);
  TL7(1, 2, KX(3, 0), KW(3, 0, 0), KW(3, 0, 1))
  TL(2, 0, KW(3, 0, 2), KW(3, 0, 3), KW(3, 0, 4)) TL(2, 1, fkr = __hip_atomic_load((gu64*)(ws + WS_FKVR) + hq * 16 + l15, RLX_AGENT), NOP, NOP) TL(2, 2, NOP, NOP, NOP)
  PRIO(2);
  if (role) oa_load();
  TL(2, 3, NOP, NOP, NOP)
  if (role && w2) oa_part();
  TL(2, 4, NOP, NOP, NOP)
  PRIO(1);
  if (role && !w2) oa_part();
  TL(2, 5, WA(w, 0), WA(w, 1), WA(w, 2))
  PRIO(0);
  TL6(2, WA(w, 3), WA(w, 4))
  WAIT_BAR(5);
  STAMP(13);
  PRIO(3);
  if (role) set_flag(ws, WS_FOA + ((rt * 8 + hq) * 8 + w) * 8, tag, lane);
  TL7(2, 0, NOP, NOP, NOP)
  TL(0, 0, WA(8 + w, 0), NOP, WA(8 + w, 1)) TL(0, 1, WA(8 + w, 2), NOP, WA(8 + w, 3)) TL(0, 2, WA(8 + w, 4), NOP, WX(0))
  TL(0, 3, WX(1), NOP, WX(2))
  PRIO(2);
  TL(0, 4, WX(3), NOP, WX(4))
  u64 foa;
  TL(0, 5, foa = __hip_atomic_load((gu64*)(ws + WS_FOA) + rt * 64 + lane, RLX_AGENT), NOP, NOP)
  PRIO(1);
  ARRIVE(1);
  TL6(0, NOP, NOP)
  PRIO(0);
  const int tp = w & 1, kq = w >> 1;
  const unsigned ofr = (rt * 8 + role * 4 + tp * 2) * OT_B + kq * 5120 + lane * 16;
  const int R0 = rt * 128 + role * 64 + tp * 32 + l15;
  u32x4 of[2][5]; float gt0, gt1;
  asm volatile("" : "+v"(foa));
  if (!__all(foa == tag)) wait_flags<64>(ws, WS_FOA + rt * 512, tag, lane);
  asm volatile("" ::: "memory");
#define OFL(u) do { _Pragma("unroll") for (int i = 0; i < 5; i++) of[u][i] = LD16(WS_OA + ofr + (u) * OT_B + i * 1024); } while (0)
  TL7(0, 0, OFL(0), (gt0 = __uint_as_float(__builtin_amdgcn_raw_buffer_load_b32(wsr, WS_G + R0 * 4, 0, 16)), gt1 = __uint_as_float(__builtin_amdgcn_raw_buffer_load_b32(wsr, WS_G + R0 * 4 + 64, 0, 16))), OFL(1))
  pB2[2] = pk(e0, e1); pB2[3] = pk(e2, e3);
  OB = mfma32(v2, pB2, OB);
#undef TL
#undef TL6
#undef TL7
#undef TILE
#undef G
  STAMP(7);
  { LAS f32x4* xb = (LAS f32x4*)(lp + (w3 >> 1) * CH_B) + (w3 & 1) * (64 * 6);
#pragma unroll
    for (int i = 0; i < 12; i++) { auto rr = __builtin_amdgcn_permlane16_swap(__float_as_uint(OA[i]), __float_as_uint(OB[i]), false, false); OA[i] = __uint_as_float(rr[0]); OB[i] = __uint_as_float(rr[1]); }
    const f32x16 snd = w2 ? OA : OB;
    AWAIT(1, 8);
#pragma unroll
    for (int c = 0; c < 3; c++) xb[(w2 * 3 + c) * 64 + lane] = (f32x4){snd[4 * c], snd[4 * c + 1], snd[4 * c + 2], snd[4 * c + 3]};
    asm volatile("s_waitcnt vmcnt(12) lgkmcnt(0)\n\ts_barrier" ::: "memory");
    f32x16 O = w2 ? OB : OA;
#pragma unroll
    for (int c = 0; c < 3; c++) { const f32x4 t = xb[((w2 ^ 1) * 3 + c) * 64 + lane]; O[4 * c] += t[0]; O[4 * c + 1] += t[1]; O[4 * c + 2] += t[2]; O[4 * c + 3] += t[3]; }
    float l = from_upper(O[8]);
    float amax = 0.f;
#pragma unroll
    for (int i = 0; i < 12; i++) amax += fabsf(O[i]);
    const bool good = (l >= 0x1p-30f) && (l <= 0x1p100f) && (amax <= 0x1p126f);
    if (__builtin_expect(!__all(good), 0)) {
      u32x4 q1; u32x2 q2;
#pragma unroll
      for (int i = 0; i < 4; i++) { auto rr = __builtin_amdgcn_permlane16_swap(q1A[i], q1B[i], false, false); q1[i] = w2 ? rr[1] : rr[0]; }
#pragma unroll
      for (int i = 0; i < 2; i++) { auto rr = __builtin_amdgcn_permlane16_swap(q2A[i], q2B[i], false, false); q2[i] = h ? 0u : (w2 ? rr[1] : rr[0]); }
      float m = -INFINITY;
#pragma unroll 1
      for (int t = 0; t < 64; t++) {
        f32x16 s = mfma32(LD16(WS_KV + t * TILE_B + ok1), q1, Z); s = mfma32k8(LD8(WS_KV + t * TILE_B + ok2), q2, s);
#pragma unroll
        for (int i = 0; i < 16; i++) m = fmaxf(m, s[i]);
      }
      m = half_swap_max(m);
      O = Z;
#pragma unroll 1
      for (int t = 0; t < 64; t++) {
        f32x16 s = mfma32(LD16(WS_KV + t * TILE_B + ok1), q1, Z); s = mfma32k8(LD8(WS_KV + t * TILE_B + ok2), q2, s);
#pragma unroll
        for (int i = 0; i < 16; i++) s[i] = ex2(s[i] - m);
        O = mfma32(LD16(WS_KV + t * TILE_B + ov), (u32x4){pk(s[0], s[1]), pk(s[2], s[3]), pk(s[4], s[5]), pk(s[6], s[7])}, O);
        O = mfma32(LD16(WS_KV + t * TILE_B + ov + 2 * VB), (u32x4){pk(s[8], s[9]), pk(s[10], s[11]), pk(s[12], s[13]), pk(s[14], s[15])}, O);
      }
      l = from_upper(O[8]);
    }
    const float inv = __builtin_amdgcn_rcpf(l);
    LAS char* tb = lp + w2 * CH_B + 12288 + w3 * 1536;
#pragma unroll
    for (int c = 0; c < 3; c++) { const int k0 = (g & 1) * 20 + 8 * c + 4 * h;
      *(LAS u32x2*)(tb + ((c == 2 && h) ? 1280 + (g & 1) * 8 : (k0 >> 3) * 256 + (k0 & 4) * 2) + l15 * 16) = (u32x2){nz(pk(O[4 * c] * inv, O[4 * c + 1] * inv)), nz(pk(O[4 * c + 2] * inv, O[4 * c + 3] * inv))}; }
    const unsigned ob = WS_OB + (rt * 8 + w3 * 2 + w2) * OT_B + hp * 1280 + lane * 16;
    const u32x4 z0 = ldsr16(tb + lane * 16), z1 = ldsr16(tb + 1024 + l15 * 16);
    ST16(z0, ob); if (lane < 16) ST16(z1, ob + 1024);
  }
  STAMP(8);
  u32x4 wf[5][5];
#pragma unroll
  for (int i = 0; i < 5; i++) { const LAS char* wb = lp + L_WO + (kq * 5 + i) * 5120 + wlane;
#pragma unroll
    for (int t = 0; t < 5; t++) wf[i][t] = trd2(wb + t * 8, wb + 1280 + t * 8); }
  BAR_L();
  f32x4 ca[2][5], cb[2][5];
#pragma unroll
  for (int t = 0; t < 5; t++) { ca[0][t] = ca[1][t] = cb[0][t] = cb[1][t] = (f32x4){0.f, 0.f, 0.f, 0.f}; }
#pragma unroll
  for (int i = 0; i < 5; i++) {
    WOP(woB, w, i, L_WO + w * 5120); SBAR();
#pragma unroll
    for (int t = 0; t < 5; t++) ca[0][t] = mfma16(wf[i][t], of[0][i], ca[0][t]);
    SBAR(); WOP(woB, 8 + w, i, L_WO + (8 + w) * 5120); if (w == i) WOP(woB, 16, i, L_WO + 16 * 5120); SBAR();
#pragma unroll
    for (int t = 0; t < 5; t++) ca[1][t] = mfma16(wf[i][t], of[1][i], ca[1][t]);
    SBAR();
  }
  STAMP(9);
#pragma unroll
  for (int u = 0; u < 2; u++)
#pragma unroll
    for (int i = 0; i < 5; i++) of[u][i] = LD16(WS_OB + ofr + u * OT_B + i * 1024);
  WAIT_BAR(10);
#pragma unroll
  for (int i = 0; i < 5; i++) { const int gr = kq * 5 + i; const LAS char* wb = lp + (gr < 17 ? L_WO + gr * 5120 : 145408 + (gr - 17) * 5120) + wlane;
#pragma unroll
    for (int t = 0; t < 5; t++) wf[i][t] = trd2(wb + t * 8, wb + 1280 + t * 8); }
#pragma unroll
  for (int t = 0; t < 5; t++) { ca[0][t] *= gt0; ca[1][t] *= gt1; }
  BAR_L();
  SBAR();
  STAMP(10);
#pragma unroll
  for (int u = 0; u < 2; u++)
#pragma unroll
    for (int i = 0; i < 5; i++) {
      if (__builtin_expect(missing(of[u][i]), 0)) {
#pragma unroll 1
        for (unsigned spin = 0; spin < SPIN_CAP; ++spin) {
          asm volatile("" ::: "memory");
#pragma unroll
          for (int j = u * 5 + i; j < 10; j++) of[j / 5][j % 5] = LD16(WS_OB + ofr + (j / 5) * OT_B + (j % 5) * 1024);
          if (!missing(of[u][i])) break;
          __builtin_amdgcn_s_sleep(1);
        }
      }
#pragma unroll
      for (int t = 0; t < 5; t++) cb[u][t] = mfma16(wf[i][t], of[u][i], cb[u][t]);
      SBAR();
    }
  {
    LAS f32x4* xb = (LAS f32x4*)(lp + (tp ? L_WO : 0)) + lane;
    f32x4 v[10];
#pragma unroll
    for (int it = 0; it < 10; it++) { const float gg = 1.0f - (it < 5 ? gt0 : gt1); v[it] = cb[it / 5][it % 5] * gg + ca[it / 5][it % 5]; }
#pragma unroll
    for (int it = 0; it < 10; it++) if (((it * 13) >> 5) != kq) xb[(it * 4 + kq) * 64] = v[it];
    BAR_L();
    u16* op = out + (size_t)R0 * DM + C0 + g * 20;
#pragma unroll
    for (int it = 0; it < 10; it++) if (((it * 13) >> 5) == kq) { const int o_ = (it * 13) >> 5;
      const f32x4 z = (v[it] + xb[(it * 4 + ((o_ + 1) & 3)) * 64]) + (xb[(it * 4 + ((o_ + 2) & 3)) * 64] + xb[(it * 4 + ((o_ + 3) & 3)) * 64]);
      *(u32x2*)(op + (it / 5) * 16 * DM + (it % 5) * 4) = (u32x2){pk(z[0], z[1]), pk(z[2], z[3])}; }
  }
  STAMP(11);
}
}

extern "C" void kernel_launch(void* const* d_in, const int* in_sizes, int n_in, void* d_out, int out_size, void* d_ws, size_t ws_size, hipStream_t stream) {
  using namespace gm;
  fused<<<256, 512, 0, stream>>>((const u16*)d_in[0], (const u16*)d_in[1], (const u16*)d_in[2], (const u16*)d_in[3], (const u16*)d_in[5], (const u16*)d_in[6],
                                 (const u16*)d_in[7], (const u16*)d_in[8], (const u16*)d_in[9], (u16*)d_out, (char*)d_ws);
}
```

```cpp
#include <hip/hip_runtime.h>
#include <hip/hip_bf16.h>
#include <stdint.h>

namespace gm {
typedef float f32x16 __attribute__((ext_vector_type(16)));
typedef float f32x4 __attribute__((ext_vector_type(4)));
typedef float f32x2 __attribute__((ext_vector_type(2)));
typedef short s16x4 __attribute__((ext_vector_type(4)));
typedef __bf16 b16x2 __attribute__((ext_vector_type(2)));
typedef __bf16 b16x8 __attribute__((ext_vector_type(8)));
typedef unsigned u32x4 __attribute__((ext_vector_type(4)));
typedef unsigned u32x2 __attribute__((ext_vector_type(2)));
typedef unsigned long long u64;
typedef unsigned short u16;
#define DI __device__ __forceinline__
#define LAS __attribute__((address_space(3)))
#define GAS __attribute__((address_space(1)))
typedef GAS u64 gu64;

constexpr int DM = 640;
constexpr unsigned WS_EPOCH = 0, WS_FKV = 2048, WS_FKVR = 4096, WS_FQ = 8192, WS_FPART = 16384, WS_FOA = 24576, WS_G = 57344;
constexpr unsigned WS_KVR = 8912896, KVR_B = 10240, WS_KV = 131072, WS_QX = 327680, QX_B = 10240, WS_PART = 1703936, PART_B = 15360;
constexpr unsigned WS_OA = 3670016, WS_OB = 6291456, WS_END = 8994816, OT_B = 20480;
constexpr int VB = 352, TILE_B = 1280 + 4 * VB, CH_B = TILE_B * 8, STEP_B = 2 * CH_B;
constexpr int STAGE_B = 36864, ST_W1 = 10240, ST_X = 20480;
constexpr int L_BUF = 43008, L_P0W = 0, L_P0X0 = 25600, L_P0X1 = 153600, L_RED = 158720, L_RED1 = 40960;
constexpr int L_HO = 86016, L_QL = 96256, L_VIM = 96256, L_PHK = 116736, L_WPHI = 158592, L_ZERO = 161792, L_CNT = 163712, L_TOTAL = 163840;
constexpr int L_WO = 43008;
constexpr float LOG2E = 1.4426950408889634f;
constexpr float QSCALE = 0.22360679774997896f * LOG2E;
constexpr unsigned SPIN_CAP = 20000, LSPIN_CAP = 400000;

DI unsigned pk(float a, float b) { f32x2 v = {a, b}; b16x2 r = __builtin_convertvector(v, b16x2); return __builtin_bit_cast(unsigned, r); }
DI f32x16 mfma32(u32x4 a, u32x4 b, f32x16 c) { return __builtin_amdgcn_mfma_f32_32x32x16_bf16(__builtin_bit_cast(b16x8, a), __builtin_bit_cast(b16x8, b), c, 0, 0, 0); }
DI f32x16 mfma32k8(u32x2 a, u32x2 b, f32x16 c) { return __builtin_amdgcn_mfma_f32_32x32x8bf16_1k(__builtin_bit_cast(s16x4, a), __builtin_bit_cast(s16x4, b), c, 0, 0, 0); }
DI f32x4 mfma16(u32x4 a, u32x4 b, f32x4 c) { return __builtin_amdgcn_mfma_f32_16x16x32_bf16(__builtin_bit_cast(b16x8, a), __builtin_bit_cast(b16x8, b), c, 0, 0, 0); }
DI u32x2 trd(const LAS char* p) { return __builtin_bit_cast(u32x2, __builtin_amdgcn_ds_read_tr16_b64_v4i16((LAS s16x4*)p)); }
DI u32x4 trd2(const LAS char* p0, const LAS char* p1) { u32x2 a = trd(p0), b = trd(p1); return (u32x4){a.x, a.y, b.x, b.y}; }
DI u32x4 ldsr16(const LAS char* p) { return *(const LAS u32x4*)p; }
DI u32x2 ldsr8(const LAS char* p) { return *(const LAS u32x2*)p; }
DI float ex2(float x) { return __builtin_amdgcn_exp2f(x); }
DI void dma16s(const void* base, unsigned voff, unsigned lds_base) {
  asm volatile("s_mov_b32 m0, %2\n\ts_nop 0\n\tglobal_load_lds_dwordx4 %0, %1" :: "v"(voff), "s"(base), "s"(lds_base) : "memory"); }
DI void dma16s_sc1(const void* base, unsigned voff, unsigned lds_base) {
  asm volatile("s_mov_b32 m0, %2\n\ts_nop 0\n\tglobal_load_lds_dwordx4 %0, %1 sc1" :: "v"(voff), "s"(base), "s"(lds_base) : "memory"); }
DI void dmag_open(unsigned lds_base) { asm volatile("s_mov_b32 m0, %0\n\ts_nop 0" :: "s"(lds_base) : "memory"); }
template <int J> DI void dmag(const void* base, unsigned voff) { asm volatile("global_load_lds_dwordx4 %0, %1 offset:%2" :: "v"(voff), "s"(base), "i"((J & 3) * 1024) : "memory"); }
template <int J> DI void dmag_sc1(const void* base, unsigned voff) { asm volatile("global_load_lds_dwordx4 %0, %1 offset:%2 sc1" :: "v"(voff), "s"(base), "i"((J & 3) * 1024) : "memory"); }
DI void dma4s(const void* base, unsigned voff, unsigned lds_base) {
  asm volatile("s_mov_b32 m0, %2\n\ts_nop 0\n\tglobal_load_lds_dword %0, %1" :: "v"(voff), "s"(base), "s"(lds_base) : "memory"); }
DI u32x4 ld16_async(const void* p) { u32x4 r; asm volatile("global_load_dwordx4 %0, %1, off" : "=v"(r) : "v"(p) : "memory"); return r; }
DI u32x2 ld8_sc1_async(const void* p) { u32x2 r; asm volatile("global_load_dwordx2 %0, %1, off sc1" : "=v"(r) : "v"(p) : "memory"); return r; }
#define BAR_L() asm volatile("s_waitcnt lgkmcnt(0)\n\ts_barrier" ::: "memory")
#define WAIT_BAR(N) asm volatile("s_waitcnt vmcnt(" #N ") lgkmcnt(0)\n\ts_barrier" ::: "memory")
#define WAIT_VM(N) asm volatile("s_waitcnt vmcnt(" #N ")" ::: "memory")
#define SBAR() __builtin_amdgcn_sched_barrier(0)
#define RLX_AGENT __ATOMIC_RELAXED, __HIP_MEMORY_SCOPE_AGENT
DI float half_swap_max(float m) { auto rr = __builtin_amdgcn_permlane32_swap(__float_as_uint(m), __float_as_uint(m), false, false); return fmaxf(__uint_as_float(rr[0]), __uint_as_float(rr[1])); }
DI float from_upper(float v) { auto rr = __builtin_amdgcn_permlane32_swap(__float_as_uint(v), __float_as_uint(v), false, false); return __uint_as_float(rr[1]); }

template <int NF>
DI bool flags_set(const char* ws, unsigned off, u64 tag, int lane) {
  gu64* f = (gu64*)(ws + off);
  bool ok = __hip_atomic_load(f + (NF < 64 ? (lane & (NF - 1)) : lane), RLX_AGENT) == tag;
  if (NF >= 128) ok &= __hip_atomic_load(f + 64 + lane, RLX_AGENT) == tag;
  if (NF >= 256) { ok &= __hip_atomic_load(f + 128 + lane, RLX_AGENT) == tag; ok &= __hip_atomic_load(f + 192 + lane, RLX_AGENT) == tag; }
  return ok;
}
template <int NF>
DI void wait_flags(const char* ws, unsigned off, u64 tag, int lane) {
#pragma unroll 1
  for (unsigned spin = 0; spin < SPIN_CAP; ++spin) {
    if (__all(flags_set<NF>(ws, off, tag, lane))) break;
    __builtin_amdgcn_s_sleep(2);
  }
  asm volatile("" ::: "memory");
}
DI void set_flag(char* ws, unsigned off, u64 tag, int lane) { if (lane == 0) __hip_atomic_store((gu64*)(ws + off), tag, RLX_AGENT); }
DI unsigned nz(unsigned v) { return v < 0xFFFFFFFEu ? v : 0xFFFFFFFEu; }
DI unsigned umax3(unsigned a, unsigned b, unsigned c) { unsigned m = a > b ? a : b; return m > c ? m : c; }
DI bool missing(u32x4 v) { return __any(umax3(umax3(v.x, v.y, v.z), v.w, 0u) == 0xFFFFFFFFu); }
#define ONES ((u32x4){0xFFFFFFFFu, 0xFFFFFFFFu, 0xFFFFFFFFu, 0xFFFFFFFFu})
#define ST16(v, off) __builtin_amdgcn_raw_buffer_store_b128((v), wsr, (off), 0, 16)
#define ST8(v, off) __builtin_amdgcn_raw_buffer_store_b64((v), wsr, (off), 0, 16)
#define LD16(off) __builtin_amdgcn_raw_buffer_load_b128(wsr, (off), 0, 16)
#define LD8(off) __builtin_amdgcn_raw_buffer_load_b64(wsr, (off), 0, 16)

#define STAMP(i) do {} while (0)

__global__ __launch_bounds__(512, 2) void fused(const u16* __restrict__ x, const u16* __restrict__ wqkv, const u16* __restrict__ wphi, const u16* __restrict__ woA,
                                                const u16* __restrict__ wq, const u16* __restrict__ wk, const u16* __restrict__ wv, const u16* __restrict__ woB,
                                                const u16* __restrict__ gatew, u16* __restrict__ out, char* ws) {
  __shared__ __attribute__((aligned(16))) char lds[L_TOTAL];
  const int tid = threadIdx.x, lane = tid & 63;
  const int w = __builtin_amdgcn_readfirstlane(tid >> 6) & 7;
  const int l15 = lane & 15, g = lane >> 4, q4 = l15 >> 2, p4 = lane & 3, r = lane & 31, h = lane >> 5;
  const int bx = blockIdx.x & 7, bi = blockIdx.x >> 3;
  const int rt = (bx & 1) * 8 + (bi & 7), hp = (bx >> 1) * 4 + ((bi >> 3) & 3), hq = hp >> 1, role = hp & 1;
  const int w3 = w & 3, w2 = w >> 2;
  LAS char* lp = (LAS char*)lds;
  const unsigned lds0 = (unsigned)(uintptr_t)lds;
  const __amdgpu_buffer_rsrc_t wsr = __builtin_amdgcn_make_buffer_rsrc(ws, 0, WS_END, 0x00020000);
  LAS unsigned* cnt = (LAS unsigned*)(lp + L_CNT);
#define ARRIVE_NW(c) do { asm volatile("" ::: "memory"); if (lane == 0) __hip_atomic_fetch_add(cnt + (c), 1u, __ATOMIC_RELAXED, __HIP_MEMORY_SCOPE_WORKGROUP); asm volatile("" ::: "memory"); } while (0)
#define PEEK(c) ((unsigned)__builtin_amdgcn_readfirstlane(__hip_atomic_load(cnt + (c), __ATOMIC_RELAXED, __HIP_MEMORY_SCOPE_WORKGROUP)))
#define AWAIT(c, n) do { _Pragma("unroll 1") for (unsigned sp_ = 0; sp_ < LSPIN_CAP; ++sp_) { if (PEEK(c) >= (unsigned)(n)) break; __builtin_amdgcn_s_sleep(1); } asm volatile("" ::: "memory"); } while (0)
  STAMP(0);

  const int T0 = rt * 128 + hq * 16;
  const int C0 = hq * 80;
  const int wlane = ((g >> 1) * 16 + (g & 1) * 4 + q4) * 160 + p4 * 40;
  u64 tag;
  unsigned o0[10];
#define KVP(s, slot, p) dma16s_sc1(ws + WS_KV + (s) * STEP_B + (p) * 1024, lane * 16, lds0 + (slot) * STEP_B + (p) * 1024)
#define KVL(s, slot) do { _Pragma("unroll") for (int j = 0; j < 11; j++) if (j < 10 || w3 < 2) KVP(s, slot, w3 + 4 * j); } while (0)
#define KW(s, slot, j) do { if ((j) == 4) KVP(s, slot, w * 5 + 4); else { if ((j) == 0) dmag_open(lds0 + (slot) * STEP_B + w * 5120); dmag_sc1<(j)>(ws + WS_KV + (s) * STEP_B + w * 5120, lane * 16); } } while (0)
#define KX(s, slot) do { if (w < 2) KVP(s, slot, 40 + w); } while (0)
  if (w2 == 0) {
    u32x4 gf0, gf1, gf2, gf3, gf4, wpc; u32x2 tch;
    u32x2 ep = ld8_sc1_async(ws + WS_EPOCH);
    const int lx = w3 < 3 ? L_P0X0 + w3 * 5120 : L_P0X1;
    { const u16* wsrc = (role ? wv : wk) + w3 * 3200;
#pragma unroll
      for (int j = 0; j < 5; j++) { const int n = j * 64 + lane, row = (n * 3277) >> 16, cc = n - row * 20; dma16s(x + T0 * DM + w3 * 160, row * 1280 + cc * 16, lds0 + lx + j * 1024); }
#pragma unroll
      for (int j = 0; j < 6; j++) dma16s(wsrc + j * 512, lane * 16, lds0 + L_P0W + w3 * 6400 + j * 1024);
      if (lane < 16) dma16s(wsrc + 6 * 512, lane * 16, lds0 + L_P0W + w3 * 6400 + 6 * 1024);
      const u16* gp = gatew + w3 * 160 + g * 8;
      gf0 = ld16_async(gp); gf1 = ld16_async(gp + 32); gf2 = ld16_async(gp + 64); gf3 = ld16_async(gp + 96); gf4 = ld16_async(gp + 128);
      wpc = ld16_async(wphi + hq * 1600 + (tid < 200 ? tid : 199) * 8);
      const char* tp = ws + (lane & 7) * 1114112;
      if (lane >= 8) tp = ws + (lane < 16 ? WS_FKV + blockIdx.x * 128 : WS_KVR + blockIdx.x * 320 + (lane & 3) * 96);
      if (w3 == 1) tp = (const char*)(woA + C0 + (lane & 7) * 51200);
      if (w3 == 2) tp = (const char*)(woB + C0 + (lane & 7) * 51200);
      if (w3 == 3) tp = (const char*)(out + (rt * 128 + role * 64) * DM + C0);
      tch = ld8_sc1_async(tp);
    }
    asm volatile("s_waitcnt vmcnt(1)" : "+v"(gf0), "+v"(gf1), "+v"(gf2), "+v"(gf3), "+v"(gf4), "+v"(wpc), "+v"(ep) :: "memory");
    tag = (((u64)(unsigned)__builtin_amdgcn_readfirstlane(ep.y) << 32) | (unsigned)__builtin_amdgcn_readfirstlane(ep.x)) + 1ull;
    STAMP(1);
    { const LAS char* wb = lp + L_P0W + (w3 * 160 + g * 8 + q4) * 40;
      const LAS char* xb = lp + lx + l15 * 320 + g * 16;
      const bool gl = l15 == 4;
      f32x4 c0 = {}, c1 = {};
#define P0STEP(s, GF) { const u32x4 b = ldsr16(xb + (s) * 64); c0 = mfma16(trd2(wb + p4 * 8 + (s) * 1280, wb + p4 * 8 + (s) * 1280 + 160), b, c0); \
        u32x4 a = trd2(wb + 32 + (s) * 1280, wb + 32 + (s) * 1280 + 160); if (gl) a = GF; c1 = mfma16(a, b, c1); }
      P0STEP(0, gf0) P0STEP(1, gf1) P0STEP(2, gf2) P0STEP(3, gf3) P0STEP(4, gf4)
#undef P0STEP
      *(LAS f32x4*)(lp + L_RED + tid * 16) = c0;
      if (lane < 32) *(LAS f32x4*)(lp + L_RED1 + (w3 * 32 + lane) * 16) = c1; }
    STAMP(2);
    f32x16 acc[5];
#pragma unroll
    for (int b = 0; b < 5; b++) acc[b] = (f32x16){};
    const int wm = g & 1, whd = p4;
    const int wo0 = wm * ST_W1 + (8 * h + 4 * wm + q4) * 160 + whd * 40, wo1 = wm * ST_W1 + (8 * h + 4 * (wm ^ 1) + q4) * 160 + whd * 40;
    const int xo = ST_X + (w3 * 32 + r) * 128 + ((h ^ ((r >> 1) & 7)) * 16);
    u32x4 fa[5], fb[5], xa, xb;
#pragma unroll
    for (int j = 0; j < 5; j++) fb[j] = (u32x4){0u, 0u, 0u, 0u};
    xb = (u32x4){0u, 0u, 0u, 0u};
#define KSTEP(FN, XN, s, FC, XC) { XN = ldsr16(sb + (xo ^ ((s) * 32))); \
      _Pragma("unroll") for (int j = 0; j < 5; j++) { acc[j] = mfma32(FC[j], XC, acc[j]); FN[j] = trd2(sb + wo0 + (s) * 2560 + j * 8, sb + wo1 + (s) * 2560 + j * 8); SBAR(); } }
    int bsel = 2;
#pragma unroll 1
    for (int i = 0; i < 10; i++) {
      BAR_L();
      if (i == 1 && role) {
        const unsigned qb = WS_QX + (rt * 8 + hq) * QX_B + w3 * 1280 + lane * 16;
        ST16(ONES, qb); ST16(ONES, qb + 5120); if (lane < 32) ST16(ONES, qb + 1024 + (lane >> 4) * (5120 - 256)); }
      if (i == 4) { asm volatile("s_waitcnt vmcnt(0)" : "+v"(tch) :: "memory"); if (role) set_flag(ws, WS_FQ + ((rt * 8 + hq) * 4 + w3) * 8, tag, lane);
                    if (tid < 200) *(LAS u32x4*)(lp + L_WPHI + tid * 16) = wpc; }
      const LAS char* sb = lp + L_BUF + bsel * STAGE_B;
      bsel = bsel == 2 ? 0 : bsel + 1;
      KSTEP(fa, xa, 0, fb, xb) KSTEP(fb, xb, 1, fa, xa) KSTEP(fa, xa, 2, fb, xb)
      { xb = ldsr16(sb + (xo ^ 96));
        acc[0] = mfma32(fa[0], xa, acc[0]); fb[0] = trd2(sb + wo0 + 7680, sb + wo1 + 7680); fb[1] = trd2(sb + wo0 + 7688, sb + wo1 + 7688); SBAR();
        acc[1] = mfma32(fa[1], xa, acc[1]); fb[2] = trd2(sb + wo0 + 7696, sb + wo1 + 7696); fb[3] = trd2(sb + wo0 + 7704, sb + wo1 + 7704); SBAR();
        acc[2] = mfma32(fa[2], xa, acc[2]); fb[4] = trd2(sb + wo0 + 7712, sb + wo1 + 7712); SBAR();
        acc[3] = mfma32(fa[3], xa, acc[3]); acc[4] = mfma32(fa[4], xa, acc[4]); SBAR(); }
    }
#pragma unroll
    for (int j = 0; j < 5; j++) acc[j] = mfma32(fb[j], xb, acc[j]);
#undef KSTEP
    STAMP(3);
#define PK2(t, i, sc) pk(acc[t][i] * (sc), acc[t][(i) + 1] * (sc))
#define ROW(i, sc) const u32x4 ra = {PK2(0, i, sc), PK2(0, (i) + 2, sc), PK2(1, i, sc), PK2(1, (i) + 2, sc)}, rb = {PK2(2, i, sc), PK2(2, (i) + 2, sc), PK2(3, i, sc), PK2(3, (i) + 2, sc)}; const u32x2 rc = {PK2(4, i, sc), PK2(4, (i) + 2, sc)}
    { ROW(4, 1.0f);
      *(LAS u32x4*)(lp + L_HO + tid * 16) = ra; *(LAS u32x4*)(lp + L_HO + 4096 + tid * 16) = rb; *(LAS u32x2*)(lp + L_HO + 8192 + tid * 8) = rc; }
    BAR_L();
    const int qfo = (h * 4 + w3) * 1280;
    if (role) {
      { ROW(8, QSCALE); const unsigned qb = WS_QX + (rt * 8 + hq) * QX_B + qfo;
        ST16(((u32x4){nz(ra.x), nz(ra.y), nz(ra.z), nz(ra.w)}), qb + r * 16); ST16(((u32x4){nz(rb.x), nz(rb.y), nz(rb.z), nz(rb.w)}), qb + 512 + r * 16); ST8(((u32x2){nz(rc.x), nz(rc.y)}), qb + 1024 + r * 8); }
      { ROW(12, QSCALE); LAS char* d = lp + L_QL + qfo;
        *(LAS u32x4*)(d + r * 16) = ra; *(LAS u32x4*)(d + 512 + r * 16) = rb; *(LAS u32x2*)(d + 1024 + r * 8) = rc; }
    } else { LAS char* d = lp + L_VIM + (h * 128 + w3 * 32 + r) * 40;
#pragma unroll
      for (int t = 0; t < 5; t++) { *(LAS u32x2*)(d + t * 8) = (u32x2){PK2(t, 8, 1.0f), PK2(t, 10, 1.0f)}; *(LAS u32x2*)(d + 10240 + t * 8) = (u32x2){PK2(t, 12, 1.0f), PK2(t, 14, 1.0f)}; } }
#pragma unroll
    for (int t = 0; t < 5; t++) { o0[2 * t] = PK2(t, 0, 1.0f); o0[2 * t + 1] = PK2(t, 2, 1.0f); }
#undef ROW
#undef PK2
  } else {
    u32x2 ep = ld8_sc1_async(ws + WS_EPOCH);
    const bool second = w3 >> 1;
    const unsigned rs = (second && role) ? 1280u : 3840u;
    const char* wsrc = (second ? (role ? (const char*)(wq + C0) : (const char*)(wqkv + 1280 + C0)) : (const char*)(wqkv + (role ? 0 : 640) + C0)) + (w3 & 1) * 32 * rs;
    const char* xsrc = (const char*)(x + (rt * 128 + w3 * 32) * DM);
    int rot = (bi & 7) + 3 * ((bi >> 3) & 3); rot = rot >= 10 ? rot - 10 : rot;
    unsigned so[5], xoff[2];
#pragma unroll
    for (int j = 0; j < 5; j++) { const int c = j * 64 + lane, rho = (c * 6554) >> 16, cc = c - rho * 10; so[j] = (rho ^ (second ? 4 : 0)) * rs + cc * 16; }
#pragma unroll
    for (int j = 0; j < 2; j++) { const int row = j * 8 + (lane >> 3); xoff[j] = row * 1280 + (((lane & 7) ^ ((row >> 1) & 7)) * 16); }
    int bsel = 2, kc = rot;
    auto stage = [&]() {
      const unsigned lb = lds0 + L_BUF + bsel * STAGE_B;
      const char* bw = wsrc + kc * 64 * rs; const char* bxp = xsrc + kc * 128;
#pragma unroll
      for (int j = 0; j < 5; j++) { dma16s(bw, so[j], lb + (w3 * 5 + j) * 1024); if (j < 4) dma16s(bxp + (j >> 1) * 16 * 1280, xoff[j & 1], lb + ST_X + (w3 * 4 + j) * 1024); }
      bsel = bsel == 2 ? 0 : bsel + 1; kc = kc == 9 ? 0 : kc + 1;
    };
    stage(); stage();
    asm volatile("s_waitcnt vmcnt(9)" : "+v"(ep) :: "memory");
    tag = (((u64)(unsigned)__builtin_amdgcn_readfirstlane(ep.y) << 32) | (unsigned)__builtin_amdgcn_readfirstlane(ep.x)) + 1ull;
    STAMP(1); STAMP(2);
#pragma unroll 1
    for (int i = 0; i < 8; i++) {
      WAIT_BAR(9);
      stage();
      if (i == 0 && w3 == 0) {
        const unsigned tb = WS_KV + (T0 >> 5) * TILE_B; const int s16 = hq & 1;
        if (!role) {
          const LAS f32x4* rp = (const LAS f32x4*)(lp + (h ? L_RED1 : L_RED)) + (h ? (g & 1) * 16 : g * 32) + l15;
          const int st = h ? 32 : 64, o2 = h ? 0 : 16;
          const f32x4 a = (rp[0] + rp[st]) + (rp[2 * st] + rp[3 * st]), b = (rp[o2] + rp[st + o2]) + (rp[2 * st + o2] + rp[3 * st + o2]);
          const u32x4 v = {pk(a[0], a[1]), pk(a[2], a[3]), pk(b[0], b[1]), pk(b[2], b[3])};
          if (lane < 32) ST16(v, tb + (g * 32 + s16 * 16 + l15) * 16);
          else if (lane < 48) ST8(((u32x2){v.x, v.y}), tb + 1024 + (s16 * 16 + l15) * 8);
          else { const float gv = 1.0f / (1.0f + __expf(-a[0])); __builtin_amdgcn_raw_buffer_store_b32(__float_as_uint(gv), wsr, WS_G + (T0 + l15) * 4, 0, 16); }
        } else if (lane < 44) {
          const int hh = lane >= 22, d = lane - hh * 22, dd = d < 20 ? d : 0, st = dd >= 16 ? 128 : 256;
          const LAS float* rp = (const LAS float*)(lp + (dd >= 16 ? L_RED1 : L_RED)) + (((dd & 15) >> 2) * 16 + 4 * hh) * 4 + (dd & 3);
          float e[8];
#pragma unroll
          for (int j = 0; j < 8; j++) { const int tk = (8 * (j >> 2) + (j & 3)) * 4; e[j] = (rp[tk] + rp[tk + st]) + (rp[tk + 2 * st] + rp[tk + 3 * st]); }
          u32x4 v = {pk(e[0], e[1]), pk(e[2], e[3]), pk(e[4], e[5]), pk(e[6], e[7])};
          if (d == 20) v = (u32x4){0x3F803F80u, 0x3F803F80u, 0x3F803F80u, 0x3F803F80u};
          if (d == 21) v = (u32x4){0u, 0u, 0u, 0u};
          ST16(v, tb + 1280 + (s16 * 2 + hh) * VB + d * 16);
        }
      }
      if (i == 2) { if (w3 == 0) set_flag(ws, WS_FKV + blockIdx.x * 8, tag, lane);
                    if (w3 >= 2) *(LAS u32x4*)(lp + L_ZERO + (tid - 384) * 16) = (u32x4){0u, 0u, 0u, 0u}; }
    }
    u64 fa, fb, fc, fd, fe;
    { gu64* f = (gu64*)(ws + WS_FKV) + lane;
      fa = __hip_atomic_load(f, RLX_AGENT); fb = __hip_atomic_load(f + 64, RLX_AGENT); fc = __hip_atomic_load(f + 128, RLX_AGENT); fd = __hip_atomic_load(f + 192, RLX_AGENT);
      fe = __hip_atomic_load((gu64*)(ws + WS_FQ) + (rt * 8 + hq) * 4 + p4, RLX_AGENT); }
    WAIT_BAR(14);
    asm volatile("" : "+v"(fa), "+v"(fb), "+v"(fc), "+v"(fd), "+v"(fe));
    const bool kvok = __all((fa == tag) & (fb == tag) & (fc == tag) & (fd == tag) & (fe == tag));
    asm volatile("" ::: "memory");
    BAR_L();
    if (!kvok) { wait_flags<256>(ws, WS_FKV, tag, lane); wait_flags<4>(ws, WS_FQ + (rt * 8 + hq) * 32, tag, lane); }
    KVL(0, 0);
    if (blockIdx.x == 0 && w3 == 0) set_flag(ws, WS_EPOCH, tag, lane);
    STAMP(3);
    BAR_L();
    const int ht = tid - 256;
    const u32x4 a = ldsr16(lp + L_HO + ht * 16), b = ldsr16(lp + L_HO + 4096 + ht * 16); const u32x2 c = ldsr8(lp + L_HO + 8192 + ht * 8);
    o0[0] = a.x; o0[1] = a.y; o0[2] = a.z; o0[3] = a.w; o0[4] = b.x; o0[5] = b.y; o0[6] = b.z; o0[7] = b.w; o0[8] = c.x; o0[9] = c.y;
  }
  STAMP(4);
  u32x4 ph[5];
  { const LAS char* pb = ((p4 & 1) == h) ? lp + L_WPHI + (2 * w2 + h) * 800 + q4 * 40 + (2 * (g & 1) + (p4 >> 1)) * 8 : lp + L_ZERO;
    const u32x4 b0 = {o0[0], o0[1], o0[2], o0[3]}, b1 = {o0[4], o0[5], o0[6], o0[7]}, b2 = {o0[8], o0[9], 0u, 0u};
    const u32x2 c0 = trd(pb + 640), c1 = trd(pb + 672);
    f32x16 P0 = {}, P1 = {};
    P0 = mfma32(trd2(pb, pb + 160), b0, P0); P1 = mfma32(trd2(pb + 32, pb + 192), b0, P1);
    P0 = mfma32(trd2(pb + 320, pb + 480), b1, P0); P1 = mfma32(trd2(pb + 352, pb + 512), b1, P1);
    P0 = mfma32((u32x4){c0.x, c0.y, 0u, 0u}, b2, P0); P1 = mfma32((u32x4){c1.x, c1.y, 0u, 0u}, b2, P1);
    KX(1, 1); KW(1, 1, 0); SBAR();
    f32x2 p[10];
#pragma unroll
    for (int f = 0; f < 10; f++) p[f] = f < 8 ? (f32x2){P0[(2 * f) & 15], P0[(2 * f + 1) & 15]} : (f32x2){P1[(2 * f) & 3], P1[(2 * f + 1) & 3]};
    float mx = fmaxf(p[0][0], p[0][1]), mn = fminf(p[0][0], p[0][1]);
#pragma unroll
    for (int f = 1; f < 10; f++) { mx = fmaxf(fmaxf(mx, p[f][0]), p[f][1]); mn = fminf(fminf(mn, p[f][0]), p[f][1]); }
    const f32x2 ma = {-mx * LOG2E, -mx * LOG2E}, mb = {mn * LOG2E, mn * LOG2E}, ca_ = {LOG2E, LOG2E}, cb_ = {-LOG2E, -LOG2E};
    f32x2 ea[10], eb[10], sa = {0.f, 0.f}, sb = {0.f, 0.f};
#pragma unroll
    for (int f = 0; f < 10; f++) { if (f % 2 == 0 && f < 8) { SBAR(); if (f == 0) KW(1, 1, 1); if (f == 2) KW(1, 1, 2); if (f == 4) KW(1, 1, 3); if (f == 6) KW(1, 1, 4); SBAR(); }
      const f32x2 xa = __builtin_elementwise_fma(p[f], ca_, ma), xb = __builtin_elementwise_fma(p[f], cb_, mb);
      ea[f] = (f32x2){ex2(xa[0]), ex2(xa[1])}; eb[f] = (f32x2){ex2(xb[0]), ex2(xb[1])}; sa += ea[f]; sb += eb[f]; }
    const float ia = __builtin_amdgcn_rcpf(sa[0] + sa[1]), ib = __builtin_amdgcn_rcpf(sb[0] + sb[1]);
    unsigned o[20];
#pragma unroll
    for (int f = 0; f < 10; f++) { const f32x2 ya = ea[f] * ia, yb = eb[f] * ib; o[f] = pk(ya[0], ya[1]); o[10 + f] = pk(yb[0], yb[1]); }
#pragma unroll
    for (int c = 0; c < 5; c++) ph[c] = (u32x4){o[4 * c], o[4 * c + 1], o[4 * c + 2], o[4 * c + 3]};
    asm volatile("" : "+v"(ph[0]), "+v"(ph[1]), "+v"(ph[2]), "+v"(ph[3]), "+v"(ph[4]));
  }
  u32x4 q1A, q1B; u32x2 q2A, q2B;
  if (role) {
    WAIT_BAR(0);
    STAMP(5);
    const LAS char* qa = lp + L_QL + w3 * 1280;
    q1A = ldsr16(qa + lane * 16); q1B = ldsr16(qa + 5120 + lane * 16); q2A = ldsr8(qa + 1024 + r * 8); q2B = ldsr8(qa + 6144 + r * 8);
    ARRIVE_NW(0);
  } else {
    const unsigned qb = WS_QX + (rt * 8 + hq) * QX_B + w3 * 1280;
    q1A = LD16(qb + lane * 16); q1B = LD16(qb + 5120 + lane * 16); q2A = LD8(qb + 1024 + r * 8); q2B = LD8(qb + 6144 + r * 8);
    { LAS char* d = lp + L_PHK + ((2 * w2 + h) * 128 + w3 * 32 + r) * 80;
#pragma unroll
      for (int c = 0; c < 5; c++) *(LAS u32x4*)(d + c * 16) = ph[c]; }
    WAIT_BAR(4);
    STAMP(5);
#pragma unroll 1
    for (unsigned spin = 0; spin < SPIN_CAP; ++spin) {
      if (!(missing(q1A) | missing(q1B) | missing((u32x4){q2A.x, q2A.y, q2B.x, q2B.y}))) break;
      __builtin_amdgcn_s_sleep(1);
      asm volatile("" ::: "memory");
      q1A = LD16(qb + lane * 16); q1B = LD16(qb + 5120 + lane * 16); q2A = LD8(qb + 1024 + r * 8); q2B = LD8(qb + 6144 + r * 8);
    }
  }
  STAMP(6);
  const int vr = r <= 21 ? r : 21;
  const int ok1 = lane * 16, ok2 = h ? (1280 + 20 * 16) : (1024 + r * 8), ov = 1280 + h * VB + vr * 16;
  const f32x16 Z = {};
  if (h) { q2A = (u32x2){0u, 0u}; q2B = (u32x2){0u, 0u}; }
  { const LAS char* t0 = lp;
    const u32x4 k1 = ldsr16(t0 + ok1); const u32x2 k2 = ldsr8(t0 + ok2);
    f32x16 sa = mfma32(k1, q1A, Z); sa = mfma32k8(k2, q2A, sa);
    f32x16 sb = mfma32(k1, q1B, Z); sb = mfma32k8(k2, q2B, sb);
    float ma = sa[0], mb = sb[0];
#pragma unroll
    for (int i = 1; i < 16; i++) { ma = fmaxf(ma, sa[i]); mb = fmaxf(mb, sb[i]); }
    ma = half_swap_max(ma); mb = half_swap_max(mb);
    const float mah = __uint_as_float(__float_as_uint(ma) & 0xFFFF0000u), mbh = __uint_as_float(__float_as_uint(mb) & 0xFFFF0000u);
    if (h) { q2A.x = pk(-mah, mah - ma); q2B.x = pk(-mbh, mbh - mb); } }
  f32x16 OA = {}, OB = {}, SA, SB;
  u32x4 k1, v1, v2, v2n, pA1, pA2, pB1, pB2; u32x2 k2;
  float e0, e1, e2, e3;
  const LAS char* c0p = lp + w2 * CH_B + ok1; const LAS char* c1p = lp + w2 * CH_B + ok2; const LAS char* c2p = lp + w2 * CH_B + ov;
  k1 = ldsr16(c0p); k2 = ldsr8(c1p);
  SA = mfma32(k1, q1A, Z); SA = mfma32k8(k2, q2A, SA);
  v1 = ldsr16(c2p); v2 = v1;
  e0 = e1 = e2 = e3 = 0.f; pB2 = (u32x4){0u, 0u, 0u, 0u};
#define G(MF, LD, C0_, C1_, X, S_, i) MF; LD; SBAR(); C0_; C1_; SBAR(); X##0 = ex2(S_[i]); X##1 = ex2(S_[i + 1]); X##2 = ex2(S_[i + 2]); X##3 = ex2(S_[i + 3]); asm volatile("" : "+v"(X##0), "+v"(X##1), "+v"(X##2), "+v"(X##3)); SBAR();
#define TILE(TB, TN, V2, H0, H1, H2) { float a0, a1, a2, a3; \
    G(SB = mfma32(k1, q1B, Z),    k1 = ldsr16(c0p + (TN)), pB2[2] = pk(e0, e1), pB2[3] = pk(e2, e3), a, SA, 0) \
    G(OB = mfma32(v2, pB2, OB),   V2,                      pA1[0] = pk(a0, a1), pA1[1] = pk(a2, a3), e, SA, 4) \
    G(SB = mfma32k8(k2, q2B, SB), k2 = ldsr8(c1p + (TN)),  pA1[2] = pk(e0, e1), pA1[3] = pk(e2, e3), a, SA, 8) \
    G(OA = mfma32(v1, pA1, OA),   H0,                      pA2[0] = pk(a0, a1), pA2[1] = pk(a2, a3), e, SA, 12) \
    G(SA = mfma32(k1, q1A, Z),    H1,                      pA2[2] = pk(e0, e1), pA2[3] = pk(e2, e3), a, SB, 0) \
    G(OA = mfma32(v2, pA2, OA),   H2,                      pB1[0] = pk(a0, a1), pB1[1] = pk(a2, a3), e, SB, 4) \
    G(SA = mfma32k8(k2, q2A, SA), (void)0,                 pB1[2] = pk(e0, e1), pB1[3] = pk(e2, e3), a, SB, 8) \
    G(OB = mfma32(v1, pB1, OB),   v1 = ldsr16(c2p + (TN)), pB2[0] = pk(a0, a1), pB2[1] = pk(a2, a3), e, SB, 12) }
#define NOP (void)0
#define TL(sl, t, H0, H1, H2) TILE((sl) * STEP_B + (t) * TILE_B, (sl) * STEP_B + ((t) + 1) * TILE_B, v2 = ldsr16(c2p + (sl) * STEP_B + (t) * TILE_B + 2 * VB), H0, H1, H2)
#define TL6(sl, H0, H1) TL(sl, 6, H0, H1, v2n = ldsr16(c2p + (sl) * STEP_B + 7 * TILE_B + 2 * VB))
#define TL7(sl, sn, H0, H1, H2) TILE((sl) * STEP_B + 7 * TILE_B, (sn) * STEP_B, v2 = v2n, H0, H1, H2)
  unsigned woff[5];
#pragma unroll
  for (int j = 0; j < 5; j++) { const int c = j * 64 + lane, rho = (c * 6554) >> 16, cc = c - rho * 10; const int k = (rho & ~12) | ((rho & 4) << 1) | ((rho & 8) >> 1); woff[j] = k * 1280 + cc * 16 + (4 - j) * 1024; }
#define WOG(wo, gr, j, dst) do { if ((j) == 4) WOP(wo, gr, 4, dst); else { if ((j) == 0) dmag_open(lds0 + (dst)); dmag<(j)>((wo) + C0 + (gr) * 32 * 640 - 2048, woff[j]); } } while (0)
#define WOP(wo, gr, j, dst) dma16s((wo) + C0 + (gr) * 32 * 640 - (4 - (j)) * 512, woff[j], lds0 + (dst) + (j) * 1024)
#define WA(gr, j) WOG(woA, gr, j, L_WO + (gr) * 5120)
  const u16* wxs = w < 4 ? woA + (16 + w3) * 32 * 640 : woB + (w < 7 ? 17 + w3 : 19) * 32 * 640;
  const int wxd = w < 4 ? L_WO + (16 + w3) * 5120 : 145408 + (w < 7 ? w3 : 2) * 5120;
#define WX(j) WOG(wxs, 0, j, wxd)
#define PRIO(n) __builtin_amdgcn_s_setprio(n)
  const unsigned fo = (rt * 8 + hq) * OT_B + lane * 16;
#define FILL(j) do { const int n_ = w * 3 + ((j) >> 1); ST16(ONES, (((j) & 1) ? WS_OB : WS_OA) + fo + (n_ < 20 ? n_ : 19) * 1024); } while (0)
#define CLR_VM() __builtin_amdgcn_s_waitcnt(0x0F70)
  const bool red = !role && w == 4;
  f32x4 part[16]; u64 fp0, fp1;
  auto red_load = [&]() {
    asm volatile("" : "+v"(fp0), "+v"(fp1));
    if (!__all((fp0 == tag) & (fp1 == tag))) wait_flags<128>(ws, WS_FPART + hq * 1024, tag, lane);
    asm volatile("" ::: "memory");
#pragma unroll
    for (int j = 0; j < 16; j++) part[j] = __builtin_bit_cast(f32x4, LD16(WS_PART + (hq * 16 + j) * PART_B + (rt * 60 + (lane < 60 ? lane : 59)) * 16));
  };
  auto red_store = [&]() {
    f32x4 a = ((part[0] + part[1]) + (part[2] + part[3])) + ((part[4] + part[5]) + (part[6] + part[7]));
    a += ((part[8] + part[9]) + (part[10] + part[11])) + ((part[12] + part[13]) + (part[14] + part[15]));
    const int hd = rt >> 2, u = (rt & 3) * 60 + lane, hi = u >= 160, fq = hi ? (u - 160) >> 3 : u >> 4, di = hi ? u & 7 : u & 15;
    if (lane < 60) ST8(((u32x2){pk(a[0], a[1]), pk(a[2], a[3])}), WS_KVR + hq * KVR_B + (((hd >> 1) * 2 + hi) * 5 + (fq >> 1)) * 512 + ((hd & 1) * 16 + di) * 16 + (fq & 1) * 8);
  };
  u32x4 ka[10]; u64 fkr;
  auto oa_load = [&]() {
    asm volatile("" : "+v"(fkr));
    if (!__all(fkr == tag)) wait_flags<16>(ws, WS_FKVR + hq * 128, tag, lane);
    asm volatile("" ::: "memory");
    const unsigned kb = (((r >> 2) & 1) == h) ? WS_KVR + hq * KVR_B + w2 * 5120 + (h * 16 + (r & 3) + 4 * (r >> 3)) * 16 : 0x7FFF0000u;
#pragma unroll
    for (int j = 0; j < 10; j++) ka[j] = LD16(kb + j * 512);
  };
  u32x4 ja[2][3], jb[2]; f32x4 jc[3];
  const LAS char* jab = lp + L_PHK + ((w >> 1) * 128 + g * 8 + q4) * 80 + p4 * 8;
  const LAS char* jbb = lp + L_VIM + ((w >> 1) * 128 + g * 8 + q4) * 40 + (w & 1) * 32 + p4 * 8;
  auto kv_rd = [&](int k0) {
#pragma unroll
    for (int ks = 0; ks < 2; ks++) { jb[ks] = trd2(jbb + (k0 + ks) * 1280, jbb + (k0 + ks) * 1280 + 160);
#pragma unroll
      for (int ft = 0; ft < 3; ft++) ja[ks][ft] = trd2(jab + (k0 + ks) * 2560 + ft * 32, jab + (k0 + ks) * 2560 + 320 + ft * 32); } };
  auto kv_mm = [&](bool first) {
#pragma unroll
    for (int ks = 0; ks < 2; ks++) { if ((w & 1) && l15 == 4) jb[ks] = (u32x4){0x3F803F80u, 0x3F803F80u, 0x3F803F80u, 0x3F803F80u};
#pragma unroll
      for (int ft = 0; ft < 3; ft++) jc[ft] = mfma16(ja[ks][ft], jb[ks], (first && !ks) ? (f32x4){0.f, 0.f, 0.f, 0.f} : jc[ft]); } };
  auto kv_st = [&]() { const int dt = w & 1; const unsigned pb = WS_PART + (hq * 16 + rt) * PART_B + (w >> 1) * 3840 + (dt ? 2560 + g * 128 : g * 256) + l15 * 16;
#pragma unroll
    for (int ft = 0; ft < 3; ft++) if ((!dt || l15 < 8) && ft * 4 + g < 10) ST16(__builtin_bit_cast(u32x4, jc[ft]), pb + ft * (dt ? 512 : 1024)); };
  f32x16 oa0, oa1; u32x4 z0, z1, z2;
  LAS char* otb = lp + L_WO + (8 + w) * 5120;
  auto oa_mm = [&](int s) { oa0 = mfma32(ka[s], ph[s], s ? oa0 : Z); oa1 = mfma32(ka[5 + s], ph[s], s ? oa1 : Z); };
  auto oa_norm = [&]() { const float inv = __builtin_amdgcn_rcpf(oa1[4] + 1e-6f);
#pragma unroll
    for (int t = 0; t < 5; t++) { const int k0 = h * 20 + 4 * t;
      const u32x2 v = t < 4 ? (u32x2){nz(pk(oa0[(4 * t) & 15] * inv, oa0[(4 * t + 1) & 15] * inv)), nz(pk(oa0[(4 * t + 2) & 15] * inv, oa0[(4 * t + 3) & 15] * inv))} : (u32x2){nz(pk(oa1[0] * inv, oa1[1] * inv)), nz(pk(oa1[2] * inv, oa1[3] * inv))};
      *(LAS u32x2*)(otb + (r >> 4) * 1280 + (k0 >> 3) * 256 + (k0 & 4) * 2 + l15 * 16) = v; }
    z0 = ldsr16(otb + lane * 16); z1 = ldsr16(otb + 1280 + lane * 16); z2 = ldsr16(otb + (r >> 4) * 1280 + 1024 + l15 * 16); };
  auto oa_st = [&]() { const unsigned oa = WS_OA + (rt * 8 + w3 * 2) * OT_B + (hq * 2 + w2) * 1280;
    ST16(z0, oa + lane * 16); ST16(z1, oa + OT_B + lane * 16); if (!h) ST16(z2, oa + (r >> 4) * OT_B + 1024 + l15 * 16); };
  unsigned cpk;
  PRIO(3);
  TL(0, 0, if (!role) kv_rd(0); else FILL(0), NOP, NOP)
  TL(0, 1, if (!role) { kv_mm(true); kv_rd(2); ARRIVE_NW(0); } else FILL(1), NOP, NOP)
  TL(0, 2, if (!role) kv_mm(false); else FILL(2), NOP, if (!role) kv_st())
  TL(0, 3, NOP, NOP, cpk = __hip_atomic_load(cnt, __ATOMIC_RELAXED, __HIP_MEMORY_SCOPE_WORKGROUP))
  STAMP(14);
  if ((unsigned)__builtin_amdgcn_readfirstlane(cpk) < 8u) AWAIT(0, 8);
  asm volatile("" ::: "memory");
  TL(0, 4, NOP, NOP, if (!role) { WAIT_VM(0); set_flag(ws, WS_FPART + ((hq * 16 + rt) * 8 + w) * 8, tag, lane); } else FILL(3))
  PRIO(2);
  TL(0, 5, KX(2, 2), NOP, KW(2, 2, 0)) TL6(0, KW(2, 2, 1), KW(2, 2, 2))
  TL7(0, 1, KW(2, 2, 3), NOP, KW(2, 2, 4))
  STAMP(15);
  TL(1, 0, if (role) FILL(4), NOP, if (red) { fp0 = __hip_atomic_load((gu64*)(ws + WS_FPART) + hq * 128 + lane, RLX_AGENT); fp1 = __hip_atomic_load((gu64*)(ws + WS_FPART) + hq * 128 + 64 + lane, RLX_AGENT); })
  TL(1, 1, if (w < 4) { const int row = (w & 1) * 32 + (lane >> 1);
                      dma4s((w & 2 ? woB : woA) + C0 + ((rt & 7) * 2 + role) * 40 * 640, (row < 40 ? row : 39) * 1280 + (lane & 1) * 156, lds0 + 157696); }, NOP, if (role) FILL(5))
  TL(1, 2, NOP, NOP, if (red) red_load())
  PRIO(1);
  TL(1, 3, NOP, NOP, NOP)
  TL(1, 4, NOP, CLR_VM(), if (red) red_store())
  TL(1, 5, NOP, NOP, NOP)
  PRIO(0);
  TL6(1, if (red) { WAIT_VM(0); set_flag(ws, WS_FKVR + (hq * 16 + rt) * 8, tag, lane); }, NOP)
  if (red) WAIT_BAR(1); else WAIT_BAR(0);
  STAMP(12);
  PRIO(3);
  if (role) set_flag(ws, WS_FOA + ((rt * 8 + hq) * 8 + w) * 8, tag, lane);
  u64 foa;
  TL7(1, 2, KX(3, 0), KW(3, 0, 0), KW(3, 0, 1))
  TL(2, 0, KW(3, 0, 2), KW(3, 0, 3), KW(3, 0, 4)) TL(2, 1, WA(w, 0), WA(w, 1), WA(w, 2))
  TL(2, 2, WA(w, 3), WA(w, 4), (foa = __hip_atomic_load((gu64*)(ws + WS_FOA) + rt * 64 + lane, RLX_AGENT), fkr = __hip_atomic_load((gu64*)(ws + WS_FKVR) + hq * 16 + l15, RLX_AGENT)))
  PRIO(2);
  TL(2, 3, NOP, NOP, NOP)
  auto chk = [&]() { asm volatile("" : "+v"(foa), "+v"(fkr));
    if (!__all(foa == tag)) wait_flags<64>(ws, WS_FOA + rt * 512, tag, lane);
    asm volatile("" ::: "memory"); if (role) oa_load(); };
  TL(2, 4, chk(), NOP, NOP)
  PRIO(1);
  TL(2, 5, NOP, NOP, if (role) oa_mm(0))
  PRIO(0);
  TL6(2, if (role) oa_mm(1), if (role) oa_mm(2))
  BAR_L();
  STAMP(13);
  PRIO(3);
  TL7(2, 0, if (role) oa_mm(3), { if (role) oa_mm(4); CLR_VM(); }, WX(0))
  TL(0, 0, if (role) oa_norm(), WX(1), WX(2))
  TL(0, 1, if (role) oa_st(), WX(3), WX(4))
  TL(0, 2, WA(8 + w, 0), WA(8 + w, 1), WA(8 + w, 2))
  u32x4 wbr[11];
  const char* wbs = (const char*)(woB + C0 + w * 32 * 640) - 4096;
#define WB2(j) do { wbr[j] = *(const u32x4*)(wbs + woff[j] + (j) * 1024); wbr[5 + (j)] = *(const u32x4*)(wbs + 8 * 32 * 1280 + woff[j] + (j) * 1024); } while (0)
  TL(0, 3, WA(8 + w, 3), WA(8 + w, 4), WB2(0))
  PRIO(2);
  TL(0, 4, WB2(1), WB2(2), WB2(3))
  const int w5 = w < 5 ? w : 4;
  const unsigned wo16 = (w5 == 0 ? woff[0] : w5 == 1 ? woff[1] : w5 == 2 ? woff[2] : w5 == 3 ? woff[3] : woff[4]) + w5 * 1024;
  TL(0, 5, WB2(4), NOP, wbr[10] = *(const u32x4*)((const char*)(woB + C0 + 16 * 32 * 640) - 4096 + wo16))
  PRIO(1);
  ARRIVE_NW(1);
  const int tp = w & 1, kq = w >> 1;
  const unsigned ofr = (rt * 8 + role * 4 + tp * 2) * OT_B + kq * 5120 + lane * 16;
  const int R0 = rt * 128 + role * 64 + tp * 32 + l15;
  u32x4 of[2][5]; float gt0, gt1;
#define OFL(u) do { _Pragma("unroll") for (int i = 0; i < 5; i++) of[u][i] = LD16(WS_OA + ofr + (u) * OT_B + i * 1024); } while (0)
  TL6(0, OFL(0), (gt0 = __uint_as_float(__builtin_amdgcn_raw_buffer_load_b32(wsr, WS_G + R0 * 4, 0, 16)), gt1 = __uint_as_float(__builtin_amdgcn_raw_buffer_load_b32(wsr, WS_G + R0 * 4 + 64, 0, 16))))
  PRIO(0);
  TL7(0, 0, OFL(1), NOP, NOP)
  pB2[2] = pk(e0, e1); pB2[3] = pk(e2, e3);
  OB = mfma32(v2, pB2, OB);
#undef TL
#undef TL6
#undef TL7
#undef TILE
#undef G
  STAMP(7);
  { LAS f32x4* xb = (LAS f32x4*)(lp + (w3 >> 1) * CH_B) + (w3 & 1) * (64 * 6);
#pragma unroll
    for (int i = 0; i < 12; i++) { auto rr = __builtin_amdgcn_permlane16_swap(__float_as_uint(OA[i]), __float_as_uint(OB[i]), false, false); OA[i] = __uint_as_float(rr[0]); OB[i] = __uint_as_float(rr[1]); }
    const f32x16 snd = w2 ? OA : OB;
    AWAIT(1, 8);
#pragma unroll
    for (int c = 0; c < 3; c++) xb[(w2 * 3 + c) * 64 + lane] = (f32x4){snd[4 * c], snd[4 * c + 1], snd[4 * c + 2], snd[4 * c + 3]};
    asm volatile("s_waitcnt vmcnt(23) lgkmcnt(0)\n\ts_barrier" ::: "memory");
    f32x16 O = w2 ? OB : OA;
#pragma unroll
    for (int c = 0; c < 3; c++) { const f32x4 t = xb[((w2 ^ 1) * 3 + c) * 64 + lane]; O[4 * c] += t[0]; O[4 * c + 1] += t[1]; O[4 * c + 2] += t[2]; O[4 * c + 3] += t[3]; }
    float l = from_upper(O[8]);
    float amax = 0.f;
#pragma unroll
    for (int i = 0; i < 12; i++) amax += fabsf(O[i]);
    const bool good = (l >= 0x1p-30f) && (l <= 0x1p100f) && (amax <= 0x1p126f);
    if (__builtin_expect(!__all(good), 0)) {
      u32x4 q1; u32x2 q2;
#pragma unroll
      for (int i = 0; i < 4; i++) { auto rr = __builtin_amdgcn_permlane16_swap(q1A[i], q1B[i], false, false); q1[i] = w2 ? rr[1] : rr[0]; }
#pragma unroll
      for (int i = 0; i < 2; i++) { auto rr = __builtin_amdgcn_permlane16_swap(q2A[i], q2B[i], false, false); q2[i] = h ? 0u : (w2 ? rr[1] : rr[0]); }
      float m = -INFINITY;
#pragma unroll 1
      for (int t = 0; t < 64; t++) {
        f32x16 s = mfma32(LD16(WS_KV + t * TILE_B + ok1), q1, Z); s = mfma32k8(LD8(WS_KV + t * TILE_B + ok2), q2, s);
#pragma unroll
        for (int i = 0; i < 16; i++) m = fmaxf(m, s[i]);
      }
      m = half_swap_max(m);
      O = Z;
#pragma unroll 1
      for (int t = 0; t < 64; t++) {
        f32x16 s = mfma32(LD16(WS_KV + t * TILE_B + ok1), q1, Z); s = mfma32k8(LD8(WS_KV + t * TILE_B + ok2), q2, s);
#pragma unroll
        for (int i = 0; i < 16; i++) s[i] = ex2(s[i] - m);
        O = mfma32(LD16(WS_KV + t * TILE_B + ov), (u32x4){pk(s[0], s[1]), pk(s[2], s[3]), pk(s[4], s[5]), pk(s[6], s[7])}, O);
        O = mfma32(LD16(WS_KV + t * TILE_B + ov + 2 * VB), (u32x4){pk(s[8], s[9]), pk(s[10], s[11]), pk(s[12], s[13]), pk(s[14], s[15])}, O);
      }
      l = from_upper(O[8]);
    }
    const float inv = __builtin_amdgcn_rcpf(l);
    LAS char* tb = lp + w2 * CH_B + 12288 + w3 * 1536;
#pragma unroll
    for (int c = 0; c < 3; c++) { const int k0 = (g & 1) * 20 + 8 * c + 4 * h;
      *(LAS u32x2*)(tb + ((c == 2 && h) ? 1280 + (g & 1) * 8 : (k0 >> 3) * 256 + (k0 & 4) * 2) + l15 * 16) = (u32x2){nz(pk(O[4 * c] * inv, O[4 * c + 1] * inv)), nz(pk(O[4 * c + 2] * inv, O[4 * c + 3] * inv))}; }
    const unsigned ob = WS_OB + (rt * 8 + w3 * 2 + w2) * OT_B + hp * 1280 + lane * 16;
    const u32x4 z0 = ldsr16(tb + lane * 16), z1 = ldsr16(tb + 1024 + l15 * 16);
    ST16(z0, ob); ST16(z1, lane < 16 ? ob + 1024 : 0x7FFF0000u);
  }
  STAMP(8);
  u32x4 wf[5][5];
#pragma unroll
  for (int i = 0; i < 5; i++) { const LAS char* wb = lp + L_WO + (kq * 5 + i) * 5120 + wlane;
#pragma unroll
    for (int t = 0; t < 5; t++) wf[i][t] = trd2(wb + t * 8, wb + 1280 + t * 8); }
  BAR_L();
  auto of_missing = [&]() { u32x4 mm = of[0][0];
#pragma unroll
    for (int j = 1; j < 10; j++) mm = __builtin_elementwise_max(mm, of[j / 5][j % 5]);
    return missing(mm); };
  if (__builtin_expect(of_missing(), 0)) {
#pragma unroll 1
    for (unsigned spin = 0; spin < SPIN_CAP; ++spin) {
      asm volatile("" ::: "memory");
      OFL(0); OFL(1);
      if (!of_missing()) break;
      __builtin_amdgcn_s_sleep(1);
    }
  }
  f32x4 ca[2][5], cb[2][5];
#pragma unroll
  for (int t = 0; t < 5; t++) { ca[0][t] = ca[1][t] = cb[0][t] = cb[1][t] = (f32x4){0.f, 0.f, 0.f, 0.f}; }
  { LAS char* d = lp + L_WO + w * 5120 + lane * 16;
#pragma unroll
    for (int i = 0; i < 5; i++) {
      *(LAS u32x4*)(d + i * 1024) = wbr[i]; *(LAS u32x4*)(d + 40960 + i * 1024) = wbr[5 + i]; SBAR();
#pragma unroll
      for (int t = 0; t < 5; t++) ca[0][t] = mfma16(wf[i][t], of[0][i], ca[0][t]);
      SBAR();
    }
    *(LAS u32x4*)(lp + L_WO + 16 * 5120 + w5 * 1024 + lane * 16) = wbr[10];
#pragma unroll
    for (int i = 0; i < 5; i++) {
#pragma unroll
      for (int t = 0; t < 5; t++) ca[1][t] = mfma16(wf[i][t], of[1][i], ca[1][t]);
      SBAR(); of[0][i] = LD16(WS_OB + ofr + i * 1024); SBAR();
    } }
  STAMP(9);
  BAR_L();
#pragma unroll
  for (int i = 0; i < 5; i++) { const int gr = kq * 5 + i; const LAS char* wb = lp + (gr < 17 ? L_WO + gr * 5120 : 145408 + (gr - 17) * 5120) + wlane;
#pragma unroll
    for (int t = 0; t < 5; t++) wf[i][t] = trd2(wb + t * 8, wb + 1280 + t * 8);
    SBAR(); of[1][i] = LD16(WS_OB + ofr + OT_B + i * 1024); SBAR(); }
  ARRIVE_NW(2);
#pragma unroll
  for (int t = 0; t < 5; t++) { ca[0][t] *= gt0; ca[1][t] *= gt1; }
  SBAR();
  STAMP(10);
#pragma unroll
  for (int u = 0; u < 2; u++)
#pragma unroll
    for (int i = 0; i < 5; i++) {
      if (__builtin_expect(missing(of[u][i]), 0)) {
#pragma unroll 1
        for (unsigned spin = 0; spin < SPIN_CAP; ++spin) {
          asm volatile("" ::: "memory");
#pragma unroll
          for (int j = u * 5 + i; j < 10; j++) of[j / 5][j % 5] = LD16(WS_OB + ofr + (j / 5) * OT_B + (j % 5) * 1024);
          if (!missing(of[u][i])) break;
          __builtin_amdgcn_s_sleep(1);
        }
      }
#pragma unroll
      for (int t = 0; t < 5; t++) cb[u][t] = mfma16(wf[i][t], of[u][i], cb[u][t]);
      SBAR();
    }
  {
    LAS f32x4* xb = (LAS f32x4*)(lp + (tp ? L_WO : 0)) + lane;
    f32x4 v[10];
#pragma unroll
    for (int it = 0; it < 10; it++) { const float gg = 1.0f - (it < 5 ? gt0 : gt1); v[it] = cb[it / 5][it % 5] * gg + ca[it / 5][it % 5]; }
    if (tp) AWAIT(2, 8);
#pragma unroll
    for (int it = 0; it < 10; it++) if (((it * 13) >> 5) != kq) xb[(it * 4 + kq) * 64] = v[it];
    BAR_L();
    u16* op = out + (size_t)R0 * DM + C0 + g * 20;
#pragma unroll
    for (int it = 0; it < 10; it++) if (((it * 13) >> 5) == kq) { const int o_ = (it * 13) >> 5;
      const f32x4 z = (v[it] + xb[(it * 4 + ((o_ + 1) & 3)) * 64]) + (xb[(it * 4 + ((o_ + 2) & 3)) * 64] + xb[(it * 4 + ((o_ + 3) & 3)) * 64]);
      *(u32x2*)(op + (it / 5) * 16 * DM + (it % 5) * 4) = (u32x2){pk(z[0], z[1]), pk(z[2], z[3])}; }
  }
  STAMP(11);
}
}

extern "C" void kernel_launch(void* const* d_in, const int* in_sizes, int n_in, void* d_out, int out_size, void* d_ws, size_t ws_size, hipStream_t stream) {
  using namespace gm;
  fused<<<256, 512, 0, stream>>>((const u16*)d_in[0], (const u16*)d_in[1], (const u16*)d_in[2], (const u16*)d_in[3], (const u16*)d_in[5], (const u16*)d_in[6],
                                 (const u16*)d_in[7], (const u16*)d_in[8], (const u16*)d_in[9], (u16*)d_out, (char*)d_ws);
}
```

```cpp
#include <hip/hip_runtime.h>
#include <hip/hip_bf16.h>
#include <stdint.h>

namespace gm {
typedef float f32x16 __attribute__((ext_vector_type(16)));
typedef float f32x4 __attribute__((ext_vector_type(4)));
typedef float f32x2 __attribute__((ext_vector_type(2)));
typedef short s16x4 __attribute__((ext_vector_type(4)));
typedef __bf16 b16x2 __attribute__((ext_vector_type(2)));
typedef __bf16 b16x8 __attribute__((ext_vector_type(8)));
typedef unsigned u32x4 __attribute__((ext_vector_type(4)));
typedef unsigned u32x2 __attribute__((ext_vector_type(2)));
typedef unsigned long long u64;
typedef unsigned short u16;
#define DI __device__ __forceinline__
#define LAS __attribute__((address_space(3)))
#define GAS __attribute__((address_space(1)))
typedef GAS u64 gu64;

constexpr int DM = 640;
constexpr unsigned WS_EPOCH = 0, WS_FKV = 2048, WS_FKVR = 4096, WS_FQ = 8192, WS_FPART = 16384, WS_FOA = 24576, WS_G = 57344;
constexpr unsigned WS_KVR = 8912896, KVR_B = 10240, WS_KV = 131072, WS_QX = 327680, QX_B = 10240, WS_PART = 1703936, PART_B = 15360;
constexpr unsigned WS_OA = 3670016, WS_OB = 6291456, WS_END = 8994816, OT_B = 20480;
constexpr int VB = 352, TILE_B = 1280 + 4 * VB, CH_B = TILE_B * 8, STEP_B = 2 * CH_B;
constexpr int STAGE_B = 36864, ST_W1 = 10240, ST_X = 20480;
constexpr int L_BUF = 43008, L_P0W = 0, L_P0X0 = 25600, L_P0X1 = 153600, L_RED = 158720, L_RED1 = 40960;
constexpr int L_HO = 86016, L_QL = 96256, L_VIM = 96256, L_PHK = 116736, L_WPHI = 158592, L_ZERO = 162816, L_CNT = 163712, L_TOTAL = 163840;
constexpr int L_WO = 43008;
constexpr float LOG2E = 1.4426950408889634f;
constexpr float QSCALE = 0.22360679774997896f * LOG2E;
constexpr unsigned SPIN_CAP = 20000, LSPIN_CAP = 400000;

DI unsigned pk(float a, float b) { f32x2 v = {a, b}; b16x2 r = __builtin_convertvector(v, b16x2); return __builtin_bit_cast(unsigned, r); }
DI f32x16 mfma32(u32x4 a, u32x4 b, f32x16 c) { return __builtin_amdgcn_mfma_f32_32x32x16_bf16(__builtin_bit_cast(b16x8, a), __builtin_bit_cast(b16x8, b), c, 0, 0, 0); }
DI f32x16 mfma32k8(u32x2 a, u32x2 b, f32x16 c) { return __builtin_amdgcn_mfma_f32_32x32x8bf16_1k(__builtin_bit_cast(s16x4, a), __builtin_bit_cast(s16x4, b), c, 0, 0, 0); }
DI f32x4 mfma16(u32x4 a, u32x4 b, f32x4 c) { return __builtin_amdgcn_mfma_f32_16x16x32_bf16(__builtin_bit_cast(b16x8, a), __builtin_bit_cast(b16x8, b), c, 0, 0, 0); }
DI u32x2 trd(const LAS char* p) { return __builtin_bit_cast(u32x2, __builtin_amdgcn_ds_read_tr16_b64_v4i16((LAS s16x4*)p)); }
DI u32x4 trd2(const LAS char* p0, const LAS char* p1) { u32x2 a = trd(p0), b = trd(p1); return (u32x4){a.x, a.y, b.x, b.y}; }
DI u32x4 ldsr16(const LAS char* p) { return *(const LAS u32x4*)p; }
DI u32x2 ldsr8(const LAS char* p) { return *(const LAS u32x2*)p; }
DI float ex2(float x) { return __builtin_amdgcn_exp2f(x); }
DI void dma16s(const void* base, unsigned voff, unsigned lds_base) {
  asm volatile("s_mov_b32 m0, %2\n\ts_nop 0\n\tglobal_load_lds_dwordx4 %0, %1" :: "v"(voff), "s"(base), "s"(lds_base) : "memory"); }
DI void dma16s_sc1(const void* base, unsigned voff, unsigned lds_base) {
  asm volatile("s_mov_b32 m0, %2\n\ts_nop 0\n\tglobal_load_lds_dwordx4 %0, %1 sc1" :: "v"(voff), "s"(base), "s"(lds_base) : "memory"); }
DI void dmag_open(unsigned lds_base) { asm volatile("s_mov_b32 m0, %0\n\ts_nop 0" :: "s"(lds_base) : "memory"); }
template <int J> DI void dmag(const void* base, unsigned voff) { asm volatile("global_load_lds_dwordx4 %0, %1 offset:%2" :: "v"(voff), "s"(base), "i"((J & 3) * 1024) : "memory"); }
template <int J> DI void dmag_sc1(const void* base, unsigned voff) { asm volatile("global_load_lds_dwordx4 %0, %1 offset:%2 sc1" :: "v"(voff), "s"(base), "i"((J & 3) * 1024) : "memory"); }
DI void dma4s(const void* base, unsigned voff, unsigned lds_base) {
  asm volatile("s_mov_b32 m0, %2\n\ts_nop 0\n\tglobal_load_lds_dword %0, %1" :: "v"(voff), "s"(base), "s"(lds_base) : "memory"); }
DI u32x4 ld16_async(const void* p) { u32x4 r; asm volatile("global_load_dwordx4 %0, %1, off" : "=v"(r) : "v"(p) : "memory"); return r; }
DI u32x2 ld8_sc1_async(const void* p) { u32x2 r; asm volatile("global_load_dwordx2 %0, %1, off sc1" : "=v"(r) : "v"(p) : "memory"); return r; }
#define BAR_L() asm volatile("s_waitcnt lgkmcnt(0)\n\ts_barrier" ::: "memory")
#define WAIT_BAR(N) asm volatile("s_waitcnt vmcnt(" #N ") lgkmcnt(0)\n\ts_barrier" ::: "memory")
#define WAIT_VM(N) asm volatile("s_waitcnt vmcnt(" #N ")" ::: "memory")
#define SBAR() __builtin_amdgcn_sched_barrier(0)
#define RLX_AGENT __ATOMIC_RELAXED, __HIP_MEMORY_SCOPE_AGENT
DI float half_swap_max(float m) { auto rr = __builtin_amdgcn_permlane32_swap(__float_as_uint(m), __float_as_uint(m), false, false); return fmaxf(__uint_as_float(rr[0]), __uint_as_float(rr[1])); }
DI float from_upper(float v) { auto rr = __builtin_amdgcn_permlane32_swap(__float_as_uint(v), __float_as_uint(v), false, false); return __uint_as_float(rr[1]); }

template <int NF>
DI bool flags_set(const char* ws, unsigned off, u64 tag, int lane) {
  gu64* f = (gu64*)(ws + off);
  bool ok = __hip_atomic_load(f + (NF < 64 ? (lane & (NF - 1)) : lane), RLX_AGENT) == tag;
  if (NF >= 128) ok &= __hip_atomic_load(f + 64 + lane, RLX_AGENT) == tag;
  if (NF >= 256) { ok &= __hip_atomic_load(f + 128 + lane, RLX_AGENT) == tag; ok &= __hip_atomic_load(f + 192 + lane, RLX_AGENT) == tag; }
  return ok;
}
template <int NF>
DI void wait_flags(const char* ws, unsigned off, u64 tag, int lane) {
#pragma unroll 1
  for (unsigned spin = 0; spin < SPIN_CAP; ++spin) {
    if (__all(flags_set<NF>(ws, off, tag, lane))) break;
    __builtin_amdgcn_s_sleep(2);
  }
  asm volatile("" ::: "memory");
}
DI void set_flag(char* ws, unsigned off, u64 tag, int lane) { if (lane == 0) __hip_atomic_store((gu64*)(ws + off), tag, RLX_AGENT); }
DI unsigned nz(unsigned v) { return v < 0xFFFFFFFEu ? v : 0xFFFFFFFEu; }
DI unsigned umax3(unsigned a, unsigned b, unsigned c) { unsigned m = a > b ? a : b; return m > c ? m : c; }
DI bool missing(u32x4 v) { return __any(umax3(umax3(v.x, v.y, v.z), v.w, 0u) == 0xFFFFFFFFu); }
#define ONES ((u32x4){0xFFFFFFFFu, 0xFFFFFFFFu, 0xFFFFFFFFu, 0xFFFFFFFFu})
#define ST16(v, off) __builtin_amdgcn_raw_buffer_store_b128((v), wsr, (off), 0, 16)
#define ST8(v, off) __builtin_amdgcn_raw_buffer_store_b64((v), wsr, (off), 0, 16)
#define LD16(off) __builtin_amdgcn_raw_buffer_load_b128(wsr, (off), 0, 16)
#define LD8(off) __builtin_amdgcn_raw_buffer_load_b64(wsr, (off), 0, 16)

#define STAMP(i) do {} while (0)

__global__ __launch_bounds__(512, 2) void fused(const u16* __restrict__ x, const u16* __restrict__ wqkv, const u16* __restrict__ wphi, const u16* __restrict__ woA,
                                                const u16* __restrict__ wq, const u16* __restrict__ wk, const u16* __restrict__ wv, const u16* __restrict__ woB,
                                                const u16* __restrict__ gatew, u16* __restrict__ out, char* ws) {
  __shared__ __attribute__((aligned(16))) char lds[L_TOTAL];
  const int tid = threadIdx.x, lane = tid & 63;
  const int w = __builtin_amdgcn_readfirstlane(tid >> 6) & 7;
  const int l15 = lane & 15, g = lane >> 4, q4 = l15 >> 2, p4 = lane & 3, r = lane & 31, h = lane >> 5;
  const int bx = blockIdx.x & 7, bi = blockIdx.x >> 3;
  const int rt = (bx & 1) * 8 + (bi & 7), hp = (bx >> 1) * 4 + ((bi >> 3) & 3), hq = hp >> 1, role = hp & 1;
  const int w3 = w & 3, w2 = w >> 2;
  LAS char* lp = (LAS char*)lds;
  const unsigned lds0 = (unsigned)(uintptr_t)lds;
  const __amdgpu_buffer_rsrc_t wsr = __builtin_amdgcn_make_buffer_rsrc(ws, 0, WS_END, 0x00020000);
  LAS unsigned* cnt = (LAS unsigned*)(lp + L_CNT);
#define ARRIVE_NW(c) do { asm volatile("" ::: "memory"); if (lane == 0) __hip_atomic_fetch_add(cnt + (c), 1u, __ATOMIC_RELAXED, __HIP_MEMORY_SCOPE_WORKGROUP); asm volatile("" ::: "memory"); } while (0)
#define PEEK(c) ((unsigned)__builtin_amdgcn_readfirstlane(__hip_atomic_load(cnt + (c), __ATOMIC_RELAXED, __HIP_MEMORY_SCOPE_WORKGROUP)))
#define AWAIT(c, n) do { _Pragma("unroll 1") for (unsigned sp_ = 0; sp_ < LSPIN_CAP; ++sp_) { if (PEEK(c) >= (unsigned)(n)) break; __builtin_amdgcn_s_sleep(1); } asm volatile("" ::: "memory"); } while (0)
#define POST(c, v) do { asm volatile("" ::: "memory"); __hip_atomic_store(cnt + (c) + w3, (unsigned)(v), __ATOMIC_RELAXED, __HIP_MEMORY_SCOPE_WORKGROUP); asm volatile("" ::: "memory"); } while (0)
#define PEEK4(c) __hip_atomic_load(cnt + (c) + p4, __ATOMIC_RELAXED, __HIP_MEMORY_SCOPE_WORKGROUP)
#define AWAIT4(c, n) do { _Pragma("unroll 1") for (unsigned sp_ = 0; sp_ < LSPIN_CAP; ++sp_) { if (__all(PEEK4(c) >= (unsigned)(n))) break; __builtin_amdgcn_s_sleep(1); } asm volatile("" ::: "memory"); } while (0)
  STAMP(0);
  if (w == 0) *(LAS u32x4*)(lp + L_ZERO + lane * 16) = (u32x4){0u, 0u, 0u, 0u};

  const int T0 = rt * 128 + hq * 16;
  const int C0 = hq * 80;
  const int wlane = ((g >> 1) * 16 + (g & 1) * 4 + q4) * 160 + p4 * 40;
  u64 tag;
  unsigned o0[10];
#define KVP(s, slot, p) dma16s_sc1(ws + WS_KV + (s) * STEP_B + (p) * 1024, lane * 16, lds0 + (slot) * STEP_B + (p) * 1024)
#define KVL(s, slot) do { _Pragma("unroll") for (int j = 0; j < 11; j++) if (j < 10 || w3 < 2) KVP(s, slot, w3 + 4 * j); } while (0)
#define KW(s, slot, j) do { if ((j) == 4) KVP(s, slot, w * 5 + 4); else { if ((j) == 0) dmag_open(lds0 + (slot) * STEP_B + w * 5120); dmag_sc1<(j)>(ws + WS_KV + (s) * STEP_B + w * 5120, lane * 16); } } while (0)
#define KX(s, slot) do { if (w < 2) KVP(s, slot, 40 + w); } while (0)
  if (w2 == 0) {
    u32x4 gf0, gf1, gf2, gf3, gf4, wpc; u32x2 tch;
    u32x2 ep = ld8_sc1_async(ws + WS_EPOCH);
    const int lx = w3 < 3 ? L_P0X0 + w3 * 5120 : L_P0X1;
    { const u16* wsrc = (role ? wv : wk) + w3 * 3200;
#pragma unroll
      for (int j = 0; j < 5; j++) { const int n = j * 64 + lane, row = (n * 3277) >> 16, cc = n - row * 20; dma16s(x + T0 * DM + w3 * 160, row * 1280 + cc * 16, lds0 + lx + j * 1024); }
#pragma unroll
      for (int j = 0; j < 6; j++) dma16s(wsrc + j * 512, lane * 16, lds0 + L_P0W + w3 * 6400 + j * 1024);
      if (lane < 16) dma16s(wsrc + 6 * 512, lane * 16, lds0 + L_P0W + w3 * 6400 + 6 * 1024);
      const u16* gp = gatew + w3 * 160 + g * 8;
      gf0 = ld16_async(gp); gf1 = ld16_async(gp + 32); gf2 = ld16_async(gp + 64); gf3 = ld16_async(gp + 96); gf4 = ld16_async(gp + 128);
      wpc = ld16_async(wphi + hq * 1600 + (tid < 200 ? tid : 199) * 8);
      const char* tp = ws + (lane & 7) * 1114112;
      if (lane >= 8) tp = ws + (lane < 16 ? WS_FKV + blockIdx.x * 128 : WS_KVR + blockIdx.x * 320 + (lane & 3) * 96);
      if (w3 == 1) tp = (const char*)(woA + C0 + (lane & 7) * 51200);
      if (w3 == 2) tp = (const char*)(woB + C0 + (lane & 7) * 51200);
      if (w3 == 3) tp = (const char*)(out + (rt * 128 + role * 64) * DM + C0);
      tch = ld8_sc1_async(tp);
    }
    asm volatile("s_waitcnt vmcnt(1)" : "+v"(gf0), "+v"(gf1), "+v"(gf2), "+v"(gf3), "+v"(gf4), "+v"(wpc), "+v"(ep) :: "memory");
    tag = (((u64)(unsigned)__builtin_amdgcn_readfirstlane(ep.y) << 32) | (unsigned)__builtin_amdgcn_readfirstlane(ep.x)) + 1ull;
    STAMP(1);
    { const LAS char* wb = lp + L_P0W + (w3 * 160 + g * 8 + q4) * 40;
      const LAS char* xb = lp + lx + l15 * 320 + g * 16;
      const bool gl = l15 == 4;
      f32x4 c0 = {}, c1 = {};
#define P0STEP(s, GF) { const u32x4 b = ldsr16(xb + (s) * 64); c0 = mfma16(trd2(wb + p4 * 8 + (s) * 1280, wb + p4 * 8 + (s) * 1280 + 160), b, c0); \
        u32x4 a = trd2(wb + 32 + (s) * 1280, wb + 32 + (s) * 1280 + 160); if (gl) a = GF; c1 = mfma16(a, b, c1); }
      P0STEP(0, gf0) P0STEP(1, gf1) P0STEP(2, gf2) P0STEP(3, gf3) P0STEP(4, gf4)
#undef P0STEP
      *(LAS f32x4*)(lp + L_RED + tid * 16) = c0;
      if (lane < 32) *(LAS f32x4*)(lp + L_RED1 + (w3 * 32 + lane) * 16) = c1; }
    f32x16 acc[5];
#pragma unroll
    for (int b = 0; b < 5; b++) acc[b] = (f32x16){};
    const int wm = g & 1, whd = p4;
    const int wo0 = wm * ST_W1 + (8 * h + 4 * wm + q4) * 160 + whd * 40, wo1 = wm * ST_W1 + (8 * h + 4 * (wm ^ 1) + q4) * 160 + whd * 40;
    const int xo = ST_X + (w3 * 32 + r) * 128 + ((h ^ ((r >> 1) & 7)) * 16);
    u32x4 fr[4][5], xr[4];
#define FRD(s, j) fr[s][j] = trd2(sb + wo0 + (s) * 2560 + (j) * 8, sb + wo1 + (s) * 2560 + (j) * 8)
#define FMM(s, j) acc[j] = mfma32(fr[s][j], xr[s], acc[j])
#define KSTEP(s) { xr[s] = ldsr16(sb + (xo ^ ((s) * 32))); \
      _Pragma("unroll") for (int j = 0; j < 5; j++) { if (i > 0 || (s) >= 2) FMM((s) ^ 2, j); FRD(s, j); SBAR(); } }
    unsigned lnd = 1;
    BAR_L();
    STAMP(2);
#pragma unroll
    for (int i = 0; i < 10; i++) {
      if (__builtin_expect(!__all(lnd >= (unsigned)(i + 1)), 0)) AWAIT4(8, i + 1);
      asm volatile("" ::: "memory");
      const LAS char* sb = lp + L_BUF + ((i + 2) % 3) * STAGE_B;
      KSTEP(0)
      if (i == 1 && role) {
        const unsigned qb = WS_QX + (rt * 8 + hq) * QX_B + w3 * 1280 + lane * 16;
        ST16(ONES, qb); ST16(ONES, qb + 5120); ST16(ONES, lane < 32 ? qb + 1024 + (lane >> 4) * (5120 - 256) : 0x7FFF0000u); }
      if (i == 4) { asm volatile("s_waitcnt vmcnt(0)" : "+v"(tch) :: "memory"); if (role) set_flag(ws, WS_FQ + ((rt * 8 + hq) * 4 + w3) * 8, tag, lane);
                    if (tid < 200) *(LAS u32x4*)(lp + L_WPHI + tid * 16) = wpc; }
      KSTEP(1) KSTEP(2)
      lnd = PEEK4(i < 9 ? 8 : 12);
      KSTEP(3)
      POST(12, i + 1);
    }
#pragma unroll
    for (int j = 0; j < 10; j++) FMM(2 + j / 5, j % 5);
#undef FRD
#undef FMM
#undef KSTEP
    STAMP(3);
#define PK2(t, i, sc) pk(acc[t][i] * (sc), acc[t][(i) + 1] * (sc))
#define ROW(i, sc) const u32x4 ra = {PK2(0, i, sc), PK2(0, (i) + 2, sc), PK2(1, i, sc), PK2(1, (i) + 2, sc)}, rb = {PK2(2, i, sc), PK2(2, (i) + 2, sc), PK2(3, i, sc), PK2(3, (i) + 2, sc)}; const u32x2 rc = {PK2(4, i, sc), PK2(4, (i) + 2, sc)}
    { ROW(4, 1.0f);
      if (__builtin_expect(!__all(lnd >= 9u), 0)) AWAIT4(12, 9);
      asm volatile("" ::: "memory");
      *(LAS u32x4*)(lp + L_HO + tid * 16) = ra; *(LAS u32x4*)(lp + L_HO + 4096 + tid * 16) = rb; *(LAS u32x2*)(lp + L_HO + 8192 + tid * 8) = rc; }
    BAR_L();
    const int qfo = (h * 4 + w3) * 1280;
    if (role) {
      { ROW(8, QSCALE); const unsigned qb = WS_QX + (rt * 8 + hq) * QX_B + qfo;
        ST16(((u32x4){nz(ra.x), nz(ra.y), nz(ra.z), nz(ra.w)}), qb + r * 16); ST16(((u32x4){nz(rb.x), nz(rb.y), nz(rb.z), nz(rb.w)}), qb + 512 + r * 16); ST8(((u32x2){nz(rc.x), nz(rc.y)}), qb + 1024 + r * 8); }
      { ROW(12, QSCALE); LAS char* d = lp + L_QL + qfo;
        *(LAS u32x4*)(d + r * 16) = ra; *(LAS u32x4*)(d + 512 + r * 16) = rb; *(LAS u32x2*)(d + 1024 + r * 8) = rc; }
    } else { LAS char* d = lp + L_VIM + (h * 128 + w3 * 32 + r) * 40;
#pragma unroll
      for (int t = 0; t < 5; t++) { *(LAS u32x2*)(d + t * 8) = (u32x2){PK2(t, 8, 1.0f), PK2(t, 10, 1.0f)}; *(LAS u32x2*)(d + 10240 + t * 8) = (u32x2){PK2(t, 12, 1.0f), PK2(t, 14, 1.0f)}; } }
#pragma unroll
    for (int t = 0; t < 5; t++) { o0[2 * t] = PK2(t, 0, 1.0f); o0[2 * t + 1] = PK2(t, 2, 1.0f); }
#undef ROW
#undef PK2
  } else {
    u32x2 ep = ld8_sc1_async(ws + WS_EPOCH);
    const bool second = w3 >> 1;
    const unsigned rs = (second && role) ? 1280u : 3840u;
    const char* wsrc = (second ? (role ? (const char*)(wq + C0) : (const char*)(wqkv + 1280 + C0)) : (const char*)(wqkv + (role ? 0 : 640) + C0)) + (w3 & 1) * 32 * rs;
    const char* xsrc = (const char*)(x + (rt * 128 + w3 * 32) * DM);
    int rot = (bi & 7) + 3 * ((bi >> 3) & 3); rot = rot >= 10 ? rot - 10 : rot;
    unsigned so[5], xoff[2];
#pragma unroll
    for (int j = 0; j < 5; j++) { const int c = j * 64 + lane, rho = (c * 6554) >> 16, cc = c - rho * 10; so[j] = (rho ^ (second ? 4 : 0)) * rs + cc * 16; }
#pragma unroll
    for (int j = 0; j < 2; j++) { const int row = j * 8 + (lane >> 3); xoff[j] = row * 1280 + (((lane & 7) ^ ((row >> 1) & 7)) * 16); }
    int bsel = 2, kc = rot;
    auto stage = [&](int n) {
      const unsigned lb = lds0 + L_BUF + bsel * STAGE_B;
      const char* bw = wsrc + kc * 64 * rs; const char* bxp = xsrc + kc * 128;
#pragma unroll
      for (int j = 0; j < 5; j++) { dma16s(bw, so[j], lb + (w3 * 5 + j) * 1024); if (j < 4) dma16s(bxp + (j >> 1) * 16 * 1280, xoff[j & 1], lb + ST_X + (w3 * 4 + j) * 1024);
        if (j == 1 && n > 0) { WAIT_VM(4); POST(8, n); } }
      bsel = bsel == 2 ? 0 : bsel + 1; kc = kc == 9 ? 0 : kc + 1;
    };
    stage(0); stage(0);
    asm volatile("s_waitcnt vmcnt(9)\n\ts_barrier" : "+v"(ep) :: "memory");
    tag = (((u64)(unsigned)__builtin_amdgcn_readfirstlane(ep.y) << 32) | (unsigned)__builtin_amdgcn_readfirstlane(ep.x)) + 1ull;
    STAMP(1); STAMP(2);
#pragma unroll 1
    for (int i = 0; i < 8; i++) {
      if (i >= 1) AWAIT4(12, i);
      stage(i + 2);
      if (i == 0 && w3 == 0) {
        const unsigned tb = WS_KV + (T0 >> 5) * TILE_B; const int s16 = hq & 1;
        if (!role) {
          const LAS f32x4* rp = (const LAS f32x4*)(lp + (h ? L_RED1 : L_RED)) + (h ? (g & 1) * 16 : g * 32) + l15;
          const int st = h ? 32 : 64, o2 = h ? 0 : 16;
          const f32x4 a = (rp[0] + rp[st]) + (rp[2 * st] + rp[3 * st]), b = (rp[o2] + rp[st + o2]) + (rp[2 * st + o2] + rp[3 * st + o2]);
          const u32x4 v = {pk(a[0], a[1]), pk(a[2], a[3]), pk(b[0], b[1]), pk(b[2], b[3])};
          if (lane < 32) ST16(v, tb + (g * 32 + s16 * 16 + l15) * 16);
          else if (lane < 48) ST8(((u32x2){v.x, v.y}), tb + 1024 + (s16 * 16 + l15) * 8);
          else { const float gv = 1.0f / (1.0f + __expf(-a[0])); __builtin_amdgcn_raw_buffer_store_b32(__float_as_uint(gv), wsr, WS_G + (T0 + l15) * 4, 0, 16); }
        } else if (lane < 44) {
          const int hh = lane >= 22, d = lane - hh * 22, dd = d < 20 ? d : 0, st = dd >= 16 ? 128 : 256;
          const LAS float* rp = (const LAS float*)(lp + (dd >= 16 ? L_RED1 : L_RED)) + (((dd & 15) >> 2) * 16 + 4 * hh) * 4 + (dd & 3);
          float e[8];
#pragma unroll
          for (int j = 0; j < 8; j++) { const int tk = (8 * (j >> 2) + (j & 3)) * 4; e[j] = (rp[tk] + rp[tk + st]) + (rp[tk + 2 * st] + rp[tk + 3 * st]); }
          u32x4 v = {pk(e[0], e[1]), pk(e[2], e[3]), pk(e[4], e[5]), pk(e[6], e[7])};
          if (d == 20) v = (u32x4){0x3F803F80u, 0x3F803F80u, 0x3F803F80u, 0x3F803F80u};
          if (d == 21) v = (u32x4){0u, 0u, 0u, 0u};
          ST16(v, tb + 1280 + (s16 * 2 + hh) * VB + d * 16);
        }
      }
      if (i == 1 && w3 == 0) set_flag(ws, WS_FKV + blockIdx.x * 8, tag, lane);
    }
    u64 fa, fb, fc, fd, fe;
    { gu64* f = (gu64*)(ws + WS_FKV) + lane;
      fa = __hip_atomic_load(f, RLX_AGENT); fb = __hip_atomic_load(f + 64, RLX_AGENT); fc = __hip_atomic_load(f + 128, RLX_AGENT); fd = __hip_atomic_load(f + 192, RLX_AGENT);
      fe = __hip_atomic_load((gu64*)(ws + WS_FQ) + (rt * 8 + hq) * 4 + p4, RLX_AGENT); }
    WAIT_VM(5); POST(8, 10);
    asm volatile("" : "+v"(fa), "+v"(fb), "+v"(fc), "+v"(fd), "+v"(fe));
    const bool kvok = __all((fa == tag) & (fb == tag) & (fc == tag) & (fd == tag) & (fe == tag));
    asm volatile("" ::: "memory");
    if (!kvok) { wait_flags<256>(ws, WS_FKV, tag, lane); wait_flags<4>(ws, WS_FQ + (rt * 8 + hq) * 32, tag, lane); }
    KVL(0, 0);
    if (blockIdx.x == 0 && w3 == 0) set_flag(ws, WS_EPOCH, tag, lane);
    STAMP(3);
    BAR_L();
    const int ht = tid - 256;
    const u32x4 a = ldsr16(lp + L_HO + ht * 16), b = ldsr16(lp + L_HO + 4096 + ht * 16); const u32x2 c = ldsr8(lp + L_HO + 8192 + ht * 8);
    o0[0] = a.x; o0[1] = a.y; o0[2] = a.z; o0[3] = a.w; o0[4] = b.x; o0[5] = b.y; o0[6] = b.z; o0[7] = b.w; o0[8] = c.x; o0[9] = c.y;
  }
  STAMP(4);
  u32x4 ph[5];
  { const LAS char* pb = ((p4 & 1) == h) ? lp + L_WPHI + (2 * w2 + h) * 800 + q4 * 40 + (2 * (g & 1) + (p4 >> 1)) * 8 : lp + L_ZERO;
    const u32x4 b0 = {o0[0], o0[1], o0[2], o0[3]}, b1 = {o0[4], o0[5], o0[6], o0[7]}, b2 = {o0[8], o0[9], 0u, 0u};
    const u32x2 c0 = trd(pb + 640), c1 = trd(pb + 672);
    f32x16 P0 = {}, P1 = {};
    P0 = mfma32(trd2(pb, pb + 160), b0, P0); P1 = mfma32(trd2(pb + 32, pb + 192), b0, P1);
    P0 = mfma32(trd2(pb + 320, pb + 480), b1, P0); P1 = mfma32(trd2(pb + 352, pb + 512), b1, P1);
    P0 = mfma32((u32x4){c0.x, c0.y, 0u, 0u}, b2, P0); P1 = mfma32((u32x4){c1.x, c1.y, 0u, 0u}, b2, P1);
    KX(1, 1); KW(1, 1, 0); SBAR();
    f32x2 p[10];
#pragma unroll
    for (int f = 0; f < 10; f++) p[f] = f < 8 ? (f32x2){P0[(2 * f) & 15], P0[(2 * f + 1) & 15]} : (f32x2){P1[(2 * f) & 3], P1[(2 * f + 1) & 3]};
    float mx = fmaxf(p[0][0], p[0][1]), mn = fminf(p[0][0], p[0][1]);
#pragma unroll
    for (int f = 1; f < 10; f++) { mx = fmaxf(fmaxf(mx, p[f][0]), p[f][1]); mn = fminf(fminf(mn, p[f][0]), p[f][1]); }
    const f32x2 ma = {-mx * LOG2E, -mx * LOG2E}, mb = {mn * LOG2E, mn * LOG2E}, ca_ = {LOG2E, LOG2E}, cb_ = {-LOG2E, -LOG2E};
    f32x2 ea[10], eb[10], sa = {0.f, 0.f}, sb = {0.f, 0.f};
#pragma unroll
    for (int f = 0; f < 10; f++) { if (f % 2 == 0 && f < 8) { SBAR(); if (f == 0) KW(1, 1, 1); if (f == 2) KW(1, 1, 2); if (f == 4) KW(1, 1, 3); if (f == 6) KW(1, 1, 4); SBAR(); }
      const f32x2 xa = __builtin_elementwise_fma(p[f], ca_, ma), xb = __builtin_elementwise_fma(p[f], cb_, mb);
      ea[f] = (f32x2){ex2(xa[0]), ex2(xa[1])}; eb[f] = (f32x2){ex2(xb[0]), ex2(xb[1])}; sa += ea[f]; sb += eb[f]; }
    const float ia = __builtin_amdgcn_rcpf(sa[0] + sa[1]), ib = __builtin_amdgcn_rcpf(sb[0] + sb[1]);
    unsigned o[20];
#pragma unroll
    for (int f = 0; f < 10; f++) { const f32x2 ya = ea[f] * ia, yb = eb[f] * ib; o[f] = pk(ya[0], ya[1]); o[10 + f] = pk(yb[0], yb[1]); }
#pragma unroll
    for (int c = 0; c < 5; c++) ph[c] = (u32x4){o[4 * c], o[4 * c + 1], o[4 * c + 2], o[4 * c + 3]};
    asm volatile("" : "+v"(ph[0]), "+v"(ph[1]), "+v"(ph[2]), "+v"(ph[3]), "+v"(ph[4]));
  }
  u32x4 q1A, q1B; u32x2 q2A, q2B;
  if (role) {
    WAIT_BAR(0);
    STAMP(5);
    const LAS char* qa = lp + L_QL + w3 * 1280;
    q1A = ldsr16(qa + lane * 16); q1B = ldsr16(qa + 5120 + lane * 16); q2A = ldsr8(qa + 1024 + r * 8); q2B = ldsr8(qa + 6144 + r * 8);
    ARRIVE_NW(0);
  } else {
    const unsigned qb = WS_QX + (rt * 8 + hq) * QX_B + w3 * 1280;
    q1A = LD16(qb + lane * 16); q1B = LD16(qb + 5120 + lane * 16); q2A = LD8(qb + 1024 + r * 8); q2B = LD8(qb + 6144 + r * 8);
    { LAS char* d = lp + L_PHK + ((2 * w2 + h) * 128 + w3 * 32 + r) * 80;
#pragma unroll
      for (int c = 0; c < 5; c++) *(LAS u32x4*)(d + c * 16) = ph[c]; }
    WAIT_BAR(4);
    STAMP(5);
#pragma unroll 1
    for (unsigned spin = 0; spin < SPIN_CAP; ++spin) {
      if (!(missing(q1A) | missing(q1B) | missing((u32x4){q2A.x, q2A.y, q2B.x, q2B.y}))) break;
      __builtin_amdgcn_s_sleep(1);
      asm volatile("" ::: "memory");
      q1A = LD16(qb + lane * 16); q1B = LD16(qb + 5120 + lane * 16); q2A = LD8(qb + 1024 + r * 8); q2B = LD8(qb + 6144 + r * 8);
    }
  }
  STAMP(6);
  const int vr = r <= 21 ? r : 21;
  const int ok1 = lane * 16, ok2 = h ? (1280 + 20 * 16) : (1024 + r * 8), ov = 1280 + h * VB + vr * 16;
  const f32x16 Z = {};
  if (h) { q2A = (u32x2){0u, 0u}; q2B = (u32x2){0u, 0u}; }
  { const LAS char* t0 = lp;
    const u32x4 k1 = ldsr16(t0 + ok1); const u32x2 k2 = ldsr8(t0 + ok2);
    f32x16 sa = mfma32(k1, q1A, Z); sa = mfma32k8(k2, q2A, sa);
    f32x16 sb = mfma32(k1, q1B, Z); sb = mfma32k8(k2, q2B, sb);
    float ma = sa[0], mb = sb[0];
#pragma unroll
    for (int i = 1; i < 16; i++) { ma = fmaxf(ma, sa[i]); mb = fmaxf(mb, sb[i]); }
    ma = half_swap_max(ma); mb = half_swap_max(mb);
    const float mah = __uint_as_float(__float_as_uint(ma) & 0xFFFF0000u), mbh = __uint_as_float(__float_as_uint(mb) & 0xFFFF0000u);
    if (h) { q2A.x = pk(-mah, mah - ma); q2B.x = pk(-mbh, mbh - mb); } }
  f32x16 OA = {}, OB = {}, SA, SB;
  u32x4 k1, v1, v2, v2n, pA1, pA2, pB1, pB2; u32x2 k2;
  float e0, e1, e2, e3;
  const LAS char* c0p = lp + w2 * CH_B + ok1; const LAS char* c1p = lp + w2 * CH_B + ok2; const LAS char* c2p = lp + w2 * CH_B + ov;
  k1 = ldsr16(c0p); k2 = ldsr8(c1p);
  SA = mfma32(k1, q1A, Z); SA = mfma32k8(k2, q2A, SA);
  v1 = ldsr16(c2p); v2 = v1;
  e0 = e1 = e2 = e3 = 0.f; pB2 = (u32x4){0u, 0u, 0u, 0u};
#define G(MF, LD, C0_, C1_, X, S_, i) MF; LD; SBAR(); C0_; C1_; SBAR(); X##0 = ex2(S_[i]); X##1 = ex2(S_[i + 1]); X##2 = ex2(S_[i + 2]); X##3 = ex2(S_[i + 3]); asm volatile("" : "+v"(X##0), "+v"(X##1), "+v"(X##2), "+v"(X##3)); SBAR();
#define TILE(TB, TN, V2, H0, H1, H2) { float a0, a1, a2, a3; \
    G(SB = mfma32(k1, q1B, Z),    k1 = ldsr16(c0p + (TN)), pB2[2] = pk(e0, e1), pB2[3] = pk(e2, e3), a, SA, 0) \
    G(OB = mfma32(v2, pB2, OB),   V2,                      pA1[0] = pk(a0, a1), pA1[1] = pk(a2, a3), e, SA, 4) \
    G(SB = mfma32k8(k2, q2B, SB), k2 = ldsr8(c1p + (TN)),  pA1[2] = pk(e0, e1), pA1[3] = pk(e2, e3), a, SA, 8) \
    G(OA = mfma32(v1, pA1, OA),   H0,                      pA2[0] = pk(a0, a1), pA2[1] = pk(a2, a3), e, SA, 12) \
    G(SA = mfma32(k1, q1A, Z),    H1,                      pA2[2] = pk(e0, e1), pA2[3] = pk(e2, e3), a, SB, 0) \
    G(OA = mfma32(v2, pA2, OA),   H2,                      pB1[0] = pk(a0, a1), pB1[1] = pk(a2, a3), e, SB, 4) \
    G(SA = mfma32k8(k2, q2A, SA), (void)0,                 pB1[2] = pk(e0, e1), pB1[3] = pk(e2, e3), a, SB, 8) \
    G(OB = mfma32(v1, pB1, OB),   v1 = ldsr16(c2p + (TN)), pB2[0] = pk(a0, a1), pB2[1] = pk(a2, a3), e, SB, 12) }
#define NOP (void)0
#define TL(sl, t, H0, H1, H2) TILE((sl) * STEP_B + (t) * TILE_B, (sl) * STEP_B + ((t) + 1) * TILE_B, v2 = ldsr16(c2p + (sl) * STEP_B + (t) * TILE_B + 2 * VB), H0, H1, H2)
#define TL6(sl, H0, H1) TL(sl, 6, H0, H1, v2n = ldsr16(c2p + (sl) * STEP_B + 7 * TILE_B + 2 * VB))
#define TL7(sl, sn, H0, H1, H2) TILE((sl) * STEP_B + 7 * TILE_B, (sn) * STEP_B, v2 = v2n, H0, H1, H2)
  unsigned woff[5];
#pragma unroll
  for (int j = 0; j < 5; j++) { const int c = j * 64 + lane, rho = (c * 6554) >> 16, cc = c - rho * 10; const int k = (rho & ~12) | ((rho & 4) << 1) | ((rho & 8) >> 1); woff[j] = k * 1280 + cc * 16 + (4 - j) * 1024; }
#define WOG(wo, gr, j, dst) do { if ((j) == 4) WOP(wo, gr, 4, dst); else { if ((j) == 0) dmag_open(lds0 + (dst)); dmag<(j)>((wo) + C0 + (gr) * 32 * 640 - 2048, woff[j]); } } while (0)
#define WOP(wo, gr, j, dst) dma16s((wo) + C0 + (gr) * 32 * 640 - (4 - (j)) * 512, woff[j], lds0 + (dst) + (j) * 1024)
#define WA(gr, j) WOG(woA, gr, j, L_WO + (gr) * 5120)
  const u16* wxs = w < 4 ? woA + (16 + w3) * 32 * 640 : woB + (w < 7 ? 17 + w3 : 19) * 32 * 640;
  const int wxd = w < 4 ? L_WO + (16 + w3) * 5120 : 145408 + (w < 7 ? w3 : 2) * 5120;
#define WX(j) WOG(wxs, 0, j, wxd)
#define PRIO(n) __builtin_amdgcn_s_setprio(n)
  const unsigned fo = (rt * 8 + hq) * OT_B + lane * 16;
#define FILL(j) do { const int n_ = w * 3 + ((j) >> 1); ST16(ONES, (((j) & 1) ? WS_OB : WS_OA) + fo + (n_ < 20 ? n_ : 19) * 1024); } while (0)
#define CLR_VM() __builtin_amdgcn_s_waitcnt(0x0F70)
  const bool red = !role && w == 4;
  f32x4 part[16]; u64 fp0, fp1;
  auto red_load = [&]() {
    asm volatile("" : "+v"(fp0), "+v"(fp1));
    if (!__all((fp0 == tag) & (fp1 == tag))) wait_flags<128>(ws, WS_FPART + hq * 1024, tag, lane);
    asm volatile("" ::: "memory");
#pragma unroll
    for (int j = 0; j < 16; j++) part[j] = __builtin_bit_cast(f32x4, LD16(WS_PART + (hq * 16 + j) * PART_B + (rt * 60 + (lane < 60 ? lane : 59)) * 16));
  };
  auto red_store = [&]() {
    f32x4 a = ((part[0] + part[1]) + (part[2] + part[3])) + ((part[4] + part[5]) + (part[6] + part[7]));
    a += ((part[8] + part[9]) + (part[10] + part[11])) + ((part[12] + part[13]) + (part[14] + part[15]));
    const int hd = rt >> 2, u = (rt & 3) * 60 + lane, hi = u >= 160, fq = hi ? (u - 160) >> 3 : u >> 4, di = hi ? u & 7 : u & 15;
    if (lane < 60) ST8(((u32x2){pk(a[0], a[1]), pk(a[2], a[3])}), WS_KVR + hq * KVR_B + (((hd >> 1) * 2 + hi) * 5 + (fq >> 1)) * 512 + ((hd & 1) * 16 + di) * 16 + (fq & 1) * 8);
  };
  u32x4 ka[10]; u64 fkr;
  auto oa_load = [&]() {
    asm volatile("" : "+v"(fkr));
    if (!__all(fkr == tag)) wait_flags<16>(ws, WS_FKVR + hq * 128, tag, lane);
    asm volatile("" ::: "memory");
    const unsigned kb = (((r >> 2) & 1) == h) ? WS_KVR + hq * KVR_B + w2 * 5120 + (h * 16 + (r & 3) + 4 * (r >> 3)) * 16 : 0x7FFF0000u;
#pragma unroll
    for (int j = 0; j < 10; j++) ka[j] = LD16(kb + j * 512);
  };
  u32x4 ja[2][3], jb[2]; f32x4 jc[3];
  const LAS char* jab = lp + L_PHK + ((w >> 1) * 128 + g * 8 + q4) * 80 + p4 * 8;
  const LAS char* jbb = lp + L_VIM + ((w >> 1) * 128 + g * 8 + q4) * 40 + (w & 1) * 32 + p4 * 8;
  auto kv_rd = [&](int k0) {
#pragma unroll
    for (int ks = 0; ks < 2; ks++) { jb[ks] = trd2(jbb + (k0 + ks) * 1280, jbb + (k0 + ks) * 1280 + 160);
#pragma unroll
      for (int ft = 0; ft < 3; ft++) ja[ks][ft] = trd2(jab + (k0 + ks) * 2560 + ft * 32, jab + (k0 + ks) * 2560 + 320 + ft * 32); } };
  auto kv_mm = [&](bool first) {
#pragma unroll
    for (int ks = 0; ks < 2; ks++) { if ((w & 1) && l15 == 4) jb[ks] = (u32x4){0x3F803F80u, 0x3F803F80u, 0x3F803F80u, 0x3F803F80u};
#pragma unroll
      for (int ft = 0; ft < 3; ft++) jc[ft] = mfma16(ja[ks][ft], jb[ks], (first && !ks) ? (f32x4){0.f, 0.f, 0.f, 0.f} : jc[ft]); } };
  auto kv_st = [&]() { const int dt = w & 1; const unsigned pb = WS_PART + (hq * 16 + rt) * PART_B + (w >> 1) * 3840 + (dt ? 2560 + g * 128 : g * 256) + l15 * 16;
#pragma unroll
    for (int ft = 0; ft < 3; ft++) if ((!dt || l15 < 8) && ft * 4 + g < 10) ST16(__builtin_bit_cast(u32x4, jc[ft]), pb + ft * (dt ? 512 : 1024)); };
  f32x16 oa0, oa1; u32x4 z0, z1, z2;
  LAS char* otb = lp + L_WO + (8 + w) * 5120;
  auto oa_mm = [&](int s) { oa0 = mfma32(ka[s], ph[s], s ? oa0 : Z); oa1 = mfma32(ka[5 + s], ph[s], s ? oa1 : Z); };
  auto oa_norm = [&]() { const float inv = __builtin_amdgcn_rcpf(oa1[4] + 1e-6f);
#pragma unroll
    for (int t = 0; t < 5; t++) { const int k0 = h * 20 + 4 * t;
      const u32x2 v = t < 4 ? (u32x2){nz(pk(oa0[(4 * t) & 15] * inv, oa0[(4 * t + 1) & 15] * inv)), nz(pk(oa0[(4 * t + 2) & 15] * inv, oa0[(4 * t + 3) & 15] * inv))} : (u32x2){nz(pk(oa1[0] * inv, oa1[1] * inv)), nz(pk(oa1[2] * inv, oa1[3] * inv))};
      *(LAS u32x2*)(otb + (r >> 4) * 1280 + (k0 >> 3) * 256 + (k0 & 4) * 2 + l15 * 16) = v; }
    z0 = ldsr16(otb + lane * 16); z1 = ldsr16(otb + 1280 + lane * 16); z2 = ldsr16(otb + (r >> 4) * 1280 + 1024 + l15 * 16); };
  auto oa_st = [&]() { const unsigned oa = WS_OA + (rt * 8 + w3 * 2) * OT_B + (hq * 2 + w2) * 1280;
    ST16(z0, oa + lane * 16); ST16(z1, oa + OT_B + lane * 16); if (!h) ST16(z2, oa + (r >> 4) * OT_B + 1024 + l15 * 16); };
  unsigned cpk;
  PRIO(3);
  TL(0, 0, if (!role) kv_rd(0); else FILL(0), NOP, NOP)
  TL(0, 1, if (!role) { kv_mm(true); kv_rd(2); ARRIVE_NW(0); } else FILL(1), NOP, NOP)
  TL(0, 2, if (!role) kv_mm(false); else FILL(2), NOP, if (!role) kv_st())
  TL(0, 3, NOP, NOP, cpk = __hip_atomic_load(cnt, __ATOMIC_RELAXED, __HIP_MEMORY_SCOPE_WORKGROUP))
  STAMP(14);
  if ((unsigned)__builtin_amdgcn_readfirstlane(cpk) < 8u) AWAIT(0, 8);
  asm volatile("" ::: "memory");
  TL(0, 4, NOP, NOP, if (!role) { WAIT_VM(0); set_flag(ws, WS_FPART + ((hq * 16 + rt) * 8 + w) * 8, tag, lane); } else FILL(3))
  PRIO(2);
  TL(0, 5, KX(2, 2), NOP, KW(2, 2, 0)) TL6(0, KW(2, 2, 1), KW(2, 2, 2))
  TL7(0, 1, KW(2, 2, 3), NOP, KW(2, 2, 4))
  STAMP(15);
  TL(1, 0, if (role) FILL(4), NOP, if (red) { fp0 = __hip_atomic_load((gu64*)(ws + WS_FPART) + hq * 128 + lane, RLX_AGENT); fp1 = __hip_atomic_load((gu64*)(ws + WS_FPART) + hq * 128 + 64 + lane, RLX_AGENT); })
  TL(1, 1, if (w < 4) { const int row = (w & 1) * 32 + (lane >> 1);
                      dma4s((w & 2 ? woB : woA) + C0 + ((rt & 7) * 2 + role) * 40 * 640, (row < 40 ? row : 39) * 1280 + (lane & 1) * 156, lds0 + 157696); }, NOP, if (role) FILL(5))
  TL(1, 2, NOP, NOP, if (red) red_load())
  PRIO(1);
  TL(1, 3, NOP, NOP, NOP)
  TL(1, 4, NOP, CLR_VM(), if (red) red_store())
  TL(1, 5, NOP, NOP, NOP)
  PRIO(0);
  TL6(1, if (red) { WAIT_VM(0); set_flag(ws, WS_FKVR + (hq * 16 + rt) * 8, tag, lane); }, NOP)
  if (red) WAIT_BAR(1); else WAIT_BAR(0);
  STAMP(12);
  PRIO(3);
  if (role) set_flag(ws, WS_FOA + ((rt * 8 + hq) * 8 + w) * 8, tag, lane);
  u64 foa;
  TL7(1, 2, KX(3, 0), KW(3, 0, 0), KW(3, 0, 1))
  TL(2, 0, KW(3, 0, 2), KW(3, 0, 3), KW(3, 0, 4)) TL(2, 1, WA(w, 0), WA(w, 1), WA(w, 2))
  TL(2, 2, WA(w, 3), WA(w, 4), (foa = __hip_atomic_load((gu64*)(ws + WS_FOA) + rt * 64 + lane, RLX_AGENT), fkr = __hip_atomic_load((gu64*)(ws + WS_FKVR) + hq * 16 + l15, RLX_AGENT)))
  PRIO(2);
  TL(2, 3, NOP, NOP, NOP)
  auto chk = [&]() { asm volatile("" : "+v"(foa), "+v"(fkr));
    if (!__all(foa == tag)) wait_flags<64>(ws, WS_FOA + rt * 512, tag, lane);
    asm volatile("" ::: "memory"); if (role) oa_load(); };
  TL(2, 4, chk(), NOP, NOP)
  PRIO(1);
  TL(2, 5, NOP, NOP, if (role) oa_mm(0))
  PRIO(0);
  TL6(2, if (role) oa_mm(1), if (role) oa_mm(2))
  BAR_L();
  STAMP(13);
  PRIO(3);
  TL7(2, 0, if (role) oa_mm(3), { if (role) oa_mm(4); CLR_VM(); }, WX(0))
  TL(0, 0, if (role) oa_norm(), WX(1), WX(2))
  TL(0, 1, if (role) oa_st(), WX(3), WX(4))
  TL(0, 2, WA(8 + w, 0), WA(8 + w, 1), WA(8 + w, 2))
  u32x4 wbr[11];
  const char* wbs = (const char*)(woB + C0 + w * 32 * 640) - 4096;
#define WB2(j) do { wbr[j] = *(const u32x4*)(wbs + woff[j] + (j) * 1024); wbr[5 + (j)] = *(const u32x4*)(wbs + 8 * 32 * 1280 + woff[j] + (j) * 1024); } while (0)
  TL(0, 3, WA(8 + w, 3), WA(8 + w, 4), WB2(0))
  PRIO(2);
  TL(0, 4, WB2(1), WB2(2), WB2(3))
  const int w5 = w < 5 ? w : 4;
  const unsigned wo16 = (w5 == 0 ? woff[0] : w5 == 1 ? woff[1] : w5 == 2 ? woff[2] : w5 == 3 ? woff[3] : woff[4]) + w5 * 1024;
  TL(0, 5, WB2(4), NOP, wbr[10] = *(const u32x4*)((const char*)(woB + C0 + 16 * 32 * 640) - 4096 + wo16))
  PRIO(1);
  ARRIVE_NW(1);
  const int tp = w & 1, kq = w >> 1;
  const unsigned ofr = (rt * 8 + role * 4 + tp * 2) * OT_B + kq * 5120 + lane * 16;
  const int R0 = rt * 128 + role * 64 + tp * 32 + l15;
  u32x4 of[2][5]; float gt0, gt1;
#define OFL(u) do { _Pragma("unroll") for (int i = 0; i < 5; i++) of[u][i] = LD16(WS_OA + ofr + (u) * OT_B + i * 1024); } while (0)
  TL6(0, OFL(0), (gt0 = __uint_as_float(__builtin_amdgcn_raw_buffer_load_b32(wsr, WS_G + R0 * 4, 0, 16)), gt1 = __uint_as_float(__builtin_amdgcn_raw_buffer_load_b32(wsr, WS_G + R0 * 4 + 64, 0, 16))))
  PRIO(0);
  TL7(0, 0, OFL(1), NOP, NOP)
  pB2[2] = pk(e0, e1); pB2[3] = pk(e2, e3);
  OB = mfma32(v2, pB2, OB);
#undef TL
#undef TL6
#undef TL7
#undef TILE
#undef G
  STAMP(7);
  { LAS f32x4* xb = (LAS f32x4*)(lp + (w3 >> 1) * CH_B) + (w3 & 1) * (64 * 6);
#pragma unroll
    for (int i = 0; i < 12; i++) { auto rr = __builtin_amdgcn_permlane16_swap(__float_as_uint(OA[i]), __float_as_uint(OB[i]), false, false); OA[i] = __uint_as_float(rr[0]); OB[i] = __uint_as_float(rr[1]); }
    const f32x16 snd = w2 ? OA : OB;
    AWAIT(1, 8);
#pragma unroll
    for (int c = 0; c < 3; c++) xb[(w2 * 3 + c) * 64 + lane] = (f32x4){snd[4 * c], snd[4 * c + 1], snd[4 * c + 2], snd[4 * c + 3]};
    asm volatile("s_waitcnt vmcnt(23) lgkmcnt(0)\n\ts_barrier" ::: "memory");
    f32x16 O = w2 ? OB : OA;
#pragma unroll
    for (int c = 0; c < 3; c++) { const f32x4 t = xb[((w2 ^ 1) * 3 + c) * 64 + lane]; O[4 * c] += t[0]; O[4 * c + 1] += t[1]; O[4 * c + 2] += t[2]; O[4 * c + 3] += t[3]; }
    float l = from_upper(O[8]);
    float amax = 0.f;
#pragma unroll
    for (int i = 0; i < 12; i++) amax += fabsf(O[i]);
    const bool good = (l >= 0x1p-30f) && (l <= 0x1p100f) && (amax <= 0x1p126f);
    if (__builtin_expect(!__all(good), 0)) {
      u32x4 q1; u32x2 q2;
#pragma unroll
      for (int i = 0; i < 4; i++) { auto rr = __builtin_amdgcn_permlane16_swap(q1A[i], q1B[i], false, false); q1[i] = w2 ? rr[1] : rr[0]; }
#pragma unroll
      for (int i = 0; i < 2; i++) { auto rr = __builtin_amdgcn_permlane16_swap(q2A[i], q2B[i], false, false); q2[i] = h ? 0u : (w2 ? rr[1] : rr[0]); }
      float m = -INFINITY;
#pragma unroll 1
      for (int t = 0; t < 64; t++) {
        f32x16 s = mfma32(LD16(WS_KV + t * TILE_B + ok1), q1, Z); s = mfma32k8(LD8(WS_KV + t * TILE_B + ok2), q2, s);
#pragma unroll
        for (int i = 0; i < 16; i++) m = fmaxf(m, s[i]);
      }
      m = half_swap_max(m);
      O = Z;
#pragma unroll 1
      for (int t = 0; t < 64; t++) {
        f32x16 s = mfma32(LD16(WS_KV + t * TILE_B + ok1), q1, Z); s = mfma32k8(LD8(WS_KV + t * TILE_B + ok2), q2, s);
#pragma unroll
        for (int i = 0; i < 16; i++) s[i] = ex2(s[i] - m);
        O = mfma32(LD16(WS_KV + t * TILE_B + ov), (u32x4){pk(s[0], s[1]), pk(s[2], s[3]), pk(s[4], s[5]), pk(s[6], s[7])}, O);
        O = mfma32(LD16(WS_KV + t * TILE_B + ov + 2 * VB), (u32x4){pk(s[8], s[9]), pk(s[10], s[11]), pk(s[12], s[13]), pk(s[14], s[15])}, O);
      }
      l = from_upper(O[8]);
    }
    const float inv = __builtin_amdgcn_rcpf(l);
    LAS char* tb = lp + w2 * CH_B + 12288 + w3 * 1536;
#pragma unroll
    for (int c = 0; c < 3; c++) { const int k0 = (g & 1) * 20 + 8 * c + 4 * h;
      *(LAS u32x2*)(tb + ((c == 2 && h) ? 1280 + (g & 1) * 8 : (k0 >> 3) * 256 + (k0 & 4) * 2) + l15 * 16) = (u32x2){nz(pk(O[4 * c] * inv, O[4 * c + 1] * inv)), nz(pk(O[4 * c + 2] * inv, O[4 * c + 3] * inv))}; }
    const unsigned ob = WS_OB + (rt * 8 + w3 * 2 + w2) * OT_B + hp * 1280 + lane * 16;
    const u32x4 z0 = ldsr16(tb + lane * 16), z1 = ldsr16(tb + 1024 + l15 * 16);
    ST16(z0, ob); ST16(z1, lane < 16 ? ob + 1024 : 0x7FFF0000u);
  }
  STAMP(8);
  u32x4 wf[5][5];
#pragma unroll
  for (int i = 0; i < 5; i++) { const LAS char* wb = lp + L_WO + (kq * 5 + i) * 5120 + wlane;
#pragma unroll
    for (int t = 0; t < 5; t++) wf[i][t] = trd2(wb + t * 8, wb + 1280 + t * 8); }
  BAR_L();
  auto of_missing = [&]() { u32x4 mm = of[0][0];
#pragma unroll
    for (int j = 1; j < 10; j++) mm = __builtin_elementwise_max(mm, of[j / 5][j % 5]);
    return missing(mm); };
  if (__builtin_expect(of_missing(), 0)) {
#pragma unroll 1
    for (unsigned spin = 0; spin < SPIN_CAP; ++spin) {
      asm volatile("" ::: "memory");
      OFL(0); OFL(1);
      if (!of_missing()) break;
      __builtin_amdgcn_s_sleep(1);
    }
  }
  f32x4 ca[2][5], cb[2][5];
#pragma unroll
  for (int t = 0; t < 5; t++) { ca[0][t] = ca[1][t] = cb[0][t] = cb[1][t] = (f32x4){0.f, 0.f, 0.f, 0.f}; }
  { LAS char* d = lp + L_WO + w * 5120 + lane * 16;
#pragma unroll
    for (int i = 0; i < 5; i++) {
      *(LAS u32x4*)(d + i * 1024) = wbr[i]; *(LAS u32x4*)(d + 40960 + i * 1024) = wbr[5 + i]; SBAR();
#pragma unroll
      for (int t = 0; t < 5; t++) ca[0][t] = mfma16(wf[i][t], of[0][i], ca[0][t]);
      SBAR();
    }
    *(LAS u32x4*)(lp + L_WO + 16 * 5120 + w5 * 1024 + lane * 16) = wbr[10];
#pragma unroll
    for (int i = 0; i < 5; i++) {
#pragma unroll
      for (int t = 0; t < 5; t++) ca[1][t] = mfma16(wf[i][t], of[1][i], ca[1][t]);
      SBAR(); of[0][i] = LD16(WS_OB + ofr + i * 1024); SBAR();
    } }
  STAMP(9);
  BAR_L();
#pragma unroll
  for (int i = 0; i < 5; i++) { const int gr = kq * 5 + i; const LAS char* wb = lp + (gr < 17 ? L_WO + gr * 5120 : 145408 + (gr - 17) * 5120) + wlane;
#pragma unroll
    for (int t = 0; t < 5; t++) wf[i][t] = trd2(wb + t * 8, wb + 1280 + t * 8);
    SBAR(); of[1][i] = LD16(WS_OB + ofr + OT_B + i * 1024); SBAR(); }
  ARRIVE_NW(2);
#pragma unroll
  for (int t = 0; t < 5; t++) { ca[0][t] *= gt0; ca[1][t] *= gt1; }
  SBAR();
  STAMP(10);
#pragma unroll
  for (int u = 0; u < 2; u++)
#pragma unroll
    for (int i = 0; i < 5; i++) {
      if (__builtin_expect(missing(of[u][i]), 0)) {
#pragma unroll 1
        for (unsigned spin = 0; spin < SPIN_CAP; ++spin) {
          asm volatile("" ::: "memory");
#pragma unroll
          for (int j = u * 5 + i; j < 10; j++) of[j / 5][j % 5] = LD16(WS_OB + ofr + (j / 5) * OT_B + (j % 5) * 1024);
          if (!missing(of[u][i])) break;
          __builtin_amdgcn_s_sleep(1);
        }
      }
#pragma unroll
      for (int t = 0; t < 5; t++) cb[u][t] = mfma16(wf[i][t], of[u][i], cb[u][t]);
      SBAR();
    }
  {
    LAS f32x4* xb = (LAS f32x4*)(lp + (tp ? L_WO : 0)) + lane;
    f32x4 v[10];
#pragma unroll
    for (int it = 0; it < 10; it++) { const float gg = 1.0f - (it < 5 ? gt0 : gt1); v[it] = cb[it / 5][it % 5] * gg + ca[it / 5][it % 5]; }
    if (tp) AWAIT(2, 8);
#pragma unroll
    for (int it = 0; it < 10; it++) if (((it * 13) >> 5) != kq) xb[(it * 4 + kq) * 64] = v[it];
    BAR_L();
    u16* op = out + (size_t)R0 * DM + C0 + g * 20;
#pragma unroll
    for (int it = 0; it < 10; it++) if (((it * 13) >> 5) == kq) { const int o_ = (it * 13) >> 5;
      const f32x4 z = (v[it] + xb[(it * 4 + ((o_ + 1) & 3)) * 64]) + (xb[(it * 4 + ((o_ + 2) & 3)) * 64] + xb[(it * 4 + ((o_ + 3) & 3)) * 64]);
      *(u32x2*)(op + (it / 5) * 16 * DM + (it % 5) * 4) = (u32x2){pk(z[0], z[1]), pk(z[2], z[3])}; }
  }
  STAMP(11);
}
}

extern "C" void kernel_launch(void* const* d_in, const int* in_sizes, int n_in, void* d_out, int out_size, void* d_ws, size_t ws_size, hipStream_t stream) {
  using namespace gm;
  fused<<<256, 512, 0, stream>>>((const u16*)d_in[0], (const u16*)d_in[1], (const u16*)d_in[2], (const u16*)d_in[3], (const u16*)d_in[5], (const u16*)d_in[6],
                                 (const u16*)d_in[7], (const u16*)d_in[8], (const u16*)d_in[9], (u16*)d_out, (char*)d_ws);
}
```

```cpp
#include <hip/hip_runtime.h>
#include <hip/hip_bf16.h>
#include <stdint.h>

namespace gm {
typedef float f32x16 __attribute__((ext_vector_type(16)));
typedef float f32x4 __attribute__((ext_vector_type(4)));
typedef float f32x2 __attribute__((ext_vector_type(2)));
typedef short s16x4 __attribute__((ext_vector_type(4)));
typedef __bf16 b16x2 __attribute__((ext_vector_type(2)));
typedef __bf16 b16x8 __attribute__((ext_vector_type(8)));
typedef unsigned u32x4 __attribute__((ext_vector_type(4)));
typedef unsigned u32x2 __attribute__((ext_vector_type(2)));
typedef unsigned long long u64;
typedef unsigned short u16;
#define DI __device__ __forceinline__
#define LAS __attribute__((address_space(3)))
#define GAS __attribute__((address_space(1)))
typedef GAS u64 gu64;

constexpr int DM = 640;
constexpr unsigned WS_EPOCH = 0, WS_FKV = 2048, WS_FKVR = 4096, WS_FQ = 8192, WS_FPART = 16384, WS_FOA = 24576, WS_G = 57344;
constexpr unsigned WS_KVR = 8912896, KVR_B = 10240, WS_KV = 131072, WS_QX = 327680, QX_B = 10240, WS_PART = 1703936, PART_B = 15360;
constexpr unsigned WS_OA = 3670016, WS_OB = 6291456, WS_END = 8994816, OT_B = 20480;
constexpr int VB = 352, TILE_B = 1280 + 4 * VB, CH_B = TILE_B * 8, STEP_B = 2 * CH_B;
constexpr int STAGE_B = 36864, ST_W1 = 10240, ST_X = 20480;
constexpr int L_BUF = 43008, L_P0W = 0, L_P0X0 = 25600, L_P0X1 = 153600, L_RED = 158720, L_RED1 = 40960;
constexpr int L_HO = 86016, L_QL = 96256, L_VIM = 96256, L_PHK = 116736, L_WPHI = 158592, L_ZERO = 162816, L_CNT = 163712, L_TOTAL = 163840;
constexpr int L_WO = 43008;
constexpr float LOG2E = 1.4426950408889634f;
constexpr float QSCALE = 0.22360679774997896f * LOG2E;
constexpr unsigned SPIN_CAP = 20000, LSPIN_CAP = 400000;

DI unsigned pk(float a, float b) { f32x2 v = {a, b}; b16x2 r = __builtin_convertvector(v, b16x2); return __builtin_bit_cast(unsigned, r); }
DI f32x16 mfma32(u32x4 a, u32x4 b, f32x16 c) { return __builtin_amdgcn_mfma_f32_32x32x16_bf16(__builtin_bit_cast(b16x8, a), __builtin_bit_cast(b16x8, b), c, 0, 0, 0); }
DI f32x16 mfma32k8(u32x2 a, u32x2 b, f32x16 c) { return __builtin_amdgcn_mfma_f32_32x32x8bf16_1k(__builtin_bit_cast(s16x4, a), __builtin_bit_cast(s16x4, b), c, 0, 0, 0); }
DI f32x4 mfma16(u32x4 a, u32x4 b, f32x4 c) { return __builtin_amdgcn_mfma_f32_16x16x32_bf16(__builtin_bit_cast(b16x8, a), __builtin_bit_cast(b16x8, b), c, 0, 0, 0); }
DI u32x2 trd(const LAS char* p) { return __builtin_bit_cast(u32x2, __builtin_amdgcn_ds_read_tr16_b64_v4i16((LAS s16x4*)p)); }
DI u32x4 trd2(const LAS char* p0, const LAS char* p1) { u32x2 a = trd(p0), b = trd(p1); return (u32x4){a.x, a.y, b.x, b.y}; }
DI u32x4 ldsr16(const LAS char* p) { return *(const LAS u32x4*)p; }
DI u32x2 ldsr8(const LAS char* p) { return *(const LAS u32x2*)p; }
DI float ex2(float x) { return __builtin_amdgcn_exp2f(x); }
DI void dma16s(const void* base, unsigned voff, unsigned lds_base) {
  asm volatile("s_mov_b32 m0, %2\n\ts_nop 0\n\tglobal_load_lds_dwordx4 %0, %1" :: "v"(voff), "s"(base), "s"(lds_base) : "memory"); }
DI void dma16s_sc1(const void* base, unsigned voff, unsigned lds_base) {
  asm volatile("s_mov_b32 m0, %2\n\ts_nop 0\n\tglobal_load_lds_dwordx4 %0, %1 sc1" :: "v"(voff), "s"(base), "s"(lds_base) : "memory"); }
DI void dmag_open(unsigned lds_base) { asm volatile("s_mov_b32 m0, %0\n\ts_nop 0" :: "s"(lds_base) : "memory"); }
template <int J> DI void dmag(const void* base, unsigned voff) { asm volatile("global_load_lds_dwordx4 %0, %1 offset:%2" :: "v"(voff), "s"(base), "i"((J & 3) * 1024) : "memory"); }
template <int J> DI void dmag_sc1(const void* base, unsigned voff) { asm volatile("global_load_lds_dwordx4 %0, %1 offset:%2 sc1" :: "v"(voff), "s"(base), "i"((J & 3) * 1024) : "memory"); }
DI void dma4s(const void* base, unsigned voff, unsigned lds_base) {
  asm volatile("s_mov_b32 m0, %2\n\ts_nop 0\n\tglobal_load_lds_dword %0, %1" :: "v"(voff), "s"(base), "s"(lds_base) : "memory"); }
DI u32x4 ld16_async(const void* p) { u32x4 r; asm volatile("global_load_dwordx4 %0, %1, off" : "=v"(r) : "v"(p) : "memory"); return r; }
DI u32x2 ld8_sc1_async(const void* p) { u32x2 r; asm volatile("global_load_dwordx2 %0, %1, off sc1" : "=v"(r) : "v"(p) : "memory"); return r; }
#define BAR_L() asm volatile("s_waitcnt lgkmcnt(0)\n\ts_barrier" ::: "memory")
#define WAIT_BAR(N) asm volatile("s_waitcnt vmcnt(" #N ") lgkmcnt(0)\n\ts_barrier" ::: "memory")
#define WAIT_VM(N) asm volatile("s_waitcnt vmcnt(" #N ")" ::: "memory")
#define SBAR() __builtin_amdgcn_sched_barrier(0)
#define RLX_AGENT __ATOMIC_RELAXED, __HIP_MEMORY_SCOPE_AGENT
DI float half_swap_max(float m) { auto rr = __builtin_amdgcn_permlane32_swap(__float_as_uint(m), __float_as_uint(m), false, false); return fmaxf(__uint_as_float(rr[0]), __uint_as_float(rr[1])); }
DI float from_upper(float v) { auto rr = __builtin_amdgcn_permlane32_swap(__float_as_uint(v), __float_as_uint(v), false, false); return __uint_as_float(rr[1]); }

template <int NF>
DI bool flags_set(const char* ws, unsigned off, u64 tag, int lane) {
  gu64* f = (gu64*)(ws + off);
  bool ok = __hip_atomic_load(f + (NF < 64 ? (lane & (NF - 1)) : lane), RLX_AGENT) == tag;
  if (NF >= 128) ok &= __hip_atomic_load(f + 64 + lane, RLX_AGENT) == tag;
  if (NF >= 256) { ok &= __hip_atomic_load(f + 128 + lane, RLX_AGENT) == tag; ok &= __hip_atomic_load(f + 192 + lane, RLX_AGENT) == tag; }
  return ok;
}
template <int NF>
DI void wait_flags(const char* ws, unsigned off, u64 tag, int lane) {
#pragma unroll 1
  for (unsigned spin = 0; spin < SPIN_CAP; ++spin) {
    if (__all(flags_set<NF>(ws, off, tag, lane))) break;
    __builtin_amdgcn_s_sleep(2);
  }
  asm volatile("" ::: "memory");
}
DI void set_flag(char* ws, unsigned off, u64 tag, int lane) { if (lane == 0) __hip_atomic_store((gu64*)(ws + off), tag, RLX_AGENT); }
DI unsigned nz(unsigned v) { return v < 0xFFFFFFFEu ? v : 0xFFFFFFFEu; }
DI unsigned umax3(unsigned a, unsigned b, unsigned c) { unsigned m = a > b ? a : b; return m > c ? m : c; }
DI bool missing(u32x4 v) { return __any(umax3(umax3(v.x, v.y, v.z), v.w, 0u) == 0xFFFFFFFFu); }
#define ONES ((u32x4){0xFFFFFFFFu, 0xFFFFFFFFu, 0xFFFFFFFFu, 0xFFFFFFFFu})
#define ST16(v, off) __builtin_amdgcn_raw_buffer_store_b128((v), wsr, (off), 0, 16)
#define ST8(v, off) __builtin_amdgcn_raw_buffer_store_b64((v), wsr, (off), 0, 16)
#define LD16(off) __builtin_amdgcn_raw_buffer_load_b128(wsr, (off), 0, 16)
#define LD8(off) __builtin_amdgcn_raw_buffer_load_b64(wsr, (off), 0, 16)

#define STAMP(i) do {} while (0)

__global__ __launch_bounds__(512, 2) void fused(const u16* x, const u16* wqkv, const u16* wphi, const u16* woA,
                                                const u16* wq, const u16* wk, const u16* wv, const u16* woB,
                                                const u16* gatew, u16* out, char* ws) {
  __shared__ __attribute__((aligned(16))) char lds[L_TOTAL];
  const int tid = threadIdx.x, lane = tid & 63;
  const int w = __builtin_amdgcn_readfirstlane(tid >> 6) & 7;
  const int l15 = lane & 15, g = lane >> 4, q4 = l15 >> 2, p4 = lane & 3, r = lane & 31, h = lane >> 5;
  const int bx = blockIdx.x & 7, bi = blockIdx.x >> 3;
  const int rt = (bx & 1) * 8 + (bi & 7), hp = (bx >> 1) * 4 + ((bi >> 3) & 3), hq = hp >> 1, role = hp & 1;
  const int w3 = w & 3, w2 = w >> 2;
  LAS char* lp = (LAS char*)lds;
  const unsigned lds0 = (unsigned)(uintptr_t)lds;
  const bool second = w3 >> 1;
  const unsigned rs = (second && role) ? 1280u : 3840u;
  unsigned so[5], xoff[2];
#pragma unroll
  for (int j = 0; j < 5; j++) { const int c = j * 64 + lane, rho = (c * 6554) >> 16, cc = c - rho * 10; so[j] = (rho ^ (second ? 4 : 0)) * rs + cc * 16; }
#pragma unroll
  for (int j = 0; j < 2; j++) { const int row = j * 8 + (lane >> 3); xoff[j] = row * 1280 + (((lane & 7) ^ ((row >> 1) & 7)) * 16); }
  asm volatile("" : "+v"(so[0]), "+v"(so[1]), "+v"(so[2]), "+v"(so[3]), "+v"(so[4]), "+v"(xoff[0]), "+v"(xoff[1]));
  asm volatile("" : "+s"(x), "+s"(wqkv), "+s"(wphi), "+s"(woA), "+s"(wq), "+s"(wk), "+s"(wv), "+s"(woB), "+s"(gatew), "+s"(out), "+s"(ws));
  const __amdgpu_buffer_rsrc_t wsr = __builtin_amdgcn_make_buffer_rsrc(ws, 0, WS_END, 0x00020000);
  LAS unsigned* cnt = (LAS unsigned*)(lp + L_CNT);
#define ARRIVE_NW(c) do { asm volatile("" ::: "memory"); if (lane == 0) __hip_atomic_fetch_add(cnt + (c), 1u, __ATOMIC_RELAXED, __HIP_MEMORY_SCOPE_WORKGROUP); asm volatile("" ::: "memory"); } while (0)
#define PEEK(c) ((unsigned)__builtin_amdgcn_readfirstlane(__hip_atomic_load(cnt + (c), __ATOMIC_RELAXED, __HIP_MEMORY_SCOPE_WORKGROUP)))
#define AWAIT(c, n) do { _Pragma("unroll 1") for (unsigned sp_ = 0; sp_ < LSPIN_CAP; ++sp_) { if (PEEK(c) >= (unsigned)(n)) break; __builtin_amdgcn_s_sleep(1); } asm volatile("" ::: "memory"); } while (0)
#define POST(c, v) do { asm volatile("" ::: "memory"); __hip_atomic_store(cnt + (c) + w3, (unsigned)(v), __ATOMIC_RELAXED, __HIP_MEMORY_SCOPE_WORKGROUP); asm volatile("" ::: "memory"); } while (0)
#define PEEK4(c) __hip_atomic_load(cnt + (c) + p4, __ATOMIC_RELAXED, __HIP_MEMORY_SCOPE_WORKGROUP)
#define AWAIT4(c, n) do { _Pragma("unroll 1") for (unsigned sp_ = 0; sp_ < LSPIN_CAP; ++sp_) { if (__all(PEEK4(c) >= (unsigned)(n))) break; __builtin_amdgcn_s_sleep(1); } asm volatile("" ::: "memory"); } while (0)
  STAMP(0);
  if (w == 0) *(LAS u32x4*)(lp + L_ZERO + lane * 16) = (u32x4){0u, 0u, 0u, 0u};

  const int T0 = rt * 128 + hq * 16;
  const int C0 = hq * 80;
  const int wlane = ((g >> 1) * 16 + (g & 1) * 4 + q4) * 160 + p4 * 40;
  u64 tag;
  unsigned o0[10];
#define KVP(s, slot, p) dma16s_sc1(ws + WS_KV + (s) * STEP_B + (p) * 1024, lane * 16, lds0 + (slot) * STEP_B + (p) * 1024)
#define KVL(s, slot) do { _Pragma("unroll") for (int j = 0; j < 11; j++) if (j < 10 || w3 < 2) KVP(s, slot, w3 + 4 * j); } while (0)
#define KW(s, slot, j) do { if ((j) == 4) KVP(s, slot, w * 5 + 4); else { if ((j) == 0) dmag_open(lds0 + (slot) * STEP_B + w * 5120); dmag_sc1<(j)>(ws + WS_KV + (s) * STEP_B + w * 5120, lane * 16); } } while (0)
#define KX(s, slot) do { if (w < 2) KVP(s, slot, 40 + w); } while (0)
  if (w2 == 0) {
    u32x4 gf0, gf1, gf2, gf3, gf4, wpc; u32x2 tch;
    u32x2 ep = ld8_sc1_async(ws + WS_EPOCH);
    const int lx = w3 < 3 ? L_P0X0 + w3 * 5120 : L_P0X1;
    { const u16* wsrc = (role ? wv : wk) + w3 * 3200;
#pragma unroll
      for (int j = 0; j < 5; j++) { const int n = j * 64 + lane, row = (n * 3277) >> 16, cc = n - row * 20; dma16s(x + T0 * DM + w3 * 160, row * 1280 + cc * 16, lds0 + lx + j * 1024); }
#pragma unroll
      for (int j = 0; j < 6; j++) dma16s(wsrc + j * 512, lane * 16, lds0 + L_P0W + w3 * 6400 + j * 1024);
      if (lane < 16) dma16s(wsrc + 6 * 512, lane * 16, lds0 + L_P0W + w3 * 6400 + 6 * 1024);
      const u16* gp = gatew + w3 * 160 + g * 8;
      gf0 = ld16_async(gp); gf1 = ld16_async(gp + 32); gf2 = ld16_async(gp + 64); gf3 = ld16_async(gp + 96); gf4 = ld16_async(gp + 128);
      wpc = ld16_async(wphi + hq * 1600 + (tid < 200 ? tid : 199) * 8);
      const char* tp = ws + (lane & 7) * 1114112;
      if (lane >= 8) tp = ws + (lane < 16 ? WS_FKV + blockIdx.x * 128 : WS_KVR + blockIdx.x * 320 + (lane & 3) * 96);
      if (w3 == 1) tp = (const char*)(woA + C0 + (lane & 7) * 51200);
      if (w3 == 2) tp = (const char*)(woB + C0 + (lane & 7) * 51200);
      if (w3 == 3) tp = (const char*)(out + (rt * 128 + role * 64) * DM + C0);
      tch = ld8_sc1_async(tp);
    }
    asm volatile("s_waitcnt vmcnt(1)" : "+v"(gf0), "+v"(gf1), "+v"(gf2), "+v"(gf3), "+v"(gf4), "+v"(wpc), "+v"(ep) :: "memory");
    tag = (((u64)(unsigned)__builtin_amdgcn_readfirstlane(ep.y) << 32) | (unsigned)__builtin_amdgcn_readfirstlane(ep.x)) + 1ull;
    STAMP(1);
    { const LAS char* wb = lp + L_P0W + (w3 * 160 + g * 8 + q4) * 40;
      const LAS char* xb = lp + lx + l15 * 320 + g * 16;
      const bool gl = l15 == 4;
      f32x4 c0 = {}, c1 = {};
#define P0STEP(s, GF) { const u32x4 b = ldsr16(xb + (s) * 64); c0 = mfma16(trd2(wb + p4 * 8 + (s) * 1280, wb + p4 * 8 + (s) * 1280 + 160), b, c0); \
        u32x4 a = trd2(wb + 32 + (s) * 1280, wb + 32 + (s) * 1280 + 160); if (gl) a = GF; c1 = mfma16(a, b, c1); }
      P0STEP(0, gf0) P0STEP(1, gf1) P0STEP(2, gf2) P0STEP(3, gf3) P0STEP(4, gf4)
#undef P0STEP
      *(LAS f32x4*)(lp + L_RED + tid * 16) = c0;
      if (lane < 32) *(LAS f32x4*)(lp + L_RED1 + (w3 * 32 + lane) * 16) = c1; }
    f32x16 acc[5];
#pragma unroll
    for (int b = 0; b < 5; b++) acc[b] = (f32x16){};
    const int wm = g & 1, whd = p4;
    const int wo0 = wm * ST_W1 + (8 * h + 4 * wm + q4) * 160 + whd * 40, wo1 = wm * ST_W1 + (8 * h + 4 * (wm ^ 1) + q4) * 160 + whd * 40;
    const int xo = ST_X + (w3 * 32 + r) * 128 + ((h ^ ((r >> 1) & 7)) * 16);
    u32x4 fr[4][5], xr[4];
#define FRD(s, j) fr[s][j] = trd2(sb + wo0 + (s) * 2560 + (j) * 8, sb + wo1 + (s) * 2560 + (j) * 8)
#define FMM(s, j) acc[j] = mfma32(fr[s][j], xr[s], acc[j])
#define KSTEP(s) { xr[s] = ldsr16(sb + (xo ^ ((s) * 32))); \
      _Pragma("unroll") for (int j = 0; j < 5; j++) { if (i > 0 || (s) >= 2) FMM((s) ^ 2, j); FRD(s, j); SBAR(); } }
    unsigned lnd = 1;
    BAR_L();
    STAMP(2);
#pragma unroll
    for (int i = 0; i < 10; i++) {
      if (__builtin_expect(!__all(lnd >= (unsigned)(i + 1)), 0)) AWAIT4(8, i + 1);
      asm volatile("" ::: "memory");
      const LAS char* sb = lp + L_BUF + ((i + 2) % 3) * STAGE_B;
      KSTEP(0)
      if (i == 1 && role) {
        const unsigned qb = WS_QX + (rt * 8 + hq) * QX_B + w3 * 1280 + lane * 16;
        ST16(ONES, qb); ST16(ONES, qb + 5120); ST16(ONES, lane < 32 ? qb + 1024 + (lane >> 4) * (5120 - 256) : 0x7FFF0000u); }
      if (i == 4) { asm volatile("s_waitcnt vmcnt(0)" : "+v"(tch) :: "memory"); if (role) set_flag(ws, WS_FQ + ((rt * 8 + hq) * 4 + w3) * 8, tag, lane);
                    if (tid < 200) *(LAS u32x4*)(lp + L_WPHI + tid * 16) = wpc; }
      KSTEP(1) KSTEP(2)
      lnd = PEEK4(i < 9 ? 8 : 12);
      KSTEP(3)
      POST(12, i + 1);
    }
#pragma unroll
    for (int j = 0; j < 10; j++) FMM(2 + j / 5, j % 5);
#undef FRD
#undef FMM
#undef KSTEP
    STAMP(3);
#define PK2(t, i, sc) pk(acc[t][i] * (sc), acc[t][(i) + 1] * (sc))
#define ROW(i, sc) const u32x4 ra = {PK2(0, i, sc), PK2(0, (i) + 2, sc), PK2(1, i, sc), PK2(1, (i) + 2, sc)}, rb = {PK2(2, i, sc), PK2(2, (i) + 2, sc), PK2(3, i, sc), PK2(3, (i) + 2, sc)}; const u32x2 rc = {PK2(4, i, sc), PK2(4, (i) + 2, sc)}
    { ROW(4, 1.0f);
      if (__builtin_expect(!__all(lnd >= 9u), 0)) AWAIT4(12, 9);
      asm volatile("" ::: "memory");
      *(LAS u32x4*)(lp + L_HO + tid * 16) = ra; *(LAS u32x4*)(lp + L_HO + 4096 + tid * 16) = rb; *(LAS u32x2*)(lp + L_HO + 8192 + tid * 8) = rc; }
    BAR_L();
    const int qfo = (h * 4 + w3) * 1280;
    if (role) {
      { ROW(8, QSCALE); const unsigned qb = WS_QX + (rt * 8 + hq) * QX_B + qfo;
        ST16(((u32x4){nz(ra.x), nz(ra.y), nz(ra.z), nz(ra.w)}), qb + r * 16); ST16(((u32x4){nz(rb.x), nz(rb.y), nz(rb.z), nz(rb.w)}), qb + 512 + r * 16); ST8(((u32x2){nz(rc.x), nz(rc.y)}), qb + 1024 + r * 8); }
      { ROW(12, QSCALE); LAS char* d = lp + L_QL + qfo;
        *(LAS u32x4*)(d + r * 16) = ra; *(LAS u32x4*)(d + 512 + r * 16) = rb; *(LAS u32x2*)(d + 1024 + r * 8) = rc; }
    } else { LAS char* d = lp + L_VIM + (h * 128 + w3 * 32 + r) * 40;
#pragma unroll
      for (int t = 0; t < 5; t++) { *(LAS u32x2*)(d + t * 8) = (u32x2){PK2(t, 8, 1.0f), PK2(t, 10, 1.0f)}; *(LAS u32x2*)(d + 10240 + t * 8) = (u32x2){PK2(t, 12, 1.0f), PK2(t, 14, 1.0f)}; } }
#pragma unroll
    for (int t = 0; t < 5; t++) { o0[2 * t] = PK2(t, 0, 1.0f); o0[2 * t + 1] = PK2(t, 2, 1.0f); }
#undef ROW
#undef PK2
  } else {
    u32x2 ep = ld8_sc1_async(ws + WS_EPOCH);
    const char* wsrc = (second ? (role ? (const char*)(wq + C0) : (const char*)(wqkv + 1280 + C0)) : (const char*)(wqkv + (role ? 0 : 640) + C0)) + (w3 & 1) * 32 * rs;
    const char* xsrc = (const char*)(x + (rt * 128 + w3 * 32) * DM);
    int rot = (bi & 7) + 3 * ((bi >> 3) & 3) + (bx & 6) + 5 * (bx & 1); rot = rot >= 20 ? rot - 20 : (rot >= 10 ? rot - 10 : rot);
    int bsel = 2, kc = rot;
    auto stage = [&](int n) {
      const unsigned lb = lds0 + L_BUF + bsel * STAGE_B;
      const char* bw = wsrc + kc * 64 * rs; const char* bxp = xsrc + kc * 128;
#pragma unroll
      for (int j = 0; j < 5; j++) { dma16s(bw, so[j], lb + (w3 * 5 + j) * 1024); if (j < 4) dma16s(bxp + (j >> 1) * 16 * 1280, xoff[j & 1], lb + ST_X + (w3 * 4 + j) * 1024);
        if (j == 1 && n > 0) { WAIT_VM(4); POST(8, n); } }
      bsel = bsel == 2 ? 0 : bsel + 1; kc = kc == 9 ? 0 : kc + 1;
    };
    stage(0); stage(0);
    asm volatile("s_waitcnt vmcnt(9)\n\ts_barrier" : "+v"(ep) :: "memory");
    tag = (((u64)(unsigned)__builtin_amdgcn_readfirstlane(ep.y) << 32) | (unsigned)__builtin_amdgcn_readfirstlane(ep.x)) + 1ull;
    STAMP(1); STAMP(2);
#pragma unroll 1
    for (int i = 0; i < 8; i++) {
      if (i >= 1) AWAIT4(12, i);
      stage(i + 2);
      if (i == 0 && w3 == 0) {
        const unsigned tb = WS_KV + (T0 >> 5) * TILE_B; const int s16 = hq & 1;
        if (!role) {
          const LAS f32x4* rp = (const LAS f32x4*)(lp + (h ? L_RED1 : L_RED)) + (h ? (g & 1) * 16 : g * 32) + l15;
          const int st = h ? 32 : 64, o2 = h ? 0 : 16;
          const f32x4 a = (rp[0] + rp[st]) + (rp[2 * st] + rp[3 * st]), b = (rp[o2] + rp[st + o2]) + (rp[2 * st + o2] + rp[3 * st + o2]);
          const u32x4 v = {pk(a[0], a[1]), pk(a[2], a[3]), pk(b[0], b[1]), pk(b[2], b[3])};
          if (lane < 32) ST16(v, tb + (g * 32 + s16 * 16 + l15) * 16);
          else if (lane < 48) ST8(((u32x2){v.x, v.y}), tb + 1024 + (s16 * 16 + l15) * 8);
          else { const float gv = 1.0f / (1.0f + __expf(-a[0])); __builtin_amdgcn_raw_buffer_store_b32(__float_as_uint(gv), wsr, WS_G + (T0 + l15) * 4, 0, 16); }
        } else if (lane < 44) {
          const int hh = lane >= 22, d = lane - hh * 22, dd = d < 20 ? d : 0, st = dd >= 16 ? 128 : 256;
          const LAS float* rp = (const LAS float*)(lp + (dd >= 16 ? L_RED1 : L_RED)) + (((dd & 15) >> 2) * 16 + 4 * hh) * 4 + (dd & 3);
          float e[8];
#pragma unroll
          for (int j = 0; j < 8; j++) { const int tk = (8 * (j >> 2) + (j & 3)) * 4; e[j] = (rp[tk] + rp[tk + st]) + (rp[tk + 2 * st] + rp[tk + 3 * st]); }
          u32x4 v = {pk(e[0], e[1]), pk(e[2], e[3]), pk(e[4], e[5]), pk(e[6], e[7])};
          if (d == 20) v = (u32x4){0x3F803F80u, 0x3F803F80u, 0x3F803F80u, 0x3F803F80u};
          if (d == 21) v = (u32x4){0u, 0u, 0u, 0u};
          ST16(v, tb + 1280 + (s16 * 2 + hh) * VB + d * 16);
        }
      }
      if (i == 1 && w3 == 0) set_flag(ws, WS_FKV + blockIdx.x * 8, tag, lane);
    }
    u64 fa, fb, fc, fd, fe;
    { gu64* f = (gu64*)(ws + WS_FKV) + lane;
      fa = __hip_atomic_load(f, RLX_AGENT); fb = __hip_atomic_load(f + 64, RLX_AGENT); fc = __hip_atomic_load(f + 128, RLX_AGENT); fd = __hip_atomic_load(f + 192, RLX_AGENT);
      fe = __hip_atomic_load((gu64*)(ws + WS_FQ) + (rt * 8 + hq) * 4 + p4, RLX_AGENT); }
    WAIT_VM(5); POST(8, 10);
    asm volatile("" : "+v"(fa), "+v"(fb), "+v"(fc), "+v"(fd), "+v"(fe));
    const bool kvok = __all((fa == tag) & (fb == tag) & (fc == tag) & (fd == tag) & (fe == tag));
    asm volatile("" ::: "memory");
    if (!kvok) { wait_flags<256>(ws, WS_FKV, tag, lane); wait_flags<4>(ws, WS_FQ + (rt * 8 + hq) * 32, tag, lane); }
    KVL(0, 0);
    if (blockIdx.x == 0 && w3 == 0) set_flag(ws, WS_EPOCH, tag, lane);
    STAMP(3);
    BAR_L();
    const int ht = tid - 256;
    const u32x4 a = ldsr16(lp + L_HO + ht * 16), b = ldsr16(lp + L_HO + 4096 + ht * 16); const u32x2 c = ldsr8(lp + L_HO + 8192 + ht * 8);
    o0[0] = a.x; o0[1] = a.y; o0[2] = a.z; o0[3] = a.w; o0[4] = b.x; o0[5] = b.y; o0[6] = b.z; o0[7] = b.w; o0[8] = c.x; o0[9] = c.y;
  }
  STAMP(4);
  u32x4 ph[5];
  { const LAS char* pb = ((p4 & 1) == h) ? lp + L_WPHI + (2 * w2 + h) * 800 + q4 * 40 + (2 * (g & 1) + (p4 >> 1)) * 8 : lp + L_ZERO;
    const u32x4 b0 = {o0[0], o0[1], o0[2], o0[3]}, b1 = {o0[4], o0[5], o0[6], o0[7]}, b2 = {o0[8], o0[9], 0u, 0u};
    const u32x2 c0 = trd(pb + 640), c1 = trd(pb + 672);
    f32x16 P0 = {}, P1 = {};
    P0 = mfma32(trd2(pb, pb + 160), b0, P0); P1 = mfma32(trd2(pb + 32, pb + 192), b0, P1);
    P0 = mfma32(trd2(pb + 320, pb + 480), b1, P0); P1 = mfma32(trd2(pb + 352, pb + 512), b1, P1);
    P0 = mfma32((u32x4){c0.x, c0.y, 0u, 0u}, b2, P0); P1 = mfma32((u32x4){c1.x, c1.y, 0u, 0u}, b2, P1);
    KX(1, 1); KW(1, 1, 0); SBAR();
    f32x2 p[10];
#pragma unroll
    for (int f = 0; f < 10; f++) p[f] = f < 8 ? (f32x2){P0[(2 * f) & 15], P0[(2 * f + 1) & 15]} : (f32x2){P1[(2 * f) & 3], P1[(2 * f + 1) & 3]};
    float mx = fmaxf(p[0][0], p[0][1]), mn = fminf(p[0][0], p[0][1]);
#pragma unroll
    for (int f = 1; f < 10; f++) { mx = fmaxf(fmaxf(mx, p[f][0]), p[f][1]); mn = fminf(fminf(mn, p[f][0]), p[f][1]); }
    const f32x2 ma = {-mx * LOG2E, -mx * LOG2E}, mb = {mn * LOG2E, mn * LOG2E}, ca_ = {LOG2E, LOG2E}, cb_ = {-LOG2E, -LOG2E};
    f32x2 ea[10], eb[10], sa = {0.f, 0.f}, sb = {0.f, 0.f};
#pragma unroll
    for (int f = 0; f < 10; f++) { if (f % 2 == 0 && f < 8) { SBAR(); if (f == 0) KW(1, 1, 1); if (f == 2) KW(1, 1, 2); if (f == 4) KW(1, 1, 3); if (f == 6) KW(1, 1, 4); SBAR(); }
      const f32x2 xa = __builtin_elementwise_fma(p[f], ca_, ma), xb = __builtin_elementwise_fma(p[f], cb_, mb);
      ea[f] = (f32x2){ex2(xa[0]), ex2(xa[1])}; eb[f] = (f32x2){ex2(xb[0]), ex2(xb[1])}; sa += ea[f]; sb += eb[f]; }
    const float ia = __builtin_amdgcn_rcpf(sa[0] + sa[1]), ib = __builtin_amdgcn_rcpf(sb[0] + sb[1]);
    unsigned o[20];
#pragma unroll
    for (int f = 0; f < 10; f++) { const f32x2 ya = ea[f] * ia, yb = eb[f] * ib; o[f] = pk(ya[0], ya[1]); o[10 + f] = pk(yb[0], yb[1]); }
#pragma unroll
    for (int c = 0; c < 5; c++) ph[c] = (u32x4){o[4 * c], o[4 * c + 1], o[4 * c + 2], o[4 * c + 3]};
    asm volatile("" : "+v"(ph[0]), "+v"(ph[1]), "+v"(ph[2]), "+v"(ph[3]), "+v"(ph[4]));
  }
  u32x4 q1A, q1B; u32x2 q2A, q2B;
  if (role) {
    WAIT_BAR(0);
    STAMP(5);
    const LAS char* qa = lp + L_QL + w3 * 1280;
    q1A = ldsr16(qa + lane * 16); q1B = ldsr16(qa + 5120 + lane * 16); q2A = ldsr8(qa + 1024 + r * 8); q2B = ldsr8(qa + 6144 + r * 8);
    ARRIVE_NW(0);
  } else {
    const unsigned qb = WS_QX + (rt * 8 + hq) * QX_B + w3 * 1280;
    q1A = LD16(qb + lane * 16); q1B = LD16(qb + 5120 + lane * 16); q2A = LD8(qb + 1024 + r * 8); q2B = LD8(qb + 6144 + r * 8);
    { LAS char* d = lp + L_PHK + ((2 * w2 + h) * 128 + w3 * 32 + r) * 80;
#pragma unroll
      for (int c = 0; c < 5; c++) *(LAS u32x4*)(d + c * 16) = ph[c]; }
    WAIT_BAR(4);
    STAMP(5);
#pragma unroll 1
    for (unsigned spin = 0; spin < SPIN_CAP; ++spin) {
      if (!(missing(q1A) | missing(q1B) | missing((u32x4){q2A.x, q2A.y, q2B.x, q2B.y}))) break;
      __builtin_amdgcn_s_sleep(1);
      asm volatile("" ::: "memory");
      q1A = LD16(qb + lane * 16); q1B = LD16(qb + 5120 + lane * 16); q2A = LD8(qb + 1024 + r * 8); q2B = LD8(qb + 6144 + r * 8);
    }
  }
  STAMP(6);
  const int vr = r <= 21 ? r : 21;
  const int ok1 = lane * 16, ok2 = h ? (1280 + 20 * 16) : (1024 + r * 8), ov = 1280 + h * VB + vr * 16;
  const f32x16 Z = {};
  if (h) { q2A = (u32x2){0u, 0u}; q2B = (u32x2){0u, 0u}; }
  { const LAS char* t0 = lp;
    const u32x4 k1 = ldsr16(t0 + ok1); const u32x2 k2 = ldsr8(t0 + ok2);
    f32x16 sa = mfma32(k1, q1A, Z); sa = mfma32k8(k2, q2A, sa);
    f32x16 sb = mfma32(k1, q1B, Z); sb = mfma32k8(k2, q2B, sb);
    float ma = sa[0], mb = sb[0];
#pragma unroll
    for (int i = 1; i < 16; i++) { ma = fmaxf(ma, sa[i]); mb = fmaxf(mb, sb[i]); }
    ma = half_swap_max(ma); mb = half_swap_max(mb);
    const float mah = __uint_as_float(__float_as_uint(ma) & 0xFFFF0000u), mbh = __uint_as_float(__float_as_uint(mb) & 0xFFFF0000u);
    if (h) { q2A.x = pk(-mah, mah - ma); q2B.x = pk(-mbh, mbh - mb); } }
  f32x16 OA = {}, OB = {}, SA, SB;
  u32x4 k1, v1, v2, v2n, pA1, pA2, pB1, pB2; u32x2 k2;
  float e0, e1, e2, e3;
  const LAS char* c0p = lp + w2 * CH_B + ok1; const LAS char* c1p = lp + w2 * CH_B + ok2; const LAS char* c2p = lp + w2 * CH_B + ov;
  k1 = ldsr16(c0p); k2 = ldsr8(c1p);
  SA = mfma32(k1, q1A, Z); SA = mfma32k8(k2, q2A, SA);
  v1 = ldsr16(c2p); v2 = v1;
  e0 = e1 = e2 = e3 = 0.f; pB2 = (u32x4){0u, 0u, 0u, 0u};
#define G(MF, LD, C0_, C1_, X, S_, i) MF; LD; SBAR(); C0_; C1_; SBAR(); X##0 = ex2(S_[i]); X##1 = ex2(S_[i + 1]); X##2 = ex2(S_[i + 2]); X##3 = ex2(S_[i + 3]); asm volatile("" : "+v"(X##0), "+v"(X##1), "+v"(X##2), "+v"(X##3)); SBAR();
#define TILE(TB, TN, V2, H0, H1, H2) { float a0, a1, a2, a3; \
    G(SB = mfma32(k1, q1B, Z),    k1 = ldsr16(c0p + (TN)), pB2[2] = pk(e0, e1), pB2[3] = pk(e2, e3), a, SA, 0) \
    G(OB = mfma32(v2, pB2, OB),   V2,                      pA1[0] = pk(a0, a1), pA1[1] = pk(a2, a3), e, SA, 4) \
    G(SB = mfma32k8(k2, q2B, SB), k2 = ldsr8(c1p + (TN)),  pA1[2] = pk(e0, e1), pA1[3] = pk(e2, e3), a, SA, 8) \
    G(OA = mfma32(v1, pA1, OA),   H0,                      pA2[0] = pk(a0, a1), pA2[1] = pk(a2, a3), e, SA, 12) \
    G(SA = mfma32(k1, q1A, Z),    H1,                      pA2[2] = pk(e0, e1), pA2[3] = pk(e2, e3), a, SB, 0) \
    G(OA = mfma32(v2, pA2, OA),   H2,                      pB1[0] = pk(a0, a1), pB1[1] = pk(a2, a3), e, SB, 4) \
    G(SA = mfma32k8(k2, q2A, SA), (void)0,                 pB1[2] = pk(e0, e1), pB1[3] = pk(e2, e3), a, SB, 8) \
    G(OB = mfma32(v1, pB1, OB),   v1 = ldsr16(c2p + (TN)), pB2[0] = pk(a0, a1), pB2[1] = pk(a2, a3), e, SB, 12) }
#define NOP (void)0
#define TL(sl, t, H0, H1, H2) TILE((sl) * STEP_B + (t) * TILE_B, (sl) * STEP_B + ((t) + 1) * TILE_B, v2 = ldsr16(c2p + (sl) * STEP_B + (t) * TILE_B + 2 * VB), H0, H1, H2)
#define TL6(sl, H0, H1) TL(sl, 6, H0, H1, v2n = ldsr16(c2p + (sl) * STEP_B + 7 * TILE_B + 2 * VB))
#define TL7(sl, sn, H0, H1, H2) TILE((sl) * STEP_B + 7 * TILE_B, (sn) * STEP_B, v2 = v2n, H0, H1, H2)
  unsigned woff[5];
#pragma unroll
  for (int j = 0; j < 5; j++) { const int c = j * 64 + lane, rho = (c * 6554) >> 16, cc = c - rho * 10; const int k = (rho & ~12) | ((rho & 4) << 1) | ((rho & 8) >> 1); woff[j] = k * 1280 + cc * 16 + (4 - j) * 1024; }
#define WOG(wo, gr, j, dst) do { if ((j) == 4) WOP(wo, gr, 4, dst); else { if ((j) == 0) dmag_open(lds0 + (dst)); dmag<(j)>((wo) + C0 + (gr) * 32 * 640 - 2048, woff[j]); } } while (0)
#define WOP(wo, gr, j, dst) dma16s((wo) + C0 + (gr) * 32 * 640 - (4 - (j)) * 512, woff[j], lds0 + (dst) + (j) * 1024)
#define WA(gr, j) WOG(woA, gr, j, L_WO + (gr) * 5120)
  const u16* wxs = w < 4 ? woA + (16 + w3) * 32 * 640 : woB + (w < 7 ? 17 + w3 : 19) * 32 * 640;
  const int wxd = w < 4 ? L_WO + (16 + w3) * 5120 : 145408 + (w < 7 ? w3 : 2) * 5120;
#define WX(j) WOG(wxs, 0, j, wxd)
#define PRIO(n) __builtin_amdgcn_s_setprio(n)
  const unsigned fo = (rt * 8 + hq) * OT_B + lane * 16;
#define FILL(j) do { const int n_ = w * 3 + ((j) >> 1); ST16(ONES, (((j) & 1) ? WS_OB : WS_OA) + fo + (n_ < 20 ? n_ : 19) * 1024); } while (0)
#define CLR_VM() __builtin_amdgcn_s_waitcnt(0x0F70)
  const bool red = !role && w == 4;
  f32x4 part[16]; u64 fp0, fp1;
  auto red_load = [&]() {
    asm volatile("" : "+v"(fp0), "+v"(fp1));
    if (!__all((fp0 == tag) & (fp1 == tag))) wait_flags<128>(ws, WS_FPART + hq * 1024, tag, lane);
    asm volatile("" ::: "memory");
#pragma unroll
    for (int j = 0; j < 16; j++) part[j] = __builtin_bit_cast(f32x4, LD16(WS_PART + (hq * 16 + j) * PART_B + (rt * 60 + (lane < 60 ? lane : 59)) * 16));
  };
  auto red_store = [&]() {
    f32x4 a = ((part[0] + part[1]) + (part[2] + part[3])) + ((part[4] + part[5]) + (part[6] + part[7]));
    a += ((part[8] + part[9]) + (part[10] + part[11])) + ((part[12] + part[13]) + (part[14] + part[15]));
    const int hd = rt >> 2, u = (rt & 3) * 60 + lane, hi = u >= 160, fq = hi ? (u - 160) >> 3 : u >> 4, di = hi ? u & 7 : u & 15;
    if (lane < 60) ST8(((u32x2){pk(a[0], a[1]), pk(a[2], a[3])}), WS_KVR + hq * KVR_B + (((hd >> 1) * 2 + hi) * 5 + (fq >> 1)) * 512 + ((hd & 1) * 16 + di) * 16 + (fq & 1) * 8);
  };
  u32x4 ka[10]; u64 fkr;
  auto oa_load = [&]() {
    asm volatile("" : "+v"(fkr));
    if (!__all(fkr == tag)) wait_flags<16>(ws, WS_FKVR + hq * 128, tag, lane);
    asm volatile("" ::: "memory");
    const unsigned kb = (((r >> 2) & 1) == h) ? WS_KVR + hq * KVR_B + w2 * 5120 + (h * 16 + (r & 3) + 4 * (r >> 3)) * 16 : 0x7FFF0000u;
#pragma unroll
    for (int j = 0; j < 10; j++) ka[j] = LD16(kb + j * 512);
  };
  u32x4 ja[2][3], jb[2]; f32x4 jc[3];
  const LAS char* jab = lp + L_PHK + ((w >> 1) * 128 + g * 8 + q4) * 80 + p4 * 8;
  const LAS char* jbb = lp + L_VIM + ((w >> 1) * 128 + g * 8 + q4) * 40 + (w & 1) * 32 + p4 * 8;
  auto kv_rd = [&](int k0) {
#pragma unroll
    for (int ks = 0; ks < 2; ks++) { jb[ks] = trd2(jbb + (k0 + ks) * 1280, jbb + (k0 + ks) * 1280 + 160);
#pragma unroll
      for (int ft = 0; ft < 3; ft++) ja[ks][ft] = trd2(jab + (k0 + ks) * 2560 + ft * 32, jab + (k0 + ks) * 2560 + 320 + ft * 32); } };
  auto kv_mm = [&](bool first) {
#pragma unroll
    for (int ks = 0; ks < 2; ks++) { if ((w & 1) && l15 == 4) jb[ks] = (u32x4){0x3F803F80u, 0x3F803F80u, 0x3F803F80u, 0x3F803F80u};
#pragma unroll
      for (int ft = 0; ft < 3; ft++) jc[ft] = mfma16(ja[ks][ft], jb[ks], (first && !ks) ? (f32x4){0.f, 0.f, 0.f, 0.f} : jc[ft]); } };
  auto kv_st = [&]() { const int dt = w & 1; const unsigned pb = WS_PART + (hq * 16 + rt) * PART_B + (w >> 1) * 3840 + (dt ? 2560 + g * 128 : g * 256) + l15 * 16;
#pragma unroll
    for (int ft = 0; ft < 3; ft++) if ((!dt || l15 < 8) && ft * 4 + g < 10) ST16(__builtin_bit_cast(u32x4, jc[ft]), pb + ft * (dt ? 512 : 1024)); };
  f32x16 oa0, oa1; u32x4 z0, z1, z2;
  LAS char* otb = lp + L_WO + (8 + w) * 5120;
  auto oa_mm = [&](int s) { oa0 = mfma32(ka[s], ph[s], s ? oa0 : Z); oa1 = mfma32(ka[5 + s], ph[s], s ? oa1 : Z); };
  auto oa_norm = [&]() { const float inv = __builtin_amdgcn_rcpf(oa1[4] + 1e-6f);
#pragma unroll
    for (int t = 0; t < 5; t++) { const int k0 = h * 20 + 4 * t;
      const u32x2 v = t < 4 ? (u32x2){nz(pk(oa0[(4 * t) & 15] * inv, oa0[(4 * t + 1) & 15] * inv)), nz(pk(oa0[(4 * t + 2) & 15] * inv, oa0[(4 * t + 3) & 15] * inv))} : (u32x2){nz(pk(oa1[0] * inv, oa1[1] * inv)), nz(pk(oa1[2] * inv, oa1[3] * inv))};
      *(LAS u32x2*)(otb + (r >> 4) * 1280 + (k0 >> 3) * 256 + (k0 & 4) * 2 + l15 * 16) = v; }
    z0 = ldsr16(otb + lane * 16); z1 = ldsr16(otb + 1280 + lane * 16); z2 = ldsr16(otb + (r >> 4) * 1280 + 1024 + l15 * 16); };
  auto oa_st = [&]() { const unsigned oa = WS_OA + (rt * 8 + w3 * 2) * OT_B + (hq * 2 + w2) * 1280;
    ST16(z0, oa + lane * 16); ST16(z1, oa + OT_B + lane * 16); if (!h) ST16(z2, oa + (r >> 4) * OT_B + 1024 + l15 * 16); };
  unsigned cpk;
  PRIO(3);
  TL(0, 0, if (!role) kv_rd(0); else FILL(0), NOP, NOP)
  TL(0, 1, if (!role) { kv_mm(true); kv_rd(2); ARRIVE_NW(0); } else FILL(1), NOP, NOP)
  TL(0, 2, if (!role) kv_mm(false); else FILL(2), NOP, if (!role) kv_st())
  TL(0, 3, NOP, NOP, cpk = __hip_atomic_load(cnt, __ATOMIC_RELAXED, __HIP_MEMORY_SCOPE_WORKGROUP))
  STAMP(14);
  if ((unsigned)__builtin_amdgcn_readfirstlane(cpk) < 8u) AWAIT(0, 8);
  asm volatile("" ::: "memory");
  TL(0, 4, NOP, NOP, if (!role) { WAIT_VM(0); set_flag(ws, WS_FPART + ((hq * 16 + rt) * 8 + w) * 8, tag, lane); } else FILL(3))
  PRIO(2);
  TL(0, 5, KX(2, 2), NOP, KW(2, 2, 0)) TL6(0, KW(2, 2, 1), KW(2, 2, 2))
  TL7(0, 1, KW(2, 2, 3), NOP, KW(2, 2, 4))
  STAMP(15);
  TL(1, 0, if (role) FILL(4), NOP, if (red) { fp0 = __hip_atomic_load((gu64*)(ws + WS_FPART) + hq * 128 + lane, RLX_AGENT); fp1 = __hip_atomic_load((gu64*)(ws + WS_FPART) + hq * 128 + 64 + lane, RLX_AGENT); })
  TL(1, 1, if (w < 4) { const int row = (w & 1) * 32 + (lane >> 1);
                      dma4s((w & 2 ? woB : woA) + C0 + ((rt & 7) * 2 + role) * 40 * 640, (row < 40 ? row : 39) * 1280 + (lane & 1) * 156, lds0 + 157696); }, NOP, if (role) FILL(5))
  TL(1, 2, NOP, NOP, if (red) red_load())
  PRIO(1);
  TL(1, 3, NOP, NOP, NOP)
  TL(1, 4, NOP, CLR_VM(), if (red) red_store())
  TL(1, 5, NOP, NOP, NOP)
  PRIO(0);
  TL6(1, if (red) { WAIT_VM(0); set_flag(ws, WS_FKVR + (hq * 16 + rt) * 8, tag, lane); }, NOP)
  if (red) WAIT_BAR(1); else WAIT_BAR(0);
  STAMP(12);
  PRIO(3);
  if (role) set_flag(ws, WS_FOA + ((rt * 8 + hq) * 8 + w) * 8, tag, lane);
  u64 foa;
  TL7(1, 2, KX(3, 0), KW(3, 0, 0), KW(3, 0, 1))
  TL(2, 0, KW(3, 0, 2), KW(3, 0, 3), KW(3, 0, 4)) TL(2, 1, WA(w, 0), WA(w, 1), WA(w, 2))
  TL(2, 2, WA(w, 3), WA(w, 4), (foa = __hip_atomic_load((gu64*)(ws + WS_FOA) + rt * 64 + lane, RLX_AGENT), fkr = __hip_atomic_load((gu64*)(ws + WS_FKVR) + hq * 16 + l15, RLX_AGENT)))
  PRIO(2);
  TL(2, 3, NOP, NOP, NOP)
  auto chk = [&]() { asm volatile("" : "+v"(foa), "+v"(fkr));
    if (!__all(foa == tag)) wait_flags<64>(ws, WS_FOA + rt * 512, tag, lane);
    asm volatile("" ::: "memory"); if (role) oa_load(); };
  TL(2, 4, chk(), NOP, NOP)
  PRIO(1);
  TL(2, 5, NOP, NOP, if (role) oa_mm(0))
  PRIO(0);
  TL6(2, if (role) oa_mm(1), if (role) oa_mm(2))
  BAR_L();
  STAMP(13);
  PRIO(3);
  TL7(2, 0, if (role) oa_mm(3), { if (role) oa_mm(4); CLR_VM(); }, WX(0))
  TL(0, 0, if (role) oa_norm(), WX(1), WX(2))
  TL(0, 1, if (role) oa_st(), WX(3), WX(4))
  TL(0, 2, WA(8 + w, 0), WA(8 + w, 1), WA(8 + w, 2))
  u32x4 wbr[11];
  const char* wbs = (const char*)(woB + C0 + w * 32 * 640) - 4096;
#define WB2(j) do { wbr[j] = *(const GAS u32x4*)(wbs + woff[j] + (j) * 1024); wbr[5 + (j)] = *(const GAS u32x4*)(wbs + 8 * 32 * 1280 + woff[j] + (j) * 1024); } while (0)
  TL(0, 3, WA(8 + w, 3), WA(8 + w, 4), WB2(0))
  PRIO(2);
  TL(0, 4, WB2(1), WB2(2), WB2(3))
  const int w5 = w < 5 ? w : 4;
  const unsigned wo16 = (w5 == 0 ? woff[0] : w5 == 1 ? woff[1] : w5 == 2 ? woff[2] : w5 == 3 ? woff[3] : woff[4]) + w5 * 1024;
  TL(0, 5, WB2(4), NOP, wbr[10] = *(const GAS u32x4*)((const char*)(woB + C0 + 16 * 32 * 640) - 4096 + wo16))
  PRIO(1);
  ARRIVE_NW(1);
  const int tp = w & 1, kq = w >> 1;
  const unsigned ofr = (rt * 8 + role * 4 + tp * 2) * OT_B + kq * 5120 + lane * 16;
  const int R0 = rt * 128 + role * 64 + tp * 32 + l15;
  u32x4 of[2][5]; float gt0, gt1;
#define OFL(u) do { _Pragma("unroll") for (int i = 0; i < 5; i++) of[u][i] = LD16(WS_OA + ofr + (u) * OT_B + i * 1024); } while (0)
  TL6(0, OFL(0), (gt0 = __uint_as_float(__builtin_amdgcn_raw_buffer_load_b32(wsr, WS_G + R0 * 4, 0, 16)), gt1 = __uint_as_float(__builtin_amdgcn_raw_buffer_load_b32(wsr, WS_G + R0 * 4 + 64, 0, 16))))
  PRIO(0);
  TL7(0, 0, OFL(1), NOP, NOP)
  pB2[2] = pk(e0, e1); pB2[3] = pk(e2, e3);
  OB = mfma32(v2, pB2, OB);
#undef TL
#undef TL6
#undef TL7
#undef TILE
#undef G
  STAMP(7);
  { LAS f32x4* xb = (LAS f32x4*)(lp + (w3 >> 1) * CH_B) + (w3 & 1) * (64 * 6);
#pragma unroll
    for (int i = 0; i < 12; i++) { auto rr = __builtin_amdgcn_permlane16_swap(__float_as_uint(OA[i]), __float_as_uint(OB[i]), false, false); OA[i] = __uint_as_float(rr[0]); OB[i] = __uint_as_float(rr[1]); }
    const f32x16 snd = w2 ? OA : OB;
    AWAIT(1, 8);
#pragma unroll
    for (int c = 0; c < 3; c++) xb[(w2 * 3 + c) * 64 + lane] = (f32x4){snd[4 * c], snd[4 * c + 1], snd[4 * c + 2], snd[4 * c + 3]};
    asm volatile("s_waitcnt vmcnt(23) lgkmcnt(0)\n\ts_barrier" ::: "memory");
    f32x16 O = w2 ? OB : OA;
#pragma unroll
    for (int c = 0; c < 3; c++) { const f32x4 t = xb[((w2 ^ 1) * 3 + c) * 64 + lane]; O[4 * c] += t[0]; O[4 * c + 1] += t[1]; O[4 * c + 2] += t[2]; O[4 * c + 3] += t[3]; }
    float l = from_upper(O[8]);
    float amax = 0.f;
#pragma unroll
    for (int i = 0; i < 12; i++) amax += fabsf(O[i]);
    const bool good = (l >= 0x1p-30f) && (l <= 0x1p100f) && (amax <= 0x1p126f);
    if (__builtin_expect(!__all(good), 0)) {
      u32x4 q1; u32x2 q2;
#pragma unroll
      for (int i = 0; i < 4; i++) { auto rr = __builtin_amdgcn_permlane16_swap(q1A[i], q1B[i], false, false); q1[i] = w2 ? rr[1] : rr[0]; }
#pragma unroll
      for (int i = 0; i < 2; i++) { auto rr = __builtin_amdgcn_permlane16_swap(q2A[i], q2B[i], false, false); q2[i] = h ? 0u : (w2 ? rr[1] : rr[0]); }
      float m = -INFINITY;
#pragma unroll 1
      for (int t = 0; t < 64; t++) {
        f32x16 s = mfma32(LD16(WS_KV + t * TILE_B + ok1), q1, Z); s = mfma32k8(LD8(WS_KV + t * TILE_B + ok2), q2, s);
#pragma unroll
        for (int i = 0; i < 16; i++) m = fmaxf(m, s[i]);
      }
      m = half_swap_max(m);
      O = Z;
#pragma unroll 1
      for (int t = 0; t < 64; t++) {
        f32x16 s = mfma32(LD16(WS_KV + t * TILE_B + ok1), q1, Z); s = mfma32k8(LD8(WS_KV + t * TILE_B + ok2), q2, s);
#pragma unroll
        for (int i = 0; i < 16; i++) s[i] = ex2(s[i] - m);
        O = mfma32(LD16(WS_KV + t * TILE_B + ov), (u32x4){pk(s[0], s[1]), pk(s[2], s[3]), pk(s[4], s[5]), pk(s[6], s[7])}, O);
        O = mfma32(LD16(WS_KV + t * TILE_B + ov + 2 * VB), (u32x4){pk(s[8], s[9]), pk(s[10], s[11]), pk(s[12], s[13]), pk(s[14], s[15])}, O);
      }
      l = from_upper(O[8]);
    }
    const float inv = __builtin_amdgcn_rcpf(l);
    LAS char* tb = lp + w2 * CH_B + 12288 + w3 * 1536;
#pragma unroll
    for (int c = 0; c < 3; c++) { const int k0 = (g & 1) * 20 + 8 * c + 4 * h;
      *(LAS u32x2*)(tb + ((c == 2 && h) ? 1280 + (g & 1) * 8 : (k0 >> 3) * 256 + (k0 & 4) * 2) + l15 * 16) = (u32x2){nz(pk(O[4 * c] * inv, O[4 * c + 1] * inv)), nz(pk(O[4 * c + 2] * inv, O[4 * c + 3] * inv))}; }
    const unsigned ob = WS_OB + (rt * 8 + w3 * 2 + w2) * OT_B + hp * 1280 + lane * 16;
    const u32x4 z0 = ldsr16(tb + lane * 16), z1 = ldsr16(tb + 1024 + l15 * 16);
    ST16(z0, ob); ST16(z1, lane < 16 ? ob + 1024 : 0x7FFF0000u);
  }
  STAMP(8);
  u32x4 wf[5][5];
#pragma unroll
  for (int i = 0; i < 5; i++) { const LAS char* wb = lp + L_WO + (kq * 5 + i) * 5120 + wlane;
#pragma unroll
    for (int t = 0; t < 5; t++) wf[i][t] = trd2(wb + t * 8, wb + 1280 + t * 8); }
  BAR_L();
  auto of_missing = [&]() { u32x4 mm = of[0][0];
#pragma unroll
    for (int j = 1; j < 10; j++) mm = __builtin_elementwise_max(mm, of[j / 5][j % 5]);
    return missing(mm); };
  if (__builtin_expect(of_missing(), 0)) {
#pragma unroll 1
    for (unsigned spin = 0; spin < SPIN_CAP; ++spin) {
      asm volatile("" ::: "memory");
      OFL(0); OFL(1);
      if (!of_missing()) break;
      __builtin_amdgcn_s_sleep(1);
    }
  }
  f32x4 ca[2][5], cb[2][5];
#pragma unroll
  for (int t = 0; t < 5; t++) { ca[0][t] = ca[1][t] = cb[0][t] = cb[1][t] = (f32x4){0.f, 0.f, 0.f, 0.f}; }
  { LAS char* d = lp + L_WO + w * 5120 + lane * 16;
#pragma unroll
    for (int i = 0; i < 5; i++) {
      *(LAS u32x4*)(d + i * 1024) = wbr[i]; *(LAS u32x4*)(d + 40960 + i * 1024) = wbr[5 + i]; SBAR();
#pragma unroll
      for (int t = 0; t < 5; t++) ca[0][t] = mfma16(wf[i][t], of[0][i], ca[0][t]);
      SBAR();
    }
    *(LAS u32x4*)(lp + L_WO + 16 * 5120 + w5 * 1024 + lane * 16) = wbr[10];
#pragma unroll
    for (int i = 0; i < 5; i++) {
#pragma unroll
      for (int t = 0; t < 5; t++) ca[1][t] = mfma16(wf[i][t], of[1][i], ca[1][t]);
      SBAR(); of[0][i] = LD16(WS_OB + ofr + i * 1024); SBAR();
    } }
  STAMP(9);
  BAR_L();
#pragma unroll
  for (int i = 0; i < 5; i++) { const int gr = kq * 5 + i; const LAS char* wb = lp + (gr < 17 ? L_WO + gr * 5120 : 145408 + (gr - 17) * 5120) + wlane;
#pragma unroll
    for (int t = 0; t < 5; t++) wf[i][t] = trd2(wb + t * 8, wb + 1280 + t * 8);
    SBAR(); of[1][i] = LD16(WS_OB + ofr + OT_B + i * 1024); SBAR(); }
  ARRIVE_NW(2);
#pragma unroll
  for (int t = 0; t < 5; t++) { ca[0][t] *= gt0; ca[1][t] *= gt1; }
  SBAR();
  STAMP(10);
#pragma unroll
  for (int u = 0; u < 2; u++)
#pragma unroll
    for (int i = 0; i < 5; i++) {
      if (__builtin_expect(missing(of[u][i]), 0)) {
#pragma unroll 1
        for (unsigned spin = 0; spin < SPIN_CAP; ++spin) {
          asm volatile("" ::: "memory");
#pragma unroll
          for (int j = u * 5 + i; j < 10; j++) of[j / 5][j % 5] = LD16(WS_OB + ofr + (j / 5) * OT_B + (j % 5) * 1024);
          if (!missing(of[u][i])) break;
          __builtin_amdgcn_s_sleep(1);
        }
      }
#pragma unroll
      for (int t = 0; t < 5; t++) cb[u][t] = mfma16(wf[i][t], of[u][i], cb[u][t]);
      SBAR();
    }
  {
    LAS f32x4* xb = (LAS f32x4*)(lp + (tp ? L_WO : 0)) + lane;
    f32x4 v[10];
#pragma unroll
    for (int it = 0; it < 10; it++) { const float gg = 1.0f - (it < 5 ? gt0 : gt1); v[it] = cb[it / 5][it % 5] * gg + ca[it / 5][it % 5]; }
    if (tp) AWAIT(2, 8);
#pragma unroll
    for (int it = 0; it < 10; it++) if (((it * 13) >> 5) != kq) xb[(it * 4 + kq) * 64] = v[it];
    BAR_L();
    u16* op = out + (size_t)R0 * DM + C0 + g * 20;
#pragma unroll
    for (int it = 0; it < 10; it++) if (((it * 13) >> 5) == kq) { const int o_ = (it * 13) >> 5;
      const f32x4 z = (v[it] + xb[(it * 4 + ((o_ + 1) & 3)) * 64]) + (xb[(it * 4 + ((o_ + 2) & 3)) * 64] + xb[(it * 4 + ((o_ + 3) & 3)) * 64]);
      *(GAS u32x2*)(op + (it / 5) * 16 * DM + (it % 5) * 4) = (u32x2){pk(z[0], z[1]), pk(z[2], z[3])}; }
  }
  STAMP(11);
}
}

extern "C" void kernel_launch(void* const* d_in, const int* in_sizes, int n_in, void* d_out, int out_size, void* d_ws, size_t ws_size, hipStream_t stream) {
  using namespace gm;
  fused<<<256, 512, 0, stream>>>((const u16*)d_in[0], (const u16*)d_in[1], (const u16*)d_in[2], (const u16*)d_in[3], (const u16*)d_in[5], (const u16*)d_in[6],
                                 (const u16*)d_in[7], (const u16*)d_in[8], (const u16*)d_in[9], (u16*)d_out, (char*)d_ws);
}
```

```cpp
#include <hip/hip_runtime.h>
#include <hip/hip_bf16.h>
#include <stdint.h>

namespace gm {
typedef float f32x16 __attribute__((ext_vector_type(16)));
typedef float f32x4 __attribute__((ext_vector_type(4)));
typedef float f32x2 __attribute__((ext_vector_type(2)));
typedef short s16x4 __attribute__((ext_vector_type(4)));
typedef __bf16 b16x2 __attribute__((ext_vector_type(2)));
typedef __bf16 b16x8 __attribute__((ext_vector_type(8)));
typedef unsigned u32x4 __attribute__((ext_vector_type(4)));
typedef unsigned u32x2 __attribute__((ext_vector_type(2)));
typedef unsigned long long u64;
typedef unsigned short u16;
#define DI __device__ __forceinline__
#define LAS __attribute__((address_space(3)))
#define GAS __attribute__((address_space(1)))
typedef GAS u64 gu64;

constexpr int DM = 640;
constexpr unsigned WS_EPOCH = 0, WS_FKV = 2048, WS_FKVR = 4096, WS_FQ = 8192, WS_FPART = 16384, WS_FOA = 24576, WS_G = 57344;
constexpr unsigned WS_KVR = 8912896, KVR_B = 10240, WS_KV = 131072, WS_QX = 327680, QX_B = 10240, WS_PART = 1703936, PART_B = 15360;
constexpr unsigned WS_OA = 3670016, WS_OB = 6291456, WS_END = 8994816, OT_B = 20480;
constexpr int VB = 352, TILE_B = 1280 + 4 * VB, CH_B = TILE_B * 8, STEP_B = 2 * CH_B;
constexpr int STAGE_B = 36864, ST_W1 = 10240, ST_X = 20480;
constexpr int L_BUF = 43008, L_P0W = 0, L_P0X0 = 25600, L_P0X1 = 153600, L_RED = 158720, L_RED1 = 40960;
constexpr int L_HO = 86016, L_QL = 96256, L_VIM = 96256, L_PHK = 116736, L_WPHI = 158592, L_ZERO = 162816, L_CNT = 163712, L_TOTAL = 163840;
constexpr int L_WO = 43008;
constexpr float LOG2E = 1.4426950408889634f;
constexpr float QSCALE = 0.22360679774997896f * LOG2E;
constexpr unsigned SPIN_CAP = 20000, LSPIN_CAP = 400000;

DI unsigned pk(float a, float b) { f32x2 v = {a, b}; b16x2 r = __builtin_convertvector(v, b16x2); return __builtin_bit_cast(unsigned, r); }
DI f32x16 mfma32(u32x4 a, u32x4 b, f32x16 c) { return __builtin_amdgcn_mfma_f32_32x32x16_bf16(__builtin_bit_cast(b16x8, a), __builtin_bit_cast(b16x8, b), c, 0, 0, 0); }
DI f32x16 mfma32k8(u32x2 a, u32x2 b, f32x16 c) { return __builtin_amdgcn_mfma_f32_32x32x8bf16_1k(__builtin_bit_cast(s16x4, a), __builtin_bit_cast(s16x4, b), c, 0, 0, 0); }
DI f32x4 mfma16(u32x4 a, u32x4 b, f32x4 c) { return __builtin_amdgcn_mfma_f32_16x16x32_bf16(__builtin_bit_cast(b16x8, a), __builtin_bit_cast(b16x8, b), c, 0, 0, 0); }
DI u32x2 trd(const LAS char* p) { return __builtin_bit_cast(u32x2, __builtin_amdgcn_ds_read_tr16_b64_v4i16((LAS s16x4*)p)); }
DI u32x4 trd2(const LAS char* p0, const LAS char* p1) { u32x2 a = trd(p0), b = trd(p1); return (u32x4){a.x, a.y, b.x, b.y}; }
DI u32x4 ldsr16(const LAS char* p) { return *(const LAS u32x4*)p; }
DI u32x2 ldsr8(const LAS char* p) { return *(const LAS u32x2*)p; }
DI float ex2(float x) { return __builtin_amdgcn_exp2f(x); }
DI void dma16s(const void* base, unsigned voff, unsigned lds_base) {
  asm volatile("s_mov_b32 m0, %2\n\ts_nop 0\n\tglobal_load_lds_dwordx4 %0, %1" :: "v"(voff), "s"(base), "s"(lds_base) : "memory"); }
DI void dma16s_sc1(const void* base, unsigned voff, unsigned lds_base) {
  asm volatile("s_mov_b32 m0, %2\n\ts_nop 0\n\tglobal_load_lds_dwordx4 %0, %1 sc1" :: "v"(voff), "s"(base), "s"(lds_base) : "memory"); }
DI void dmag_open(unsigned lds_base) { asm volatile("s_mov_b32 m0, %0\n\ts_nop 0" :: "s"(lds_base) : "memory"); }
template <int J> DI void dmag(const void* base, unsigned voff) { asm volatile("global_load_lds_dwordx4 %0, %1 offset:%2" :: "v"(voff), "s"(base), "i"((J & 3) * 1024) : "memory"); }
template <int J> DI void dmag_sc1(const void* base, unsigned voff) { asm volatile("global_load_lds_dwordx4 %0, %1 offset:%2 sc1" :: "v"(voff), "s"(base), "i"((J & 3) * 1024) : "memory"); }
DI void dma4s(const void* base, unsigned voff, unsigned lds_base) {
  asm volatile("s_mov_b32 m0, %2\n\ts_nop 0\n\tglobal_load_lds_dword %0, %1" :: "v"(voff), "s"(base), "s"(lds_base) : "memory"); }
DI u32x4 ld16_async(const void* p) { u32x4 r; asm volatile("global_load_dwordx4 %0, %1, off" : "=v"(r) : "v"(p) : "memory"); return r; }
DI u32x2 ld8_sc1_async(const void* p) { u32x2 r; asm volatile("global_load_dwordx2 %0, %1, off sc1" : "=v"(r) : "v"(p) : "memory"); return r; }
#define BAR_L() asm volatile("s_waitcnt lgkmcnt(0)\n\ts_barrier" ::: "memory")
#define WAIT_BAR(N) asm volatile("s_waitcnt vmcnt(" #N ") lgkmcnt(0)\n\ts_barrier" ::: "memory")
#define WAIT_VM(N) asm volatile("s_waitcnt vmcnt(" #N ")" ::: "memory")
#define SBAR() __builtin_amdgcn_sched_barrier(0)
#define RLX_AGENT __ATOMIC_RELAXED, __HIP_MEMORY_SCOPE_AGENT
DI float half_swap_max(float m) { auto rr = __builtin_amdgcn_permlane32_swap(__float_as_uint(m), __float_as_uint(m), false, false); return fmaxf(__uint_as_float(rr[0]), __uint_as_float(rr[1])); }
DI float from_upper(float v) { auto rr = __builtin_amdgcn_permlane32_swap(__float_as_uint(v), __float_as_uint(v), false, false); return __uint_as_float(rr[1]); }

template <int NF>
DI bool flags_set(const char* ws, unsigned off, u64 tag, int lane) {
  gu64* f = (gu64*)(ws + off);
  bool ok = __hip_atomic_load(f + (NF < 64 ? (lane & (NF - 1)) : lane), RLX_AGENT) == tag;
  if (NF >= 128) ok &= __hip_atomic_load(f + 64 + lane, RLX_AGENT) == tag;
  if (NF >= 256) { ok &= __hip_atomic_load(f + 128 + lane, RLX_AGENT) == tag; ok &= __hip_atomic_load(f + 192 + lane, RLX_AGENT) == tag; }
  return ok;
}
template <int NF>
DI void wait_flags(const char* ws, unsigned off, u64 tag, int lane) {
#pragma unroll 1
  for (unsigned spin = 0; spin < SPIN_CAP; ++spin) {
    if (__all(flags_set<NF>(ws, off, tag, lane))) break;
    __builtin_amdgcn_s_sleep(2);
  }
  asm volatile("" ::: "memory");
}
DI void set_flag(char* ws, unsigned off, u64 tag, int lane) { if (lane == 0) __hip_atomic_store((gu64*)(ws + off), tag, RLX_AGENT); }
DI unsigned nz(unsigned v) { return v < 0xFFFFFFFEu ? v : 0xFFFFFFFEu; }
DI unsigned umax3(unsigned a, unsigned b, unsigned c) { unsigned m = a > b ? a : b; return m > c ? m : c; }
DI bool missing(u32x4 v) { return __any(umax3(umax3(v.x, v.y, v.z), v.w, 0u) == 0xFFFFFFFFu); }
#define ONES ((u32x4){0xFFFFFFFFu, 0xFFFFFFFFu, 0xFFFFFFFFu, 0xFFFFFFFFu})
#define ST16(v, off) __builtin_amdgcn_raw_buffer_store_b128((v), wsr, (off), 0, 16)
#define ST8(v, off) __builtin_amdgcn_raw_buffer_store_b64((v), wsr, (off), 0, 16)
#define LD16(off) __builtin_amdgcn_raw_buffer_load_b128(wsr, (off), 0, 16)
#define LD8(off) __builtin_amdgcn_raw_buffer_load_b64(wsr, (off), 0, 16)

#define STAMP(i) do { if ((i) == 13) pa_ = __builtin_amdgcn_s_memtime(); if ((i) == 7) pb_ = __builtin_amdgcn_s_memtime(); if ((i) == 11) { u64 x_ = (blockIdx.x == 37 && w == 1) ? (u64)((unsigned)(pb_ - pa_) / 25u) : 0ull; \
    while (__builtin_amdgcn_s_memrealtime() - tstart < 5000ull + x_) __builtin_amdgcn_s_sleep(2); } } while (0)

__global__ __launch_bounds__(512, 2) void fused(const u16* __restrict__ x, const u16* __restrict__ wqkv, const u16* __restrict__ wphi, const u16* __restrict__ woA,
                                                const u16* __restrict__ wq, const u16* __restrict__ wk, const u16* __restrict__ wv, const u16* __restrict__ woB,
                                                const u16* __restrict__ gatew, u16* __restrict__ out, char* ws) {
  __shared__ __attribute__((aligned(16))) char lds[L_TOTAL];
  const int tid = threadIdx.x, lane = tid & 63;
  const int w = __builtin_amdgcn_readfirstlane(tid >> 6) & 7;
  const int l15 = lane & 15, g = lane >> 4, q4 = l15 >> 2, p4 = lane & 3, r = lane & 31, h = lane >> 5;
  const int bx = blockIdx.x & 7, bi = blockIdx.x >> 3;
  const int rt = (bx & 1) * 8 + (bi & 7), hp = (bx >> 1) * 4 + ((bi >> 3) & 3), hq = hp >> 1, role = hp & 1;
  const int w3 = w & 3, w2 = w >> 2;
  LAS char* lp = (LAS char*)lds;
  const unsigned lds0 = (unsigned)(uintptr_t)lds;
  const __amdgpu_buffer_rsrc_t wsr = __builtin_amdgcn_make_buffer_rsrc(ws, 0, WS_END, 0x00020000);
  LAS unsigned* cnt = (LAS unsigned*)(lp + L_CNT);
#define ARRIVE_NW(c) do { asm volatile("" ::: "memory"); if (lane == 0) __hip_atomic_fetch_add(cnt + (c), 1u, __ATOMIC_RELAXED, __HIP_MEMORY_SCOPE_WORKGROUP); asm volatile("" ::: "memory"); } while (0)
#define PEEK(c) ((unsigned)__builtin_amdgcn_readfirstlane(__hip_atomic_load(cnt + (c), __ATOMIC_RELAXED, __HIP_MEMORY_SCOPE_WORKGROUP)))
#define AWAIT(c, n) do { _Pragma("unroll 1") for (unsigned sp_ = 0; sp_ < LSPIN_CAP; ++sp_) { if (PEEK(c) >= (unsigned)(n)) break; __builtin_amdgcn_s_sleep(1); } asm volatile("" ::: "memory"); } while (0)
#define POST(c, v) do { asm volatile("" ::: "memory"); __hip_atomic_store(cnt + (c) + w3, (unsigned)(v), __ATOMIC_RELAXED, __HIP_MEMORY_SCOPE_WORKGROUP); asm volatile("" ::: "memory"); } while (0)
#define PEEK4(c) __hip_atomic_load(cnt + (c) + p4, __ATOMIC_RELAXED, __HIP_MEMORY_SCOPE_WORKGROUP)
#define AWAIT4(c, n) do { _Pragma("unroll 1") for (unsigned sp_ = 0; sp_ < LSPIN_CAP; ++sp_) { if (__all(PEEK4(c) >= (unsigned)(n))) break; __builtin_amdgcn_s_sleep(1); } asm volatile("" ::: "memory"); } while (0)
  const u64 tstart = __builtin_amdgcn_s_memrealtime(); u64 pa_ = 0, pb_ = 0;
  STAMP(0);
  if (w == 0) *(LAS u32x4*)(lp + L_ZERO + lane * 16) = (u32x4){0u, 0u, 0u, 0u};

  const int T0 = rt * 128 + hq * 16;
  const int C0 = hq * 80;
  const int wlane = ((g >> 1) * 16 + (g & 1) * 4 + q4) * 160 + p4 * 40;
  u64 tag;
  unsigned o0[10];
#define KVP(s, slot, p) dma16s_sc1(ws + WS_KV + (s) * STEP_B + (p) * 1024, lane * 16, lds0 + (slot) * STEP_B + (p) * 1024)
#define KVL(s, slot) do { _Pragma("unroll") for (int j = 0; j < 11; j++) if (j < 10 || w3 < 2) KVP(s, slot, w3 + 4 * j); } while (0)
#define KW(s, slot, j) do { if ((j) == 4) KVP(s, slot, w * 5 + 4); else { if ((j) == 0) dmag_open(lds0 + (slot) * STEP_B + w * 5120); dmag_sc1<(j)>(ws + WS_KV + (s) * STEP_B + w * 5120, lane * 16); } } while (0)
#define KX(s, slot) do { if (w < 2) KVP(s, slot, 40 + w); } while (0)
  if (w2 == 0) {
    u32x4 gf0, gf1, gf2, gf3, gf4, wpc; u32x2 tch;
    u32x2 ep = ld8_sc1_async(ws + WS_EPOCH);
    const int lx = w3 < 3 ? L_P0X0 + w3 * 5120 : L_P0X1;
    { const u16* wsrc = (role ? wv : wk) + w3 * 3200;
#pragma unroll
      for (int j = 0; j < 5; j++) { const int n = j * 64 + lane, row = (n * 3277) >> 16, cc = n - row * 20; dma16s(x + T0 * DM + w3 * 160, row * 1280 + cc * 16, lds0 + lx + j * 1024); }
#pragma unroll
      for (int j = 0; j < 6; j++) dma16s(wsrc + j * 512, lane * 16, lds0 + L_P0W + w3 * 6400 + j * 1024);
      if (lane < 16) dma16s(wsrc + 6 * 512, lane * 16, lds0 + L_P0W + w3 * 6400 + 6 * 1024);
      const u16* gp = gatew + w3 * 160 + g * 8;
      gf0 = ld16_async(gp); gf1 = ld16_async(gp + 32); gf2 = ld16_async(gp + 64); gf3 = ld16_async(gp + 96); gf4 = ld16_async(gp + 128);
      wpc = ld16_async(wphi + hq * 1600 + (tid < 200 ? tid : 199) * 8);
      const char* tp = ws + (lane & 7) * 1114112;
      if (lane >= 8) tp = ws + (lane < 16 ? WS_FKV + blockIdx.x * 128 : WS_KVR + blockIdx.x * 320 + (lane & 3) * 96);
      if (w3 == 1) tp = (const char*)(woA + C0 + (lane & 7) * 51200);
      if (w3 == 2) tp = (const char*)(woB + C0 + (lane & 7) * 51200);
      if (w3 == 3) tp = (const char*)(out + (rt * 128 + role * 64) * DM + C0);
      tch = ld8_sc1_async(tp);
    }
    asm volatile("s_waitcnt vmcnt(1)" : "+v"(gf0), "+v"(gf1), "+v"(gf2), "+v"(gf3), "+v"(gf4), "+v"(wpc), "+v"(ep) :: "memory");
    tag = (((u64)(unsigned)__builtin_amdgcn_readfirstlane(ep.y) << 32) | (unsigned)__builtin_amdgcn_readfirstlane(ep.x)) + 1ull;
    STAMP(1);
    { const LAS char* wb = lp + L_P0W + (w3 * 160 + g * 8 + q4) * 40;
      const LAS char* xb = lp + lx + l15 * 320 + g * 16;
      const bool gl = l15 == 4;
      f32x4 c0 = {}, c1 = {};
#define P0STEP(s, GF) { const u32x4 b = ldsr16(xb + (s) * 64); c0 = mfma16(trd2(wb + p4 * 8 + (s) * 1280, wb + p4 * 8 + (s) * 1280 + 160), b, c0); \
        u32x4 a = trd2(wb + 32 + (s) * 1280, wb + 32 + (s) * 1280 + 160); if (gl) a = GF; c1 = mfma16(a, b, c1); }
      P0STEP(0, gf0) P0STEP(1, gf1) P0STEP(2, gf2) P0STEP(3, gf3) P0STEP(4, gf4)
#undef P0STEP
      *(LAS f32x4*)(lp + L_RED + tid * 16) = c0;
      if (lane < 32) *(LAS f32x4*)(lp + L_RED1 + (w3 * 32 + lane) * 16) = c1; }
    f32x16 acc[5];
#pragma unroll
    for (int b = 0; b < 5; b++) acc[b] = (f32x16){};
    const int wm = g & 1, whd = p4;
    const int wo0 = wm * ST_W1 + (8 * h + 4 * wm + q4) * 160 + whd * 40, wo1 = wm * ST_W1 + (8 * h + 4 * (wm ^ 1) + q4) * 160 + whd * 40;
    const int xo = ST_X + (w3 * 32 + r) * 128 + ((h ^ ((r >> 1) & 7)) * 16);
    u32x4 fr[4][5], xr[4];
#define FRD(s, j) fr[s][j] = trd2(sb + wo0 + (s) * 2560 + (j) * 8, sb + wo1 + (s) * 2560 + (j) * 8)
#define FMM(s, j) acc[j] = mfma32(fr[s][j], xr[s], acc[j])
#define KSTEP(s) { xr[s] = ldsr16(sb + (xo ^ ((s) * 32))); \
      _Pragma("unroll") for (int j = 0; j < 5; j++) { if (i > 0 || (s) >= 2) FMM((s) ^ 2, j); FRD(s, j); SBAR(); } }
    unsigned lnd = 1;
    BAR_L();
    STAMP(2);
#pragma unroll
    for (int i = 0; i < 10; i++) {
      if (__builtin_expect(!__all(lnd >= (unsigned)(i + 1)), 0)) AWAIT4(8, i + 1);
      asm volatile("" ::: "memory");
      const LAS char* sb = lp + L_BUF + ((i + 2) % 3) * STAGE_B;
      KSTEP(0)
      if (i == 1 && role) {
        const unsigned qb = WS_QX + (rt * 8 + hq) * QX_B + w3 * 1280 + lane * 16;
        ST16(ONES, qb); ST16(ONES, qb + 5120); ST16(ONES, lane < 32 ? qb + 1024 + (lane >> 4) * (5120 - 256) : 0x7FFF0000u); }
      if (i == 4) { asm volatile("s_waitcnt vmcnt(0)" : "+v"(tch) :: "memory"); if (role) set_flag(ws, WS_FQ + ((rt * 8 + hq) * 4 + w3) * 8, tag, lane);
                    if (tid < 200) *(LAS u32x4*)(lp + L_WPHI + tid * 16) = wpc; }
      KSTEP(1) KSTEP(2)
      lnd = PEEK4(i < 9 ? 8 : 12);
      KSTEP(3)
      POST(12, i + 1);
    }
#pragma unroll
    for (int j = 0; j < 10; j++) FMM(2 + j / 5, j % 5);
#undef FRD
#undef FMM
#undef KSTEP
    STAMP(3);
#define PK2(t, i, sc) pk(acc[t][i] * (sc), acc[t][(i) + 1] * (sc))
#define ROW(i, sc) const u32x4 ra = {PK2(0, i, sc), PK2(0, (i) + 2, sc), PK2(1, i, sc), PK2(1, (i) + 2, sc)}, rb = {PK2(2, i, sc), PK2(2, (i) + 2, sc), PK2(3, i, sc), PK2(3, (i) + 2, sc)}; const u32x2 rc = {PK2(4, i, sc), PK2(4, (i) + 2, sc)}
    { ROW(4, 1.0f);
      if (__builtin_expect(!__all(lnd >= 9u), 0)) AWAIT4(12, 9);
      asm volatile("" ::: "memory");
      *(LAS u32x4*)(lp + L_HO + tid * 16) = ra; *(LAS u32x4*)(lp + L_HO + 4096 + tid * 16) = rb; *(LAS u32x2*)(lp + L_HO + 8192 + tid * 8) = rc; }
    BAR_L();
    const int qfo = (h * 4 + w3) * 1280;
    if (role) {
      { ROW(8, QSCALE); const unsigned qb = WS_QX + (rt * 8 + hq) * QX_B + qfo;
        ST16(((u32x4){nz(ra.x), nz(ra.y), nz(ra.z), nz(ra.w)}), qb + r * 16); ST16(((u32x4){nz(rb.x), nz(rb.y), nz(rb.z), nz(rb.w)}), qb + 512 + r * 16); ST8(((u32x2){nz(rc.x), nz(rc.y)}), qb + 1024 + r * 8); }
      { ROW(12, QSCALE); LAS char* d = lp + L_QL + qfo;
        *(LAS u32x4*)(d + r * 16) = ra; *(LAS u32x4*)(d + 512 + r * 16) = rb; *(LAS u32x2*)(d + 1024 + r * 8) = rc; }
    } else { LAS char* d = lp + L_VIM + (h * 128 + w3 * 32 + r) * 40;
#pragma unroll
      for (int t = 0; t < 5; t++) { *(LAS u32x2*)(d + t * 8) = (u32x2){PK2(t, 8, 1.0f), PK2(t, 10, 1.0f)}; *(LAS u32x2*)(d + 10240 + t * 8) = (u32x2){PK2(t, 12, 1.0f), PK2(t, 14, 1.0f)}; } }
#pragma unroll
    for (int t = 0; t < 5; t++) { o0[2 * t] = PK2(t, 0, 1.0f); o0[2 * t + 1] = PK2(t, 2, 1.0f); }
#undef ROW
#undef PK2
  } else {
    u32x2 ep = ld8_sc1_async(ws + WS_EPOCH);
    const bool second = w3 >> 1;
    const unsigned rs = (second && role) ? 1280u : 3840u;
    const char* wsrc = (second ? (role ? (const char*)(wq + C0) : (const char*)(wqkv + 1280 + C0)) : (const char*)(wqkv + (role ? 0 : 640) + C0)) + (w3 & 1) * 32 * rs;
    const char* xsrc = (const char*)(x + (rt * 128 + w3 * 32) * DM);
    int rot = (bi & 7) + 3 * ((bi >> 3) & 3); rot = rot >= 10 ? rot - 10 : rot;
    unsigned so[5], xoff[2];
#pragma unroll
    for (int j = 0; j < 5; j++) { const int c = j * 64 + lane, rho = (c * 6554) >> 16, cc = c - rho * 10; so[j] = (rho ^ (second ? 4 : 0)) * rs + cc * 16; }
#pragma unroll
    for (int j = 0; j < 2; j++) { const int row = j * 8 + (lane >> 3); xoff[j] = row * 1280 + (((lane & 7) ^ ((row >> 1) & 7)) * 16); }
    int bsel = 2, kc = rot;
    auto stage = [&](int n) {
      const unsigned lb = lds0 + L_BUF + bsel * STAGE_B;
      const char* bw = wsrc + kc * 64 * rs; const char* bxp = xsrc + kc * 128;
#pragma unroll
      for (int j = 0; j < 5; j++) { dma16s(bw, so[j], lb + (w3 * 5 + j) * 1024); if (j < 4) dma16s(bxp + (j >> 1) * 16 * 1280, xoff[j & 1], lb + ST_X + (w3 * 4 + j) * 1024);
        if (j == 1 && n > 0) { WAIT_VM(4); POST(8, n); } }
      bsel = bsel == 2 ? 0 : bsel + 1; kc = kc == 9 ? 0 : kc + 1;
    };
    stage(0); stage(0);
    asm volatile("s_waitcnt vmcnt(9)\n\ts_barrier" : "+v"(ep) :: "memory");
    tag = (((u64)(unsigned)__builtin_amdgcn_readfirstlane(ep.y) << 32) | (unsigned)__builtin_amdgcn_readfirstlane(ep.x)) + 1ull;
    STAMP(1); STAMP(2);
#pragma unroll 1
    for (int i = 0; i < 8; i++) {
      if (i >= 1) AWAIT4(12, i);
      stage(i + 2);
      if (i == 0 && w3 == 0) {
        const unsigned tb = WS_KV + (T0 >> 5) * TILE_B; const int s16 = hq & 1;
        if (!role) {
          const LAS f32x4* rp = (const LAS f32x4*)(lp + (h ? L_RED1 : L_RED)) + (h ? (g & 1) * 16 : g * 32) + l15;
          const int st = h ? 32 : 64, o2 = h ? 0 : 16;
          const f32x4 a = (rp[0] + rp[st]) + (rp[2 * st] + rp[3 * st]), b = (rp[o2] + rp[st + o2]) + (rp[2 * st + o2] + rp[3 * st + o2]);
          const u32x4 v = {pk(a[0], a[1]), pk(a[2], a[3]), pk(b[0], b[1]), pk(b[2], b[3])};
          if (lane < 32) ST16(v, tb + (g * 32 + s16 * 16 + l15) * 16);
          else if (lane < 48) ST8(((u32x2){v.x, v.y}), tb + 1024 + (s16 * 16 + l15) * 8);
          else { const float gv = 1.0f / (1.0f + __expf(-a[0])); __builtin_amdgcn_raw_buffer_store_b32(__float_as_uint(gv), wsr, WS_G + (T0 + l15) * 4, 0, 16); }
        } else if (lane < 44) {
          const int hh = lane >= 22, d = lane - hh * 22, dd = d < 20 ? d : 0, st = dd >= 16 ? 128 : 256;
          const LAS float* rp = (const LAS float*)(lp + (dd >= 16 ? L_RED1 : L_RED)) + (((dd & 15) >> 2) * 16 + 4 * hh) * 4 + (dd & 3);
          float e[8];
#pragma unroll
          for (int j = 0; j < 8; j++) { const int tk = (8 * (j >> 2) + (j & 3)) * 4; e[j] = (rp[tk] + rp[tk + st]) + (rp[tk + 2 * st] + rp[tk + 3 * st]); }
          u32x4 v = {pk(e[0], e[1]), pk(e[2], e[3]), pk(e[4], e[5]), pk(e[6], e[7])};
          if (d == 20) v = (u32x4){0x3F803F80u, 0x3F803F80u, 0x3F803F80u, 0x3F803F80u};
          if (d == 21) v = (u32x4){0u, 0u, 0u, 0u};
          ST16(v, tb + 1280 + (s16 * 2 + hh) * VB + d * 16);
        }
      }
      if (i == 1 && w3 == 0) set_flag(ws, WS_FKV + blockIdx.x * 8, tag, lane);
    }
    u64 fa, fb, fc, fd, fe;
    { gu64* f = (gu64*)(ws + WS_FKV) + lane;
      fa = __hip_atomic_load(f, RLX_AGENT); fb = __hip_atomic_load(f + 64, RLX_AGENT); fc = __hip_atomic_load(f + 128, RLX_AGENT); fd = __hip_atomic_load(f + 192, RLX_AGENT);
      fe = __hip_atomic_load((gu64*)(ws + WS_FQ) + (rt * 8 + hq) * 4 + p4, RLX_AGENT); }
    WAIT_VM(5); POST(8, 10);
    asm volatile("" : "+v"(fa), "+v"(fb), "+v"(fc), "+v"(fd), "+v"(fe));
    const bool kvok = __all((fa == tag) & (fb == tag) & (fc == tag) & (fd == tag) & (fe == tag));
    asm volatile("" ::: "memory");
    if (!kvok) { wait_flags<256>(ws, WS_FKV, tag, lane); wait_flags<4>(ws, WS_FQ + (rt * 8 + hq) * 32, tag, lane); }
    KVL(0, 0);
    if (blockIdx.x == 0 && w3 == 0) set_flag(ws, WS_EPOCH, tag, lane);
    STAMP(3);
    BAR_L();
    const int ht = tid - 256;
    const u32x4 a = ldsr16(lp + L_HO + ht * 16), b = ldsr16(lp + L_HO + 4096 + ht * 16); const u32x2 c = ldsr8(lp + L_HO + 8192 + ht * 8);
    o0[0] = a.x; o0[1] = a.y; o0[2] = a.z; o0[3] = a.w; o0[4] = b.x; o0[5] = b.y; o0[6] = b.z; o0[7] = b.w; o0[8] = c.x; o0[9] = c.y;
  }
  STAMP(4);
  u32x4 ph[5];
  { const LAS char* pb = ((p4 & 1) == h) ? lp + L_WPHI + (2 * w2 + h) * 800 + q4 * 40 + (2 * (g & 1) + (p4 >> 1)) * 8 : lp + L_ZERO;
    const u32x4 b0 = {o0[0], o0[1], o0[2], o0[3]}, b1 = {o0[4], o0[5], o0[6], o0[7]}, b2 = {o0[8], o0[9], 0u, 0u};
    const u32x2 c0 = trd(pb + 640), c1 = trd(pb + 672);
    f32x16 P0 = {}, P1 = {};
    P0 = mfma32(trd2(pb, pb + 160), b0, P0); P1 = mfma32(trd2(pb + 32, pb + 192), b0, P1);
    P0 = mfma32(trd2(pb + 320, pb + 480), b1, P0); P1 = mfma32(trd2(pb + 352, pb + 512), b1, P1);
    P0 = mfma32((u32x4){c0.x, c0.y, 0u, 0u}, b2, P0); P1 = mfma32((u32x4){c1.x, c1.y, 0u, 0u}, b2, P1);
    KX(1, 1); KW(1, 1, 0); SBAR();
    f32x2 p[10];
#pragma unroll
    for (int f = 0; f < 10; f++) p[f] = f < 8 ? (f32x2){P0[(2 * f) & 15], P0[(2 * f + 1) & 15]} : (f32x2){P1[(2 * f) & 3], P1[(2 * f + 1) & 3]};
    float mx = fmaxf(p[0][0], p[0][1]), mn = fminf(p[0][0], p[0][1]);
#pragma unroll
    for (int f = 1; f < 10; f++) { mx = fmaxf(fmaxf(mx, p[f][0]), p[f][1]); mn = fminf(fminf(mn, p[f][0]), p[f][1]); }
    const f32x2 ma = {-mx * LOG2E, -mx * LOG2E}, mb = {mn * LOG2E, mn * LOG2E}, ca_ = {LOG2E, LOG2E}, cb_ = {-LOG2E, -LOG2E};
    f32x2 ea[10], eb[10], sa = {0.f, 0.f}, sb = {0.f, 0.f};
#pragma unroll
    for (int f = 0; f < 10; f++) { if (f % 2 == 0 && f < 8) { SBAR(); if (f == 0) KW(1, 1, 1); if (f == 2) KW(1, 1, 2); if (f == 4) KW(1, 1, 3); if (f == 6) KW(1, 1, 4); SBAR(); }
      const f32x2 xa = __builtin_elementwise_fma(p[f], ca_, ma), xb = __builtin_elementwise_fma(p[f], cb_, mb);
      ea[f] = (f32x2){ex2(xa[0]), ex2(xa[1])}; eb[f] = (f32x2){ex2(xb[0]), ex2(xb[1])}; sa += ea[f]; sb += eb[f]; }
    const float ia = __builtin_amdgcn_rcpf(sa[0] + sa[1]), ib = __builtin_amdgcn_rcpf(sb[0] + sb[1]);
    unsigned o[20];
#pragma unroll
    for (int f = 0; f < 10; f++) { const f32x2 ya = ea[f] * ia, yb = eb[f] * ib; o[f] = pk(ya[0], ya[1]); o[10 + f] = pk(yb[0], yb[1]); }
#pragma unroll
    for (int c = 0; c < 5; c++) ph[c] = (u32x4){o[4 * c], o[4 * c + 1], o[4 * c + 2], o[4 * c + 3]};
    asm volatile("" : "+v"(ph[0]), "+v"(ph[1]), "+v"(ph[2]), "+v"(ph[3]), "+v"(ph[4]));
  }
  u32x4 q1A, q1B; u32x2 q2A, q2B;
  if (role) {
    WAIT_BAR(0);
    STAMP(5);
    const LAS char* qa = lp + L_QL + w3 * 1280;
    q1A = ldsr16(qa + lane * 16); q1B = ldsr16(qa + 5120 + lane * 16); q2A = ldsr8(qa + 1024 + r * 8); q2B = ldsr8(qa + 6144 + r * 8);
    ARRIVE_NW(0);
  } else {
    const unsigned qb = WS_QX + (rt * 8 + hq) * QX_B + w3 * 1280;
    q1A = LD16(qb + lane * 16); q1B = LD16(qb + 5120 + lane * 16); q2A = LD8(qb + 1024 + r * 8); q2B = LD8(qb + 6144 + r * 8);
    { LAS char* d = lp + L_PHK + ((2 * w2 + h) * 128 + w3 * 32 + r) * 80;
#pragma unroll
      for (int c = 0; c < 5; c++) *(LAS u32x4*)(d + c * 16) = ph[c]; }
    WAIT_BAR(4);
    STAMP(5);
#pragma unroll 1
    for (unsigned spin = 0; spin < SPIN_CAP; ++spin) {
      if (!(missing(q1A) | missing(q1B) | missing((u32x4){q2A.x, q2A.y, q2B.x, q2B.y}))) break;
      __builtin_amdgcn_s_sleep(1);
      asm volatile("" ::: "memory");
      q1A = LD16(qb + lane * 16); q1B = LD16(qb + 5120 + lane * 16); q2A = LD8(qb + 1024 + r * 8); q2B = LD8(qb + 6144 + r * 8);
    }
  }
  STAMP(6);
  const int vr = r <= 21 ? r : 21;
  const int ok1 = lane * 16, ok2 = h ? (1280 + 20 * 16) : (1024 + r * 8), ov = 1280 + h * VB + vr * 16;
  const f32x16 Z = {};
  if (h) { q2A = (u32x2){0u, 0u}; q2B = (u32x2){0u, 0u}; }
  { const LAS char* t0 = lp;
    const u32x4 k1 = ldsr16(t0 + ok1); const u32x2 k2 = ldsr8(t0 + ok2);
    f32x16 sa = mfma32(k1, q1A, Z); sa = mfma32k8(k2, q2A, sa);
    f32x16 sb = mfma32(k1, q1B, Z); sb = mfma32k8(k2, q2B, sb);
    float ma = sa[0], mb = sb[0];
#pragma unroll
    for (int i = 1; i < 16; i++) { ma = fmaxf(ma, sa[i]); mb = fmaxf(mb, sb[i]); }
    ma = half_swap_max(ma); mb = half_swap_max(mb);
    const float mah = __uint_as_float(__float_as_uint(ma) & 0xFFFF0000u), mbh = __uint_as_float(__float_as_uint(mb) & 0xFFFF0000u);
    if (h) { q2A.x = pk(-mah, mah - ma); q2B.x = pk(-mbh, mbh - mb); } }
  f32x16 OA = {}, OB = {}, SA, SB;
  u32x4 k1, v1, v2, v2n, pA1, pA2, pB1, pB2; u32x2 k2;
  float e0, e1, e2, e3;
  const LAS char* c0p = lp + w2 * CH_B + ok1; const LAS char* c1p = lp + w2 * CH_B + ok2; const LAS char* c2p = lp + w2 * CH_B + ov;
  k1 = ldsr16(c0p); k2 = ldsr8(c1p);
  SA = mfma32(k1, q1A, Z); SA = mfma32k8(k2, q2A, SA);
  v1 = ldsr16(c2p); v2 = v1;
  e0 = e1 = e2 = e3 = 0.f; pB2 = (u32x4){0u, 0u, 0u, 0u};
#define G(MF, LD, C0_, C1_, X, S_, i) MF; LD; SBAR(); C0_; C1_; SBAR(); X##0 = ex2(S_[i]); X##1 = ex2(S_[i + 1]); X##2 = ex2(S_[i + 2]); X##3 = ex2(S_[i + 3]); asm volatile("" : "+v"(X##0), "+v"(X##1), "+v"(X##2), "+v"(X##3)); SBAR();
#define TILE(TB, TN, V2, H0, H1, H2) { float a0, a1, a2, a3; \
    G(SB = mfma32(k1, q1B, Z),    k1 = ldsr16(c0p + (TN)), pB2[2] = pk(e0, e1), pB2[3] = pk(e2, e3), a, SA, 0) \
    G(OB = mfma32(v2, pB2, OB),   V2,                      pA1[0] = pk(a0, a1), pA1[1] = pk(a2, a3), e, SA, 4) \
    G(SB = mfma32k8(k2, q2B, SB), k2 = ldsr8(c1p + (TN)),  pA1[2] = pk(e0, e1), pA1[3] = pk(e2, e3), a, SA, 8) \
    G(OA = mfma32(v1, pA1, OA),   H0,                      pA2[0] = pk(a0, a1), pA2[1] = pk(a2, a3), e, SA, 12) \
    G(SA = mfma32(k1, q1A, Z),    H1,                      pA2[2] = pk(e0, e1), pA2[3] = pk(e2, e3), a, SB, 0) \
    G(OA = mfma32(v2, pA2, OA),   H2,                      pB1[0] = pk(a0, a1), pB1[1] = pk(a2, a3), e, SB, 4) \
    G(SA = mfma32k8(k2, q2A, SA), (void)0,                 pB1[2] = pk(e0, e1), pB1[3] = pk(e2, e3), a, SB, 8) \
    G(OB = mfma32(v1, pB1, OB),   v1 = ldsr16(c2p + (TN)), pB2[0] = pk(a0, a1), pB2[1] = pk(a2, a3), e, SB, 12) }
#define NOP (void)0
#define TL(sl, t, H0, H1, H2) TILE((sl) * STEP_B + (t) * TILE_B, (sl) * STEP_B + ((t) + 1) * TILE_B, v2 = ldsr16(c2p + (sl) * STEP_B + (t) * TILE_B + 2 * VB), H0, H1, H2)
#define TL6(sl, H0, H1) TL(sl, 6, H0, H1, v2n = ldsr16(c2p + (sl) * STEP_B + 7 * TILE_B + 2 * VB))
#define TL7(sl, sn, H0, H1, H2) TILE((sl) * STEP_B + 7 * TILE_B, (sn) * STEP_B, v2 = v2n, H0, H1, H2)
  unsigned woff[5];
#pragma unroll
  for (int j = 0; j < 5; j++) { const int c = j * 64 + lane, rho = (c * 6554) >> 16, cc = c - rho * 10; const int k = (rho & ~12) | ((rho & 4) << 1) | ((rho & 8) >> 1); woff[j] = k * 1280 + cc * 16 + (4 - j) * 1024; }
#define WOG(wo, gr, j, dst) do { if ((j) == 4) WOP(wo, gr, 4, dst); else { if ((j) == 0) dmag_open(lds0 + (dst)); dmag<(j)>((wo) + C0 + (gr) * 32 * 640 - 2048, woff[j]); } } while (0)
#define WOP(wo, gr, j, dst) dma16s((wo) + C0 + (gr) * 32 * 640 - (4 - (j)) * 512, woff[j], lds0 + (dst) + (j) * 1024)
#define WA(gr, j) WOG(woA, gr, j, L_WO + (gr) * 5120)
  const u16* wxs = w < 4 ? woA + (16 + w3) * 32 * 640 : woB + (w < 7 ? 17 + w3 : 19) * 32 * 640;
  const int wxd = w < 4 ? L_WO + (16 + w3) * 5120 : 145408 + (w < 7 ? w3 : 2) * 5120;
#define WX(j) WOG(wxs, 0, j, wxd)
#define PRIO(n) __builtin_amdgcn_s_setprio(n)
  const unsigned fo = (rt * 8 + hq) * OT_B + lane * 16;
#define FILL(j) do { const int n_ = w * 3 + ((j) >> 1); ST16(ONES, (((j) & 1) ? WS_OB : WS_OA) + fo + (n_ < 20 ? n_ : 19) * 1024); } while (0)
#define CLR_VM() __builtin_amdgcn_s_waitcnt(0x0F70)
  const bool red = !role && w == 4;
  f32x4 part[16]; u64 fp0, fp1;
  auto red_load = [&]() {
    asm volatile("" : "+v"(fp0), "+v"(fp1));
    if (!__all((fp0 == tag) & (fp1 == tag))) wait_flags<128>(ws, WS_FPART + hq * 1024, tag, lane);
    asm volatile("" ::: "memory");
#pragma unroll
    for (int j = 0; j < 16; j++) part[j] = __builtin_bit_cast(f32x4, LD16(WS_PART + (hq * 16 + j) * PART_B + (rt * 60 + (lane < 60 ? lane : 59)) * 16));
  };
  auto red_store = [&]() {
    f32x4 a = ((part[0] + part[1]) + (part[2] + part[3])) + ((part[4] + part[5]) + (part[6] + part[7]));
    a += ((part[8] + part[9]) + (part[10] + part[11])) + ((part[12] + part[13]) + (part[14] + part[15]));
    const int hd = rt >> 2, u = (rt & 3) * 60 + lane, hi = u >= 160, fq = hi ? (u - 160) >> 3 : u >> 4, di = hi ? u & 7 : u & 15;
    if (lane < 60) ST8(((u32x2){pk(a[0], a[1]), pk(a[2], a[3])}), WS_KVR + hq * KVR_B + (((hd >> 1) * 2 + hi) * 5 + (fq >> 1)) * 512 + ((hd & 1) * 16 + di) * 16 + (fq & 1) * 8);
  };
  u32x4 ka[10]; u64 fkr;
  auto oa_load = [&]() {
    asm volatile("" : "+v"(fkr));
    if (!__all(fkr == tag)) wait_flags<16>(ws, WS_FKVR + hq * 128, tag, lane);
    asm volatile("" ::: "memory");
    const unsigned kb = (((r >> 2) & 1) == h) ? WS_KVR + hq * KVR_B + w2 * 5120 + (h * 16 + (r & 3) + 4 * (r >> 3)) * 16 : 0x7FFF0000u;
#pragma unroll
    for (int j = 0; j < 10; j++) ka[j] = LD16(kb + j * 512);
  };
  u32x4 ja[2][3], jb[2]; f32x4 jc[3];
  const LAS char* jab = lp + L_PHK + ((w >> 1) * 128 + g * 8 + q4) * 80 + p4 * 8;
  const LAS char* jbb = lp + L_VIM + ((w >> 1) * 128 + g * 8 + q4) * 40 + (w & 1) * 32 + p4 * 8;
  auto kv_rd = [&](int k0) {
#pragma unroll
    for (int ks = 0; ks < 2; ks++) { jb[ks] = trd2(jbb + (k0 + ks) * 1280, jbb + (k0 + ks) * 1280 + 160);
#pragma unroll
      for (int ft = 0; ft < 3; ft++) ja[ks][ft] = trd2(jab + (k0 + ks) * 2560 + ft * 32, jab + (k0 + ks) * 2560 + 320 + ft * 32); } };
  auto kv_mm = [&](bool first) {
#pragma unroll
    for (int ks = 0; ks < 2; ks++) { if ((w & 1) && l15 == 4) jb[ks] = (u32x4){0x3F803F80u, 0x3F803F80u, 0x3F803F80u, 0x3F803F80u};
#pragma unroll
      for (int ft = 0; ft < 3; ft++) jc[ft] = mfma16(ja[ks][ft], jb[ks], (first && !ks) ? (f32x4){0.f, 0.f, 0.f, 0.f} : jc[ft]); } };
  auto kv_st = [&]() { const int dt = w & 1; const unsigned pb = WS_PART + (hq * 16 + rt) * PART_B + (w >> 1) * 3840 + (dt ? 2560 + g * 128 : g * 256) + l15 * 16;
#pragma unroll
    for (int ft = 0; ft < 3; ft++) if ((!dt || l15 < 8) && ft * 4 + g < 10) ST16(__builtin_bit_cast(u32x4, jc[ft]), pb + ft * (dt ? 512 : 1024)); };
  f32x16 oa0, oa1; u32x4 z0, z1, z2;
  LAS char* otb = lp + L_WO + (8 + w) * 5120;
  auto oa_mm = [&](int s) { oa0 = mfma32(ka[s], ph[s], s ? oa0 : Z); oa1 = mfma32(ka[5 + s], ph[s], s ? oa1 : Z); };
  auto oa_norm = [&]() { const float inv = __builtin_amdgcn_rcpf(oa1[4] + 1e-6f);
#pragma unroll
    for (int t = 0; t < 5; t++) { const int k0 = h * 20 + 4 * t;
      const u32x2 v = t < 4 ? (u32x2){nz(pk(oa0[(4 * t) & 15] * inv, oa0[(4 * t + 1) & 15] * inv)), nz(pk(oa0[(4 * t + 2) & 15] * inv, oa0[(4 * t + 3) & 15] * inv))} : (u32x2){nz(pk(oa1[0] * inv, oa1[1] * inv)), nz(pk(oa1[2] * inv, oa1[3] * inv))};
      *(LAS u32x2*)(otb + (r >> 4) * 1280 + (k0 >> 3) * 256 + (k0 & 4) * 2 + l15 * 16) = v; }
    z0 = ldsr16(otb + lane * 16); z1 = ldsr16(otb + 1280 + lane * 16); z2 = ldsr16(otb + (r >> 4) * 1280 + 1024 + l15 * 16); };
  auto oa_st = [&]() { const unsigned oa = WS_OA + (rt * 8 + w3 * 2) * OT_B + (hq * 2 + w2) * 1280;
    ST16(z0, oa + lane * 16); ST16(z1, oa + OT_B + lane * 16); if (!h) ST16(z2, oa + (r >> 4) * OT_B + 1024 + l15 * 16); };
  unsigned cpk;
  PRIO(3);
  TL(0, 0, if (!role) kv_rd(0); else FILL(0), NOP, NOP)
  TL(0, 1, if (!role) { kv_mm(true); kv_rd(2); ARRIVE_NW(0); } else FILL(1), NOP, NOP)
  TL(0, 2, if (!role) kv_mm(false); else FILL(2), NOP, if (!role) kv_st())
  TL(0, 3, NOP, NOP, cpk = __hip_atomic_load(cnt, __ATOMIC_RELAXED, __HIP_MEMORY_SCOPE_WORKGROUP))
  STAMP(14);
  if ((unsigned)__builtin_amdgcn_readfirstlane(cpk) < 8u) AWAIT(0, 8);
  asm volatile("" ::: "memory");
  TL(0, 4, NOP, NOP, if (!role) { WAIT_VM(0); set_flag(ws, WS_FPART + ((hq * 16 + rt) * 8 + w) * 8, tag, lane); } else FILL(3))
  PRIO(2);
  TL(0, 5, KX(2, 2), NOP, KW(2, 2, 0)) TL6(0, KW(2, 2, 1), KW(2, 2, 2))
  TL7(0, 1, KW(2, 2, 3), NOP, KW(2, 2, 4))
  STAMP(15);
  TL(1, 0, if (role) FILL(4), NOP, if (red) { fp0 = __hip_atomic_load((gu64*)(ws + WS_FPART) + hq * 128 + lane, RLX_AGENT); fp1 = __hip_atomic_load((gu64*)(ws + WS_FPART) + hq * 128 + 64 + lane, RLX_AGENT); })
  TL(1, 1, if (w < 4) { const int row = (w & 1) * 32 + (lane >> 1);
                      dma4s((w & 2 ? woB : woA) + C0 + ((rt & 7) * 2 + role) * 40 * 640, (row < 40 ? row : 39) * 1280 + (lane & 1) * 156, lds0 + 157696); }, NOP, if (role) FILL(5))
  TL(1, 2, NOP, NOP, if (red) red_load())
  PRIO(1);
  TL(1, 3, NOP, NOP, NOP)
  TL(1, 4, NOP, CLR_VM(), if (red) red_store())
  TL(1, 5, NOP, NOP, NOP)
  PRIO(0);
  TL6(1, if (red) { WAIT_VM(0); set_flag(ws, WS_FKVR + (hq * 16 + rt) * 8, tag, lane); }, NOP)
  if (red) WAIT_BAR(1); else WAIT_BAR(0);
  STAMP(12);
  PRIO(3);
  if (role) set_flag(ws, WS_FOA + ((rt * 8 + hq) * 8 + w) * 8, tag, lane);
  u64 foa;
  TL7(1, 2, KX(3, 0), KW(3, 0, 0), KW(3, 0, 1))
  TL(2, 0, KW(3, 0, 2), KW(3, 0, 3), KW(3, 0, 4)) TL(2, 1, WA(w, 0), WA(w, 1), WA(w, 2))
  TL(2, 2, WA(w, 3), WA(w, 4), (foa = __hip_atomic_load((gu64*)(ws + WS_FOA) + rt * 64 + lane, RLX_AGENT), fkr = __hip_atomic_load((gu64*)(ws + WS_FKVR) + hq * 16 + l15, RLX_AGENT)))
  PRIO(2);
  TL(2, 3, NOP, NOP, NOP)
  auto chk = [&]() { asm volatile("" : "+v"(foa), "+v"(fkr));
    if (!__all(foa == tag)) wait_flags<64>(ws, WS_FOA + rt * 512, tag, lane);
    asm volatile("" ::: "memory"); if (role) oa_load(); };
  TL(2, 4, chk(), NOP, NOP)
  PRIO(1);
  TL(2, 5, NOP, NOP, if (role) oa_mm(0))
  PRIO(0);
  TL6(2, if (role) oa_mm(1), if (role) oa_mm(2))
  BAR_L();
  STAMP(13);
  PRIO(3);
  TL7(2, 0, if (role) oa_mm(3), { if (role) oa_mm(4); CLR_VM(); }, WX(0))
  TL(0, 0, if (role) oa_norm(), WX(1), WX(2))
  TL(0, 1, if (role) oa_st(), WX(3), WX(4))
  TL(0, 2, WA(8 + w, 0), WA(8 + w, 1), WA(8 + w, 2))
  u32x4 wbr[11];
  const char* wbs = (const char*)(woB + C0 + w * 32 * 640) - 4096;
#define WB2(j) do { wbr[j] = *(const u32x4*)(wbs + woff[j] + (j) * 1024); wbr[5 + (j)] = *(const u32x4*)(wbs + 8 * 32 * 1280 + woff[j] + (j) * 1024); } while (0)
  TL(0, 3, WA(8 + w, 3), WA(8 + w, 4), WB2(0))
  PRIO(2);
  TL(0, 4, WB2(1), WB2(2), WB2(3))
  const int w5 = w < 5 ? w : 4;
  const unsigned wo16 = (w5 == 0 ? woff[0] : w5 == 1 ? woff[1] : w5 == 2 ? woff[2] : w5 == 3 ? woff[3] : woff[4]) + w5 * 1024;
  TL(0, 5, WB2(4), NOP, wbr[10] = *(const u32x4*)((const char*)(woB + C0 + 16 * 32 * 640) - 4096 + wo16))
  PRIO(1);
  ARRIVE_NW(1);
  const int tp = w & 1, kq = w >> 1;
  const unsigned ofr = (rt * 8 + role * 4 + tp * 2) * OT_B + kq * 5120 + lane * 16;
  const int R0 = rt * 128 + role * 64 + tp * 32 + l15;
  u32x4 of[2][5]; float gt0, gt1;
#define OFL(u) do { _Pragma("unroll") for (int i = 0; i < 5; i++) of[u][i] = LD16(WS_OA + ofr + (u) * OT_B + i * 1024); } while (0)
  TL6(0, OFL(0), (gt0 = __uint_as_float(__builtin_amdgcn_raw_buffer_load_b32(wsr, WS_G + R0 * 4, 0, 16)), gt1 = __uint_as_float(__builtin_amdgcn_raw_buffer_load_b32(wsr, WS_G + R0 * 4 + 64, 0, 16))))
  PRIO(0);
  TL7(0, 0, OFL(1), NOP, NOP)
  pB2[2] = pk(e0, e1); pB2[3] = pk(e2, e3);
  OB = mfma32(v2, pB2, OB);
#undef TL
#undef TL6
#undef TL7
#undef TILE
#undef G
  STAMP(7);
  { LAS f32x4* xb = (LAS f32x4*)(lp + (w3 >> 1) * CH_B) + (w3 & 1) * (64 * 6);
#pragma unroll
    for (int i = 0; i < 12; i++) { auto rr = __builtin_amdgcn_permlane16_swap(__float_as_uint(OA[i]), __float_as_uint(OB[i]), false, false); OA[i] = __uint_as_float(rr[0]); OB[i] = __uint_as_float(rr[1]); }
    const f32x16 snd = w2 ? OA : OB;
    AWAIT(1, 8);
#pragma unroll
    for (int c = 0; c < 3; c++) xb[(w2 * 3 + c) * 64 + lane] = (f32x4){snd[4 * c], snd[4 * c + 1], snd[4 * c + 2], snd[4 * c + 3]};
    asm volatile("s_waitcnt vmcnt(23) lgkmcnt(0)\n\ts_barrier" ::: "memory");
    f32x16 O = w2 ? OB : OA;
#pragma unroll
    for (int c = 0; c < 3; c++) { const f32x4 t = xb[((w2 ^ 1) * 3 + c) * 64 + lane]; O[4 * c] += t[0]; O[4 * c + 1] += t[1]; O[4 * c + 2] += t[2]; O[4 * c + 3] += t[3]; }
    float l = from_upper(O[8]);
    float amax = 0.f;
#pragma unroll
    for (int i = 0; i < 12; i++) amax += fabsf(O[i]);
    const bool good = (l >= 0x1p-30f) && (l <= 0x1p100f) && (amax <= 0x1p126f);
    if (__builtin_expect(!__all(good), 0)) {
      u32x4 q1; u32x2 q2;
#pragma unroll
      for (int i = 0; i < 4; i++) { auto rr = __builtin_amdgcn_permlane16_swap(q1A[i], q1B[i], false, false); q1[i] = w2 ? rr[1] : rr[0]; }
#pragma unroll
      for (int i = 0; i < 2; i++) { auto rr = __builtin_amdgcn_permlane16_swap(q2A[i], q2B[i], false, false); q2[i] = h ? 0u : (w2 ? rr[1] : rr[0]); }
      float m = -INFINITY;
#pragma unroll 1
      for (int t = 0; t < 64; t++) {
        f32x16 s = mfma32(LD16(WS_KV + t * TILE_B + ok1), q1, Z); s = mfma32k8(LD8(WS_KV + t * TILE_B + ok2), q2, s);
#pragma unroll
        for (int i = 0; i < 16; i++) m = fmaxf(m, s[i]);
      }
      m = half_swap_max(m);
      O = Z;
#pragma unroll 1
      for (int t = 0; t < 64; t++) {
        f32x16 s = mfma32(LD16(WS_KV + t * TILE_B + ok1), q1, Z); s = mfma32k8(LD8(WS_KV + t * TILE_B + ok2), q2, s);
#pragma unroll
        for (int i = 0; i < 16; i++) s[i] = ex2(s[i] - m);
        O = mfma32(LD16(WS_KV + t * TILE_B + ov), (u32x4){pk(s[0], s[1]), pk(s[2], s[3]), pk(s[4], s[5]), pk(s[6], s[7])}, O);
        O = mfma32(LD16(WS_KV + t * TILE_B + ov + 2 * VB), (u32x4){pk(s[8], s[9]), pk(s[10], s[11]), pk(s[12], s[13]), pk(s[14], s[15])}, O);
      }
      l = from_upper(O[8]);
    }
    const float inv = __builtin_amdgcn_rcpf(l);
    LAS char* tb = lp + w2 * CH_B + 12288 + w3 * 1536;
#pragma unroll
    for (int c = 0; c < 3; c++) { const int k0 = (g & 1) * 20 + 8 * c + 4 * h;
      *(LAS u32x2*)(tb + ((c == 2 && h) ? 1280 + (g & 1) * 8 : (k0 >> 3) * 256 + (k0 & 4) * 2) + l15 * 16) = (u32x2){nz(pk(O[4 * c] * inv, O[4 * c + 1] * inv)), nz(pk(O[4 * c + 2] * inv, O[4 * c + 3] * inv))}; }
    const unsigned ob = WS_OB + (rt * 8 + w3 * 2 + w2) * OT_B + hp * 1280 + lane * 16;
    const u32x4 z0 = ldsr16(tb + lane * 16), z1 = ldsr16(tb + 1024 + l15 * 16);
    ST16(z0, ob); ST16(z1, lane < 16 ? ob + 1024 : 0x7FFF0000u);
  }
  STAMP(8);
  u32x4 wf[5][5];
#pragma unroll
  for (int i = 0; i < 5; i++) { const LAS char* wb = lp + L_WO + (kq * 5 + i) * 5120 + wlane;
#pragma unroll
    for (int t = 0; t < 5; t++) wf[i][t] = trd2(wb + t * 8, wb + 1280 + t * 8); }
  BAR_L();
  auto of_missing = [&]() { u32x4 mm = of[0][0];
#pragma unroll
    for (int j = 1; j < 10; j++) mm = __builtin_elementwise_max(mm, of[j / 5][j % 5]);
    return missing(mm); };
  if (__builtin_expect(of_missing(), 0)) {
#pragma unroll 1
    for (unsigned spin = 0; spin < SPIN_CAP; ++spin) {
      asm volatile("" ::: "memory");
      OFL(0); OFL(1);
      if (!of_missing()) break;
      __builtin_amdgcn_s_sleep(1);
    }
  }
  f32x4 ca[2][5], cb[2][5];
#pragma unroll
  for (int t = 0; t < 5; t++) { ca[0][t] = ca[1][t] = cb[0][t] = cb[1][t] = (f32x4){0.f, 0.f, 0.f, 0.f}; }
  { LAS char* d = lp + L_WO + w * 5120 + lane * 16;
#pragma unroll
    for (int i = 0; i < 5; i++) {
      *(LAS u32x4*)(d + i * 1024) = wbr[i]; *(LAS u32x4*)(d + 40960 + i * 1024) = wbr[5 + i]; SBAR();
#pragma unroll
      for (int t = 0; t < 5; t++) ca[0][t] = mfma16(wf[i][t], of[0][i], ca[0][t]);
      SBAR();
    }
    *(LAS u32x4*)(lp + L_WO + 16 * 5120 + w5 * 1024 + lane * 16) = wbr[10];
#pragma unroll
    for (int i = 0; i < 5; i++) {
#pragma unroll
      for (int t = 0; t < 5; t++) ca[1][t] = mfma16(wf[i][t], of[1][i], ca[1][t]);
      SBAR(); of[0][i] = LD16(WS_OB + ofr + i * 1024); SBAR();
    } }
  STAMP(9);
  BAR_L();
#pragma unroll
  for (int i = 0; i < 5; i++) { const int gr = kq * 5 + i; const LAS char* wb = lp + (gr < 17 ? L_WO + gr * 5120 : 145408 + (gr - 17) * 5120) + wlane;
#pragma unroll
    for (int t = 0; t < 5; t++) wf[i][t] = trd2(wb + t * 8, wb + 1280 + t * 8);
    SBAR(); of[1][i] = LD16(WS_OB + ofr + OT_B + i * 1024); SBAR(); }
  ARRIVE_NW(2);
#pragma unroll
  for (int t = 0; t < 5; t++) { ca[0][t] *= gt0; ca[1][t] *= gt1; }
  SBAR();
  STAMP(10);
#pragma unroll
  for (int u = 0; u < 2; u++)
#pragma unroll
    for (int i = 0; i < 5; i++) {
      if (__builtin_expect(missing(of[u][i]), 0)) {
#pragma unroll 1
        for (unsigned spin = 0; spin < SPIN_CAP; ++spin) {
          asm volatile("" ::: "memory");
#pragma unroll
          for (int j = u * 5 + i; j < 10; j++) of[j / 5][j % 5] = LD16(WS_OB + ofr + (j / 5) * OT_B + (j % 5) * 1024);
          if (!missing(of[u][i])) break;
          __builtin_amdgcn_s_sleep(1);
        }
      }
#pragma unroll
      for (int t = 0; t < 5; t++) cb[u][t] = mfma16(wf[i][t], of[u][i], cb[u][t]);
      SBAR();
    }
  {
    LAS f32x4* xb = (LAS f32x4*)(lp + (tp ? L_WO : 0)) + lane;
    f32x4 v[10];
#pragma unroll
    for (int it = 0; it < 10; it++) { const float gg = 1.0f - (it < 5 ? gt0 : gt1); v[it] = cb[it / 5][it % 5] * gg + ca[it / 5][it % 5]; }
    if (tp) AWAIT(2, 8);
#pragma unroll
    for (int it = 0; it < 10; it++) if (((it * 13) >> 5) != kq) xb[(it * 4 + kq) * 64] = v[it];
    BAR_L();
    u16* op = out + (size_t)R0 * DM + C0 + g * 20;
#pragma unroll
    for (int it = 0; it < 10; it++) if (((it * 13) >> 5) == kq) { const int o_ = (it * 13) >> 5;
      const f32x4 z = (v[it] + xb[(it * 4 + ((o_ + 1) & 3)) * 64]) + (xb[(it * 4 + ((o_ + 2) & 3)) * 64] + xb[(it * 4 + ((o_ + 3) & 3)) * 64]);
      *(u32x2*)(op + (it / 5) * 16 * DM + (it % 5) * 4) = (u32x2){pk(z[0], z[1]), pk(z[2], z[3])}; }
  }
  STAMP(11);
}
}

extern "C" void kernel_launch(void* const* d_in, const int* in_sizes, int n_in, void* d_out, int out_size, void* d_ws, size_t ws_size, hipStream_t stream) {
  using namespace gm;
  fused<<<256, 512, 0, stream>>>((const u16*)d_in[0], (const u16*)d_in[1], (const u16*)d_in[2], (const u16*)d_in[3], (const u16*)d_in[5], (const u16*)d_in[6],
                                 (const u16*)d_in[7], (const u16*)d_in[8], (const u16*)d_in[9], (u16*)d_out, (char*)d_ws);
}
```

```cpp
#include <hip/hip_runtime.h>
#include <hip/hip_bf16.h>
#include <stdint.h>

namespace gm {
typedef float f32x16 __attribute__((ext_vector_type(16)));
typedef float f32x4 __attribute__((ext_vector_type(4)));
typedef float f32x2 __attribute__((ext_vector_type(2)));
typedef short s16x4 __attribute__((ext_vector_type(4)));
typedef __bf16 b16x2 __attribute__((ext_vector_type(2)));
typedef __bf16 b16x8 __attribute__((ext_vector_type(8)));
typedef unsigned u32x4 __attribute__((ext_vector_type(4)));
typedef unsigned u32x2 __attribute__((ext_vector_type(2)));
typedef unsigned long long u64;
typedef unsigned short u16;
#define DI __device__ __forceinline__
#define LAS __attribute__((address_space(3)))
#define GAS __attribute__((address_space(1)))
typedef GAS u64 gu64;

constexpr int DM = 640;
constexpr unsigned WS_EPOCH = 0, WS_FKV = 2048, WS_FKVR = 4096, WS_FQ = 8192, WS_FPART = 16384, WS_FOA = 24576, WS_G = 57344;
constexpr unsigned WS_KVR = 8912896, KVR_B = 10240, WS_KV = 131072, WS_QX = 327680, QX_B = 10240, WS_PART = 1703936, PART_B = 15360;
constexpr unsigned WS_OA = 3670016, WS_OB = 6291456, WS_END = 8994816, OT_B = 20480;
constexpr int VB = 352, TILE_B = 1280 + 4 * VB, CH_B = TILE_B * 8, STEP_B = 2 * CH_B;
constexpr int STAGE_B = 36864, ST_W1 = 10240, ST_X = 20480;
constexpr int L_BUF = 43008, L_P0W = 0, L_P0X0 = 25600, L_P0X1 = 153600, L_RED = 158720, L_RED1 = 40960;
constexpr int L_HO = 86016, L_QL = 96256, L_VIM = 96256, L_PHK = 116736, L_WPHI = 158592, L_ZERO = 162816, L_CNT = 163712, L_TOTAL = 163840;
constexpr int L_WO = 43008;
constexpr float LOG2E = 1.4426950408889634f;
constexpr float QSCALE = 0.22360679774997896f * LOG2E;
constexpr unsigned SPIN_CAP = 20000, LSPIN_CAP = 400000;

DI unsigned pk(float a, float b) { f32x2 v = {a, b}; b16x2 r = __builtin_convertvector(v, b16x2); return __builtin_bit_cast(unsigned, r); }
DI f32x16 mfma32(u32x4 a, u32x4 b, f32x16 c) { return __builtin_amdgcn_mfma_f32_32x32x16_bf16(__builtin_bit_cast(b16x8, a), __builtin_bit_cast(b16x8, b), c, 0, 0, 0); }
DI f32x16 mfma32k8(u32x2 a, u32x2 b, f32x16 c) { return __builtin_amdgcn_mfma_f32_32x32x8bf16_1k(__builtin_bit_cast(s16x4, a), __builtin_bit_cast(s16x4, b), c, 0, 0, 0); }
DI f32x4 mfma16(u32x4 a, u32x4 b, f32x4 c) { return __builtin_amdgcn_mfma_f32_16x16x32_bf16(__builtin_bit_cast(b16x8, a), __builtin_bit_cast(b16x8, b), c, 0, 0, 0); }
DI u32x2 trd(const LAS char* p) { return __builtin_bit_cast(u32x2, __builtin_amdgcn_ds_read_tr16_b64_v4i16((LAS s16x4*)p)); }
DI u32x4 trd2(const LAS char* p0, const LAS char* p1) { u32x2 a = trd(p0), b = trd(p1); return (u32x4){a.x, a.y, b.x, b.y}; }
DI u32x4 ldsr16(const LAS char* p) { return *(const LAS u32x4*)p; }
DI u32x2 ldsr8(const LAS char* p) { return *(const LAS u32x2*)p; }
DI float ex2(float x) { return __builtin_amdgcn_exp2f(x); }
DI void dma16s(const void* base, unsigned voff, unsigned lds_base) {
  asm volatile("s_mov_b32 m0, %2\n\ts_nop 0\n\tglobal_load_lds_dwordx4 %0, %1" :: "v"(voff), "s"(base), "s"(lds_base) : "memory"); }
DI void dma16s_sc1(const void* base, unsigned voff, unsigned lds_base) {
  asm volatile("s_mov_b32 m0, %2\n\ts_nop 0\n\tglobal_load_lds_dwordx4 %0, %1 sc1" :: "v"(voff), "s"(base), "s"(lds_base) : "memory"); }
DI void dmag_open(unsigned lds_base) { asm volatile("s_mov_b32 m0, %0\n\ts_nop 0" :: "s"(lds_base) : "memory"); }
template <int J> DI void dmag(const void* base, unsigned voff) { asm volatile("global_load_lds_dwordx4 %0, %1 offset:%2" :: "v"(voff), "s"(base), "i"((J & 3) * 1024) : "memory"); }
template <int J> DI void dmag_sc1(const void* base, unsigned voff) { asm volatile("global_load_lds_dwordx4 %0, %1 offset:%2 sc1" :: "v"(voff), "s"(base), "i"((J & 3) * 1024) : "memory"); }
DI void dma4s(const void* base, unsigned voff, unsigned lds_base) {
  asm volatile("s_mov_b32 m0, %2\n\ts_nop 0\n\tglobal_load_lds_dword %0, %1" :: "v"(voff), "s"(base), "s"(lds_base) : "memory"); }
DI u32x4 ld16_async(const void* p) { u32x4 r; asm volatile("global_load_dwordx4 %0, %1, off" : "=v"(r) : "v"(p) : "memory"); return r; }
DI u32x2 ld8_sc1_async(const void* p) { u32x2 r; asm volatile("global_load_dwordx2 %0, %1, off sc1" : "=v"(r) : "v"(p) : "memory"); return r; }
#define BAR_L() asm volatile("s_waitcnt lgkmcnt(0)\n\ts_barrier" ::: "memory")
#define WAIT_BAR(N) asm volatile("s_waitcnt vmcnt(" #N ") lgkmcnt(0)\n\ts_barrier" ::: "memory")
#define WAIT_VM(N) asm volatile("s_waitcnt vmcnt(" #N ")" ::: "memory")
#define SBAR() __builtin_amdgcn_sched_barrier(0)
#define RLX_AGENT __ATOMIC_RELAXED, __HIP_MEMORY_SCOPE_AGENT
DI float half_swap_max(float m) { auto rr = __builtin_amdgcn_permlane32_swap(__float_as_uint(m), __float_as_uint(m), false, false); return fmaxf(__uint_as_float(rr[0]), __uint_as_float(rr[1])); }
DI float from_upper(float v) { auto rr = __builtin_amdgcn_permlane32_swap(__float_as_uint(v), __float_as_uint(v), false, false); return __uint_as_float(rr[1]); }

template <int NF>
DI bool flags_set(const char* ws, unsigned off, u64 tag, int lane) {
  gu64* f = (gu64*)(ws + off);
  bool ok = __hip_atomic_load(f + (NF < 64 ? (lane & (NF - 1)) : lane), RLX_AGENT) == tag;
  if (NF >= 128) ok &= __hip_atomic_load(f + 64 + lane, RLX_AGENT) == tag;
  if (NF >= 256) { ok &= __hip_atomic_load(f + 128 + lane, RLX_AGENT) == tag; ok &= __hip_atomic_load(f + 192 + lane, RLX_AGENT) == tag; }
  return ok;
}
template <int NF>
DI void wait_flags(const char* ws, unsigned off, u64 tag, int lane) {
#pragma unroll 1
  for (unsigned spin = 0; spin < SPIN_CAP; ++spin) {
    if (__all(flags_set<NF>(ws, off, tag, lane))) break;
    __builtin_amdgcn_s_sleep(2);
  }
  asm volatile("" ::: "memory");
}
DI void set_flag(char* ws, unsigned off, u64 tag, int lane) { if (lane == 0) __hip_atomic_store((gu64*)(ws + off), tag, RLX_AGENT); }
DI unsigned nz(unsigned v) { return v < 0xFFFFFFFEu ? v : 0xFFFFFFFEu; }
DI unsigned umax3(unsigned a, unsigned b, unsigned c) { unsigned m = a > b ? a : b; return m > c ? m : c; }
DI bool missing(u32x4 v) { return __any(umax3(umax3(v.x, v.y, v.z), v.w, 0u) == 0xFFFFFFFFu); }
#define ONES ((u32x4){0xFFFFFFFFu, 0xFFFFFFFFu, 0xFFFFFFFFu, 0xFFFFFFFFu})
#define ST16(v, off) __builtin_amdgcn_raw_buffer_store_b128((v), wsr, (off), 0, 16)
#define ST8(v, off) __builtin_amdgcn_raw_buffer_store_b64((v), wsr, (off), 0, 16)
#define LD16(off) __builtin_amdgcn_raw_buffer_load_b128(wsr, (off), 0, 16)
#define LD8(off) __builtin_amdgcn_raw_buffer_load_b64(wsr, (off), 0, 16)

#define STAMP(i) do {} while (0)

__global__ __launch_bounds__(512, 2) void fused(const u16* x, const u16* wqkv, const u16* wphi, const u16* woA,
                                                const u16* wq, const u16* wk, const u16* wv, const u16* woB,
                                                const u16* gatew, u16* out, char* ws) {
  __shared__ __attribute__((aligned(16))) char lds[L_TOTAL];
  const int tid = threadIdx.x, lane = tid & 63;
  const int w = __builtin_amdgcn_readfirstlane(tid >> 6) & 7;
  const int l15 = lane & 15, g = lane >> 4, q4 = l15 >> 2, p4 = lane & 3, r = lane & 31, h = lane >> 5;
  const int bx = blockIdx.x & 7, bi = blockIdx.x >> 3;
  const int rt = (bx & 1) * 8 + (bi & 7), hp = (bx >> 1) * 4 + ((bi >> 3) & 3), hq = hp >> 1, role = hp & 1;
  const int w3 = w & 3, w2 = w >> 2;
  LAS char* lp = (LAS char*)lds;
  const unsigned lds0 = (unsigned)(uintptr_t)lds;
  const bool second = w3 >> 1;
  const unsigned rs = (second && role) ? 1280u : 3840u;
  unsigned so[5], xoff[2];
#pragma unroll
  for (int j = 0; j < 5; j++) { const int c = j * 64 + lane, rho = (c * 6554) >> 16, cc = c - rho * 10; so[j] = (rho ^ (second ? 4 : 0)) * rs + cc * 16; }
#pragma unroll
  for (int j = 0; j < 2; j++) { const int row = j * 8 + (lane >> 3); xoff[j] = row * 1280 + (((lane & 7) ^ ((row >> 1) & 7)) * 16); }
  asm volatile("" : "+v"(so[0]), "+v"(so[1]), "+v"(so[2]), "+v"(so[3]), "+v"(so[4]), "+v"(xoff[0]), "+v"(xoff[1]));
  asm volatile("" : "+s"(x), "+s"(wqkv), "+s"(wphi), "+s"(woA), "+s"(wq), "+s"(wk), "+s"(wv), "+s"(woB), "+s"(gatew), "+s"(out), "+s"(ws));
  const __amdgpu_buffer_rsrc_t wsr = __builtin_amdgcn_make_buffer_rsrc(ws, 0, WS_END, 0x00020000);
  LAS unsigned* cnt = (LAS unsigned*)(lp + L_CNT);
#define ARRIVE_NW(c) do { asm volatile("" ::: "memory"); if (lane == 0) __hip_atomic_fetch_add(cnt + (c), 1u, __ATOMIC_RELAXED, __HIP_MEMORY_SCOPE_WORKGROUP); asm volatile("" ::: "memory"); } while (0)
#define PEEK(c) ((unsigned)__builtin_amdgcn_readfirstlane(__hip_atomic_load(cnt + (c), __ATOMIC_RELAXED, __HIP_MEMORY_SCOPE_WORKGROUP)))
#define AWAIT(c, n) do { _Pragma("unroll 1") for (unsigned sp_ = 0; sp_ < LSPIN_CAP; ++sp_) { if (PEEK(c) >= (unsigned)(n)) break; __builtin_amdgcn_s_sleep(1); } asm volatile("" ::: "memory"); } while (0)
#define POST(c, v) do { asm volatile("" ::: "memory"); __hip_atomic_store(cnt + (c) + w3, (unsigned)(v), __ATOMIC_RELAXED, __HIP_MEMORY_SCOPE_WORKGROUP); asm volatile("" ::: "memory"); } while (0)
#define PEEK4(c) __hip_atomic_load(cnt + (c) + p4, __ATOMIC_RELAXED, __HIP_MEMORY_SCOPE_WORKGROUP)
#define AWAIT4(c, n) do { _Pragma("unroll 1") for (unsigned sp_ = 0; sp_ < LSPIN_CAP; ++sp_) { if (__all(PEEK4(c) >= (unsigned)(n))) break; __builtin_amdgcn_s_sleep(1); } asm volatile("" ::: "memory"); } while (0)
  STAMP(0);
  if (w == 0) *(LAS u32x4*)(lp + L_ZERO + lane * 16) = (u32x4){0u, 0u, 0u, 0u};

  const int T0 = rt * 128 + hq * 16;
  const int C0 = hq * 80;
  const int wlane = ((g >> 1) * 16 + (g & 1) * 4 + q4) * 160 + p4 * 40;
  u64 tag;
  unsigned o0[10];
#define KVP(s, slot, p) dma16s_sc1(ws + WS_KV + (s) * STEP_B + (p) * 1024, lane * 16, lds0 + (slot) * STEP_B + (p) * 1024)
#define KVL(s, slot) do { _Pragma("unroll") for (int j = 0; j < 11; j++) if (j < 10 || w3 < 2) KVP(s, slot, w3 + 4 * j); } while (0)
#define KW(s, slot, j) do { if ((j) == 4) KVP(s, slot, w * 5 + 4); else { if ((j) == 0) dmag_open(lds0 + (slot) * STEP_B + w * 5120); dmag_sc1<(j)>(ws + WS_KV + (s) * STEP_B + w * 5120, lane * 16); } } while (0)
#define KX(s, slot) do { if (w < 2) KVP(s, slot, 40 + w); } while (0)
  if (w2 == 0) {
    u32x4 gf0, gf1, gf2, gf3, gf4, wpc; u32x2 tch;
    u32x2 ep = ld8_sc1_async(ws + WS_EPOCH);
    const int lx = w3 < 3 ? L_P0X0 + w3 * 5120 : L_P0X1;
    { const u16* wsrc = (role ? wv : wk) + w3 * 3200;
#pragma unroll
      for (int j = 0; j < 5; j++) { const int n = j * 64 + lane, row = (n * 3277) >> 16, cc = n - row * 20; dma16s(x + T0 * DM + w3 * 160, row * 1280 + cc * 16, lds0 + lx + j * 1024); }
#pragma unroll
      for (int j = 0; j < 6; j++) dma16s(wsrc + j * 512, lane * 16, lds0 + L_P0W + w3 * 6400 + j * 1024);
      if (lane < 16) dma16s(wsrc + 6 * 512, lane * 16, lds0 + L_P0W + w3 * 6400 + 6 * 1024);
      const u16* gp = gatew + w3 * 160 + g * 8;
      gf0 = ld16_async(gp); gf1 = ld16_async(gp + 32); gf2 = ld16_async(gp + 64); gf3 = ld16_async(gp + 96); gf4 = ld16_async(gp + 128);
      wpc = ld16_async(wphi + hq * 1600 + (tid < 200 ? tid : 199) * 8);
      const char* tp = ws + (lane & 7) * 1114112;
      if (lane >= 8) tp = ws + (lane < 16 ? WS_FKV + blockIdx.x * 128 : WS_KVR + blockIdx.x * 320 + (lane & 3) * 96);
      if (w3 == 1) tp = (const char*)(woA + C0 + (lane & 7) * 51200);
      if (w3 == 2) tp = (const char*)(woB + C0 + (lane & 7) * 51200);
      if (w3 == 3) tp = (const char*)(out + (rt * 128 + role * 64) * DM + C0);
      tch = ld8_sc1_async(tp);
    }
    asm volatile("s_waitcnt vmcnt(1)" : "+v"(gf0), "+v"(gf1), "+v"(gf2), "+v"(gf3), "+v"(gf4), "+v"(wpc), "+v"(ep) :: "memory");
    tag = (((u64)(unsigned)__builtin_amdgcn_readfirstlane(ep.y) << 32) | (unsigned)__builtin_amdgcn_readfirstlane(ep.x)) + 1ull;
    STAMP(1);
    { const LAS char* wb = lp + L_P0W + (w3 * 160 + g * 8 + q4) * 40;
      const LAS char* xb = lp + lx + l15 * 320 + g * 16;
      const bool gl = l15 == 4;
      f32x4 c0 = {}, c1 = {};
#define P0STEP(s, GF) { const u32x4 b = ldsr16(xb + (s) * 64); c0 = mfma16(trd2(wb + p4 * 8 + (s) * 1280, wb + p4 * 8 + (s) * 1280 + 160), b, c0); \
        u32x4 a = trd2(wb + 32 + (s) * 1280, wb + 32 + (s) * 1280 + 160); if (gl) a = GF; c1 = mfma16(a, b, c1); }
      P0STEP(0, gf0) P0STEP(1, gf1) P0STEP(2, gf2) P0STEP(3, gf3) P0STEP(4, gf4)
#undef P0STEP
      *(LAS f32x4*)(lp + L_RED + tid * 16) = c0;
      if (lane < 32) *(LAS f32x4*)(lp + L_RED1 + (w3 * 32 + lane) * 16) = c1; }
    f32x16 acc[5];
#pragma unroll
    for (int b = 0; b < 5; b++) acc[b] = (f32x16){};
    const int wm = g & 1, whd = p4;
    const int wo0 = wm * ST_W1 + (8 * h + 4 * wm + q4) * 160 + whd * 40, wo1 = wm * ST_W1 + (8 * h + 4 * (wm ^ 1) + q4) * 160 + whd * 40;
    const int xo = ST_X + (w3 * 32 + r) * 128 + ((h ^ ((r >> 1) & 7)) * 16);
    u32x4 fr[4][5], xr[4];
#define FRD(s, j) fr[s][j] = trd2(sb + wo0 + (s) * 2560 + (j) * 8, sb + wo1 + (s) * 2560 + (j) * 8)
#define FMM(s, j) acc[j] = mfma32(fr[s][j], xr[s], acc[j])
#define KSTEP(s) { xr[s] = ldsr16(sb + (xo ^ ((s) * 32))); \
      _Pragma("unroll") for (int j = 0; j < 5; j++) { if (i > 0 || (s) >= 2) FMM((s) ^ 2, j); FRD(s, j); SBAR(); } }
    unsigned lnd = 1;
    BAR_L();
    STAMP(2);
#pragma unroll
    for (int i = 0; i < 10; i++) {
      if (__builtin_expect(!__all(lnd >= (unsigned)(i + 1)), 0)) AWAIT4(8, i + 1);
      asm volatile("" ::: "memory");
      const LAS char* sb = lp + L_BUF + ((i + 2) % 3) * STAGE_B;
      KSTEP(0)
      if (i == 1 && role) {
        const unsigned qb = WS_QX + (rt * 8 + hq) * QX_B + w3 * 1280 + lane * 16;
        ST16(ONES, qb); ST16(ONES, qb + 5120); ST16(ONES, lane < 32 ? qb + 1024 + (lane >> 4) * (5120 - 256) : 0x7FFF0000u); }
      if (i == 4) { asm volatile("s_waitcnt vmcnt(0)" : "+v"(tch) :: "memory"); if (role) set_flag(ws, WS_FQ + ((rt * 8 + hq) * 4 + w3) * 8, tag, lane);
                    if (tid < 200) *(LAS u32x4*)(lp + L_WPHI + tid * 16) = wpc; }
      KSTEP(1) KSTEP(2)
      lnd = PEEK4(i < 9 ? 8 : 12);
      KSTEP(3)
      POST(12, i + 1);
    }
#pragma unroll
    for (int j = 0; j < 10; j++) FMM(2 + j / 5, j % 5);
#undef FRD
#undef FMM
#undef KSTEP
    STAMP(3);
#define PK2(t, i, sc) pk(acc[t][i] * (sc), acc[t][(i) + 1] * (sc))
#define ROW(i, sc) const u32x4 ra = {PK2(0, i, sc), PK2(0, (i) + 2, sc), PK2(1, i, sc), PK2(1, (i) + 2, sc)}, rb = {PK2(2, i, sc), PK2(2, (i) + 2, sc), PK2(3, i, sc), PK2(3, (i) + 2, sc)}; const u32x2 rc = {PK2(4, i, sc), PK2(4, (i) + 2, sc)}
    { ROW(4, 1.0f);
      if (__builtin_expect(!__all(lnd >= 9u), 0)) AWAIT4(12, 9);
      asm volatile("" ::: "memory");
      *(LAS u32x4*)(lp + L_HO + tid * 16) = ra; *(LAS u32x4*)(lp + L_HO + 4096 + tid * 16) = rb; *(LAS u32x2*)(lp + L_HO + 8192 + tid * 8) = rc; }
    BAR_L();
    const int qfo = (h * 4 + w3) * 1280;
    if (role) {
      { ROW(8, QSCALE); const unsigned qb = WS_QX + (rt * 8 + hq) * QX_B + qfo;
        ST16(((u32x4){nz(ra.x), nz(ra.y), nz(ra.z), nz(ra.w)}), qb + r * 16); ST16(((u32x4){nz(rb.x), nz(rb.y), nz(rb.z), nz(rb.w)}), qb + 512 + r * 16); ST8(((u32x2){nz(rc.x), nz(rc.y)}), qb + 1024 + r * 8); }
      { ROW(12, QSCALE); LAS char* d = lp + L_QL + qfo;
        *(LAS u32x4*)(d + r * 16) = ra; *(LAS u32x4*)(d + 512 + r * 16) = rb; *(LAS u32x2*)(d + 1024 + r * 8) = rc; }
    } else { LAS char* d = lp + L_VIM + (h * 128 + w3 * 32 + r) * 40;
#pragma unroll
      for (int t = 0; t < 5; t++) { *(LAS u32x2*)(d + t * 8) = (u32x2){PK2(t, 8, 1.0f), PK2(t, 10, 1.0f)}; *(LAS u32x2*)(d + 10240 + t * 8) = (u32x2){PK2(t, 12, 1.0f), PK2(t, 14, 1.0f)}; } }
#pragma unroll
    for (int t = 0; t < 5; t++) { o0[2 * t] = PK2(t, 0, 1.0f); o0[2 * t + 1] = PK2(t, 2, 1.0f); }
#undef ROW
#undef PK2
  } else {
    u32x2 ep = ld8_sc1_async(ws + WS_EPOCH);
    const char* wsrc = (second ? (role ? (const char*)(wq + C0) : (const char*)(wqkv + 1280 + C0)) : (const char*)(wqkv + (role ? 0 : 640) + C0)) + (w3 & 1) * 32 * rs;
    const char* xsrc = (const char*)(x + (rt * 128 + w3 * 32) * DM);
    int rot = (bi & 7) + 3 * ((bi >> 3) & 3) + (bx & 6) + 5 * (bx & 1); rot = rot >= 20 ? rot - 20 : (rot >= 10 ? rot - 10 : rot);
    int bsel = 2, kc = rot;
    auto stage = [&](int n) {
      const unsigned lb = lds0 + L_BUF + bsel * STAGE_B;
      const char* bw = wsrc + kc * 64 * rs; const char* bxp = xsrc + kc * 128;
#pragma unroll
      for (int j = 0; j < 5; j++) { dma16s(bw, so[j], lb + (w3 * 5 + j) * 1024); if (j < 4) dma16s(bxp + (j >> 1) * 16 * 1280, xoff[j & 1], lb + ST_X + (w3 * 4 + j) * 1024);
        if (j == 1 && n > 0) { WAIT_VM(4); POST(8, n); } }
      bsel = bsel == 2 ? 0 : bsel + 1; kc = kc == 9 ? 0 : kc + 1;
    };
    stage(0); stage(0);
    asm volatile("s_waitcnt vmcnt(9)\n\ts_barrier" : "+v"(ep) :: "memory");
    tag = (((u64)(unsigned)__builtin_amdgcn_readfirstlane(ep.y) << 32) | (unsigned)__builtin_amdgcn_readfirstlane(ep.x)) + 1ull;
    STAMP(1); STAMP(2);
#pragma unroll 1
    for (int i = 0; i < 8; i++) {
      if (i >= 1) AWAIT4(12, i);
      stage(i + 2);
      if (i == 0 && w3 == 0) {
        const unsigned tb = WS_KV + (T0 >> 5) * TILE_B; const int s16 = hq & 1;
        if (!role) {
          const LAS f32x4* rp = (const LAS f32x4*)(lp + (h ? L_RED1 : L_RED)) + (h ? (g & 1) * 16 : g * 32) + l15;
          const int st = h ? 32 : 64, o2 = h ? 0 : 16;
          const f32x4 a = (rp[0] + rp[st]) + (rp[2 * st] + rp[3 * st]), b = (rp[o2] + rp[st + o2]) + (rp[2 * st + o2] + rp[3 * st + o2]);
          const u32x4 v = {pk(a[0], a[1]), pk(a[2], a[3]), pk(b[0], b[1]), pk(b[2], b[3])};
          if (lane < 32) ST16(v, tb + (g * 32 + s16 * 16 + l15) * 16);
          else if (lane < 48) ST8(((u32x2){v.x, v.y}), tb + 1024 + (s16 * 16 + l15) * 8);
          else { const float gv = 1.0f / (1.0f + __expf(-a[0])); __builtin_amdgcn_raw_buffer_store_b32(__float_as_uint(gv), wsr, WS_G + (T0 + l15) * 4, 0, 16); }
        } else if (lane < 44) {
          const int hh = lane >= 22, d = lane - hh * 22, dd = d < 20 ? d : 0, st = dd >= 16 ? 128 : 256;
          const LAS float* rp = (const LAS float*)(lp + (dd >= 16 ? L_RED1 : L_RED)) + (((dd & 15) >> 2) * 16 + 4 * hh) * 4 + (dd & 3);
          float e[8];
#pragma unroll
          for (int j = 0; j < 8; j++) { const int tk = (8 * (j >> 2) + (j & 3)) * 4; e[j] = (rp[tk] + rp[tk + st]) + (rp[tk + 2 * st] + rp[tk + 3 * st]); }
          u32x4 v = {pk(e[0], e[1]), pk(e[2], e[3]), pk(e[4], e[5]), pk(e[6], e[7])};
          if (d == 20) v = (u32x4){0x3F803F80u, 0x3F803F80u, 0x3F803F80u, 0x3F803F80u};
          if (d == 21) v = (u32x4){0u, 0u, 0u, 0u};
          ST16(v, tb + 1280 + (s16 * 2 + hh) * VB + d * 16);
        }
      }
      if (i == 1 && w3 == 0) set_flag(ws, WS_FKV + blockIdx.x * 8, tag, lane);
    }
    u64 fa, fb, fc, fd, fe;
    { gu64* f = (gu64*)(ws + WS_FKV) + lane;
      fa = __hip_atomic_load(f, RLX_AGENT); fb = __hip_atomic_load(f + 64, RLX_AGENT); fc = __hip_atomic_load(f + 128, RLX_AGENT); fd = __hip_atomic_load(f + 192, RLX_AGENT);
      fe = __hip_atomic_load((gu64*)(ws + WS_FQ) + (rt * 8 + hq) * 4 + p4, RLX_AGENT); }
    WAIT_VM(5); POST(8, 10);
    asm volatile("" : "+v"(fa), "+v"(fb), "+v"(fc), "+v"(fd), "+v"(fe));
    const bool kvok = __all((fa == tag) & (fb == tag) & (fc == tag) & (fd == tag) & (fe == tag));
    asm volatile("" ::: "memory");
    if (!kvok) { wait_flags<256>(ws, WS_FKV, tag, lane); wait_flags<4>(ws, WS_FQ + (rt * 8 + hq) * 32, tag, lane); }
    KVL(0, 0);
    if (blockIdx.x == 0 && w3 == 0) set_flag(ws, WS_EPOCH, tag, lane);
    STAMP(3);
    BAR_L();
    const int ht = tid - 256;
    const u32x4 a = ldsr16(lp + L_HO + ht * 16), b = ldsr16(lp + L_HO + 4096 + ht * 16); const u32x2 c = ldsr8(lp + L_HO + 8192 + ht * 8);
    o0[0] = a.x; o0[1] = a.y; o0[2] = a.z; o0[3] = a.w; o0[4] = b.x; o0[5] = b.y; o0[6] = b.z; o0[7] = b.w; o0[8] = c.x; o0[9] = c.y;
  }
  STAMP(4);
  u32x4 ph[5];
  { const LAS char* pb = ((p4 & 1) == h) ? lp + L_WPHI + (2 * w2 + h) * 800 + q4 * 40 + (2 * (g & 1) + (p4 >> 1)) * 8 : lp + L_ZERO;
    const u32x4 b0 = {o0[0], o0[1], o0[2], o0[3]}, b1 = {o0[4], o0[5], o0[6], o0[7]}, b2 = {o0[8], o0[9], 0u, 0u};
    const u32x2 c0 = trd(pb + 640), c1 = trd(pb + 672);
    f32x16 P0 = {}, P1 = {};
    P0 = mfma32(trd2(pb, pb + 160), b0, P0); P1 = mfma32(trd2(pb + 32, pb + 192), b0, P1);
    P0 = mfma32(trd2(pb + 320, pb + 480), b1, P0); P1 = mfma32(trd2(pb + 352, pb + 512), b1, P1);
    P0 = mfma32((u32x4){c0.x, c0.y, 0u, 0u}, b2, P0); P1 = mfma32((u32x4){c1.x, c1.y, 0u, 0u}, b2, P1);
    KX(1, 1); KW(1, 1, 0); SBAR();
    f32x2 p[10];
#pragma unroll
    for (int f = 0; f < 10; f++) p[f] = f < 8 ? (f32x2){P0[(2 * f) & 15], P0[(2 * f + 1) & 15]} : (f32x2){P1[(2 * f) & 3], P1[(2 * f + 1) & 3]};
    float mx = fmaxf(p[0][0], p[0][1]), mn = fminf(p[0][0], p[0][1]);
#pragma unroll
    for (int f = 1; f < 10; f++) { mx = fmaxf(fmaxf(mx, p[f][0]), p[f][1]); mn = fminf(fminf(mn, p[f][0]), p[f][1]); }
    const f32x2 ma = {-mx * LOG2E, -mx * LOG2E}, mb = {mn * LOG2E, mn * LOG2E}, ca_ = {LOG2E, LOG2E}, cb_ = {-LOG2E, -LOG2E};
    f32x2 ea[10], eb[10], sa = {0.f, 0.f}, sb = {0.f, 0.f};
#pragma unroll
    for (int f = 0; f < 10; f++) { if (f % 2 == 0 && f < 8) { SBAR(); if (f == 0) KW(1, 1, 1); if (f == 2) KW(1, 1, 2); if (f == 4) KW(1, 1, 3); if (f == 6) KW(1, 1, 4); SBAR(); }
      const f32x2 xa = __builtin_elementwise_fma(p[f], ca_, ma), xb = __builtin_elementwise_fma(p[f], cb_, mb);
      ea[f] = (f32x2){ex2(xa[0]), ex2(xa[1])}; eb[f] = (f32x2){ex2(xb[0]), ex2(xb[1])}; sa += ea[f]; sb += eb[f]; }
    const float ia = __builtin_amdgcn_rcpf(sa[0] + sa[1]), ib = __builtin_amdgcn_rcpf(sb[0] + sb[1]);
    unsigned o[20];
#pragma unroll
    for (int f = 0; f < 10; f++) { const f32x2 ya = ea[f] * ia, yb = eb[f] * ib; o[f] = pk(ya[0], ya[1]); o[10 + f] = pk(yb[0], yb[1]); }
#pragma unroll
    for (int c = 0; c < 5; c++) ph[c] = (u32x4){o[4 * c], o[4 * c + 1], o[4 * c + 2], o[4 * c + 3]};
    asm volatile("" : "+v"(ph[0]), "+v"(ph[1]), "+v"(ph[2]), "+v"(ph[3]), "+v"(ph[4]));
  }
  u32x4 q1A, q1B; u32x2 q2A, q2B;
  if (role) {
    WAIT_BAR(0);
    STAMP(5);
    const LAS char* qa = lp + L_QL + w3 * 1280;
    q1A = ldsr16(qa + lane * 16); q1B = ldsr16(qa + 5120 + lane * 16); q2A = ldsr8(qa + 1024 + r * 8); q2B = ldsr8(qa + 6144 + r * 8);
    ARRIVE_NW(0);
  } else {
    const unsigned qb = WS_QX + (rt * 8 + hq) * QX_B + w3 * 1280;
    q1A = LD16(qb + lane * 16); q1B = LD16(qb + 5120 + lane * 16); q2A = LD8(qb + 1024 + r * 8); q2B = LD8(qb + 6144 + r * 8);
    { LAS char* d = lp + L_PHK + ((2 * w2 + h) * 128 + w3 * 32 + r) * 80;
#pragma unroll
      for (int c = 0; c < 5; c++) *(LAS u32x4*)(d + c * 16) = ph[c]; }
    WAIT_BAR(4);
    STAMP(5);
#pragma unroll 1
    for (unsigned spin = 0; spin < SPIN_CAP; ++spin) {
      if (!(missing(q1A) | missing(q1B) | missing((u32x4){q2A.x, q2A.y, q2B.x, q2B.y}))) break;
      __builtin_amdgcn_s_sleep(1);
      asm volatile("" ::: "memory");
      q1A = LD16(qb + lane * 16); q1B = LD16(qb + 5120 + lane * 16); q2A = LD8(qb + 1024 + r * 8); q2B = LD8(qb + 6144 + r * 8);
    }
  }
  STAMP(6);
  const int vr = r <= 21 ? r : 21;
  const int ok1 = lane * 16, ok2 = h ? (1280 + 20 * 16) : (1024 + r * 8), ov = 1280 + h * VB + vr * 16;
  const f32x16 Z = {};
  if (h) { q2A = (u32x2){0u, 0u}; q2B = (u32x2){0u, 0u}; }
  { const LAS char* t0 = lp;
    const u32x4 k1 = ldsr16(t0 + ok1); const u32x2 k2 = ldsr8(t0 + ok2);
    f32x16 sa = mfma32(k1, q1A, Z); sa = mfma32k8(k2, q2A, sa);
    f32x16 sb = mfma32(k1, q1B, Z); sb = mfma32k8(k2, q2B, sb);
    float ma = sa[0], mb = sb[0];
#pragma unroll
    for (int i = 1; i < 16; i++) { ma = fmaxf(ma, sa[i]); mb = fmaxf(mb, sb[i]); }
    ma = half_swap_max(ma); mb = half_swap_max(mb);
    const float mah = __uint_as_float(__float_as_uint(ma) & 0xFFFF0000u), mbh = __uint_as_float(__float_as_uint(mb) & 0xFFFF0000u);
    if (h) { q2A.x = pk(-mah, mah - ma); q2B.x = pk(-mbh, mbh - mb); } }
  f32x16 OA = {}, OB = {}, SA, SB;
  u32x4 k1, v1, v2, v2n, pA1, pA2, pB1, pB2; u32x2 k2;
  float e0, e1, e2, e3;
  const LAS char* c0p = lp + w2 * CH_B + ok1; const LAS char* c1p = lp + w2 * CH_B + ok2; const LAS char* c2p = lp + w2 * CH_B + ov;
  k1 = ldsr16(c0p); k2 = ldsr8(c1p);
  SA = mfma32(k1, q1A, Z); SA = mfma32k8(k2, q2A, SA);
  v1 = ldsr16(c2p); v2 = v1;
  e0 = e1 = e2 = e3 = 0.f; pB2 = (u32x4){0u, 0u, 0u, 0u};
#define G(MF, LD, C0_, C1_, X, S_, i) MF; LD; SBAR(); C0_; C1_; SBAR(); X##0 = ex2(S_[i]); X##1 = ex2(S_[i + 1]); X##2 = ex2(S_[i + 2]); X##3 = ex2(S_[i + 3]); asm volatile("" : "+v"(X##0), "+v"(X##1), "+v"(X##2), "+v"(X##3)); SBAR();
#define TILE(TB, TN, V2, H0, H1, H2) { float a0, a1, a2, a3; \
    G(SB = mfma32(k1, q1B, Z),    k1 = ldsr16(c0p + (TN)), pB2[2] = pk(e0, e1), pB2[3] = pk(e2, e3), a, SA, 0) \
    G(OB = mfma32(v2, pB2, OB),   V2,                      pA1[0] = pk(a0, a1), pA1[1] = pk(a2, a3), e, SA, 4) \
    G(SB = mfma32k8(k2, q2B, SB), k2 = ldsr8(c1p + (TN)),  pA1[2] = pk(e0, e1), pA1[3] = pk(e2, e3), a, SA, 8) \
    G(OA = mfma32(v1, pA1, OA),   H0,                      pA2[0] = pk(a0, a1), pA2[1] = pk(a2, a3), e, SA, 12) \
    G(SA = mfma32(k1, q1A, Z),    H1,                      pA2[2] = pk(e0, e1), pA2[3] = pk(e2, e3), a, SB, 0) \
    G(OA = mfma32(v2, pA2, OA),   H2,                      pB1[0] = pk(a0, a1), pB1[1] = pk(a2, a3), e, SB, 4) \
    G(SA = mfma32k8(k2, q2A, SA), (void)0,                 pB1[2] = pk(e0, e1), pB1[3] = pk(e2, e3), a, SB, 8) \
    G(OB = mfma32(v1, pB1, OB),   v1 = ldsr16(c2p + (TN)), pB2[0] = pk(a0, a1), pB2[1] = pk(a2, a3), e, SB, 12) }
#define NOP (void)0
#define TL(sl, t, H0, H1, H2) TILE((sl) * STEP_B + (t) * TILE_B, (sl) * STEP_B + ((t) + 1) * TILE_B, v2 = ldsr16(c2p + (sl) * STEP_B + (t) * TILE_B + 2 * VB), H0, H1, H2)
#define TL6(sl, H0, H1) TL(sl, 6, H0, H1, v2n = ldsr16(c2p + (sl) * STEP_B + 7 * TILE_B + 2 * VB))
#define TL7(sl, sn, H0, H1, H2) TILE((sl) * STEP_B + 7 * TILE_B, (sn) * STEP_B, v2 = v2n, H0, H1, H2)
  unsigned woff[5];
#pragma unroll
  for (int j = 0; j < 5; j++) { const int c = j * 64 + lane, rho = (c * 6554) >> 16, cc = c - rho * 10; const int k = (rho & ~12) | ((rho & 4) << 1) | ((rho & 8) >> 1); woff[j] = k * 1280 + cc * 16 + (4 - j) * 1024; }
#define WOG(wo, gr, j, dst) do { if ((j) == 4) WOP(wo, gr, 4, dst); else { if ((j) == 0) dmag_open(lds0 + (dst)); dmag<(j)>((wo) + C0 + (gr) * 32 * 640 - 2048, woff[j]); } } while (0)
#define WOP(wo, gr, j, dst) dma16s((wo) + C0 + (gr) * 32 * 640 - (4 - (j)) * 512, woff[j], lds0 + (dst) + (j) * 1024)
#define WA(gr, j) WOG(woA, gr, j, L_WO + (gr) * 5120)
  const u16* wxs = w < 4 ? woA + (16 + w3) * 32 * 640 : woB + (w < 7 ? 17 + w3 : 19) * 32 * 640;
  const int wxd = w < 4 ? L_WO + (16 + w3) * 5120 : 145408 + (w < 7 ? w3 : 2) * 5120;
#define WX(j) WOG(wxs, 0, j, wxd)
#define PRIO(n) __builtin_amdgcn_s_setprio(n)
  const unsigned fo = (rt * 8 + hq) * OT_B + lane * 16;
#define FILL(j) do { const int n_ = w * 3 + ((j) >> 1); ST16(ONES, (((j) & 1) ? WS_OB : WS_OA) + fo + (n_ < 20 ? n_ : 19) * 1024); } while (0)
#define CLR_VM() __builtin_amdgcn_s_waitcnt(0x0F70)
  const bool red = !role && w == 4;
  f32x4 part[16]; u64 fp0, fp1;
  auto red_load = [&]() {
    asm volatile("" : "+v"(fp0), "+v"(fp1));
    if (!__all((fp0 == tag) & (fp1 == tag))) wait_flags<128>(ws, WS_FPART + hq * 1024, tag, lane);
    asm volatile("" ::: "memory");
#pragma unroll
    for (int j = 0; j < 16; j++) part[j] = __builtin_bit_cast(f32x4, LD16(WS_PART + (hq * 16 + j) * PART_B + (rt * 60 + (lane < 60 ? lane : 59)) * 16));
  };
  auto red_store = [&]() {
    f32x4 a = ((part[0] + part[1]) + (part[2] + part[3])) + ((part[4] + part[5]) + (part[6] + part[7]));
    a += ((part[8] + part[9]) + (part[10] + part[11])) + ((part[12] + part[13]) + (part[14] + part[15]));
    const int hd = rt >> 2, u = (rt & 3) * 60 + lane, hi = u >= 160, fq = hi ? (u - 160) >> 3 : u >> 4, di = hi ? u & 7 : u & 15;
    if (lane < 60) ST8(((u32x2){pk(a[0], a[1]), pk(a[2], a[3])}), WS_KVR + hq * KVR_B + (((hd >> 1) * 2 + hi) * 5 + (fq >> 1)) * 512 + ((hd & 1) * 16 + di) * 16 + (fq & 1) * 8);
  };
  u32x4 ka[10]; u64 fkr; float gta;
  auto oa_load = [&]() {
    asm volatile("" : "+v"(fkr));
    if (!__all(fkr == tag)) wait_flags<16>(ws, WS_FKVR + hq * 128, tag, lane);
    asm volatile("" ::: "memory");
    const unsigned kb = (((r >> 2) & 1) == h) ? WS_KVR + hq * KVR_B + w2 * 5120 + (h * 16 + (r & 3) + 4 * (r >> 3)) * 16 : 0x7FFF0000u;
#pragma unroll
    for (int j = 0; j < 10; j++) ka[j] = LD16(kb + j * 512);
    gta = __uint_as_float(__builtin_amdgcn_raw_buffer_load_b32(wsr, WS_G + (rt * 128 + w3 * 32 + r) * 4, 0, 16));
  };
  u32x4 ja[2][3], jb[2]; f32x4 jc[3];
  const LAS char* jab = lp + L_PHK + ((w >> 1) * 128 + g * 8 + q4) * 80 + p4 * 8;
  const LAS char* jbb = lp + L_VIM + ((w >> 1) * 128 + g * 8 + q4) * 40 + (w & 1) * 32 + p4 * 8;
  auto kv_rd = [&](int k0) {
#pragma unroll
    for (int ks = 0; ks < 2; ks++) { jb[ks] = trd2(jbb + (k0 + ks) * 1280, jbb + (k0 + ks) * 1280 + 160);
#pragma unroll
      for (int ft = 0; ft < 3; ft++) ja[ks][ft] = trd2(jab + (k0 + ks) * 2560 + ft * 32, jab + (k0 + ks) * 2560 + 320 + ft * 32); } };
  auto kv_mm = [&](bool first) {
#pragma unroll
    for (int ks = 0; ks < 2; ks++) { if ((w & 1) && l15 == 4) jb[ks] = (u32x4){0x3F803F80u, 0x3F803F80u, 0x3F803F80u, 0x3F803F80u};
#pragma unroll
      for (int ft = 0; ft < 3; ft++) jc[ft] = mfma16(ja[ks][ft], jb[ks], (first && !ks) ? (f32x4){0.f, 0.f, 0.f, 0.f} : jc[ft]); } };
  auto kv_st = [&]() { const int dt = w & 1; const unsigned pb = WS_PART + (hq * 16 + rt) * PART_B + (w >> 1) * 3840 + (dt ? 2560 + g * 128 : g * 256) + l15 * 16;
#pragma unroll
    for (int ft = 0; ft < 3; ft++) if ((!dt || l15 < 8) && ft * 4 + g < 10) ST16(__builtin_bit_cast(u32x4, jc[ft]), pb + ft * (dt ? 512 : 1024)); };
  f32x16 oa0, oa1; u32x4 z0, z1, z2;
  LAS char* otb = lp + L_WO + (8 + w) * 5120;
  auto oa_mm = [&](int s) { oa0 = mfma32(ka[s], ph[s], s ? oa0 : Z); oa1 = mfma32(ka[5 + s], ph[s], s ? oa1 : Z); };
  auto oa_norm = [&]() { const float inv = __builtin_amdgcn_rcpf(oa1[4] + 1e-6f) * gta;
#pragma unroll
    for (int t = 0; t < 5; t++) { const int k0 = h * 20 + 4 * t;
      const u32x2 v = t < 4 ? (u32x2){nz(pk(oa0[(4 * t) & 15] * inv, oa0[(4 * t + 1) & 15] * inv)), nz(pk(oa0[(4 * t + 2) & 15] * inv, oa0[(4 * t + 3) & 15] * inv))} : (u32x2){nz(pk(oa1[0] * inv, oa1[1] * inv)), nz(pk(oa1[2] * inv, oa1[3] * inv))};
      *(LAS u32x2*)(otb + (r >> 4) * 1280 + (k0 >> 3) * 256 + (k0 & 4) * 2 + l15 * 16) = v; }
    z0 = ldsr16(otb + lane * 16); z1 = ldsr16(otb + 1280 + lane * 16); z2 = ldsr16(otb + (r >> 4) * 1280 + 1024 + l15 * 16); };
  auto oa_st = [&]() { const unsigned oa = WS_OA + (rt * 8 + w3 * 2) * OT_B + (hq * 2 + w2) * 1280;
    ST16(z0, oa + lane * 16); ST16(z1, oa + OT_B + lane * 16); if (!h) ST16(z2, oa + (r >> 4) * OT_B + 1024 + l15 * 16); };
  unsigned cpk;
  PRIO(3);
  TL(0, 0, if (!role) kv_rd(0); else FILL(0), NOP, NOP)
  TL(0, 1, if (!role) { kv_mm(true); kv_rd(2); ARRIVE_NW(0); } else FILL(1), NOP, NOP)
  TL(0, 2, if (!role) kv_mm(false); else FILL(2), NOP, if (!role) kv_st())
  TL(0, 3, NOP, NOP, cpk = __hip_atomic_load(cnt, __ATOMIC_RELAXED, __HIP_MEMORY_SCOPE_WORKGROUP))
  STAMP(14);
  if ((unsigned)__builtin_amdgcn_readfirstlane(cpk) < 8u) AWAIT(0, 8);
  asm volatile("" ::: "memory");
  TL(0, 4, NOP, NOP, if (!role) { WAIT_VM(0); set_flag(ws, WS_FPART + ((hq * 16 + rt) * 8 + w) * 8, tag, lane); } else FILL(3))
  PRIO(2);
  TL(0, 5, KX(2, 2), NOP, KW(2, 2, 0)) TL6(0, KW(2, 2, 1), KW(2, 2, 2))
  TL7(0, 1, KW(2, 2, 3), NOP, KW(2, 2, 4))
  STAMP(15);
  TL(1, 0, if (role) FILL(4), NOP, if (red) { fp0 = __hip_atomic_load((gu64*)(ws + WS_FPART) + hq * 128 + lane, RLX_AGENT); fp1 = __hip_atomic_load((gu64*)(ws + WS_FPART) + hq * 128 + 64 + lane, RLX_AGENT); })
  TL(1, 1, if (w < 4) { const int row = (w & 1) * 32 + (lane >> 1);
                      dma4s((w & 2 ? woB : woA) + C0 + ((rt & 7) * 2 + role) * 40 * 640, (row < 40 ? row : 39) * 1280 + (lane & 1) * 156, lds0 + 157696); }, NOP, if (role) FILL(5))
  TL(1, 2, NOP, NOP, if (red) red_load())
  PRIO(1);
  TL(1, 3, NOP, NOP, NOP)
  TL(1, 4, NOP, CLR_VM(), if (red) red_store())
  TL(1, 5, NOP, NOP, NOP)
  PRIO(0);
  TL6(1, if (red) { WAIT_VM(0); set_flag(ws, WS_FKVR + (hq * 16 + rt) * 8, tag, lane); }, NOP)
  if (red) WAIT_BAR(1); else WAIT_BAR(0);
  STAMP(12);
  PRIO(3);
  if (role) set_flag(ws, WS_FOA + ((rt * 8 + hq) * 8 + w) * 8, tag, lane);
  u64 foa;
  TL7(1, 2, KX(3, 0), KW(3, 0, 0), KW(3, 0, 1))
  TL(2, 0, KW(3, 0, 2), KW(3, 0, 3), KW(3, 0, 4)) TL(2, 1, WA(w, 0), WA(w, 1), WA(w, 2))
  TL(2, 2, WA(w, 3), WA(w, 4), (foa = __hip_atomic_load((gu64*)(ws + WS_FOA) + rt * 64 + lane, RLX_AGENT), fkr = __hip_atomic_load((gu64*)(ws + WS_FKVR) + hq * 16 + l15, RLX_AGENT)))
  PRIO(2);
  TL(2, 3, NOP, NOP, NOP)
  auto chk = [&]() { asm volatile("" : "+v"(foa), "+v"(fkr));
    if (!__all(foa == tag)) wait_flags<64>(ws, WS_FOA + rt * 512, tag, lane);
    asm volatile("" ::: "memory"); if (role) oa_load(); };
  TL(2, 4, chk(), NOP, NOP)
  PRIO(1);
  TL(2, 5, NOP, NOP, if (role) oa_mm(0))
  PRIO(0);
  TL6(2, if (role) oa_mm(1), if (role) oa_mm(2))
  BAR_L();
  STAMP(13);
  PRIO(3);
  TL7(2, 0, if (role) oa_mm(3), { if (role) oa_mm(4); CLR_VM(); }, WX(0))
  TL(0, 0, if (role) oa_norm(), WX(1), WX(2))
  TL(0, 1, if (role) oa_st(), WX(3), WX(4))
  TL(0, 2, WA(8 + w, 0), WA(8 + w, 1), WA(8 + w, 2))
  u32x4 wbr[11];
  const char* wbs = (const char*)(woB + C0 + w * 32 * 640) - 4096;
#define WB2(j) do { wbr[j] = *(const GAS u32x4*)(wbs + woff[j] + (j) * 1024); wbr[5 + (j)] = *(const GAS u32x4*)(wbs + 8 * 32 * 1280 + woff[j] + (j) * 1024); } while (0)
  TL(0, 3, WA(8 + w, 3), WA(8 + w, 4), WB2(0))
  PRIO(2);
  TL(0, 4, WB2(1), WB2(2), WB2(3))
  const int w5 = w < 5 ? w : 4;
  const unsigned wo16 = (w5 == 0 ? woff[0] : w5 == 1 ? woff[1] : w5 == 2 ? woff[2] : w5 == 3 ? woff[3] : woff[4]) + w5 * 1024;
  TL(0, 5, WB2(4), NOP, wbr[10] = *(const GAS u32x4*)((const char*)(woB + C0 + 16 * 32 * 640) - 4096 + wo16))
  PRIO(1);
  ARRIVE_NW(1);
  const int tp = w & 1, kq = w >> 1;
  const unsigned ofr = (rt * 8 + role * 4 + tp * 2) * OT_B + kq * 5120 + lane * 16;
  u32x4 of[2][5]; float gtb;
#define OFL(u) do { _Pragma("unroll") for (int i = 0; i < 5; i++) of[u][i] = LD16(WS_OA + ofr + (u) * OT_B + i * 1024); } while (0)
  TL6(0, OFL(0), gtb = __uint_as_float(__builtin_amdgcn_raw_buffer_load_b32(wsr, WS_G + (rt * 128 + w3 * 32 + w2 * 16 + l15) * 4, 0, 16)))
  PRIO(0);
  TL7(0, 0, OFL(1), NOP, NOP)
  pB2[2] = pk(e0, e1); pB2[3] = pk(e2, e3);
  OB = mfma32(v2, pB2, OB);
#undef TL
#undef TL6
#undef TL7
#undef TILE
#undef G
  STAMP(7);
  { LAS f32x4* xb = (LAS f32x4*)(lp + (w3 >> 1) * CH_B) + (w3 & 1) * (64 * 6);
#pragma unroll
    for (int i = 0; i < 12; i++) { auto rr = __builtin_amdgcn_permlane16_swap(__float_as_uint(OA[i]), __float_as_uint(OB[i]), false, false); OA[i] = __uint_as_float(rr[0]); OB[i] = __uint_as_float(rr[1]); }
    const f32x16 snd = w2 ? OA : OB;
    AWAIT(1, 8);
#pragma unroll
    for (int c = 0; c < 3; c++) xb[(w2 * 3 + c) * 64 + lane] = (f32x4){snd[4 * c], snd[4 * c + 1], snd[4 * c + 2], snd[4 * c + 3]};
    asm volatile("s_waitcnt vmcnt(22) lgkmcnt(0)\n\ts_barrier" ::: "memory");
    f32x16 O = w2 ? OB : OA;
#pragma unroll
    for (int c = 0; c < 3; c++) { const f32x4 t = xb[((w2 ^ 1) * 3 + c) * 64 + lane]; O[4 * c] += t[0]; O[4 * c + 1] += t[1]; O[4 * c + 2] += t[2]; O[4 * c + 3] += t[3]; }
    float l = from_upper(O[8]);
    float amax = 0.f;
#pragma unroll
    for (int i = 0; i < 12; i++) amax += fabsf(O[i]);
    const bool good = (l >= 0x1p-30f) && (l <= 0x1p100f) && (amax <= 0x1p126f);
    if (__builtin_expect(!__all(good), 0)) {
      u32x4 q1; u32x2 q2;
#pragma unroll
      for (int i = 0; i < 4; i++) { auto rr = __builtin_amdgcn_permlane16_swap(q1A[i], q1B[i], false, false); q1[i] = w2 ? rr[1] : rr[0]; }
#pragma unroll
      for (int i = 0; i < 2; i++) { auto rr = __builtin_amdgcn_permlane16_swap(q2A[i], q2B[i], false, false); q2[i] = h ? 0u : (w2 ? rr[1] : rr[0]); }
      float m = -INFINITY;
#pragma unroll 1
      for (int t = 0; t < 64; t++) {
        f32x16 s = mfma32(LD16(WS_KV + t * TILE_B + ok1), q1, Z); s = mfma32k8(LD8(WS_KV + t * TILE_B + ok2), q2, s);
#pragma unroll
        for (int i = 0; i < 16; i++) m = fmaxf(m, s[i]);
      }
      m = half_swap_max(m);
      O = Z;
#pragma unroll 1
      for (int t = 0; t < 64; t++) {
        f32x16 s = mfma32(LD16(WS_KV + t * TILE_B + ok1), q1, Z); s = mfma32k8(LD8(WS_KV + t * TILE_B + ok2), q2, s);
#pragma unroll
        for (int i = 0; i < 16; i++) s[i] = ex2(s[i] - m);
        O = mfma32(LD16(WS_KV + t * TILE_B + ov), (u32x4){pk(s[0], s[1]), pk(s[2], s[3]), pk(s[4], s[5]), pk(s[6], s[7])}, O);
        O = mfma32(LD16(WS_KV + t * TILE_B + ov + 2 * VB), (u32x4){pk(s[8], s[9]), pk(s[10], s[11]), pk(s[12], s[13]), pk(s[14], s[15])}, O);
      }
      l = from_upper(O[8]);
    }
    const float inv = __builtin_amdgcn_rcpf(l) * (1.0f - gtb);
    LAS char* tb = lp + w2 * CH_B + 12288 + w3 * 1536;
#pragma unroll
    for (int c = 0; c < 3; c++) { const int k0 = (g & 1) * 20 + 8 * c + 4 * h;
      *(LAS u32x2*)(tb + ((c == 2 && h) ? 1280 + (g & 1) * 8 : (k0 >> 3) * 256 + (k0 & 4) * 2) + l15 * 16) = (u32x2){nz(pk(O[4 * c] * inv, O[4 * c + 1] * inv)), nz(pk(O[4 * c + 2] * inv, O[4 * c + 3] * inv))}; }
    const unsigned ob = WS_OB + (rt * 8 + w3 * 2 + w2) * OT_B + hp * 1280 + lane * 16;
    const u32x4 z0 = ldsr16(tb + lane * 16), z1 = ldsr16(tb + 1024 + l15 * 16);
    ST16(z0, ob); ST16(z1, lane < 16 ? ob + 1024 : 0x7FFF0000u);
  }
  STAMP(8);
  auto of_missing = [&]() { u32x4 mm = of[0][0];
#pragma unroll
    for (int j = 1; j < 10; j++) mm = __builtin_elementwise_max(mm, of[j / 5][j % 5]);
    return missing(mm); };
  u32x4 wf[5][5];
#pragma unroll
  for (int i = 0; i < 5; i++) { const LAS char* wb = lp + L_WO + (kq * 5 + i) * 5120 + wlane;
#pragma unroll
    for (int t = 0; t < 5; t++) wf[i][t] = trd2(wb + t * 8, wb + 1280 + t * 8); }
  SBAR();
  if (__builtin_expect(of_missing(), 0)) {
#pragma unroll 1
    for (unsigned spin = 0; spin < SPIN_CAP; ++spin) {
      asm volatile("" ::: "memory");
      OFL(0); OFL(1);
      if (!of_missing()) break;
      __builtin_amdgcn_s_sleep(1);
    }
  }
  f32x4 c[2][5];
#pragma unroll
  for (int i = 0; i < 5; i++)
#pragma unroll
    for (int t = 0; t < 5; t++) c[0][t] = mfma16(wf[i][t], of[0][i], i ? c[0][t] : (f32x4){0.f, 0.f, 0.f, 0.f});
  BAR_L();
  SBAR();
  { LAS char* d = lp + L_WO + w * 5120 + lane * 16;
    *(LAS u32x4*)(lp + L_WO + 16 * 5120 + w5 * 1024 + lane * 16) = wbr[10];
#pragma unroll
    for (int i = 0; i < 5; i++) {
      *(LAS u32x4*)(d + i * 1024) = wbr[i]; *(LAS u32x4*)(d + 40960 + i * 1024) = wbr[5 + i]; of[0][i] = LD16(WS_OB + ofr + i * 1024); SBAR();
#pragma unroll
      for (int t = 0; t < 5; t++) c[1][t] = mfma16(wf[i][t], of[1][i], i ? c[1][t] : (f32x4){0.f, 0.f, 0.f, 0.f});
      SBAR();
    } }
  STAMP(9);
  BAR_L();
#pragma unroll
  for (int i = 0; i < 5; i++) { const int gr = kq * 5 + i; const LAS char* wb = lp + (gr < 17 ? L_WO + gr * 5120 : 145408 + (gr - 17) * 5120) + wlane;
#pragma unroll
    for (int t = 0; t < 5; t++) wf[i][t] = trd2(wb + t * 8, wb + 1280 + t * 8);
    SBAR(); of[1][i] = LD16(WS_OB + ofr + OT_B + i * 1024); SBAR(); }
  ARRIVE_NW(2);
  STAMP(10);
  LAS char* xw = lp + kq * 21504 + (tp * 32 + l15) * 336 + g * 80;
#pragma unroll
  for (int u = 0; u < 2; u++) {
#pragma unroll
    for (int i = 0; i < 5; i++) {
      if (__builtin_expect(missing(of[u][i]), 0)) {
#pragma unroll 1
        for (unsigned spin = 0; spin < SPIN_CAP; ++spin) {
          asm volatile("" ::: "memory");
#pragma unroll
          for (int j = u * 5 + i; j < 10; j++) of[j / 5][j % 5] = LD16(WS_OB + ofr + (j / 5) * OT_B + (j % 5) * 1024);
          if (!missing(of[u][i])) break;
          __builtin_amdgcn_s_sleep(1);
        }
      }
#pragma unroll
      for (int t = 0; t < 5; t++) c[u][t] = mfma16(wf[i][t], of[u][i], c[u][t]);
      if (u == 0 && i == 3) cpk = __hip_atomic_load(cnt + 2, __ATOMIC_RELAXED, __HIP_MEMORY_SCOPE_WORKGROUP);
      if (u == 1) { if (i == 0) { if (__builtin_expect((unsigned)__builtin_amdgcn_readfirstlane(cpk) < 8u, 0)) AWAIT(2, 8); asm volatile("" ::: "memory"); }
        *(LAS f32x4*)(xw + i * 16) = c[0][i]; }
      SBAR();
    }
  }
#pragma unroll
  for (int t = 0; t < 5; t++) *(LAS f32x4*)(xw + 16 * 336 + t * 16) = c[1][t];
  BAR_L();
  { const __amdgpu_buffer_rsrc_t orr = __builtin_amdgcn_make_buffer_rsrc(out, 0, 2048 * DM * 2, 0x00020000);
#pragma unroll
    for (int j = 0; j < 3; j++) { const int n = tid + 512 * j, nn = n < 1280 ? n : 0, row = (nn * 3277) >> 16, cq = nn - row * 20;
      const LAS f32x4* p = (const LAS f32x4*)(lp + row * 336 + cq * 16);
      const f32x4 z = (p[0] + p[1344]) + (p[2688] + p[4032]);
      __builtin_amdgcn_raw_buffer_store_b64((u32x2){pk(z[0], z[1]), pk(z[2], z[3])}, orr, n < 1280 ? ((rt * 128 + role * 64 + row) * DM + C0 + cq * 4) * 2 : 0x7FFF0000u, 0, 0); }
  }
  STAMP(11);
}
}

extern "C" void kernel_launch(void* const* d_in, const int* in_sizes, int n_in, void* d_out, int out_size, void* d_ws, size_t ws_size, hipStream_t stream) {
  using namespace gm;
  fused<<<256, 512, 0, stream>>>((const u16*)d_in[0], (const u16*)d_in[1], (const u16*)d_in[2], (const u16*)d_in[3], (const u16*)d_in[5], (const u16*)d_in[6],
                                 (const u16*)d_in[7], (const u16*)d_in[8], (const u16*)d_in[9], (u16*)d_out, (char*)d_ws);
}
```

```cpp
#include <hip/hip_runtime.h>
#include <hip/hip_bf16.h>
#include <stdint.h>

namespace gm {
typedef float f32x16 __attribute__((ext_vector_type(16)));
typedef float f32x4 __attribute__((ext_vector_type(4)));
typedef float f32x2 __attribute__((ext_vector_type(2)));
typedef short s16x4 __attribute__((ext_vector_type(4)));
typedef __bf16 b16x2 __attribute__((ext_vector_type(2)));
typedef __bf16 b16x8 __attribute__((ext_vector_type(8)));
typedef unsigned u32x4 __attribute__((ext_vector_type(4)));
typedef unsigned u32x2 __attribute__((ext_vector_type(2)));
typedef unsigned long long u64;
typedef unsigned short u16;
#define DI __device__ __forceinline__
#define LAS __attribute__((address_space(3)))
#define GAS __attribute__((address_space(1)))
typedef GAS u64 gu64;

constexpr int DM = 640;
constexpr unsigned WS_EPOCH = 0, WS_FKV = 2048, WS_FKVR = 4096, WS_FQ = 8192, WS_FPART = 16384, WS_FOA = 24576, WS_G = 57344;
constexpr unsigned WS_KVR = 8912896, KVR_B = 10240, WS_KV = 131072, WS_QX = 327680, QX_B = 10240, WS_PART = 1703936, PART_B = 15360;
constexpr unsigned WS_OA = 3670016, WS_OB = 6291456, WS_END = 8994816, OT_B = 20480;
constexpr int VB = 352, TILE_B = 1280 + 4 * VB, CH_B = TILE_B * 8, STEP_B = 2 * CH_B;
constexpr int STAGE_B = 36864, ST_W1 = 10240, ST_X = 20480;
constexpr int L_BUF = 43008, L_P0W = 0, L_P0X0 = 25600, L_P0X1 = 153600, L_RED = 158720, L_RED1 = 40960;
constexpr int L_HO = 86016, L_QL = 96256, L_VIM = 96256, L_PHK = 116736, L_WPHI = 158592, L_ZERO = 161792, L_CNT = 163712, L_TOTAL = 163840;
constexpr int L_WO = 43008;
constexpr float LOG2E = 1.4426950408889634f;
constexpr float QSCALE = 0.22360679774997896f * LOG2E;
constexpr unsigned SPIN_CAP = 20000, LSPIN_CAP = 400000;

DI unsigned pk(float a, float b) { f32x2 v = {a, b}; b16x2 r = __builtin_convertvector(v, b16x2); return __builtin_bit_cast(unsigned, r); }
DI f32x16 mfma32(u32x4 a, u32x4 b, f32x16 c) { return __builtin_amdgcn_mfma_f32_32x32x16_bf16(__builtin_bit_cast(b16x8, a), __builtin_bit_cast(b16x8, b), c, 0, 0, 0); }
DI f32x16 mfma32k8(u32x2 a, u32x2 b, f32x16 c) { return __builtin_amdgcn_mfma_f32_32x32x8bf16_1k(__builtin_bit_cast(s16x4, a), __builtin_bit_cast(s16x4, b), c, 0, 0, 0); }
DI f32x4 mfma16(u32x4 a, u32x4 b, f32x4 c) { return __builtin_amdgcn_mfma_f32_16x16x32_bf16(__builtin_bit_cast(b16x8, a), __builtin_bit_cast(b16x8, b), c, 0, 0, 0); }
DI u32x2 trd(const LAS char* p) { return __builtin_bit_cast(u32x2, __builtin_amdgcn_ds_read_tr16_b64_v4i16((LAS s16x4*)p)); }
DI u32x4 trd2(const LAS char* p0, const LAS char* p1) { u32x2 a = trd(p0), b = trd(p1); return (u32x4){a.x, a.y, b.x, b.y}; }
DI u32x4 ldsr16(const LAS char* p) { return *(const LAS u32x4*)p; }
DI u32x2 ldsr8(const LAS char* p) { return *(const LAS u32x2*)p; }
DI float ex2(float x) { return __builtin_amdgcn_exp2f(x); }
DI void dma16s(const void* base, unsigned voff, unsigned lds_base) {
  asm volatile("s_mov_b32 m0, %2\n\ts_nop 0\n\tglobal_load_lds_dwordx4 %0, %1" :: "v"(voff), "s"(base), "s"(lds_base) : "memory"); }
DI void dma16s_sc1(const void* base, unsigned voff, unsigned lds_base) {
  asm volatile("s_mov_b32 m0, %2\n\ts_nop 0\n\tglobal_load_lds_dwordx4 %0, %1 sc1" :: "v"(voff), "s"(base), "s"(lds_base) : "memory"); }
DI void dmag_open(unsigned lds_base) { asm volatile("s_mov_b32 m0, %0\n\ts_nop 0" :: "s"(lds_base) : "memory"); }
template <int J> DI void dmag(const void* base, unsigned voff) { asm volatile("global_load_lds_dwordx4 %0, %1 offset:%2" :: "v"(voff), "s"(base), "i"((J & 3) * 1024) : "memory"); }
template <int J> DI void dmag_sc1(const void* base, unsigned voff) { asm volatile("global_load_lds_dwordx4 %0, %1 offset:%2 sc1" :: "v"(voff), "s"(base), "i"((J & 3) * 1024) : "memory"); }
DI void dma4s(const void* base, unsigned voff, unsigned lds_base) {
  asm volatile("s_mov_b32 m0, %2\n\ts_nop 0\n\tglobal_load_lds_dword %0, %1" :: "v"(voff), "s"(base), "s"(lds_base) : "memory"); }
DI u32x4 ld16_async(const void* p) { u32x4 r; asm volatile("global_load_dwordx4 %0, %1, off" : "=v"(r) : "v"(p) : "memory"); return r; }
DI u32x2 ld8_sc1_async(const void* p) { u32x2 r; asm volatile("global_load_dwordx2 %0, %1, off sc1" : "=v"(r) : "v"(p) : "memory"); return r; }
#define BAR_L() asm volatile("s_waitcnt lgkmcnt(0)\n\ts_barrier" ::: "memory")
#define WAIT_BAR(N) asm volatile("s_waitcnt vmcnt(" #N ") lgkmcnt(0)\n\ts_barrier" ::: "memory")
#define WAIT_VM(N) asm volatile("s_waitcnt vmcnt(" #N ")" ::: "memory")
#define SBAR() __builtin_amdgcn_sched_barrier(0)
#define RLX_AGENT __ATOMIC_RELAXED, __HIP_MEMORY_SCOPE_AGENT
DI float half_swap_max(float m) { auto rr = __builtin_amdgcn_permlane32_swap(__float_as_uint(m), __float_as_uint(m), false, false); return fmaxf(__uint_as_float(rr[0]), __uint_as_float(rr[1])); }
DI float from_upper(float v) { auto rr = __builtin_amdgcn_permlane32_swap(__float_as_uint(v), __float_as_uint(v), false, false); return __uint_as_float(rr[1]); }

template <int NF>
DI bool flags_set(const char* ws, unsigned off, u64 tag, int lane) {
  gu64* f = (gu64*)(ws + off);
  bool ok = __hip_atomic_load(f + (NF < 64 ? (lane & (NF - 1)) : lane), RLX_AGENT) == tag;
  if (NF >= 128) ok &= __hip_atomic_load(f + 64 + lane, RLX_AGENT) == tag;
  if (NF >= 256) { ok &= __hip_atomic_load(f + 128 + lane, RLX_AGENT) == tag; ok &= __hip_atomic_load(f + 192 + lane, RLX_AGENT) == tag; }
  return ok;
}
template <int NF>
DI void wait_flags(const char* ws, unsigned off, u64 tag, int lane) {
#pragma unroll 1
  for (unsigned spin = 0; spin < SPIN_CAP; ++spin) {
    if (__all(flags_set<NF>(ws, off, tag, lane))) break;
    __builtin_amdgcn_s_sleep(2);
  }
  asm volatile("" ::: "memory");
}
DI void set_flag(char* ws, unsigned off, u64 tag, int lane) { if (lane == 0) __hip_atomic_store((gu64*)(ws + off), tag, RLX_AGENT); }
DI unsigned nz(unsigned v) { return v < 0xFFFFFFFEu ? v : 0xFFFFFFFEu; }
DI unsigned umax3(unsigned a, unsigned b, unsigned c) { unsigned m = a > b ? a : b; return m > c ? m : c; }
DI bool missing(u32x4 v) { return __any(umax3(umax3(v.x, v.y, v.z), v.w, 0u) == 0xFFFFFFFFu); }
#define ONES ((u32x4){0xFFFFFFFFu, 0xFFFFFFFFu, 0xFFFFFFFFu, 0xFFFFFFFFu})
#define ST16(v, off) __builtin_amdgcn_raw_buffer_store_b128((v), wsr, (off), 0, 16)
#define ST8(v, off) __builtin_amdgcn_raw_buffer_store_b64((v), wsr, (off), 0, 16)
#define LD16(off) __builtin_amdgcn_raw_buffer_load_b128(wsr, (off), 0, 16)
#define LD8(off) __builtin_amdgcn_raw_buffer_load_b64(wsr, (off), 0, 16)

#define STAMP(i) do {} while (0)

__global__ __launch_bounds__(512, 2) void fused(const u16* __restrict__ x, const u16* __restrict__ wqkv, const u16* __restrict__ wphi, const u16* __restrict__ woA,
                                                const u16* __restrict__ wq, const u16* __restrict__ wk, const u16* __restrict__ wv, const u16* __restrict__ woB,
                                                const u16* __restrict__ gatew, u16* __restrict__ out, char* ws) {
  __shared__ __attribute__((aligned(16))) char lds[L_TOTAL];
  const int tid = threadIdx.x, lane = tid & 63;
  const int w = __builtin_amdgcn_readfirstlane(tid >> 6) & 7;
  const int l15 = lane & 15, g = lane >> 4, q4 = l15 >> 2, p4 = lane & 3, r = lane & 31, h = lane >> 5;
  const int bx = blockIdx.x & 7, bi = blockIdx.x >> 3;
  const int rt = (bx & 1) * 8 + (bi & 7), hp = (bx >> 1) * 4 + ((bi >> 3) & 3), hq = hp >> 1, role = hp & 1;
  const int w3 = w & 3, w2 = w >> 2;
  LAS char* lp = (LAS char*)lds;
  const unsigned lds0 = (unsigned)(uintptr_t)lds;
  const __amdgpu_buffer_rsrc_t wsr = __builtin_amdgcn_make_buffer_rsrc(ws, 0, WS_END, 0x00020000);
  LAS unsigned* cnt = (LAS unsigned*)(lp + L_CNT);
#define ARRIVE_NW(c) do { asm volatile("" ::: "memory"); if (lane == 0) __hip_atomic_fetch_add(cnt + (c), 1u, __ATOMIC_RELAXED, __HIP_MEMORY_SCOPE_WORKGROUP); asm volatile("" ::: "memory"); } while (0)
#define PEEK(c) ((unsigned)__builtin_amdgcn_readfirstlane(__hip_atomic_load(cnt + (c), __ATOMIC_RELAXED, __HIP_MEMORY_SCOPE_WORKGROUP)))
#define AWAIT(c, n) do { _Pragma("unroll 1") for (unsigned sp_ = 0; sp_ < LSPIN_CAP; ++sp_) { if (PEEK(c) >= (unsigned)(n)) break; __builtin_amdgcn_s_sleep(1); } asm volatile("" ::: "memory"); } while (0)
  STAMP(0);

  const int T0 = rt * 128 + hq * 16;
  const int C0 = hq * 80;
  const int wlane = ((g >> 1) * 16 + (g & 1) * 4 + q4) * 160 + p4 * 40;
  u64 tag;
  unsigned o0[10];
#define KVP(s, slot, p) dma16s_sc1(ws + WS_KV + (s) * STEP_B + (p) * 1024, lane * 16, lds0 + (slot) * STEP_B + (p) * 1024)
#define KVL(s, slot) do { _Pragma("unroll") for (int j = 0; j < 11; j++) if (j < 10 || w3 < 2) KVP(s, slot, w3 + 4 * j); } while (0)
#define KW(s, slot, j) do { if ((j) == 4) KVP(s, slot, w * 5 + 4); else { if ((j) == 0) dmag_open(lds0 + (slot) * STEP_B + w * 5120); dmag_sc1<(j)>(ws + WS_KV + (s) * STEP_B + w * 5120, lane * 16); } } while (0)
#define KX(s, slot) do { if (w < 2) KVP(s, slot, 40 + w); } while (0)
  if (w2 == 0) {
    u32x4 gf0, gf1, gf2, gf3, gf4, wpc; u32x2 tch;
    u32x2 ep = ld8_sc1_async(ws + WS_EPOCH);
    const int lx = w3 < 3 ? L_P0X0 + w3 * 5120 : L_P0X1;
    { const u16* wsrc = (role ? wv : wk) + w3 * 3200;
#pragma unroll
      for (int j = 0; j < 5; j++) { const int n = j * 64 + lane, row = (n * 3277) >> 16, cc = n - row * 20; dma16s(x + T0 * DM + w3 * 160, row * 1280 + cc * 16, lds0 + lx + j * 1024); }
#pragma unroll
      for (int j = 0; j < 6; j++) dma16s(wsrc + j * 512, lane * 16, lds0 + L_P0W + w3 * 6400 + j * 1024);
      if (lane < 16) dma16s(wsrc + 6 * 512, lane * 16, lds0 + L_P0W + w3 * 6400 + 6 * 1024);
      const u16* gp = gatew + w3 * 160 + g * 8;
      gf0 = ld16_async(gp); gf1 = ld16_async(gp + 32); gf2 = ld16_async(gp + 64); gf3 = ld16_async(gp + 96); gf4 = ld16_async(gp + 128);
      wpc = ld16_async(wphi + hq * 1600 + (tid < 200 ? tid : 199) * 8);
      const char* tp = ws + (lane & 7) * 1114112;
      if (lane >= 8) tp = ws + (lane < 16 ? WS_FKV + blockIdx.x * 128 : WS_KVR + blockIdx.x * 320 + (lane & 3) * 96);
      if (w3 == 1) tp = (const char*)(woA + C0 + (lane & 7) * 51200);
      if (w3 == 2) tp = (const char*)(woB + C0 + (lane & 7) * 51200);
      if (w3 == 3) tp = (const char*)(out + (rt * 128 + role * 64) * DM + C0);
      tch = ld8_sc1_async(tp);
    }
    asm volatile("s_waitcnt vmcnt(1)" : "+v"(gf0), "+v"(gf1), "+v"(gf2), "+v"(gf3), "+v"(gf4), "+v"(wpc), "+v"(ep) :: "memory");
    tag = (((u64)(unsigned)__builtin_amdgcn_readfirstlane(ep.y) << 32) | (unsigned)__builtin_amdgcn_readfirstlane(ep.x)) + 1ull;
    STAMP(1);
    { const LAS char* wb = lp + L_P0W + (w3 * 160 + g * 8 + q4) * 40;
      const LAS char* xb = lp + lx + l15 * 320 + g * 16;
      const bool gl = l15 == 4;
      f32x4 c0 = {}, c1 = {};
#define P0STEP(s, GF) { const u32x4 b = ldsr16(xb + (s) * 64); c0 = mfma16(trd2(wb + p4 * 8 + (s) * 1280, wb + p4 * 8 + (s) * 1280 + 160), b, c0); \
        u32x4 a = trd2(wb + 32 + (s) * 1280, wb + 32 + (s) * 1280 + 160); if (gl) a = GF; c1 = mfma16(a, b, c1); }
      P0STEP(0, gf0) P0STEP(1, gf1) P0STEP(2, gf2) P0STEP(3, gf3) P0STEP(4, gf4)
#undef P0STEP
      *(LAS f32x4*)(lp + L_RED + tid * 16) = c0;
      if (lane < 32) *(LAS f32x4*)(lp + L_RED1 + (w3 * 32 + lane) * 16) = c1; }
    STAMP(2);
    f32x16 acc[5];
#pragma unroll
    for (int b = 0; b < 5; b++) acc[b] = (f32x16){};
    const int wm = g & 1, whd = p4;
    const int wo0 = wm * ST_W1 + (8 * h + 4 * wm + q4) * 160 + whd * 40, wo1 = wm * ST_W1 + (8 * h + 4 * (wm ^ 1) + q4) * 160 + whd * 40;
    const int xo = ST_X + (w3 * 32 + r) * 128 + ((h ^ ((r >> 1) & 7)) * 16);
    u32x4 fa[5], fb[5], xa, xb;
#pragma unroll
    for (int j = 0; j < 5; j++) fb[j] = (u32x4){0u, 0u, 0u, 0u};
    xb = (u32x4){0u, 0u, 0u, 0u};
#define KSTEP(FN, XN, s, FC, XC) { XN = ldsr16(sb + (xo ^ ((s) * 32))); \
      _Pragma("unroll") for (int j = 0; j < 5; j++) { acc[j] = mfma32(FC[j], XC, acc[j]); FN[j] = trd2(sb + wo0 + (s) * 2560 + j * 8, sb + wo1 + (s) * 2560 + j * 8); SBAR(); } }
    int bsel = 2;
#pragma unroll 1
    for (int i = 0; i < 10; i++) {
      BAR_L();
      if (i == 1 && role) {
        const unsigned qb = WS_QX + (rt * 8 + hq) * QX_B + w3 * 1280 + lane * 16;
        ST16(ONES, qb); ST16(ONES, qb + 5120); if (lane < 32) ST16(ONES, qb + 1024 + (lane >> 4) * (5120 - 256)); }
      if (i == 4) { asm volatile("s_waitcnt vmcnt(0)" : "+v"(tch) :: "memory"); if (role) set_flag(ws, WS_FQ + ((rt * 8 + hq) * 4 + w3) * 8, tag, lane);
                    if (tid < 200) *(LAS u32x4*)(lp + L_WPHI + tid * 16) = wpc; }
      const LAS char* sb = lp + L_BUF + bsel * STAGE_B;
      bsel = bsel == 2 ? 0 : bsel + 1;
      KSTEP(fa, xa, 0, fb, xb) KSTEP(fb, xb, 1, fa, xa) KSTEP(fa, xa, 2, fb, xb)
      { xb = ldsr16(sb + (xo ^ 96));
        acc[0] = mfma32(fa[0], xa, acc[0]); fb[0] = trd2(sb + wo0 + 7680, sb + wo1 + 7680); fb[1] = trd2(sb + wo0 + 7688, sb + wo1 + 7688); SBAR();
        acc[1] = mfma32(fa[1], xa, acc[1]); fb[2] = trd2(sb + wo0 + 7696, sb + wo1 + 7696); fb[3] = trd2(sb + wo0 + 7704, sb + wo1 + 7704); SBAR();
        acc[2] = mfma32(fa[2], xa, acc[2]); fb[4] = trd2(sb + wo0 + 7712, sb + wo1 + 7712); SBAR();
        acc[3] = mfma32(fa[3], xa, acc[3]); acc[4] = mfma32(fa[4], xa, acc[4]); SBAR(); }
    }
#pragma unroll
    for (int j = 0; j < 5; j++) acc[j] = mfma32(fb[j], xb, acc[j]);
#undef KSTEP
    STAMP(3);
#define PK2(t, i, sc) pk(acc[t][i] * (sc), acc[t][(i) + 1] * (sc))
#define ROW(i, sc) const u32x4 ra = {PK2(0, i, sc), PK2(0, (i) + 2, sc), PK2(1, i, sc), PK2(1, (i) + 2, sc)}, rb = {PK2(2, i, sc), PK2(2, (i) + 2, sc), PK2(3, i, sc), PK2(3, (i) + 2, sc)}; const u32x2 rc = {PK2(4, i, sc), PK2(4, (i) + 2, sc)}
    { ROW(4, 1.0f);
      *(LAS u32x4*)(lp + L_HO + tid * 16) = ra; *(LAS u32x4*)(lp + L_HO + 4096 + tid * 16) = rb; *(LAS u32x2*)(lp + L_HO + 8192 + tid * 8) = rc; }
    BAR_L();
    const int qfo = (h * 4 + w3) * 1280;
    if (role) {
      { ROW(8, QSCALE); const unsigned qb = WS_QX + (rt * 8 + hq) * QX_B + qfo;
        ST16(((u32x4){nz(ra.x), nz(ra.y), nz(ra.z), nz(ra.w)}), qb + r * 16); ST16(((u32x4){nz(rb.x), nz(rb.y), nz(rb.z), nz(rb.w)}), qb + 512 + r * 16); ST8(((u32x2){nz(rc.x), nz(rc.y)}), qb + 1024 + r * 8); }
      { ROW(12, QSCALE); LAS char* d = lp + L_QL + qfo;
        *(LAS u32x4*)(d + r * 16) = ra; *(LAS u32x4*)(d + 512 + r * 16) = rb; *(LAS u32x2*)(d + 1024 + r * 8) = rc; }
    } else { LAS char* d = lp + L_VIM + (h * 128 + w3 * 32 + r) * 40;
#pragma unroll
      for (int t = 0; t < 5; t++) { *(LAS u32x2*)(d + t * 8) = (u32x2){PK2(t, 8, 1.0f), PK2(t, 10, 1.0f)}; *(LAS u32x2*)(d + 10240 + t * 8) = (u32x2){PK2(t, 12, 1.0f), PK2(t, 14, 1.0f)}; } }
#pragma unroll
    for (int t = 0; t < 5; t++) { o0[2 * t] = PK2(t, 0, 1.0f); o0[2 * t + 1] = PK2(t, 2, 1.0f); }
#undef ROW
#undef PK2
  } else {
    u32x2 ep = ld8_sc1_async(ws + WS_EPOCH);
    const bool second = w3 >> 1;
    const unsigned rs = (second && role) ? 1280u : 3840u;
    const char* wsrc = (second ? (role ? (const char*)(wq + C0) : (const char*)(wqkv + 1280 + C0)) : (const char*)(wqkv + (role ? 0 : 640) + C0)) + (w3 & 1) * 32 * rs;
    const char* xsrc = (const char*)(x + (rt * 128 + w3 * 32) * DM);
    int rot = (bi & 7) + 3 * ((bi >> 3) & 3); rot = rot >= 10 ? rot - 10 : rot;
    unsigned so[5], xoff[2];
#pragma unroll
    for (int j = 0; j < 5; j++) { const int c = j * 64 + lane, rho = (c * 6554) >> 16, cc = c - rho * 10; so[j] = (rho ^ (second ? 4 : 0)) * rs + cc * 16; }
#pragma unroll
    for (int j = 0; j < 2; j++) { const int row = j * 8 + (lane >> 3); xoff[j] = row * 1280 + (((lane & 7) ^ ((row >> 1) & 7)) * 16); }
    int bsel = 2, kc = rot;
    auto stage = [&]() {
      const unsigned lb = lds0 + L_BUF + bsel * STAGE_B;
      const char* bw = wsrc + kc * 64 * rs; const char* bxp = xsrc + kc * 128;
#pragma unroll
      for (int j = 0; j < 5; j++) { dma16s(bw, so[j], lb + (w3 * 5 + j) * 1024); if (j < 4) dma16s(bxp + (j >> 1) * 16 * 1280, xoff[j & 1], lb + ST_X + (w3 * 4 + j) * 1024); }
      bsel = bsel == 2 ? 0 : bsel + 1; kc = kc == 9 ? 0 : kc + 1;
    };
    stage(); stage();
    asm volatile("s_waitcnt vmcnt(9)" : "+v"(ep) :: "memory");
    tag = (((u64)(unsigned)__builtin_amdgcn_readfirstlane(ep.y) << 32) | (unsigned)__builtin_amdgcn_readfirstlane(ep.x)) + 1ull;
    STAMP(1); STAMP(2);
#pragma unroll 1
    for (int i = 0; i < 8; i++) {
      WAIT_BAR(9);
      stage();
      if (i == 0 && w3 == 0) {
        const unsigned tb = WS_KV + (T0 >> 5) * TILE_B; const int s16 = hq & 1;
        if (!role) {
          const LAS f32x4* rp = (const LAS f32x4*)(lp + (h ? L_RED1 : L_RED)) + (h ? (g & 1) * 16 : g * 32) + l15;
          const int st = h ? 32 : 64, o2 = h ? 0 : 16;
          const f32x4 a = (rp[0] + rp[st]) + (rp[2 * st] + rp[3 * st]), b = (rp[o2] + rp[st + o2]) + (rp[2 * st + o2] + rp[3 * st + o2]);
          const u32x4 v = {pk(a[0], a[1]), pk(a[2], a[3]), pk(b[0], b[1]), pk(b[2], b[3])};
          if (lane < 32) ST16(v, tb + (g * 32 + s16 * 16 + l15) * 16);
          else if (lane < 48) ST8(((u32x2){v.x, v.y}), tb + 1024 + (s16 * 16 + l15) * 8);
          else { const float gv = 1.0f / (1.0f + __expf(-a[0])); __builtin_amdgcn_raw_buffer_store_b32(__float_as_uint(gv), wsr, WS_G + (T0 + l15) * 4, 0, 16); }
        } else if (lane < 44) {
          const int hh = lane >= 22, d = lane - hh * 22, dd = d < 20 ? d : 0, st = dd >= 16 ? 128 : 256;
          const LAS float* rp = (const LAS float*)(lp + (dd >= 16 ? L_RED1 : L_RED)) + (((dd & 15) >> 2) * 16 + 4 * hh) * 4 + (dd & 3);
          float e[8];
#pragma unroll
          for (int j = 0; j < 8; j++) { const int tk = (8 * (j >> 2) + (j & 3)) * 4; e[j] = (rp[tk] + rp[tk + st]) + (rp[tk + 2 * st] + rp[tk + 3 * st]); }
          u32x4 v = {pk(e[0], e[1]), pk(e[2], e[3]), pk(e[4], e[5]), pk(e[6], e[7])};
          if (d == 20) v = (u32x4){0x3F803F80u, 0x3F803F80u, 0x3F803F80u, 0x3F803F80u};
          if (d == 21) v = (u32x4){0u, 0u, 0u, 0u};
          ST16(v, tb + 1280 + (s16 * 2 + hh) * VB + d * 16);
        }
      }
      if (i == 2) { if (w3 == 0) set_flag(ws, WS_FKV + blockIdx.x * 8, tag, lane);
                    if (w3 >= 2) *(LAS u32x4*)(lp + L_ZERO + (tid - 384) * 16) = (u32x4){0u, 0u, 0u, 0u}; }
    }
    u64 fa, fb, fc, fd, fe;
    { gu64* f = (gu64*)(ws + WS_FKV) + lane;
      fa = __hip_atomic_load(f, RLX_AGENT); fb = __hip_atomic_load(f + 64, RLX_AGENT); fc = __hip_atomic_load(f + 128, RLX_AGENT); fd = __hip_atomic_load(f + 192, RLX_AGENT);
      fe = __hip_atomic_load((gu64*)(ws + WS_FQ) + (rt * 8 + hq) * 4 + p4, RLX_AGENT); }
    WAIT_BAR(14);
    asm volatile("" : "+v"(fa), "+v"(fb), "+v"(fc), "+v"(fd), "+v"(fe));
    const bool kvok = __all((fa == tag) & (fb == tag) & (fc == tag) & (fd == tag) & (fe == tag));
    asm volatile("" ::: "memory");
    BAR_L();
    if (!kvok) { wait_flags<256>(ws, WS_FKV, tag, lane); wait_flags<4>(ws, WS_FQ + (rt * 8 + hq) * 32, tag, lane); }
    KVL(0, 0);
    if (blockIdx.x == 0 && w3 == 0) set_flag(ws, WS_EPOCH, tag, lane);
    STAMP(3);
    BAR_L();
    const int ht = tid - 256;
    const u32x4 a = ldsr16(lp + L_HO + ht * 16), b = ldsr16(lp + L_HO + 4096 + ht * 16); const u32x2 c = ldsr8(lp + L_HO + 8192 + ht * 8);
    o0[0] = a.x; o0[1] = a.y; o0[2] = a.z; o0[3] = a.w; o0[4] = b.x; o0[5] = b.y; o0[6] = b.z; o0[7] = b.w; o0[8] = c.x; o0[9] = c.y;
  }
  STAMP(4);
  u32x4 ph[5];
  { const LAS char* pb = ((p4 & 1) == h) ? lp + L_WPHI + (2 * w2 + h) * 800 + q4 * 40 + (2 * (g & 1) + (p4 >> 1)) * 8 : lp + L_ZERO;
    const u32x4 b0 = {o0[0], o0[1], o0[2], o0[3]}, b1 = {o0[4], o0[5], o0[6], o0[7]}, b2 = {o0[8], o0[9], 0u, 0u};
    const u32x2 c0 = trd(pb + 640), c1 = trd(pb + 672);
    f32x16 P0 = {}, P1 = {};
    P0 = mfma32(trd2(pb, pb + 160), b0, P0); P1 = mfma32(trd2(pb + 32, pb + 192), b0, P1);
    P0 = mfma32(trd2(pb + 320, pb + 480), b1, P0); P1 = mfma32(trd2(pb + 352, pb + 512), b1, P1);
    P0 = mfma32((u32x4){c0.x, c0.y, 0u, 0u}, b2, P0); P1 = mfma32((u32x4){c1.x, c1.y, 0u, 0u}, b2, P1);
    KX(1, 1); KW(1, 1, 0); SBAR();
    f32x2 p[10];
#pragma unroll
    for (int f = 0; f < 10; f++) p[f] = f < 8 ? (f32x2){P0[(2 * f) & 15], P0[(2 * f + 1) & 15]} : (f32x2){P1[(2 * f) & 3], P1[(2 * f + 1) & 3]};
    float mx = fmaxf(p[0][0], p[0][1]), mn = fminf(p[0][0], p[0][1]);
#pragma unroll
    for (int f = 1; f < 10; f++) { mx = fmaxf(fmaxf(mx, p[f][0]), p[f][1]); mn = fminf(fminf(mn, p[f][0]), p[f][1]); }
    const f32x2 ma = {-mx * LOG2E, -mx * LOG2E}, mb = {mn * LOG2E, mn * LOG2E}, ca_ = {LOG2E, LOG2E}, cb_ = {-LOG2E, -LOG2E};
    f32x2 ea[10], eb[10], sa = {0.f, 0.f}, sb = {0.f, 0.f};
#pragma unroll
    for (int f = 0; f < 10; f++) { if (f % 2 == 0 && f < 8) { SBAR(); if (f == 0) KW(1, 1, 1); if (f == 2) KW(1, 1, 2); if (f == 4) KW(1, 1, 3); if (f == 6) KW(1, 1, 4); SBAR(); }
      const f32x2 xa = __builtin_elementwise_fma(p[f], ca_, ma), xb = __builtin_elementwise_fma(p[f], cb_, mb);
      ea[f] = (f32x2){ex2(xa[0]), ex2(xa[1])}; eb[f] = (f32x2){ex2(xb[0]), ex2(xb[1])}; sa += ea[f]; sb += eb[f]; }
    const float ia = __builtin_amdgcn_rcpf(sa[0] + sa[1]), ib = __builtin_amdgcn_rcpf(sb[0] + sb[1]);
    unsigned o[20];
#pragma unroll
    for (int f = 0; f < 10; f++) { const f32x2 ya = ea[f] * ia, yb = eb[f] * ib; o[f] = pk(ya[0], ya[1]); o[10 + f] = pk(yb[0], yb[1]); }
#pragma unroll
    for (int c = 0; c < 5; c++) ph[c] = (u32x4){o[4 * c], o[4 * c + 1], o[4 * c + 2], o[4 * c + 3]};
    asm volatile("" : "+v"(ph[0]), "+v"(ph[1]), "+v"(ph[2]), "+v"(ph[3]), "+v"(ph[4]));
  }
  u32x4 q1A, q1B; u32x2 q2A, q2B;
  if (role) {
    WAIT_BAR(0);
    STAMP(5);
    const LAS char* qa = lp + L_QL + w3 * 1280;
    q1A = ldsr16(qa + lane * 16); q1B = ldsr16(qa + 5120 + lane * 16); q2A = ldsr8(qa + 1024 + r * 8); q2B = ldsr8(qa + 6144 + r * 8);
    ARRIVE_NW(0);
  } else {
    const unsigned qb = WS_QX + (rt * 8 + hq) * QX_B + w3 * 1280;
    q1A = LD16(qb + lane * 16); q1B = LD16(qb + 5120 + lane * 16); q2A = LD8(qb + 1024 + r * 8); q2B = LD8(qb + 6144 + r * 8);
    { LAS char* d = lp + L_PHK + ((2 * w2 + h) * 128 + w3 * 32 + r) * 80;
#pragma unroll
      for (int c = 0; c < 5; c++) *(LAS u32x4*)(d + c * 16) = ph[c]; }
    WAIT_BAR(4);
    STAMP(5);
#pragma unroll 1
    for (unsigned spin = 0; spin < SPIN_CAP; ++spin) {
      if (!(missing(q1A) | missing(q1B) | missing((u32x4){q2A.x, q2A.y, q2B.x, q2B.y}))) break;
      __builtin_amdgcn_s_sleep(1);
      asm volatile("" ::: "memory");
      q1A = LD16(qb + lane * 16); q1B = LD16(qb + 5120 + lane * 16); q2A = LD8(qb + 1024 + r * 8); q2B = LD8(qb + 6144 + r * 8);
    }
  }
  STAMP(6);
  const int vr = r <= 21 ? r : 21;
  const int ok1 = lane * 16, ok2 = h ? (1280 + 20 * 16) : (1024 + r * 8), ov = 1280 + h * VB + vr * 16;
  const f32x16 Z = {};
  if (h) { q2A = (u32x2){0u, 0u}; q2B = (u32x2){0u, 0u}; }
  { const LAS char* t0 = lp;
    const u32x4 k1 = ldsr16(t0 + ok1); const u32x2 k2 = ldsr8(t0 + ok2);
    f32x16 sa = mfma32(k1, q1A, Z); sa = mfma32k8(k2, q2A, sa);
    f32x16 sb = mfma32(k1, q1B, Z); sb = mfma32k8(k2, q2B, sb);
    float ma = sa[0], mb = sb[0];
#pragma unroll
    for (int i = 1; i < 16; i++) { ma = fmaxf(ma, sa[i]); mb = fmaxf(mb, sb[i]); }
    ma = half_swap_max(ma); mb = half_swap_max(mb);
    const float mah = __uint_as_float(__float_as_uint(ma) & 0xFFFF0000u), mbh = __uint_as_float(__float_as_uint(mb) & 0xFFFF0000u);
    if (h) { q2A.x = pk(-mah, mah - ma); q2B.x = pk(-mbh, mbh - mb); } }
  f32x16 OA = {}, OB = {}, SA, SB;
  u32x4 k1, v1, v2, v2n, pA1, pA2, pB1, pB2; u32x2 k2;
  float e0, e1, e2, e3;
  const LAS char* c0p = lp + w2 * CH_B + ok1; const LAS char* c1p = lp + w2 * CH_B + ok2; const LAS char* c2p = lp + w2 * CH_B + ov;
  k1 = ldsr16(c0p); k2 = ldsr8(c1p);
  SA = mfma32(k1, q1A, Z); SA = mfma32k8(k2, q2A, SA);
  v1 = ldsr16(c2p); v2 = v1;
  e0 = e1 = e2 = e3 = 0.f; pB2 = (u32x4){0u, 0u, 0u, 0u};
#define G(MF, LD, C0_, C1_, X, S_, i) MF; LD; SBAR(); C0_; C1_; SBAR(); X##0 = ex2(S_[i]); X##1 = ex2(S_[i + 1]); X##2 = ex2(S_[i + 2]); X##3 = ex2(S_[i + 3]); asm volatile("" : "+v"(X##0), "+v"(X##1), "+v"(X##2), "+v"(X##3)); SBAR();
#define TILE(TB, TN, V2, H0, H1, H2) { float a0, a1, a2, a3; \
    G(SB = mfma32(k1, q1B, Z),    k1 = ldsr16(c0p + (TN)), pB2[2] = pk(e0, e1), pB2[3] = pk(e2, e3), a, SA, 0) \
    G(OB = mfma32(v2, pB2, OB),   V2,                      pA1[0] = pk(a0, a1), pA1[1] = pk(a2, a3), e, SA, 4) \
    G(SB = mfma32k8(k2, q2B, SB), k2 = ldsr8(c1p + (TN)),  pA1[2] = pk(e0, e1), pA1[3] = pk(e2, e3), a, SA, 8) \
    G(OA = mfma32(v1, pA1, OA),   H0,                      pA2[0] = pk(a0, a1), pA2[1] = pk(a2, a3), e, SA, 12) \
    G(SA = mfma32(k1, q1A, Z),    H1,                      pA2[2] = pk(e0, e1), pA2[3] = pk(e2, e3), a, SB, 0) \
    G(OA = mfma32(v2, pA2, OA),   H2,                      pB1[0] = pk(a0, a1), pB1[1] = pk(a2, a3), e, SB, 4) \
    G(SA = mfma32k8(k2, q2A, SA), (void)0,                 pB1[2] = pk(e0, e1), pB1[3] = pk(e2, e3), a, SB, 8) \
    G(OB = mfma32(v1, pB1, OB),   v1 = ldsr16(c2p + (TN)), pB2[0] = pk(a0, a1), pB2[1] = pk(a2, a3), e, SB, 12) }
#define NOP (void)0
#define TL(sl, t, H0, H1, H2) TILE((sl) * STEP_B + (t) * TILE_B, (sl) * STEP_B + ((t) + 1) * TILE_B, v2 = ldsr16(c2p + (sl) * STEP_B + (t) * TILE_B + 2 * VB), H0, H1, H2)
#define TL6(sl, H0, H1) TL(sl, 6, H0, H1, v2n = ldsr16(c2p + (sl) * STEP_B + 7 * TILE_B + 2 * VB))
#define TL7(sl, sn, H0, H1, H2) TILE((sl) * STEP_B + 7 * TILE_B, (sn) * STEP_B, v2 = v2n, H0, H1, H2)
  unsigned woff[5];
#pragma unroll
  for (int j = 0; j < 5; j++) { const int c = j * 64 + lane, rho = (c * 6554) >> 16, cc = c - rho * 10; const int k = (rho & ~12) | ((rho & 4) << 1) | ((rho & 8) >> 1); woff[j] = k * 1280 + cc * 16 + (4 - j) * 1024; }
#define WOG(wo, gr, j, dst) do { if ((j) == 4) WOP(wo, gr, 4, dst); else { if ((j) == 0) dmag_open(lds0 + (dst)); dmag<(j)>((wo) + C0 + (gr) * 32 * 640 - 2048, woff[j]); } } while (0)
#define WOP(wo, gr, j, dst) dma16s((wo) + C0 + (gr) * 32 * 640 - (4 - (j)) * 512, woff[j], lds0 + (dst) + (j) * 1024)
#define WA(gr, j) WOG(woA, gr, j, L_WO + (gr) * 5120)
  const u16* wxs = w < 4 ? woA + (16 + w3) * 32 * 640 : woB + (w < 7 ? 17 + w3 : 19) * 32 * 640;
  const int wxd = w < 4 ? L_WO + (16 + w3) * 5120 : 145408 + (w < 7 ? w3 : 2) * 5120;
#define WX(j) WOG(wxs, 0, j, wxd)
#define PRIO(n) __builtin_amdgcn_s_setprio(n)
  const unsigned fo = (rt * 8 + hq) * OT_B + lane * 16;
#define FILL(j) do { const int n_ = w * 3 + ((j) >> 1); ST16(ONES, (((j) & 1) ? WS_OB : WS_OA) + fo + (n_ < 20 ? n_ : 19) * 1024); } while (0)
#define CLR_VM() __builtin_amdgcn_s_waitcnt(0x0F70)
  const bool red = !role && w == 4;
  f32x4 part[16]; u64 fp0, fp1;
  auto red_load = [&]() {
    asm volatile("" : "+v"(fp0), "+v"(fp1));
    if (!__all((fp0 == tag) & (fp1 == tag))) wait_flags<128>(ws, WS_FPART + hq * 1024, tag, lane);
    asm volatile("" ::: "memory");
#pragma unroll
    for (int j = 0; j < 16; j++) part[j] = __builtin_bit_cast(f32x4, LD16(WS_PART + (hq * 16 + j) * PART_B + (rt * 60 + (lane < 60 ? lane : 59)) * 16));
  };
  auto red_store = [&]() {
    f32x4 a = ((part[0] + part[1]) + (part[2] + part[3])) + ((part[4] + part[5]) + (part[6] + part[7]));
    a += ((part[8] + part[9]) + (part[10] + part[11])) + ((part[12] + part[13]) + (part[14] + part[15]));
    const int hd = rt >> 2, u = (rt & 3) * 60 + lane, hi = u >= 160, fq = hi ? (u - 160) >> 3 : u >> 4, di = hi ? u & 7 : u & 15;
    if (lane < 60) ST8(((u32x2){pk(a[0], a[1]), pk(a[2], a[3])}), WS_KVR + hq * KVR_B + (((hd >> 1) * 2 + hi) * 5 + (fq >> 1)) * 512 + ((hd & 1) * 16 + di) * 16 + (fq & 1) * 8);
  };
  u32x4 ka[10]; u64 fkr; float gta;
  auto oa_load = [&]() {
    asm volatile("" : "+v"(fkr));
    if (!__all(fkr == tag)) wait_flags<16>(ws, WS_FKVR + hq * 128, tag, lane);
    asm volatile("" ::: "memory");
    const unsigned kb = (((r >> 2) & 1) == h) ? WS_KVR + hq * KVR_B + w2 * 5120 + (h * 16 + (r & 3) + 4 * (r >> 3)) * 16 : 0x7FFF0000u;
#pragma unroll
    for (int j = 0; j < 10; j++) ka[j] = LD16(kb + j * 512);
    gta = __uint_as_float(__builtin_amdgcn_raw_buffer_load_b32(wsr, WS_G + (rt * 128 + w3 * 32 + r) * 4, 0, 16));
  };
  u32x4 ja[2][3], jb[2]; f32x4 jc[3];
  const LAS char* jab = lp + L_PHK + ((w >> 1) * 128 + g * 8 + q4) * 80 + p4 * 8;
  const LAS char* jbb = lp + L_VIM + ((w >> 1) * 128 + g * 8 + q4) * 40 + (w & 1) * 32 + p4 * 8;
  auto kv_rd = [&](int k0) {
#pragma unroll
    for (int ks = 0; ks < 2; ks++) { jb[ks] = trd2(jbb + (k0 + ks) * 1280, jbb + (k0 + ks) * 1280 + 160);
#pragma unroll
      for (int ft = 0; ft < 3; ft++) ja[ks][ft] = trd2(jab + (k0 + ks) * 2560 + ft * 32, jab + (k0 + ks) * 2560 + 320 + ft * 32); } };
  auto kv_mm = [&](bool first) {
#pragma unroll
    for (int ks = 0; ks < 2; ks++) { if ((w & 1) && l15 == 4) jb[ks] = (u32x4){0x3F803F80u, 0x3F803F80u, 0x3F803F80u, 0x3F803F80u};
#pragma unroll
      for (int ft = 0; ft < 3; ft++) jc[ft] = mfma16(ja[ks][ft], jb[ks], (first && !ks) ? (f32x4){0.f, 0.f, 0.f, 0.f} : jc[ft]); } };
  auto kv_st = [&]() { const int dt = w & 1; const unsigned pb = WS_PART + (hq * 16 + rt) * PART_B + (w >> 1) * 3840 + (dt ? 2560 + g * 128 : g * 256) + l15 * 16;
#pragma unroll
    for (int ft = 0; ft < 3; ft++) if ((!dt || l15 < 8) && ft * 4 + g < 10) ST16(__builtin_bit_cast(u32x4, jc[ft]), pb + ft * (dt ? 512 : 1024)); };
  f32x16 oa0, oa1; u32x4 z0, z1, z2;
  LAS char* otb = lp + L_WO + (8 + w) * 5120;
  auto oa_mm = [&](int s) { oa0 = mfma32(ka[s], ph[s], s ? oa0 : Z); oa1 = mfma32(ka[5 + s], ph[s], s ? oa1 : Z); };
  auto oa_norm = [&]() { const float inv = __builtin_amdgcn_rcpf(oa1[4] + 1e-6f) * gta;
#pragma unroll
    for (int t = 0; t < 5; t++) { const int k0 = h * 20 + 4 * t;
      const u32x2 v = t < 4 ? (u32x2){nz(pk(oa0[(4 * t) & 15] * inv, oa0[(4 * t + 1) & 15] * inv)), nz(pk(oa0[(4 * t + 2) & 15] * inv, oa0[(4 * t + 3) & 15] * inv))} : (u32x2){nz(pk(oa1[0] * inv, oa1[1] * inv)), nz(pk(oa1[2] * inv, oa1[3] * inv))};
      *(LAS u32x2*)(otb + (r >> 4) * 1280 + (k0 >> 3) * 256 + (k0 & 4) * 2 + l15 * 16) = v; }
    z0 = ldsr16(otb + lane * 16); z1 = ldsr16(otb + 1280 + lane * 16); z2 = ldsr16(otb + (r >> 4) * 1280 + 1024 + l15 * 16); };
  auto oa_st = [&]() { const unsigned oa = WS_OA + (rt * 8 + w3 * 2) * OT_B + (hq * 2 + w2) * 1280;
    ST16(z0, oa + lane * 16); ST16(z1, oa + OT_B + lane * 16); if (!h) ST16(z2, oa + (r >> 4) * OT_B + 1024 + l15 * 16); };
  unsigned cpk;
  PRIO(3);
  TL(0, 0, if (!role) kv_rd(0); else FILL(0), NOP, NOP)
  TL(0, 1, if (!role) { kv_mm(true); kv_rd(2); ARRIVE_NW(0); } else FILL(1), NOP, NOP)
  TL(0, 2, if (!role) kv_mm(false); else FILL(2), NOP, if (!role) kv_st())
  TL(0, 3, NOP, NOP, cpk = __hip_atomic_load(cnt, __ATOMIC_RELAXED, __HIP_MEMORY_SCOPE_WORKGROUP))
  STAMP(14);
  if ((unsigned)__builtin_amdgcn_readfirstlane(cpk) < 8u) AWAIT(0, 8);
  asm volatile("" ::: "memory");
  TL(0, 4, NOP, NOP, if (!role) { WAIT_VM(0); set_flag(ws, WS_FPART + ((hq * 16 + rt) * 8 + w) * 8, tag, lane); } else FILL(3))
  PRIO(2);
  TL(0, 5, KX(2, 2), NOP, KW(2, 2, 0)) TL6(0, KW(2, 2, 1), KW(2, 2, 2))
  TL7(0, 1, KW(2, 2, 3), NOP, KW(2, 2, 4))
  STAMP(15);
  TL(1, 0, if (role) FILL(4), NOP, if (red) { fp0 = __hip_atomic_load((gu64*)(ws + WS_FPART) + hq * 128 + lane, RLX_AGENT); fp1 = __hip_atomic_load((gu64*)(ws + WS_FPART) + hq * 128 + 64 + lane, RLX_AGENT); })
  TL(1, 1, if (w < 4) { const int row = (w & 1) * 32 + (lane >> 1);
                      dma4s((w & 2 ? woB : woA) + C0 + ((rt & 7) * 2 + role) * 40 * 640, (row < 40 ? row : 39) * 1280 + (lane & 1) * 156, lds0 + 157696); }, NOP, if (role) FILL(5))
  TL(1, 2, NOP, NOP, if (red) red_load())
  PRIO(1);
  TL(1, 3, NOP, NOP, NOP)
  TL(1, 4, NOP, CLR_VM(), if (red) red_store())
  TL(1, 5, NOP, NOP, NOP)
  PRIO(0);
  TL6(1, if (red) { WAIT_VM(0); set_flag(ws, WS_FKVR + (hq * 16 + rt) * 8, tag, lane); }, NOP)
  if (red) WAIT_BAR(1); else WAIT_BAR(0);
  STAMP(12);
  PRIO(3);
  if (role) set_flag(ws, WS_FOA + ((rt * 8 + hq) * 8 + w) * 8, tag, lane);
  u64 foa;
  TL7(1, 2, KX(3, 0), KW(3, 0, 0), KW(3, 0, 1))
  TL(2, 0, KW(3, 0, 2), KW(3, 0, 3), KW(3, 0, 4)) TL(2, 1, WA(w, 0), WA(w, 1), WA(w, 2))
  TL(2, 2, WA(w, 3), WA(w, 4), (foa = __hip_atomic_load((gu64*)(ws + WS_FOA) + rt * 64 + lane, RLX_AGENT), fkr = __hip_atomic_load((gu64*)(ws + WS_FKVR) + hq * 16 + l15, RLX_AGENT)))
  PRIO(2);
  TL(2, 3, NOP, NOP, NOP)
  auto chk = [&]() { asm volatile("" : "+v"(foa), "+v"(fkr));
    if (!__all(foa == tag)) wait_flags<64>(ws, WS_FOA + rt * 512, tag, lane);
    asm volatile("" ::: "memory"); if (role) oa_load(); };
  TL(2, 4, chk(), NOP, NOP)
  PRIO(1);
  TL(2, 5, NOP, NOP, if (role) oa_mm(0))
  PRIO(0);
  TL6(2, if (role) oa_mm(1), if (role) oa_mm(2))
  BAR_L();
  STAMP(13);
  PRIO(3);
  TL7(2, 0, if (role) oa_mm(3), { if (role) oa_mm(4); CLR_VM(); }, WX(0))
  TL(0, 0, if (role) oa_norm(), WX(1), WX(2))
  TL(0, 1, if (role) oa_st(), WX(3), WX(4))
  TL(0, 2, WA(8 + w, 0), WA(8 + w, 1), WA(8 + w, 2))
  u32x4 wbr[11];
  const char* wbs = (const char*)(woB + C0 + w * 32 * 640) - 4096;
#define WB2(j) do { wbr[j] = *(const GAS u32x4*)(wbs + woff[j] + (j) * 1024); wbr[5 + (j)] = *(const GAS u32x4*)(wbs + 8 * 32 * 1280 + woff[j] + (j) * 1024); } while (0)
  TL(0, 3, WA(8 + w, 3), WA(8 + w, 4), WB2(0))
  PRIO(2);
  TL(0, 4, WB2(1), WB2(2), WB2(3))
  const int w5 = w < 5 ? w : 4;
  const unsigned wo16 = (w5 == 0 ? woff[0] : w5 == 1 ? woff[1] : w5 == 2 ? woff[2] : w5 == 3 ? woff[3] : woff[4]) + w5 * 1024;
  TL(0, 5, WB2(4), NOP, wbr[10] = *(const GAS u32x4*)((const char*)(woB + C0 + 16 * 32 * 640) - 4096 + wo16))
  PRIO(1);
  ARRIVE_NW(1);
  const int tp = w & 1, kq = w >> 1;
  const unsigned ofr = (rt * 8 + role * 4 + tp * 2) * OT_B + kq * 5120 + lane * 16;
  u32x4 of[2][5]; float gtb;
#define OFL(u) do { _Pragma("unroll") for (int i = 0; i < 5; i++) of[u][i] = LD16(WS_OA + ofr + (u) * OT_B + i * 1024); } while (0)
  TL6(0, OFL(0), gtb = __uint_as_float(__builtin_amdgcn_raw_buffer_load_b32(wsr, WS_G + (rt * 128 + w3 * 32 + w2 * 16 + l15) * 4, 0, 16)))
  PRIO(0);
  TL7(0, 0, OFL(1), NOP, NOP)
  pB2[2] = pk(e0, e1); pB2[3] = pk(e2, e3);
  OB = mfma32(v2, pB2, OB);
#undef TL
#undef TL6
#undef TL7
#undef TILE
#undef G
  STAMP(7);
  { LAS f32x4* xb = (LAS f32x4*)(lp + (w3 >> 1) * CH_B) + (w3 & 1) * (64 * 6);
#pragma unroll
    for (int i = 0; i < 12; i++) { auto rr = __builtin_amdgcn_permlane16_swap(__float_as_uint(OA[i]), __float_as_uint(OB[i]), false, false); OA[i] = __uint_as_float(rr[0]); OB[i] = __uint_as_float(rr[1]); }
    const f32x16 snd = w2 ? OA : OB;
    AWAIT(1, 8);
#pragma unroll
    for (int c = 0; c < 3; c++) xb[(w2 * 3 + c) * 64 + lane] = (f32x4){snd[4 * c], snd[4 * c + 1], snd[4 * c + 2], snd[4 * c + 3]};
    asm volatile("s_waitcnt vmcnt(22) lgkmcnt(0)\n\ts_barrier" ::: "memory");
    f32x16 O = w2 ? OB : OA;
#pragma unroll
    for (int c = 0; c < 3; c++) { const f32x4 t = xb[((w2 ^ 1) * 3 + c) * 64 + lane]; O[4 * c] += t[0]; O[4 * c + 1] += t[1]; O[4 * c + 2] += t[2]; O[4 * c + 3] += t[3]; }
    float l = from_upper(O[8]);
    float amax = 0.f;
#pragma unroll
    for (int i = 0; i < 12; i++) amax += fabsf(O[i]);
    const bool good = (l >= 0x1p-30f) && (l <= 0x1p100f) && (amax <= 0x1p126f);
    if (__builtin_expect(!__all(good), 0)) {
      u32x4 q1; u32x2 q2;
#pragma unroll
      for (int i = 0; i < 4; i++) { auto rr = __builtin_amdgcn_permlane16_swap(q1A[i], q1B[i], false, false); q1[i] = w2 ? rr[1] : rr[0]; }
#pragma unroll
      for (int i = 0; i < 2; i++) { auto rr = __builtin_amdgcn_permlane16_swap(q2A[i], q2B[i], false, false); q2[i] = h ? 0u : (w2 ? rr[1] : rr[0]); }
      float m = -INFINITY;
#pragma unroll 1
      for (int t = 0; t < 64; t++) {
        f32x16 s = mfma32(LD16(WS_KV + t * TILE_B + ok1), q1, Z); s = mfma32k8(LD8(WS_KV + t * TILE_B + ok2), q2, s);
#pragma unroll
        for (int i = 0; i < 16; i++) m = fmaxf(m, s[i]);
      }
      m = half_swap_max(m);
      O = Z;
#pragma unroll 1
      for (int t = 0; t < 64; t++) {
        f32x16 s = mfma32(LD16(WS_KV + t * TILE_B + ok1), q1, Z); s = mfma32k8(LD8(WS_KV + t * TILE_B + ok2), q2, s);
#pragma unroll
        for (int i = 0; i < 16; i++) s[i] = ex2(s[i] - m);
        O = mfma32(LD16(WS_KV + t * TILE_B + ov), (u32x4){pk(s[0], s[1]), pk(s[2], s[3]), pk(s[4], s[5]), pk(s[6], s[7])}, O);
        O = mfma32(LD16(WS_KV + t * TILE_B + ov + 2 * VB), (u32x4){pk(s[8], s[9]), pk(s[10], s[11]), pk(s[12], s[13]), pk(s[14], s[15])}, O);
      }
      l = from_upper(O[8]);
    }
    const float inv = __builtin_amdgcn_rcpf(l) * (1.0f - gtb);
    LAS char* tb = lp + w2 * CH_B + 12288 + w3 * 1536;
#pragma unroll
    for (int c = 0; c < 3; c++) { const int k0 = (g & 1) * 20 + 8 * c + 4 * h;
      *(LAS u32x2*)(tb + ((c == 2 && h) ? 1280 + (g & 1) * 8 : (k0 >> 3) * 256 + (k0 & 4) * 2) + l15 * 16) = (u32x2){nz(pk(O[4 * c] * inv, O[4 * c + 1] * inv)), nz(pk(O[4 * c + 2] * inv, O[4 * c + 3] * inv))}; }
    const unsigned ob = WS_OB + (rt * 8 + w3 * 2 + w2) * OT_B + hp * 1280 + lane * 16;
    const u32x4 z0 = ldsr16(tb + lane * 16), z1 = ldsr16(tb + 1024 + l15 * 16);
    ST16(z0, ob); ST16(z1, lane < 16 ? ob + 1024 : 0x7FFF0000u);
  }
  STAMP(8);
  auto of_missing = [&]() { u32x4 mm = of[0][0];
#pragma unroll
    for (int j = 1; j < 10; j++) mm = __builtin_elementwise_max(mm, of[j / 5][j % 5]);
    return missing(mm); };
  u32x4 wf[5][5];
#pragma unroll
  for (int i = 0; i < 5; i++) { const LAS char* wb = lp + L_WO + (kq * 5 + i) * 5120 + wlane;
#pragma unroll
    for (int t = 0; t < 5; t++) wf[i][t] = trd2(wb + t * 8, wb + 1280 + t * 8); }
  SBAR();
  if (__builtin_expect(of_missing(), 0)) {
#pragma unroll 1
    for (unsigned spin = 0; spin < SPIN_CAP; ++spin) {
      asm volatile("" ::: "memory");
      OFL(0); OFL(1);
      if (!of_missing()) break;
      __builtin_amdgcn_s_sleep(1);
    }
  }
  f32x4 c[2][5];
#pragma unroll
  for (int i = 0; i < 5; i++)
#pragma unroll
    for (int t = 0; t < 5; t++) c[0][t] = mfma16(wf[i][t], of[0][i], i ? c[0][t] : (f32x4){0.f, 0.f, 0.f, 0.f});
  BAR_L();
  SBAR();
  { LAS char* d = lp + L_WO + w * 5120 + lane * 16;
    *(LAS u32x4*)(lp + L_WO + 16 * 5120 + w5 * 1024 + lane * 16) = wbr[10];
#pragma unroll
    for (int i = 0; i < 5; i++) {
      *(LAS u32x4*)(d + i * 1024) = wbr[i]; *(LAS u32x4*)(d + 40960 + i * 1024) = wbr[5 + i]; of[0][i] = LD16(WS_OB + ofr + i * 1024); SBAR();
#pragma unroll
      for (int t = 0; t < 5; t++) c[1][t] = mfma16(wf[i][t], of[1][i], i ? c[1][t] : (f32x4){0.f, 0.f, 0.f, 0.f});
      SBAR();
    } }
  STAMP(9);
  BAR_L();
#pragma unroll
  for (int i = 0; i < 5; i++) { const int gr = kq * 5 + i; const LAS char* wb = lp + (gr < 17 ? L_WO + gr * 5120 : 145408 + (gr - 17) * 5120) + wlane;
#pragma unroll
    for (int t = 0; t < 5; t++) wf[i][t] = trd2(wb + t * 8, wb + 1280 + t * 8);
    SBAR(); of[1][i] = LD16(WS_OB + ofr + OT_B + i * 1024); SBAR(); }
  ARRIVE_NW(2);
  STAMP(10);
  LAS char* xw = lp + kq * 21504 + (tp * 32 + l15) * 336 + g * 80;
#pragma unroll
  for (int u = 0; u < 2; u++) {
#pragma unroll
    for (int i = 0; i < 5; i++) {
      if (__builtin_expect(missing(of[u][i]), 0)) {
#pragma unroll 1
        for (unsigned spin = 0; spin < SPIN_CAP; ++spin) {
          asm volatile("" ::: "memory");
#pragma unroll
          for (int j = u * 5 + i; j < 10; j++) of[j / 5][j % 5] = LD16(WS_OB + ofr + (j / 5) * OT_B + (j % 5) * 1024);
          if (!missing(of[u][i])) break;
          __builtin_amdgcn_s_sleep(1);
        }
      }
#pragma unroll
      for (int t = 0; t < 5; t++) c[u][t] = mfma16(wf[i][t], of[u][i], c[u][t]);
      if (u == 0 && i == 3) cpk = __hip_atomic_load(cnt + 2, __ATOMIC_RELAXED, __HIP_MEMORY_SCOPE_WORKGROUP);
      if (u == 1) { if (i == 0) { if (__builtin_expect((unsigned)__builtin_amdgcn_readfirstlane(cpk) < 8u, 0)) AWAIT(2, 8); asm volatile("" ::: "memory"); }
        *(LAS f32x4*)(xw + i * 16) = c[0][i]; }
      SBAR();
    }
  }
#pragma unroll
  for (int t = 0; t < 5; t++) *(LAS f32x4*)(xw + 16 * 336 + t * 16) = c[1][t];
  BAR_L();
  { const __amdgpu_buffer_rsrc_t orr = __builtin_amdgcn_make_buffer_rsrc(out, 0, 2048 * DM * 2, 0x00020000);
#pragma unroll
    for (int j = 0; j < 3; j++) { const int n = tid + 512 * j, nn = n < 1280 ? n : 0, row = (nn * 3277) >> 16, cq = nn - row * 20;
      const LAS f32x4* p = (const LAS f32x4*)(lp + row * 336 + cq * 16);
      const f32x4 z = (p[0] + p[1344]) + (p[2688] + p[4032]);
      __builtin_amdgcn_raw_buffer_store_b64((u32x2){pk(z[0], z[1]), pk(z[2], z[3])}, orr, n < 1280 ? ((rt * 128 + role * 64 + row) * DM + C0 + cq * 4) * 2 : 0x7FFF0000u, 0, 0); }
  }
  STAMP(11);
}
}

extern "C" void kernel_launch(void* const* d_in, const int* in_sizes, int n_in, void* d_out, int out_size, void* d_ws, size_t ws_size, hipStream_t stream) {
  using namespace gm;
  fused<<<256, 512, 0, stream>>>((const u16*)d_in[0], (const u16*)d_in[1], (const u16*)d_in[2], (const u16*)d_in[3], (const u16*)d_in[5], (const u16*)d_in[6],
                                 (const u16*)d_in[7], (const u16*)d_in[8], (const u16*)d_in[9], (u16*)d_out, (char*)d_ws);
}
```

```cpp
#include <hip/hip_runtime.h>
#include <hip/hip_bf16.h>
#include <stdint.h>

namespace gm {
typedef float f32x16 __attribute__((ext_vector_type(16)));
typedef float f32x4 __attribute__((ext_vector_type(4)));
typedef float f32x2 __attribute__((ext_vector_type(2)));
typedef short s16x4 __attribute__((ext_vector_type(4)));
typedef __bf16 b16x2 __attribute__((ext_vector_type(2)));
typedef __bf16 b16x8 __attribute__((ext_vector_type(8)));
typedef unsigned u32x4 __attribute__((ext_vector_type(4)));
typedef unsigned u32x2 __attribute__((ext_vector_type(2)));
typedef unsigned long long u64;
typedef unsigned short u16;
#define DI __device__ __forceinline__
#define LAS __attribute__((address_space(3)))
#define GAS __attribute__((address_space(1)))
typedef GAS u64 gu64;

constexpr int DM = 640;
constexpr unsigned WS_EPOCH = 0, WS_FKV = 2048, WS_FKVR = 4096, WS_FQ = 8192, WS_FPART = 16384, WS_FOA = 24576, WS_G = 57344;
constexpr unsigned WS_KVR = 8912896, KVR_B = 10240, WS_KV = 131072, WS_QX = 327680, QX_B = 10240, WS_PART = 1703936, PART_B = 15360;
constexpr unsigned WS_OA = 3670016, WS_OB = 6291456, WS_END = 8994816, OT_B = 20480;
constexpr int VB = 352, TILE_B = 1280 + 4 * VB, CH_B = TILE_B * 8, STEP_B = 2 * CH_B;
constexpr int STAGE_B = 36864, ST_W1 = 10240, ST_X = 20480;
constexpr int L_BUF = 43008, L_P0W = 0, L_P0X0 = 25600, L_P0X1 = 153600, L_RED = 158720, L_RED1 = 40960;
constexpr int L_HO = 86016, L_QL = 96256, L_VIM = 96256, L_PHK = 116736, L_WPHI = 158592, L_ZERO = 161792, L_CNT = 163712, L_TOTAL = 163840;
constexpr int L_WO = 43008;
constexpr float LOG2E = 1.4426950408889634f;
constexpr float QSCALE = 0.22360679774997896f * LOG2E;
constexpr unsigned SPIN_CAP = 20000, LSPIN_CAP = 400000;

DI unsigned pk(float a, float b) { f32x2 v = {a, b}; b16x2 r = __builtin_convertvector(v, b16x2); return __builtin_bit_cast(unsigned, r); }
DI f32x16 mfma32(u32x4 a, u32x4 b, f32x16 c) { return __builtin_amdgcn_mfma_f32_32x32x16_bf16(__builtin_bit_cast(b16x8, a), __builtin_bit_cast(b16x8, b), c, 0, 0, 0); }
DI f32x16 mfma32k8(u32x2 a, u32x2 b, f32x16 c) { return __builtin_amdgcn_mfma_f32_32x32x8bf16_1k(__builtin_bit_cast(s16x4, a), __builtin_bit_cast(s16x4, b), c, 0, 0, 0); }
DI f32x4 mfma16(u32x4 a, u32x4 b, f32x4 c) { return __builtin_amdgcn_mfma_f32_16x16x32_bf16(__builtin_bit_cast(b16x8, a), __builtin_bit_cast(b16x8, b), c, 0, 0, 0); }
DI u32x2 trd(const LAS char* p) { return __builtin_bit_cast(u32x2, __builtin_amdgcn_ds_read_tr16_b64_v4i16((LAS s16x4*)p)); }
DI u32x4 trd2(const LAS char* p0, const LAS char* p1) { u32x2 a = trd(p0), b = trd(p1); return (u32x4){a.x, a.y, b.x, b.y}; }
DI u32x4 ldsr16(const LAS char* p) { return *(const LAS u32x4*)p; }
DI u32x2 ldsr8(const LAS char* p) { return *(const LAS u32x2*)p; }
DI float ex2(float x) { return __builtin_amdgcn_exp2f(x); }
DI void dma16s(const void* base, unsigned voff, unsigned lds_base) {
  asm volatile("s_mov_b32 m0, %2\n\ts_nop 0\n\tglobal_load_lds_dwordx4 %0, %1" :: "v"(voff), "s"(base), "s"(lds_base) : "memory"); }
DI void dma16s_sc1(const void* base, unsigned voff, unsigned lds_base) {
  asm volatile("s_mov_b32 m0, %2\n\ts_nop 0\n\tglobal_load_lds_dwordx4 %0, %1 sc1" :: "v"(voff), "s"(base), "s"(lds_base) : "memory"); }
DI void dmag_open(unsigned lds_base) { asm volatile("s_mov_b32 m0, %0\n\ts_nop 0" :: "s"(lds_base) : "memory"); }
template <int J> DI void dmag(const void* base, unsigned voff) { asm volatile("global_load_lds_dwordx4 %0, %1 offset:%2" :: "v"(voff), "s"(base), "i"((J & 3) * 1024) : "memory"); }
template <int J> DI void dmag_sc1(const void* base, unsigned voff) { asm volatile("global_load_lds_dwordx4 %0, %1 offset:%2 sc1" :: "v"(voff), "s"(base), "i"((J & 3) * 1024) : "memory"); }
DI void dma4s(const void* base, unsigned voff, unsigned lds_base) {
  asm volatile("s_mov_b32 m0, %2\n\ts_nop 0\n\tglobal_load_lds_dword %0, %1" :: "v"(voff), "s"(base), "s"(lds_base) : "memory"); }
DI u32x4 ld16_async(const void* p) { u32x4 r; asm volatile("global_load_dwordx4 %0, %1, off" : "=v"(r) : "v"(p) : "memory"); return r; }
DI u32x2 ld8_sc1_async(const void* p) { u32x2 r; asm volatile("global_load_dwordx2 %0, %1, off sc1" : "=v"(r) : "v"(p) : "memory"); return r; }
#define BAR_L() asm volatile("s_waitcnt lgkmcnt(0)\n\ts_barrier" ::: "memory")
#define WAIT_BAR(N) asm volatile("s_waitcnt vmcnt(" #N ") lgkmcnt(0)\n\ts_barrier" ::: "memory")
#define WAIT_VM(N) asm volatile("s_waitcnt vmcnt(" #N ")" ::: "memory")
#define SBAR() __builtin_amdgcn_sched_barrier(0)
#define RLX_AGENT __ATOMIC_RELAXED, __HIP_MEMORY_SCOPE_AGENT
DI float half_swap_max(float m) { auto rr = __builtin_amdgcn_permlane32_swap(__float_as_uint(m), __float_as_uint(m), false, false); return fmaxf(__uint_as_float(rr[0]), __uint_as_float(rr[1])); }
DI float from_upper(float v) { auto rr = __builtin_amdgcn_permlane32_swap(__float_as_uint(v), __float_as_uint(v), false, false); return __uint_as_float(rr[1]); }

template <int NF>
DI bool flags_set(const char* ws, unsigned off, u64 tag, int lane) {
  gu64* f = (gu64*)(ws + off);
  bool ok = __hip_atomic_load(f + (NF < 64 ? (lane & (NF - 1)) : lane), RLX_AGENT) == tag;
  if (NF >= 128) ok &= __hip_atomic_load(f + 64 + lane, RLX_AGENT) == tag;
  if (NF >= 256) { ok &= __hip_atomic_load(f + 128 + lane, RLX_AGENT) == tag; ok &= __hip_atomic_load(f + 192 + lane, RLX_AGENT) == tag; }
  return ok;
}
template <int NF>
DI void wait_flags(const char* ws, unsigned off, u64 tag, int lane) {
#pragma unroll 1
  for (unsigned spin = 0; spin < SPIN_CAP; ++spin) {
    if (__all(flags_set<NF>(ws, off, tag, lane))) break;
    __builtin_amdgcn_s_sleep(2);
  }
  asm volatile("" ::: "memory");
}
DI void set_flag(char* ws, unsigned off, u64 tag, int lane) { if (lane == 0) __hip_atomic_store((gu64*)(ws + off), tag, RLX_AGENT); }
DI unsigned nz(unsigned v) { return v < 0xFFFFFFFEu ? v : 0xFFFFFFFEu; }
DI unsigned umax3(unsigned a, unsigned b, unsigned c) { unsigned m = a > b ? a : b; return m > c ? m : c; }
DI bool missing(u32x4 v) { return __any(umax3(umax3(v.x, v.y, v.z), v.w, 0u) == 0xFFFFFFFFu); }
#define ONES ((u32x4){0xFFFFFFFFu, 0xFFFFFFFFu, 0xFFFFFFFFu, 0xFFFFFFFFu})
#define ST16(v, off) __builtin_amdgcn_raw_buffer_store_b128((v), wsr, (off), 0, 16)
#define ST8(v, off) __builtin_amdgcn_raw_buffer_store_b64((v), wsr, (off), 0, 16)
#define LD16(off) __builtin_amdgcn_raw_buffer_load_b128(wsr, (off), 0, 16)
#define LD8(off) __builtin_amdgcn_raw_buffer_load_b64(wsr, (off), 0, 16)

#define STAMP(i) do {} while (0)

__global__ __launch_bounds__(512, 2) void fused(const u16* __restrict__ x, const u16* __restrict__ wqkv, const u16* __restrict__ wphi, const u16* __restrict__ woA,
                                                const u16* __restrict__ wq, const u16* __restrict__ wk, const u16* __restrict__ wv, const u16* __restrict__ woB,
                                                const u16* __restrict__ gatew, u16* __restrict__ out, char* ws) {
  __shared__ __attribute__((aligned(16))) char lds[L_TOTAL];
  const int tid = threadIdx.x, lane = tid & 63;
  const int w = __builtin_amdgcn_readfirstlane(tid >> 6) & 7;
  const int l15 = lane & 15, g = lane >> 4, q4 = l15 >> 2, p4 = lane & 3, r = lane & 31, h = lane >> 5;
  const int bx = blockIdx.x & 7, bi = blockIdx.x >> 3;
  const int rt = (bx & 1) * 8 + (bi & 7), hp = (bx >> 1) * 4 + ((bi >> 3) & 3), hq = hp >> 1, role = hp & 1;
  const int w3 = w & 3, w2 = w >> 2;
  LAS char* lp = (LAS char*)lds;
  const unsigned lds0 = (unsigned)(uintptr_t)lds;
  const __amdgpu_buffer_rsrc_t wsr = __builtin_amdgcn_make_buffer_rsrc(ws, 0, WS_END, 0x00020000);
  LAS unsigned* cnt = (LAS unsigned*)(lp + L_CNT);
#define ARRIVE_NW(c) do { asm volatile("" ::: "memory"); if (lane == 0) __hip_atomic_fetch_add(cnt + (c), 1u, __ATOMIC_RELAXED, __HIP_MEMORY_SCOPE_WORKGROUP); asm volatile("" ::: "memory"); } while (0)
#define PEEK(c) ((unsigned)__builtin_amdgcn_readfirstlane(__hip_atomic_load(cnt + (c), __ATOMIC_RELAXED, __HIP_MEMORY_SCOPE_WORKGROUP)))
#define AWAIT(c, n) do { _Pragma("unroll 1") for (unsigned sp_ = 0; sp_ < LSPIN_CAP; ++sp_) { if (PEEK(c) >= (unsigned)(n)) break; __builtin_amdgcn_s_sleep(1); } asm volatile("" ::: "memory"); } while (0)
  STAMP(0);

  const int T0 = rt * 128 + hq * 16;
  const int C0 = hq * 80;
  const int wlane = ((g >> 1) * 16 + (g & 1) * 4 + q4) * 160 + p4 * 40;
  u64 tag;
  unsigned o0[10];
#define KVP(s, slot, p) dma16s_sc1(ws + WS_KV + (s) * STEP_B + (p) * 1024, lane * 16, lds0 + (slot) * STEP_B + (p) * 1024)
#define KVL(s, slot) do { _Pragma("unroll") for (int j = 0; j < 11; j++) if (j < 10 || w3 < 2) KVP(s, slot, w3 + 4 * j); } while (0)
#define KW(s, slot, j) do { if ((j) == 4) KVP(s, slot, w * 5 + 4); else { if ((j) == 0) dmag_open(lds0 + (slot) * STEP_B + w * 5120); dmag_sc1<(j)>(ws + WS_KV + (s) * STEP_B + w * 5120, lane * 16); } } while (0)
#define KX(s, slot) do { if (w < 2) KVP(s, slot, 40 + w); } while (0)
  if (w2 == 0) {
    u32x4 gf0, gf1, gf2, gf3, gf4, wpc; u32x2 tch;
    u32x2 ep = ld8_sc1_async(ws + WS_EPOCH);
    const int lx = w3 < 3 ? L_P0X0 + w3 * 5120 : L_P0X1;
    { const u16* wsrc = (role ? wv : wk) + w3 * 3200;
#pragma unroll
      for (int j = 0; j < 5; j++) { const int n = j * 64 + lane, row = (n * 3277) >> 16, cc = n - row * 20; dma16s(x + T0 * DM + w3 * 160, row * 1280 + cc * 16, lds0 + lx + j * 1024); }
#pragma unroll
      for (int j = 0; j < 6; j++) dma16s(wsrc + j * 512, lane * 16, lds0 + L_P0W + w3 * 6400 + j * 1024);
      if (lane < 16) dma16s(wsrc + 6 * 512, lane * 16, lds0 + L_P0W + w3 * 6400 + 6 * 1024);
      const u16* gp = gatew + w3 * 160 + g * 8;
      gf0 = ld16_async(gp); gf1 = ld16_async(gp + 32); gf2 = ld16_async(gp + 64); gf3 = ld16_async(gp + 96); gf4 = ld16_async(gp + 128);
      wpc = ld16_async(wphi + hq * 1600 + (tid < 200 ? tid : 199) * 8);
      const char* tp = ws;
      tch = ld8_sc1_async(tp);
    }
    asm volatile("s_waitcnt vmcnt(1)" : "+v"(gf0), "+v"(gf1), "+v"(gf2), "+v"(gf3), "+v"(gf4), "+v"(wpc), "+v"(ep) :: "memory");
    tag = (((u64)(unsigned)__builtin_amdgcn_readfirstlane(ep.y) << 32) | (unsigned)__builtin_amdgcn_readfirstlane(ep.x)) + 1ull;
    STAMP(1);
    { const LAS char* wb = lp + L_P0W + (w3 * 160 + g * 8 + q4) * 40;
      const LAS char* xb = lp + lx + l15 * 320 + g * 16;
      const bool gl = l15 == 4;
      f32x4 c0 = {}, c1 = {};
#define P0STEP(s, GF) { const u32x4 b = ldsr16(xb + (s) * 64); c0 = mfma16(trd2(wb + p4 * 8 + (s) * 1280, wb + p4 * 8 + (s) * 1280 + 160), b, c0); \
        u32x4 a = trd2(wb + 32 + (s) * 1280, wb + 32 + (s) * 1280 + 160); if (gl) a = GF; c1 = mfma16(a, b, c1); }
      P0STEP(0, gf0) P0STEP(1, gf1) P0STEP(2, gf2) P0STEP(3, gf3) P0STEP(4, gf4)
#undef P0STEP
      *(LAS f32x4*)(lp + L_RED + tid * 16) = c0;
      if (lane < 32) *(LAS f32x4*)(lp + L_RED1 + (w3 * 32 + lane) * 16) = c1; }
    STAMP(2);
    f32x16 acc[5];
#pragma unroll
    for (int b = 0; b < 5; b++) acc[b] = (f32x16){};
    const int wm = g & 1, whd = p4;
    const int wo0 = wm * ST_W1 + (8 * h + 4 * wm + q4) * 160 + whd * 40, wo1 = wm * ST_W1 + (8 * h + 4 * (wm ^ 1) + q4) * 160 + whd * 40;
    const int xo = ST_X + (w3 * 32 + r) * 128 + ((h ^ ((r >> 1) & 7)) * 16);
    u32x4 fa[5], fb[5], xa, xb;
#pragma unroll
    for (int j = 0; j < 5; j++) fb[j] = (u32x4){0u, 0u, 0u, 0u};
    xb = (u32x4){0u, 0u, 0u, 0u};
#define KSTEP(FN, XN, s, FC, XC) { XN = ldsr16(sb + (xo ^ ((s) * 32))); \
      _Pragma("unroll") for (int j = 0; j < 5; j++) { acc[j] = mfma32(FC[j], XC, acc[j]); FN[j] = trd2(sb + wo0 + (s) * 2560 + j * 8, sb + wo1 + (s) * 2560 + j * 8); SBAR(); } }
    int bsel = 2;
#pragma unroll 1
    for (int i = 0; i < 10; i++) {
      BAR_L();
      if (i == 1 && role) {
        const unsigned qb = WS_QX + (rt * 8 + hq) * QX_B + w3 * 1280 + lane * 16;
        ST16(ONES, qb); ST16(ONES, qb + 5120); if (lane < 32) ST16(ONES, qb + 1024 + (lane >> 4) * (5120 - 256)); }
      if (i == 4) { asm volatile("s_waitcnt vmcnt(0)" : "+v"(tch) :: "memory"); if (role) set_flag(ws, WS_FQ + ((rt * 8 + hq) * 4 + w3) * 8, tag, lane);
                    if (tid < 200) *(LAS u32x4*)(lp + L_WPHI + tid * 16) = wpc; }
      const LAS char* sb = lp + L_BUF + bsel * STAGE_B;
      bsel = bsel == 2 ? 0 : bsel + 1;
      KSTEP(fa, xa, 0, fb, xb) KSTEP(fb, xb, 1, fa, xa) KSTEP(fa, xa, 2, fb, xb)
      { xb = ldsr16(sb + (xo ^ 96));
        acc[0] = mfma32(fa[0], xa, acc[0]); fb[0] = trd2(sb + wo0 + 7680, sb + wo1 + 7680); fb[1] = trd2(sb + wo0 + 7688, sb + wo1 + 7688); SBAR();
        acc[1] = mfma32(fa[1], xa, acc[1]); fb[2] = trd2(sb + wo0 + 7696, sb + wo1 + 7696); fb[3] = trd2(sb + wo0 + 7704, sb + wo1 + 7704); SBAR();
        acc[2] = mfma32(fa[2], xa, acc[2]); fb[4] = trd2(sb + wo0 + 7712, sb + wo1 + 7712); SBAR();
        acc[3] = mfma32(fa[3], xa, acc[3]); acc[4] = mfma32(fa[4], xa, acc[4]); SBAR(); }
    }
#pragma unroll
    for (int j = 0; j < 5; j++) acc[j] = mfma32(fb[j], xb, acc[j]);
#undef KSTEP
    STAMP(3);
#define PK2(t, i, sc) pk(acc[t][i] * (sc), acc[t][(i) + 1] * (sc))
#define ROW(i, sc) const u32x4 ra = {PK2(0, i, sc), PK2(0, (i) + 2, sc), PK2(1, i, sc), PK2(1, (i) + 2, sc)}, rb = {PK2(2, i, sc), PK2(2, (i) + 2, sc), PK2(3, i, sc), PK2(3, (i) + 2, sc)}; const u32x2 rc = {PK2(4, i, sc), PK2(4, (i) + 2, sc)}
    { ROW(4, 1.0f);
      *(LAS u32x4*)(lp + L_HO + tid * 16) = ra; *(LAS u32x4*)(lp + L_HO + 4096 + tid * 16) = rb; *(LAS u32x2*)(lp + L_HO + 8192 + tid * 8) = rc; }
    BAR_L();
    const int qfo = (h * 4 + w3) * 1280;
    if (role) {
      { ROW(8, QSCALE); const unsigned qb = WS_QX + (rt * 8 + hq) * QX_B + qfo;
        ST16(((u32x4){nz(ra.x), nz(ra.y), nz(ra.z), nz(ra.w)}), qb + r * 16); ST16(((u32x4){nz(rb.x), nz(rb.y), nz(rb.z), nz(rb.w)}), qb + 512 + r * 16); ST8(((u32x2){nz(rc.x), nz(rc.y)}), qb + 1024 + r * 8); }
      { ROW(12, QSCALE); LAS char* d = lp + L_QL + qfo;
        *(LAS u32x4*)(d + r * 16) = ra; *(LAS u32x4*)(d + 512 + r * 16) = rb; *(LAS u32x2*)(d + 1024 + r * 8) = rc; }
    } else { LAS char* d = lp + L_VIM + (h * 128 + w3 * 32 + r) * 40;
#pragma unroll
      for (int t = 0; t < 5; t++) { *(LAS u32x2*)(d + t * 8) = (u32x2){PK2(t, 8, 1.0f), PK2(t, 10, 1.0f)}; *(LAS u32x2*)(d + 10240 + t * 8) = (u32x2){PK2(t, 12, 1.0f), PK2(t, 14, 1.0f)}; } }
#pragma unroll
    for (int t = 0; t < 5; t++) { o0[2 * t] = PK2(t, 0, 1.0f); o0[2 * t + 1] = PK2(t, 2, 1.0f); }
#undef ROW
#undef PK2
  } else {
    u32x2 ep = ld8_sc1_async(ws + WS_EPOCH);
    const bool second = w3 >> 1;
    const unsigned rs = (second && role) ? 1280u : 3840u;
    const char* wsrc = (second ? (role ? (const char*)(wq + C0) : (const char*)(wqkv + 1280 + C0)) : (const char*)(wqkv + (role ? 0 : 640) + C0)) + (w3 & 1) * 32 * rs;
    const char* xsrc = (const char*)(x + (rt * 128 + w3 * 32) * DM);
    int rot = (bi & 7) + 3 * ((bi >> 3) & 3); rot = rot >= 10 ? rot - 10 : rot;
    unsigned so[5], xoff[2];
#pragma unroll
    for (int j = 0; j < 5; j++) { const int c = j * 64 + lane, rho = (c * 6554) >> 16, cc = c - rho * 10; so[j] = (rho ^ (second ? 4 : 0)) * rs + cc * 16; }
#pragma unroll
    for (int j = 0; j < 2; j++) { const int row = j * 8 + (lane >> 3); xoff[j] = row * 1280 + (((lane & 7) ^ ((row >> 1) & 7)) * 16); }
    int bsel = 2, kc = rot;
    auto stage = [&]() {
      const unsigned lb = lds0 + L_BUF + bsel * STAGE_B;
      const char* bw = wsrc + kc * 64 * rs; const char* bxp = xsrc + kc * 128;
#pragma unroll
      for (int j = 0; j < 5; j++) { dma16s(bw, so[j], lb + (w3 * 5 + j) * 1024); if (j < 4) dma16s(bxp + (j >> 1) * 16 * 1280, xoff[j & 1], lb + ST_X + (w3 * 4 + j) * 1024); }
      bsel = bsel == 2 ? 0 : bsel + 1; kc = kc == 9 ? 0 : kc + 1;
    };
    stage(); stage();
    asm volatile("s_waitcnt vmcnt(9)" : "+v"(ep) :: "memory");
    tag = (((u64)(unsigned)__builtin_amdgcn_readfirstlane(ep.y) << 32) | (unsigned)__builtin_amdgcn_readfirstlane(ep.x)) + 1ull;
    STAMP(1); STAMP(2);
#pragma unroll 1
    for (int i = 0; i < 8; i++) {
      WAIT_BAR(9);
      stage();
      if (i == 0 && w3 == 0) {
        const unsigned tb = WS_KV + (T0 >> 5) * TILE_B; const int s16 = hq & 1;
        if (!role) {
          const LAS f32x4* rp = (const LAS f32x4*)(lp + (h ? L_RED1 : L_RED)) + (h ? (g & 1) * 16 : g * 32) + l15;
          const int st = h ? 32 : 64, o2 = h ? 0 : 16;
          const f32x4 a = (rp[0] + rp[st]) + (rp[2 * st] + rp[3 * st]), b = (rp[o2] + rp[st + o2]) + (rp[2 * st + o2] + rp[3 * st + o2]);
          const u32x4 v = {pk(a[0], a[1]), pk(a[2], a[3]), pk(b[0], b[1]), pk(b[2], b[3])};
          if (lane < 32) ST16(v, tb + (g * 32 + s16 * 16 + l15) * 16);
          else if (lane < 48) ST8(((u32x2){v.x, v.y}), tb + 1024 + (s16 * 16 + l15) * 8);
          else { const float gv = 1.0f / (1.0f + __expf(-a[0])); __builtin_amdgcn_raw_buffer_store_b32(__float_as_uint(gv), wsr, WS_G + (T0 + l15) * 4, 0, 16); }
        } else if (lane < 44) {
          const int hh = lane >= 22, d = lane - hh * 22, dd = d < 20 ? d : 0, st = dd >= 16 ? 128 : 256;
          const LAS float* rp = (const LAS float*)(lp + (dd >= 16 ? L_RED1 : L_RED)) + (((dd & 15) >> 2) * 16 + 4 * hh) * 4 + (dd & 3);
          float e[8];
#pragma unroll
          for (int j = 0; j < 8; j++) { const int tk = (8 * (j >> 2) + (j & 3)) * 4; e[j] = (rp[tk] + rp[tk + st]) + (rp[tk + 2 * st] + rp[tk + 3 * st]); }
          u32x4 v = {pk(e[0], e[1]), pk(e[2], e[3]), pk(e[4], e[5]), pk(e[6], e[7])};
          if (d == 20) v = (u32x4){0x3F803F80u, 0x3F803F80u, 0x3F803F80u, 0x3F803F80u};
          if (d == 21) v = (u32x4){0u, 0u, 0u, 0u};
          ST16(v, tb + 1280 + (s16 * 2 + hh) * VB + d * 16);
        }
      }
      if (i == 2) { if (w3 == 0) set_flag(ws, WS_FKV + blockIdx.x * 8, tag, lane);
                    if (w3 >= 2) *(LAS u32x4*)(lp + L_ZERO + (tid - 384) * 16) = (u32x4){0u, 0u, 0u, 0u}; }
    }
    u64 fa, fb, fc, fd, fe;
    { gu64* f = (gu64*)(ws + WS_FKV) + lane;
      fa = __hip_atomic_load(f, RLX_AGENT); fb = __hip_atomic_load(f + 64, RLX_AGENT); fc = __hip_atomic_load(f + 128, RLX_AGENT); fd = __hip_atomic_load(f + 192, RLX_AGENT);
      fe = __hip_atomic_load((gu64*)(ws + WS_FQ) + (rt * 8 + hq) * 4 + p4, RLX_AGENT); }
    WAIT_BAR(14);
    asm volatile("" : "+v"(fa), "+v"(fb), "+v"(fc), "+v"(fd), "+v"(fe));
    const bool kvok = __all((fa == tag) & (fb == tag) & (fc == tag) & (fd == tag) & (fe == tag));
    asm volatile("" ::: "memory");
    BAR_L();
    if (!kvok) { wait_flags<256>(ws, WS_FKV, tag, lane); wait_flags<4>(ws, WS_FQ + (rt * 8 + hq) * 32, tag, lane); }
    KVL(0, 0);
    if (blockIdx.x == 0 && w3 == 0) set_flag(ws, WS_EPOCH, tag, lane);
    STAMP(3);
    BAR_L();
    const int ht = tid - 256;
    const u32x4 a = ldsr16(lp + L_HO + ht * 16), b = ldsr16(lp + L_HO + 4096 + ht * 16); const u32x2 c = ldsr8(lp + L_HO + 8192 + ht * 8);
    o0[0] = a.x; o0[1] = a.y; o0[2] = a.z; o0[3] = a.w; o0[4] = b.x; o0[5] = b.y; o0[6] = b.z; o0[7] = b.w; o0[8] = c.x; o0[9] = c.y;
  }
  STAMP(4);
  u32x4 ph[5];
  { const LAS char* pb = ((p4 & 1) == h) ? lp + L_WPHI + (2 * w2 + h) * 800 + q4 * 40 + (2 * (g & 1) + (p4 >> 1)) * 8 : lp + L_ZERO;
    const u32x4 b0 = {o0[0], o0[1], o0[2], o0[3]}, b1 = {o0[4], o0[5], o0[6], o0[7]}, b2 = {o0[8], o0[9], 0u, 0u};
    const u32x2 c0 = trd(pb + 640), c1 = trd(pb + 672);
    f32x16 P0 = {}, P1 = {};
    P0 = mfma32(trd2(pb, pb + 160), b0, P0); P1 = mfma32(trd2(pb + 32, pb + 192), b0, P1);
    P0 = mfma32(trd2(pb + 320, pb + 480), b1, P0); P1 = mfma32(trd2(pb + 352, pb + 512), b1, P1);
    P0 = mfma32((u32x4){c0.x, c0.y, 0u, 0u}, b2, P0); P1 = mfma32((u32x4){c1.x, c1.y, 0u, 0u}, b2, P1);
    KX(1, 1); KW(1, 1, 0); SBAR();
    f32x2 p[10];
#pragma unroll
    for (int f = 0; f < 10; f++) p[f] = f < 8 ? (f32x2){P0[(2 * f) & 15], P0[(2 * f + 1) & 15]} : (f32x2){P1[(2 * f) & 3], P1[(2 * f + 1) & 3]};
    float mx = fmaxf(p[0][0], p[0][1]), mn = fminf(p[0][0], p[0][1]);
#pragma unroll
    for (int f = 1; f < 10; f++) { mx = fmaxf(fmaxf(mx, p[f][0]), p[f][1]); mn = fminf(fminf(mn, p[f][0]), p[f][1]); }
    const f32x2 ma = {-mx * LOG2E, -mx * LOG2E}, mb = {mn * LOG2E, mn * LOG2E}, ca_ = {LOG2E, LOG2E}, cb_ = {-LOG2E, -LOG2E};
    f32x2 ea[10], eb[10], sa = {0.f, 0.f}, sb = {0.f, 0.f};
#pragma unroll
    for (int f = 0; f < 10; f++) { if (f % 2 == 0 && f < 8) { SBAR(); if (f == 0) KW(1, 1, 1); if (f == 2) KW(1, 1, 2); if (f == 4) KW(1, 1, 3); if (f == 6) KW(1, 1, 4); SBAR(); }
      const f32x2 xa = __builtin_elementwise_fma(p[f], ca_, ma), xb = __builtin_elementwise_fma(p[f], cb_, mb);
      ea[f] = (f32x2){ex2(xa[0]), ex2(xa[1])}; eb[f] = (f32x2){ex2(xb[0]), ex2(xb[1])}; sa += ea[f]; sb += eb[f]; }
    const float ia = __builtin_amdgcn_rcpf(sa[0] + sa[1]), ib = __builtin_amdgcn_rcpf(sb[0] + sb[1]);
    unsigned o[20];
#pragma unroll
    for (int f = 0; f < 10; f++) { const f32x2 ya = ea[f] * ia, yb = eb[f] * ib; o[f] = pk(ya[0], ya[1]); o[10 + f] = pk(yb[0], yb[1]); }
#pragma unroll
    for (int c = 0; c < 5; c++) ph[c] = (u32x4){o[4 * c], o[4 * c + 1], o[4 * c + 2], o[4 * c + 3]};
    asm volatile("" : "+v"(ph[0]), "+v"(ph[1]), "+v"(ph[2]), "+v"(ph[3]), "+v"(ph[4]));
  }
  u32x4 q1A, q1B; u32x2 q2A, q2B;
  if (role) {
    WAIT_BAR(0);
    STAMP(5);
    const LAS char* qa = lp + L_QL + w3 * 1280;
    q1A = ldsr16(qa + lane * 16); q1B = ldsr16(qa + 5120 + lane * 16); q2A = ldsr8(qa + 1024 + r * 8); q2B = ldsr8(qa + 6144 + r * 8);
    ARRIVE_NW(0);
  } else {
    const unsigned qb = WS_QX + (rt * 8 + hq) * QX_B + w3 * 1280;
    q1A = LD16(qb + lane * 16); q1B = LD16(qb + 5120 + lane * 16); q2A = LD8(qb + 1024 + r * 8); q2B = LD8(qb + 6144 + r * 8);
    { LAS char* d = lp + L_PHK + ((2 * w2 + h) * 128 + w3 * 32 + r) * 80;
#pragma unroll
      for (int c = 0; c < 5; c++) *(LAS u32x4*)(d + c * 16) = ph[c]; }
    WAIT_BAR(4);
    STAMP(5);
#pragma unroll 1
    for (unsigned spin = 0; spin < SPIN_CAP; ++spin) {
      if (!(missing(q1A) | missing(q1B) | missing((u32x4){q2A.x, q2A.y, q2B.x, q2B.y}))) break;
      __builtin_amdgcn_s_sleep(1);
      asm volatile("" ::: "memory");
      q1A = LD16(qb + lane * 16); q1B = LD16(qb + 5120 + lane * 16); q2A = LD8(qb + 1024 + r * 8); q2B = LD8(qb + 6144 + r * 8);
    }
  }
  STAMP(6);
  const int vr = r <= 21 ? r : 21;
  const int ok1 = lane * 16, ok2 = h ? (1280 + 20 * 16) : (1024 + r * 8), ov = 1280 + h * VB + vr * 16;
  const f32x16 Z = {};
  if (h) { q2A = (u32x2){0u, 0u}; q2B = (u32x2){0u, 0u}; }
  { const LAS char* t0 = lp;
    const u32x4 k1 = ldsr16(t0 + ok1); const u32x2 k2 = ldsr8(t0 + ok2);
    f32x16 sa = mfma32(k1, q1A, Z); sa = mfma32k8(k2, q2A, sa);
    f32x16 sb = mfma32(k1, q1B, Z); sb = mfma32k8(k2, q2B, sb);
    float ma = sa[0], mb = sb[0];
#pragma unroll
    for (int i = 1; i < 16; i++) { ma = fmaxf(ma, sa[i]); mb = fmaxf(mb, sb[i]); }
    ma = half_swap_max(ma); mb = half_swap_max(mb);
    const float mah = __uint_as_float(__float_as_uint(ma) & 0xFFFF0000u), mbh = __uint_as_float(__float_as_uint(mb) & 0xFFFF0000u);
    if (h) { q2A.x = pk(-mah, mah - ma); q2B.x = pk(-mbh, mbh - mb); } }
  f32x16 OA = {}, OB = {}, SA, SB;
  u32x4 k1, v1, v2, v2n, pA1, pA2, pB1, pB2; u32x2 k2;
  float e0, e1, e2, e3;
  const LAS char* c0p = lp + w2 * CH_B + ok1; const LAS char* c1p = lp + w2 * CH_B + ok2; const LAS char* c2p = lp + w2 * CH_B + ov;
  k1 = ldsr16(c0p); k2 = ldsr8(c1p);
  SA = mfma32(k1, q1A, Z); SA = mfma32k8(k2, q2A, SA);
  v1 = ldsr16(c2p); v2 = v1;
  e0 = e1 = e2 = e3 = 0.f; pB2 = (u32x4){0u, 0u, 0u, 0u};
#define G(MF, LD, C0_, C1_, X, S_, i) MF; LD; SBAR(); C0_; C1_; SBAR(); X##0 = ex2(S_[i]); X##1 = ex2(S_[i + 1]); X##2 = ex2(S_[i + 2]); X##3 = ex2(S_[i + 3]); asm volatile("" : "+v"(X##0), "+v"(X##1), "+v"(X##2), "+v"(X##3)); SBAR();
#define TILE(TB, TN, V2, H0, H1, H2) { float a0, a1, a2, a3; \
    G(SB = mfma32(k1, q1B, Z),    k1 = ldsr16(c0p + (TN)), pB2[2] = pk(e0, e1), pB2[3] = pk(e2, e3), a, SA, 0) \
    G(OB = mfma32(v2, pB2, OB),   V2,                      pA1[0] = pk(a0, a1), pA1[1] = pk(a2, a3), e, SA, 4) \
    G(SB = mfma32k8(k2, q2B, SB), k2 = ldsr8(c1p + (TN)),  pA1[2] = pk(e0, e1), pA1[3] = pk(e2, e3), a, SA, 8) \
    G(OA = mfma32(v1, pA1, OA),   H0,                      pA2[0] = pk(a0, a1), pA2[1] = pk(a2, a3), e, SA, 12) \
    G(SA = mfma32(k1, q1A, Z),    H1,                      pA2[2] = pk(e0, e1), pA2[3] = pk(e2, e3), a, SB, 0) \
    G(OA = mfma32(v2, pA2, OA),   H2,                      pB1[0] = pk(a0, a1), pB1[1] = pk(a2, a3), e, SB, 4) \
    G(SA = mfma32k8(k2, q2A, SA), (void)0,                 pB1[2] = pk(e0, e1), pB1[3] = pk(e2, e3), a, SB, 8) \
    G(OB = mfma32(v1, pB1, OB),   v1 = ldsr16(c2p + (TN)), pB2[0] = pk(a0, a1), pB2[1] = pk(a2, a3), e, SB, 12) }
#define NOP (void)0
#define TL(sl, t, H0, H1, H2) TILE((sl) * STEP_B + (t) * TILE_B, (sl) * STEP_B + ((t) + 1) * TILE_B, v2 = ldsr16(c2p + (sl) * STEP_B + (t) * TILE_B + 2 * VB), H0, H1, H2)
#define TL6(sl, H0, H1) TL(sl, 6, H0, H1, v2n = ldsr16(c2p + (sl) * STEP_B + 7 * TILE_B + 2 * VB))
#define TL7(sl, sn, H0, H1, H2) TILE((sl) * STEP_B + 7 * TILE_B, (sn) * STEP_B, v2 = v2n, H0, H1, H2)
  unsigned woff[5];
#pragma unroll
  for (int j = 0; j < 5; j++) { const int c = j * 64 + lane, rho = (c * 6554) >> 16, cc = c - rho * 10; const int k = (rho & ~12) | ((rho & 4) << 1) | ((rho & 8) >> 1); woff[j] = k * 1280 + cc * 16 + (4 - j) * 1024; }
#define WOG(wo, gr, j, dst) do { if ((j) == 4) WOP(wo, gr, 4, dst); else { if ((j) == 0) dmag_open(lds0 + (dst)); dmag<(j)>((wo) + C0 + (gr) * 32 * 640 - 2048, woff[j]); } } while (0)
#define WOP(wo, gr, j, dst) dma16s((wo) + C0 + (gr) * 32 * 640 - (4 - (j)) * 512, woff[j], lds0 + (dst) + (j) * 1024)
#define WA(gr, j) WOG(woA, gr, j, L_WO + (gr) * 5120)
  const u16* wxs = w < 4 ? woA + (16 + w3) * 32 * 640 : woB + (w < 7 ? 17 + w3 : 19) * 32 * 640;
  const int wxd = w < 4 ? L_WO + (16 + w3) * 5120 : 145408 + (w < 7 ? w3 : 2) * 5120;
#define WX(j) WOG(wxs, 0, j, wxd)
#define PRIO(n) __builtin_amdgcn_s_setprio(n)
  const unsigned fo = (rt * 8 + hq) * OT_B + lane * 16;
#define FILL(j) do { const int n_ = w * 3 + ((j) >> 1); ST16(ONES, (((j) & 1) ? WS_OB : WS_OA) + fo + (n_ < 20 ? n_ : 19) * 1024); } while (0)
#define CLR_VM() __builtin_amdgcn_s_waitcnt(0x0F70)
  const bool red = !role && w == 4;
  f32x4 part[16]; u64 fp0, fp1;
  auto red_load = [&]() {
    asm volatile("" : "+v"(fp0), "+v"(fp1));
    if (!__all((fp0 == tag) & (fp1 == tag))) wait_flags<128>(ws, WS_FPART + hq * 1024, tag, lane);
    asm volatile("" ::: "memory");
#pragma unroll
    for (int j = 0; j < 16; j++) part[j] = __builtin_bit_cast(f32x4, LD16(WS_PART + (hq * 16 + j) * PART_B + (rt * 60 + (lane < 60 ? lane : 59)) * 16));
  };
  auto red_store = [&]() {
    f32x4 a = ((part[0] + part[1]) + (part[2] + part[3])) + ((part[4] + part[5]) + (part[6] + part[7]));
    a += ((part[8] + part[9]) + (part[10] + part[11])) + ((part[12] + part[13]) + (part[14] + part[15]));
    const int hd = rt >> 2, u = (rt & 3) * 60 + lane, hi = u >= 160, fq = hi ? (u - 160) >> 3 : u >> 4, di = hi ? u & 7 : u & 15;
    if (lane < 60) ST8(((u32x2){pk(a[0], a[1]), pk(a[2], a[3])}), WS_KVR + hq * KVR_B + (((hd >> 1) * 2 + hi) * 5 + (fq >> 1)) * 512 + ((hd & 1) * 16 + di) * 16 + (fq & 1) * 8);
  };
  u32x4 ka[10]; u64 fkr; float gta;
  auto oa_load = [&]() {
    asm volatile("" : "+v"(fkr));
    if (!__all(fkr == tag)) wait_flags<16>(ws, WS_FKVR + hq * 128, tag, lane);
    asm volatile("" ::: "memory");
    const unsigned kb = (((r >> 2) & 1) == h) ? WS_KVR + hq * KVR_B + w2 * 5120 + (h * 16 + (r & 3) + 4 * (r >> 3)) * 16 : 0x7FFF0000u;
#pragma unroll
    for (int j = 0; j < 10; j++) ka[j] = LD16(kb + j * 512);
    gta = __uint_as_float(__builtin_amdgcn_raw_buffer_load_b32(wsr, WS_G + (rt * 128 + w3 * 32 + r) * 4, 0, 16));
  };
  u32x4 ja[2][3], jb[2]; f32x4 jc[3];
  const LAS char* jab = lp + L_PHK + ((w >> 1) * 128 + g * 8 + q4) * 80 + p4 * 8;
  const LAS char* jbb = lp + L_VIM + ((w >> 1) * 128 + g * 8 + q4) * 40 + (w & 1) * 32 + p4 * 8;
  auto kv_rd = [&](int k0) {
#pragma unroll
    for (int ks = 0; ks < 2; ks++) { jb[ks] = trd2(jbb + (k0 + ks) * 1280, jbb + (k0 + ks) * 1280 + 160);
#pragma unroll
      for (int ft = 0; ft < 3; ft++) ja[ks][ft] = trd2(jab + (k0 + ks) * 2560 + ft * 32, jab + (k0 + ks) * 2560 + 320 + ft * 32); } };
  auto kv_mm = [&](bool first) {
#pragma unroll
    for (int ks = 0; ks < 2; ks++) { if ((w & 1) && l15 == 4) jb[ks] = (u32x4){0x3F803F80u, 0x3F803F80u, 0x3F803F80u, 0x3F803F80u};
#pragma unroll
      for (int ft = 0; ft < 3; ft++) jc[ft] = mfma16(ja[ks][ft], jb[ks], (first && !ks) ? (f32x4){0.f, 0.f, 0.f, 0.f} : jc[ft]); } };
  auto kv_st = [&]() { const int dt = w & 1; const unsigned pb = WS_PART + (hq * 16 + rt) * PART_B + (w >> 1) * 3840 + (dt ? 2560 + g * 128 : g * 256) + l15 * 16;
#pragma unroll
    for (int ft = 0; ft < 3; ft++) if ((!dt || l15 < 8) && ft * 4 + g < 10) ST16(__builtin_bit_cast(u32x4, jc[ft]), pb + ft * (dt ? 512 : 1024)); };
  f32x16 oa0, oa1; u32x4 z0, z1, z2;
  LAS char* otb = lp + L_WO + (8 + w) * 5120;
  auto oa_mm = [&](int s) { oa0 = mfma32(ka[s], ph[s], s ? oa0 : Z); oa1 = mfma32(ka[5 + s], ph[s], s ? oa1 : Z); };
  auto oa_norm = [&]() { const float inv = __builtin_amdgcn_rcpf(oa1[4] + 1e-6f) * gta;
#pragma unroll
    for (int t = 0; t < 5; t++) { const int k0 = h * 20 + 4 * t;
      const u32x2 v = t < 4 ? (u32x2){nz(pk(oa0[(4 * t) & 15] * inv, oa0[(4 * t + 1) & 15] * inv)), nz(pk(oa0[(4 * t + 2) & 15] * inv, oa0[(4 * t + 3) & 15] * inv))} : (u32x2){nz(pk(oa1[0] * inv, oa1[1] * inv)), nz(pk(oa1[2] * inv, oa1[3] * inv))};
      *(LAS u32x2*)(otb + (r >> 4) * 1280 + (k0 >> 3) * 256 + (k0 & 4) * 2 + l15 * 16) = v; }
    z0 = ldsr16(otb + lane * 16); z1 = ldsr16(otb + 1280 + lane * 16); z2 = ldsr16(otb + (r >> 4) * 1280 + 1024 + l15 * 16); };
  auto oa_st = [&]() { const unsigned oa = WS_OA + (rt * 8 + w3 * 2) * OT_B + (hq * 2 + w2) * 1280;
    ST16(z0, oa + lane * 16); ST16(z1, oa + OT_B + lane * 16); if (!h) ST16(z2, oa + (r >> 4) * OT_B + 1024 + l15 * 16); };
  unsigned cpk;
  PRIO(3);
  TL(0, 0, if (!role) kv_rd(0); else FILL(0), NOP, NOP)
  TL(0, 1, if (!role) { kv_mm(true); kv_rd(2); ARRIVE_NW(0); } else FILL(1), NOP, NOP)
  TL(0, 2, if (!role) kv_mm(false); else FILL(2), NOP, if (!role) kv_st())
  TL(0, 3, NOP, NOP, cpk = __hip_atomic_load(cnt, __ATOMIC_RELAXED, __HIP_MEMORY_SCOPE_WORKGROUP))
  STAMP(14);
  if ((unsigned)__builtin_amdgcn_readfirstlane(cpk) < 8u) AWAIT(0, 8);
  asm volatile("" ::: "memory");
  TL(0, 4, NOP, NOP, if (!role) { WAIT_VM(0); set_flag(ws, WS_FPART + ((hq * 16 + rt) * 8 + w) * 8, tag, lane); } else FILL(3))
  PRIO(2);
  TL(0, 5, KX(2, 2), NOP, KW(2, 2, 0)) TL6(0, KW(2, 2, 1), KW(2, 2, 2))
  TL7(0, 1, KW(2, 2, 3), NOP, KW(2, 2, 4))
  STAMP(15);
  TL(1, 0, if (role) FILL(4), NOP, if (red) { fp0 = __hip_atomic_load((gu64*)(ws + WS_FPART) + hq * 128 + lane, RLX_AGENT); fp1 = __hip_atomic_load((gu64*)(ws + WS_FPART) + hq * 128 + 64 + lane, RLX_AGENT); })
  TL(1, 1, if (w < 4) { const int row = (w & 1) * 32 + (lane >> 1);
                      dma4s((w & 2 ? woB : woA) + C0 + ((rt & 7) * 2 + role) * 40 * 640, (row < 40 ? row : 39) * 1280 + (lane & 1) * 156, lds0 + 157696); }, NOP, if (role) FILL(5))
  TL(1, 2, NOP, NOP, if (red) red_load())
  PRIO(1);
  TL(1, 3, NOP, NOP, NOP)
  TL(1, 4, NOP, CLR_VM(), if (red) red_store())
  TL(1, 5, NOP, NOP, NOP)
  PRIO(0);
  TL6(1, if (red) { WAIT_VM(0); set_flag(ws, WS_FKVR + (hq * 16 + rt) * 8, tag, lane); }, NOP)
  if (red) WAIT_BAR(1); else WAIT_BAR(0);
  STAMP(12);
  PRIO(3);
  if (role) set_flag(ws, WS_FOA + ((rt * 8 + hq) * 8 + w) * 8, tag, lane);
  u64 foa;
  TL7(1, 2, KX(3, 0), KW(3, 0, 0), KW(3, 0, 1))
  TL(2, 0, KW(3, 0, 2), KW(3, 0, 3), KW(3, 0, 4)) TL(2, 1, WA(w, 0), WA(w, 1), WA(w, 2))
  TL(2, 2, WA(w, 3), WA(w, 4), (foa = __hip_atomic_load((gu64*)(ws + WS_FOA) + rt * 64 + lane, RLX_AGENT), fkr = __hip_atomic_load((gu64*)(ws + WS_FKVR) + hq * 16 + l15, RLX_AGENT)))
  PRIO(2);
  TL(2, 3, NOP, NOP, NOP)
  auto chk = [&]() { asm volatile("" : "+v"(foa), "+v"(fkr));
    if (!__all(foa == tag)) wait_flags<64>(ws, WS_FOA + rt * 512, tag, lane);
    asm volatile("" ::: "memory"); if (role) oa_load(); };
  TL(2, 4, chk(), NOP, NOP)
  PRIO(1);
  TL(2, 5, NOP, NOP, if (role) oa_mm(0))
  PRIO(0);
  TL6(2, if (role) oa_mm(1), if (role) oa_mm(2))
  BAR_L();
  STAMP(13);
  PRIO(3);
  TL7(2, 0, if (role) oa_mm(3), { if (role) oa_mm(4); CLR_VM(); }, WX(0))
  TL(0, 0, if (role) oa_norm(), WX(1), WX(2))
  TL(0, 1, if (role) oa_st(), WX(3), WX(4))
  TL(0, 2, WA(8 + w, 0), WA(8 + w, 1), WA(8 + w, 2))
  u32x4 wbr[11];
  const char* wbs = (const char*)(woB + C0 + w * 32 * 640) - 4096;
#define WB2(j) do { wbr[j] = *(const GAS u32x4*)(wbs + woff[j] + (j) * 1024); wbr[5 + (j)] = *(const GAS u32x4*)(wbs + 8 * 32 * 1280 + woff[j] + (j) * 1024); } while (0)
  TL(0, 3, WA(8 + w, 3), WA(8 + w, 4), WB2(0))
  PRIO(2);
  TL(0, 4, WB2(1), WB2(2), WB2(3))
  const int w5 = w < 5 ? w : 4;
  const unsigned wo16 = (w5 == 0 ? woff[0] : w5 == 1 ? woff[1] : w5 == 2 ? woff[2] : w5 == 3 ? woff[3] : woff[4]) + w5 * 1024;
  TL(0, 5, WB2(4), NOP, wbr[10] = *(const GAS u32x4*)((const char*)(woB + C0 + 16 * 32 * 640) - 4096 + wo16))
  PRIO(1);
  ARRIVE_NW(1);
  const int tp = w & 1, kq = w >> 1;
  const unsigned ofr = (rt * 8 + role * 4 + tp * 2) * OT_B + kq * 5120 + lane * 16;
  u32x4 of[2][5]; float gtb;
#define OFL(u) do { _Pragma("unroll") for (int i = 0; i < 5; i++) of[u][i] = LD16(WS_OA + ofr + (u) * OT_B + i * 1024); } while (0)
  TL6(0, OFL(0), gtb = __uint_as_float(__builtin_amdgcn_raw_buffer_load_b32(wsr, WS_G + (rt * 128 + w3 * 32 + w2 * 16 + l15) * 4, 0, 16)))
  PRIO(0);
  TL7(0, 0, OFL(1), NOP, NOP)
  pB2[2] = pk(e0, e1); pB2[3] = pk(e2, e3);
  OB = mfma32(v2, pB2, OB);
#undef TL
#undef TL6
#undef TL7
#undef TILE
#undef G
  STAMP(7);
  { LAS f32x4* xb = (LAS f32x4*)(lp + (w3 >> 1) * CH_B) + (w3 & 1) * (64 * 6);
#pragma unroll
    for (int i = 0; i < 12; i++) { auto rr = __builtin_amdgcn_permlane16_swap(__float_as_uint(OA[i]), __float_as_uint(OB[i]), false, false); OA[i] = __uint_as_float(rr[0]); OB[i] = __uint_as_float(rr[1]); }
    const f32x16 snd = w2 ? OA : OB;
    AWAIT(1, 8);
#pragma unroll
    for (int c = 0; c < 3; c++) xb[(w2 * 3 + c) * 64 + lane] = (f32x4){snd[4 * c], snd[4 * c + 1], snd[4 * c + 2], snd[4 * c + 3]};
    asm volatile("s_waitcnt vmcnt(22) lgkmcnt(0)\n\ts_barrier" ::: "memory");
    f32x16 O = w2 ? OB : OA;
#pragma unroll
    for (int c = 0; c < 3; c++) { const f32x4 t = xb[((w2 ^ 1) * 3 + c) * 64 + lane]; O[4 * c] += t[0]; O[4 * c + 1] += t[1]; O[4 * c + 2] += t[2]; O[4 * c + 3] += t[3]; }
    float l = from_upper(O[8]);
    float amax = 0.f;
#pragma unroll
    for (int i = 0; i < 12; i++) amax += fabsf(O[i]);
    const bool good = (l >= 0x1p-30f) && (l <= 0x1p100f) && (amax <= 0x1p126f);
    if (__builtin_expect(!__all(good), 0)) {
      u32x4 q1; u32x2 q2;
#pragma unroll
      for (int i = 0; i < 4; i++) { auto rr = __builtin_amdgcn_permlane16_swap(q1A[i], q1B[i], false, false); q1[i] = w2 ? rr[1] : rr[0]; }
#pragma unroll
      for (int i = 0; i < 2; i++) { auto rr = __builtin_amdgcn_permlane16_swap(q2A[i], q2B[i], false, false); q2[i] = h ? 0u : (w2 ? rr[1] : rr[0]); }
      float m = -INFINITY;
#pragma unroll 1
      for (int t = 0; t < 64; t++) {
        f32x16 s = mfma32(LD16(WS_KV + t * TILE_B + ok1), q1, Z); s = mfma32k8(LD8(WS_KV + t * TILE_B + ok2), q2, s);
#pragma unroll
        for (int i = 0; i < 16; i++) m = fmaxf(m, s[i]);
      }
      m = half_swap_max(m);
      O = Z;
#pragma unroll 1
      for (int t = 0; t < 64; t++) {
        f32x16 s = mfma32(LD16(WS_KV + t * TILE_B + ok1), q1, Z); s = mfma32k8(LD8(WS_KV + t * TILE_B + ok2), q2, s);
#pragma unroll
        for (int i = 0; i < 16; i++) s[i] = ex2(s[i] - m);
        O = mfma32(LD16(WS_KV + t * TILE_B + ov), (u32x4){pk(s[0], s[1]), pk(s[2], s[3]), pk(s[4], s[5]), pk(s[6], s[7])}, O);
        O = mfma32(LD16(WS_KV + t * TILE_B + ov + 2 * VB), (u32x4){pk(s[8], s[9]), pk(s[10], s[11]), pk(s[12], s[13]), pk(s[14], s[15])}, O);
      }
      l = from_upper(O[8]);
    }
    const float inv = __builtin_amdgcn_rcpf(l) * (1.0f - gtb);
    LAS char* tb = lp + w2 * CH_B + 12288 + w3 * 1536;
#pragma unroll
    for (int c = 0; c < 3; c++) { const int k0 = (g & 1) * 20 + 8 * c + 4 * h;
      *(LAS u32x2*)(tb + ((c == 2 && h) ? 1280 + (g & 1) * 8 : (k0 >> 3) * 256 + (k0 & 4) * 2) + l15 * 16) = (u32x2){nz(pk(O[4 * c] * inv, O[4 * c + 1] * inv)), nz(pk(O[4 * c + 2] * inv, O[4 * c + 3] * inv))}; }
    const unsigned ob = WS_OB + (rt * 8 + w3 * 2 + w2) * OT_B + hp * 1280 + lane * 16;
    const u32x4 z0 = ldsr16(tb + lane * 16), z1 = ldsr16(tb + 1024 + l15 * 16);
    ST16(z0, ob); ST16(z1, lane < 16 ? ob + 1024 : 0x7FFF0000u);
  }
  STAMP(8);
  auto of_missing = [&]() { u32x4 mm = of[0][0];
#pragma unroll
    for (int j = 1; j < 10; j++) mm = __builtin_elementwise_max(mm, of[j / 5][j % 5]);
    return missing(mm); };
  u32x4 wf[5][5];
#pragma unroll
  for (int i = 0; i < 5; i++) { const LAS char* wb = lp + L_WO + (kq * 5 + i) * 5120 + wlane;
#pragma unroll
    for (int t = 0; t < 5; t++) wf[i][t] = trd2(wb + t * 8, wb + 1280 + t * 8); }
  SBAR();
  if (__builtin_expect(of_missing(), 0)) {
#pragma unroll 1
    for (unsigned spin = 0; spin < SPIN_CAP; ++spin) {
      asm volatile("" ::: "memory");
      OFL(0); OFL(1);
      if (!of_missing()) break;
      __builtin_amdgcn_s_sleep(1);
    }
  }
  f32x4 c[2][5];
#pragma unroll
  for (int i = 0; i < 5; i++)
#pragma unroll
    for (int t = 0; t < 5; t++) c[0][t] = mfma16(wf[i][t], of[0][i], i ? c[0][t] : (f32x4){0.f, 0.f, 0.f, 0.f});
  BAR_L();
  SBAR();
  { LAS char* d = lp + L_WO + w * 5120 + lane * 16;
    *(LAS u32x4*)(lp + L_WO + 16 * 5120 + w5 * 1024 + lane * 16) = wbr[10];
#pragma unroll
    for (int i = 0; i < 5; i++) {
      *(LAS u32x4*)(d + i * 1024) = wbr[i]; *(LAS u32x4*)(d + 40960 + i * 1024) = wbr[5 + i]; of[0][i] = LD16(WS_OB + ofr + i * 1024); SBAR();
#pragma unroll
      for (int t = 0; t < 5; t++) c[1][t] = mfma16(wf[i][t], of[1][i], i ? c[1][t] : (f32x4){0.f, 0.f, 0.f, 0.f});
      SBAR();
    } }
  STAMP(9);
  BAR_L();
#pragma unroll
  for (int i = 0; i < 5; i++) { const int gr = kq * 5 + i; const LAS char* wb = lp + (gr < 17 ? L_WO + gr * 5120 : 145408 + (gr - 17) * 5120) + wlane;
#pragma unroll
    for (int t = 0; t < 5; t++) wf[i][t] = trd2(wb + t * 8, wb + 1280 + t * 8);
    SBAR(); of[1][i] = LD16(WS_OB + ofr + OT_B + i * 1024); SBAR(); }
  ARRIVE_NW(2);
  STAMP(10);
  LAS char* xw = lp + kq * 21504 + (tp * 32 + l15) * 336 + g * 80;
#pragma unroll
  for (int u = 0; u < 2; u++) {
#pragma unroll
    for (int i = 0; i < 5; i++) {
      if (__builtin_expect(missing(of[u][i]), 0)) {
#pragma unroll 1
        for (unsigned spin = 0; spin < SPIN_CAP; ++spin) {
          asm volatile("" ::: "memory");
#pragma unroll
          for (int j = u * 5 + i; j < 10; j++) of[j / 5][j % 5] = LD16(WS_OB + ofr + (j / 5) * OT_B + (j % 5) * 1024);
          if (!missing(of[u][i])) break;
          __builtin_amdgcn_s_sleep(1);
        }
      }
#pragma unroll
      for (int t = 0; t < 5; t++) c[u][t] = mfma16(wf[i][t], of[u][i], c[u][t]);
      if (u == 0 && i == 3) cpk = __hip_atomic_load(cnt + 2, __ATOMIC_RELAXED, __HIP_MEMORY_SCOPE_WORKGROUP);
      if (u == 1) { if (i == 0) { if (__builtin_expect((unsigned)__builtin_amdgcn_readfirstlane(cpk) < 8u, 0)) AWAIT(2, 8); asm volatile("" ::: "memory"); }
        *(LAS f32x4*)(xw + i * 16) = c[0][i]; }
      SBAR();
    }
  }
#pragma unroll
  for (int t = 0; t < 5; t++) *(LAS f32x4*)(xw + 16 * 336 + t * 16) = c[1][t];
  BAR_L();
  { const __amdgpu_buffer_rsrc_t orr = __builtin_amdgcn_make_buffer_rsrc(out, 0, 2048 * DM * 2, 0x00020000);
#pragma unroll
    for (int j = 0; j < 3; j++) { const int n = tid + 512 * j, nn = n < 1280 ? n : 0, row = (nn * 3277) >> 16, cq = nn - row * 20;
      const LAS f32x4* p = (const LAS f32x4*)(lp + row * 336 + cq * 16);
      const f32x4 z = (p[0] + p[1344]) + (p[2688] + p[4032]);
      __builtin_amdgcn_raw_buffer_store_b64((u32x2){pk(z[0], z[1]), pk(z[2], z[3])}, orr, n < 1280 ? ((rt * 128 + role * 64 + row) * DM + C0 + cq * 4) * 2 : 0x7FFF0000u, 0, 16); }
  }
  STAMP(11);
}
}

extern "C" void kernel_launch(void* const* d_in, const int* in_sizes, int n_in, void* d_out, int out_size, void* d_ws, size_t ws_size, hipStream_t stream) {
  using namespace gm;
  fused<<<256, 512, 0, stream>>>((const u16*)d_in[0], (const u16*)d_in[1], (const u16*)d_in[2], (const u16*)d_in[3], (const u16*)d_in[5], (const u16*)d_in[6],
                                 (const u16*)d_in[7], (const u16*)d_in[8], (const u16*)d_in[9], (u16*)d_out, (char*)d_ws);
}
```

```cpp
#include <hip/hip_runtime.h>
#include <hip/hip_bf16.h>
#include <stdint.h>

namespace gm {
typedef float f32x16 __attribute__((ext_vector_type(16)));
typedef float f32x4 __attribute__((ext_vector_type(4)));
typedef float f32x2 __attribute__((ext_vector_type(2)));
typedef short s16x4 __attribute__((ext_vector_type(4)));
typedef __bf16 b16x2 __attribute__((ext_vector_type(2)));
typedef __bf16 b16x8 __attribute__((ext_vector_type(8)));
typedef unsigned u32x4 __attribute__((ext_vector_type(4)));
typedef unsigned u32x2 __attribute__((ext_vector_type(2)));
typedef unsigned long long u64;
typedef unsigned short u16;
#define DI __device__ __forceinline__
#define LAS __attribute__((address_space(3)))
#define GAS __attribute__((address_space(1)))
typedef GAS u64 gu64;

constexpr int DM = 640;
constexpr unsigned WS_EPOCH = 0, WS_FKV = 2048, WS_FKVR = 4096, WS_FQ = 8192, WS_FPART = 16384, WS_FOA = 24576, WS_G = 57344;
constexpr unsigned WS_KVR = 8912896, KVR_B = 10240, WS_KV = 131072, WS_QX = 327680, QX_B = 10240, WS_PART = 1703936, PART_B = 15360;
constexpr unsigned WS_OA = 3670016, WS_OB = 6291456, WS_END = 8994816, OT_B = 20480;
constexpr int VB = 352, TILE_B = 1280 + 4 * VB, CH_B = TILE_B * 8, STEP_B = 2 * CH_B;
constexpr int STAGE_B = 36864, ST_W1 = 10240, ST_X = 20480;
constexpr int L_BUF = 43008, L_P0W = 0, L_P0X0 = 25600, L_P0X1 = 153600, L_RED = 158720, L_RED1 = 40960;
constexpr int L_HO = 86016, L_QL = 96256, L_VIM = 96256, L_PHK = 116736, L_WPHI = 158592, L_ZERO = 161792, L_CNT = 163712, L_TOTAL = 163840;
constexpr int L_WO = 43008;
constexpr float LOG2E = 1.4426950408889634f;
constexpr float QSCALE = 0.22360679774997896f * LOG2E;
constexpr unsigned SPIN_CAP = 20000, LSPIN_CAP = 400000;

DI unsigned pk(float a, float b) { f32x2 v = {a, b}; b16x2 r = __builtin_convertvector(v, b16x2); return __builtin_bit_cast(unsigned, r); }
DI f32x16 mfma32(u32x4 a, u32x4 b, f32x16 c) { return __builtin_amdgcn_mfma_f32_32x32x16_bf16(__builtin_bit_cast(b16x8, a), __builtin_bit_cast(b16x8, b), c, 0, 0, 0); }
DI f32x16 mfma32k8(u32x2 a, u32x2 b, f32x16 c) { return __builtin_amdgcn_mfma_f32_32x32x8bf16_1k(__builtin_bit_cast(s16x4, a), __builtin_bit_cast(s16x4, b), c, 0, 0, 0); }
DI f32x4 mfma16(u32x4 a, u32x4 b, f32x4 c) { return __builtin_amdgcn_mfma_f32_16x16x32_bf16(__builtin_bit_cast(b16x8, a), __builtin_bit_cast(b16x8, b), c, 0, 0, 0); }
DI u32x2 trd(const LAS char* p) { return __builtin_bit_cast(u32x2, __builtin_amdgcn_ds_read_tr16_b64_v4i16((LAS s16x4*)p)); }
DI u32x4 trd2(const LAS char* p0, const LAS char* p1) { u32x2 a = trd(p0), b = trd(p1); return (u32x4){a.x, a.y, b.x, b.y}; }
DI u32x4 ldsr16(const LAS char* p) { return *(const LAS u32x4*)p; }
DI u32x2 ldsr8(const LAS char* p) { return *(const LAS u32x2*)p; }
DI float ex2(float x) { return __builtin_amdgcn_exp2f(x); }
DI void dma16s(const void* base, unsigned voff, unsigned lds_base) {
  asm volatile("s_mov_b32 m0, %2\n\ts_nop 0\n\tglobal_load_lds_dwordx4 %0, %1" :: "v"(voff), "s"(base), "s"(lds_base) : "memory"); }
DI void dma16s_sc1(const void* base, unsigned voff, unsigned lds_base) {
  asm volatile("s_mov_b32 m0, %2\n\ts_nop 0\n\tglobal_load_lds_dwordx4 %0, %1 sc1" :: "v"(voff), "s"(base), "s"(lds_base) : "memory"); }
DI void dmag_open(unsigned lds_base) { asm volatile("s_mov_b32 m0, %0\n\ts_nop 0" :: "s"(lds_base) : "memory"); }
template <int J> DI void dmag(const void* base, unsigned voff) { asm volatile("global_load_lds_dwordx4 %0, %1 offset:%2" :: "v"(voff), "s"(base), "i"((J & 3) * 1024) : "memory"); }
template <int J> DI void dmag_sc1(const void* base, unsigned voff) { asm volatile("global_load_lds_dwordx4 %0, %1 offset:%2 sc1" :: "v"(voff), "s"(base), "i"((J & 3) * 1024) : "memory"); }
DI void dma4s(const void* base, unsigned voff, unsigned lds_base) {
  asm volatile("s_mov_b32 m0, %2\n\ts_nop 0\n\tglobal_load_lds_dword %0, %1" :: "v"(voff), "s"(base), "s"(lds_base) : "memory"); }
DI u32x4 ld16_async(const void* p) { u32x4 r; asm volatile("global_load_dwordx4 %0, %1, off" : "=v"(r) : "v"(p) : "memory"); return r; }
DI u32x2 ld8_sc1_async(const void* p) { u32x2 r; asm volatile("global_load_dwordx2 %0, %1, off sc1" : "=v"(r) : "v"(p) : "memory"); return r; }
#define BAR_L() asm volatile("s_waitcnt lgkmcnt(0)\n\ts_barrier" ::: "memory")
#define WAIT_BAR(N) asm volatile("s_waitcnt vmcnt(" #N ") lgkmcnt(0)\n\ts_barrier" ::: "memory")
#define WAIT_VM(N) asm volatile("s_waitcnt vmcnt(" #N ")" ::: "memory")
#define SBAR() __builtin_amdgcn_sched_barrier(0)
#define RLX_AGENT __ATOMIC_RELAXED, __HIP_MEMORY_SCOPE_AGENT
DI float half_swap_max(float m) { auto rr = __builtin_amdgcn_permlane32_swap(__float_as_uint(m), __float_as_uint(m), false, false); return fmaxf(__uint_as_float(rr[0]), __uint_as_float(rr[1])); }
DI float from_upper(float v) { auto rr = __builtin_amdgcn_permlane32_swap(__float_as_uint(v), __float_as_uint(v), false, false); return __uint_as_float(rr[1]); }

template <int NF>
DI bool flags_set(const char* ws, unsigned off, u64 tag, int lane) {
  gu64* f = (gu64*)(ws + off);
  bool ok = __hip_atomic_load(f + (NF < 64 ? (lane & (NF - 1)) : lane), RLX_AGENT) == tag;
  if (NF >= 128) ok &= __hip_atomic_load(f + 64 + lane, RLX_AGENT) == tag;
  if (NF >= 256) { ok &= __hip_atomic_load(f + 128 + lane, RLX_AGENT) == tag; ok &= __hip_atomic_load(f + 192 + lane, RLX_AGENT) == tag; }
  return ok;
}
template <int NF>
DI void wait_flags(const char* ws, unsigned off, u64 tag, int lane) {
#pragma unroll 1
  for (unsigned spin = 0; spin < SPIN_CAP; ++spin) {
    if (__all(flags_set<NF>(ws, off, tag, lane))) break;
    __builtin_amdgcn_s_sleep(2);
  }
  asm volatile("" ::: "memory");
}
DI void set_flag(char* ws, unsigned off, u64 tag, int lane) { if (lane == 0) __hip_atomic_store((gu64*)(ws + off), tag, RLX_AGENT); }
DI unsigned nz(unsigned v) { return v < 0xFFFFFFFEu ? v : 0xFFFFFFFEu; }
DI unsigned umax3(unsigned a, unsigned b, unsigned c) { unsigned m = a > b ? a : b; return m > c ? m : c; }
DI bool missing(u32x4 v) { return __any(umax3(umax3(v.x, v.y, v.z), v.w, 0u) == 0xFFFFFFFFu); }
#define ONES ((u32x4){0xFFFFFFFFu, 0xFFFFFFFFu, 0xFFFFFFFFu, 0xFFFFFFFFu})
#define ST16(v, off) __builtin_amdgcn_raw_buffer_store_b128((v), wsr, (off), 0, 16)
#define ST8(v, off) __builtin_amdgcn_raw_buffer_store_b64((v), wsr, (off), 0, 16)
#define LD16(off) __builtin_amdgcn_raw_buffer_load_b128(wsr, (off), 0, 16)
#define LD8(off) __builtin_amdgcn_raw_buffer_load_b64(wsr, (off), 0, 16)

#define STAMP(i) do {} while (0)

__global__ __launch_bounds__(512, 2) void fused(const u16* __restrict__ x, const u16* __restrict__ wqkv, const u16* __restrict__ wphi, const u16* __restrict__ woA,
                                                const u16* __restrict__ wq, const u16* __restrict__ wk, const u16* __restrict__ wv, const u16* __restrict__ woB,
                                                const u16* __restrict__ gatew, u16* __restrict__ out, char* ws) {
  __shared__ __attribute__((aligned(16))) char lds[L_TOTAL];
  const int tid = threadIdx.x, lane = tid & 63;
  const int w = __builtin_amdgcn_readfirstlane(tid >> 6) & 7;
  const int l15 = lane & 15, g = lane >> 4, q4 = l15 >> 2, p4 = lane & 3, r = lane & 31, h = lane >> 5;
  const int bx = blockIdx.x & 7, bi = blockIdx.x >> 3;
  const int rt = (bx & 1) * 8 + (bi & 7), hp = (bx >> 1) * 4 + ((bi >> 3) & 3), hq = hp >> 1, role = hp & 1;
  const int w3 = w & 3, w2 = w >> 2;
  LAS char* lp = (LAS char*)lds;
  const unsigned lds0 = (unsigned)(uintptr_t)lds;
  const __amdgpu_buffer_rsrc_t wsr = __builtin_amdgcn_make_buffer_rsrc(ws, 0, WS_END, 0x00020000);
  LAS unsigned* cnt = (LAS unsigned*)(lp + L_CNT);
#define ARRIVE_NW(c) do { asm volatile("" ::: "memory"); if (lane == 0) __hip_atomic_fetch_add(cnt + (c), 1u, __ATOMIC_RELAXED, __HIP_MEMORY_SCOPE_WORKGROUP); asm volatile("" ::: "memory"); } while (0)
#define PEEK(c) ((unsigned)__builtin_amdgcn_readfirstlane(__hip_atomic_load(cnt + (c), __ATOMIC_RELAXED, __HIP_MEMORY_SCOPE_WORKGROUP)))
#define AWAIT(c, n) do { _Pragma("unroll 1") for (unsigned sp_ = 0; sp_ < LSPIN_CAP; ++sp_) { if (PEEK(c) >= (unsigned)(n)) break; __builtin_amdgcn_s_sleep(1); } asm volatile("" ::: "memory"); } while (0)
  STAMP(0);

  const int T0 = rt * 128 + hq * 16;
  const int C0 = hq * 80;
  const int wlane = ((g >> 1) * 16 + (g & 1) * 4 + q4) * 160 + p4 * 40;
  u64 tag;
  unsigned o0[10];
#define KVP(s, slot, p) dma16s_sc1(ws + WS_KV + (s) * STEP_B + (p) * 1024, lane * 16, lds0 + (slot) * STEP_B + (p) * 1024)
#define KVL(s, slot) do { _Pragma("unroll") for (int j = 0; j < 11; j++) if (j < 10 || w3 < 2) KVP(s, slot, w3 + 4 * j); } while (0)
#define KW(s, slot, j) do { if ((j) == 4) KVP(s, slot, w * 5 + 4); else { if ((j) == 0) dmag_open(lds0 + (slot) * STEP_B + w * 5120); dmag_sc1<(j)>(ws + WS_KV + (s) * STEP_B + w * 5120, lane * 16); } } while (0)
#define KX(s, slot) do { if (w < 2) KVP(s, slot, 40 + w); } while (0)
  if (w2 == 0) {
    u32x4 gf0, gf1, gf2, gf3, gf4, wpc;
    u32x2 ep = ld8_sc1_async(ws + WS_EPOCH);
    const int lx = w3 < 3 ? L_P0X0 + w3 * 5120 : L_P0X1;
    { const u16* wsrc = (role ? wv : wk) + w3 * 3200;
#pragma unroll
      for (int j = 0; j < 5; j++) { const int n = j * 64 + lane, row = (n * 3277) >> 16, cc = n - row * 20; dma16s(x + T0 * DM + w3 * 160, row * 1280 + cc * 16, lds0 + lx + j * 1024); }
#pragma unroll
      for (int j = 0; j < 6; j++) dma16s(wsrc + j * 512, lane * 16, lds0 + L_P0W + w3 * 6400 + j * 1024);
      if (lane < 16) dma16s(wsrc + 6 * 512, lane * 16, lds0 + L_P0W + w3 * 6400 + 6 * 1024);
      const u16* gp = gatew + w3 * 160 + g * 8;
      gf0 = ld16_async(gp); gf1 = ld16_async(gp + 32); gf2 = ld16_async(gp + 64); gf3 = ld16_async(gp + 96); gf4 = ld16_async(gp + 128);
      wpc = ld16_async(wphi + hq * 1600 + (tid < 200 ? tid : 199) * 8);
    }
    asm volatile("s_waitcnt vmcnt(0)" : "+v"(gf0), "+v"(gf1), "+v"(gf2), "+v"(gf3), "+v"(gf4), "+v"(wpc), "+v"(ep) :: "memory");
    tag = (((u64)(unsigned)__builtin_amdgcn_readfirstlane(ep.y) << 32) | (unsigned)__builtin_amdgcn_readfirstlane(ep.x)) + 1ull;
    STAMP(1);
    { const LAS char* wb = lp + L_P0W + (w3 * 160 + g * 8 + q4) * 40;
      const LAS char* xb = lp + lx + l15 * 320 + g * 16;
      const bool gl = l15 == 4;
      f32x4 c0 = {}, c1 = {};
#define P0STEP(s, GF) { const u32x4 b = ldsr16(xb + (s) * 64); c0 = mfma16(trd2(wb + p4 * 8 + (s) * 1280, wb + p4 * 8 + (s) * 1280 + 160), b, c0); \
        u32x4 a = trd2(wb + 32 + (s) * 1280, wb + 32 + (s) * 1280 + 160); if (gl) a = GF; c1 = mfma16(a, b, c1); }
      P0STEP(0, gf0) P0STEP(1, gf1) P0STEP(2, gf2) P0STEP(3, gf3) P0STEP(4, gf4)
#undef P0STEP
      *(LAS f32x4*)(lp + L_RED + tid * 16) = c0;
      if (lane < 32) *(LAS f32x4*)(lp + L_RED1 + (w3 * 32 + lane) * 16) = c1; }
    STAMP(2);
    f32x16 acc[5];
#pragma unroll
    for (int b = 0; b < 5; b++) acc[b] = (f32x16){};
    const int wm = g & 1, whd = p4;
    const int wo0 = wm * ST_W1 + (8 * h + 4 * wm + q4) * 160 + whd * 40, wo1 = wm * ST_W1 + (8 * h + 4 * (wm ^ 1) + q4) * 160 + whd * 40;
    const int xo = ST_X + (w3 * 32 + r) * 128 + ((h ^ ((r >> 1) & 7)) * 16);
    u32x4 fa[5], fb[5], xa, xb;
#define KSTEP(FN, XN, s, FC, XC) { XN = ldsr16(sb + (xo ^ ((s) * 32))); \
      _Pragma("unroll") for (int j = 0; j < 5; j++) { if (i > 0 || (s) > 0) acc[j] = mfma32(FC[j], XC, acc[j]); FN[j] = trd2(sb + wo0 + (s) * 2560 + j * 8, sb + wo1 + (s) * 2560 + j * 8); SBAR(); } }
#pragma unroll
    for (int i = 0; i < 10; i++) {
      BAR_L();
      if (i == 1 && role) {
        const unsigned qb = WS_QX + (rt * 8 + hq) * QX_B + w3 * 1280 + lane * 16;
        ST16(ONES, qb); ST16(ONES, qb + 5120); if (lane < 32) ST16(ONES, qb + 1024 + (lane >> 4) * (5120 - 256)); }
      if (i == 4) { if (role) { WAIT_VM(0); set_flag(ws, WS_FQ + ((rt * 8 + hq) * 4 + w3) * 8, tag, lane); }
                    if (tid < 200) *(LAS u32x4*)(lp + L_WPHI + tid * 16) = wpc; }
      const LAS char* sb = lp + L_BUF + ((i + 2) % 3) * STAGE_B;
      KSTEP(fa, xa, 0, fb, xb) KSTEP(fb, xb, 1, fa, xa) KSTEP(fa, xa, 2, fb, xb)
      { xb = ldsr16(sb + (xo ^ 96));
        acc[0] = mfma32(fa[0], xa, acc[0]); fb[0] = trd2(sb + wo0 + 7680, sb + wo1 + 7680); fb[1] = trd2(sb + wo0 + 7688, sb + wo1 + 7688); SBAR();
        acc[1] = mfma32(fa[1], xa, acc[1]); fb[2] = trd2(sb + wo0 + 7696, sb + wo1 + 7696); fb[3] = trd2(sb + wo0 + 7704, sb + wo1 + 7704); SBAR();
        acc[2] = mfma32(fa[2], xa, acc[2]); fb[4] = trd2(sb + wo0 + 7712, sb + wo1 + 7712); SBAR();
        acc[3] = mfma32(fa[3], xa, acc[3]); acc[4] = mfma32(fa[4], xa, acc[4]); SBAR(); }
    }
#pragma unroll
    for (int j = 0; j < 5; j++) acc[j] = mfma32(fb[j], xb, acc[j]);
#undef KSTEP
    STAMP(3);
#define PK2(t, i, sc) pk(acc[t][i] * (sc), acc[t][(i) + 1] * (sc))
#define ROW(i, sc) const u32x4 ra = {PK2(0, i, sc), PK2(0, (i) + 2, sc), PK2(1, i, sc), PK2(1, (i) + 2, sc)}, rb = {PK2(2, i, sc), PK2(2, (i) + 2, sc), PK2(3, i, sc), PK2(3, (i) + 2, sc)}; const u32x2 rc = {PK2(4, i, sc), PK2(4, (i) + 2, sc)}
    { ROW(4, 1.0f);
      *(LAS u32x4*)(lp + L_HO + tid * 16) = ra; *(LAS u32x4*)(lp + L_HO + 4096 + tid * 16) = rb; *(LAS u32x2*)(lp + L_HO + 8192 + tid * 8) = rc; }
    BAR_L();
    const int qfo = (h * 4 + w3) * 1280;
    if (role) {
      { ROW(8, QSCALE); const unsigned qb = WS_QX + (rt * 8 + hq) * QX_B + qfo;
        ST16(((u32x4){nz(ra.x), nz(ra.y), nz(ra.z), nz(ra.w)}), qb + r * 16); ST16(((u32x4){nz(rb.x), nz(rb.y), nz(rb.z), nz(rb.w)}), qb + 512 + r * 16); ST8(((u32x2){nz(rc.x), nz(rc.y)}), qb + 1024 + r * 8); }
      { ROW(12, QSCALE); LAS char* d = lp + L_QL + qfo;
        *(LAS u32x4*)(d + r * 16) = ra; *(LAS u32x4*)(d + 512 + r * 16) = rb; *(LAS u32x2*)(d + 1024 + r * 8) = rc; }
    } else { LAS char* d = lp + L_VIM + (h * 128 + w3 * 32 + r) * 40;
#pragma unroll
      for (int t = 0; t < 5; t++) { *(LAS u32x2*)(d + t * 8) = (u32x2){PK2(t, 8, 1.0f), PK2(t, 10, 1.0f)}; *(LAS u32x2*)(d + 10240 + t * 8) = (u32x2){PK2(t, 12, 1.0f), PK2(t, 14, 1.0f)}; } }
#pragma unroll
    for (int t = 0; t < 5; t++) { o0[2 * t] = PK2(t, 0, 1.0f); o0[2 * t + 1] = PK2(t, 2, 1.0f); }
#undef ROW
#undef PK2
  } else {
    u32x2 ep = ld8_sc1_async(ws + WS_EPOCH);
    const bool second = w3 >> 1;
    const unsigned rs = (second && role) ? 1280u : 3840u;
    const char* wsrc = (second ? (role ? (const char*)(wq + C0) : (const char*)(wqkv + 1280 + C0)) : (const char*)(wqkv + (role ? 0 : 640) + C0)) + (w3 & 1) * 32 * rs;
    const char* xsrc = (const char*)(x + (rt * 128 + w3 * 32) * DM);
    int rot = (bi & 7) + 3 * ((bi >> 3) & 3); rot = rot >= 10 ? rot - 10 : rot;
    unsigned so[5], xoff[2];
#pragma unroll
    for (int j = 0; j < 5; j++) { const int c = j * 64 + lane, rho = (c * 6554) >> 16, cc = c - rho * 10; so[j] = (rho ^ (second ? 4 : 0)) * rs + cc * 16; }
#pragma unroll
    for (int j = 0; j < 2; j++) { const int row = j * 8 + (lane >> 3); xoff[j] = row * 1280 + (((lane & 7) ^ ((row >> 1) & 7)) * 16); }
    int bsel = 2, kc = rot;
    auto stage = [&]() {
      const unsigned lb = lds0 + L_BUF + bsel * STAGE_B;
      const char* bw = wsrc + kc * 64 * rs; const char* bxp = xsrc + kc * 128;
#pragma unroll
      for (int j = 0; j < 5; j++) { dma16s(bw, so[j], lb + (w3 * 5 + j) * 1024); if (j < 4) dma16s(bxp + (j >> 1) * 16 * 1280, xoff[j & 1], lb + ST_X + (w3 * 4 + j) * 1024); }
      bsel = bsel == 2 ? 0 : bsel + 1; kc = kc == 9 ? 0 : kc + 1;
    };
    stage(); stage();
    asm volatile("s_waitcnt vmcnt(9)" : "+v"(ep) :: "memory");
    tag = (((u64)(unsigned)__builtin_amdgcn_readfirstlane(ep.y) << 32) | (unsigned)__builtin_amdgcn_readfirstlane(ep.x)) + 1ull;
    STAMP(1); STAMP(2);
#pragma unroll 1
    for (int i = 0; i < 8; i++) {
      WAIT_BAR(9);
      stage();
      if (i == 0 && w3 == 0) {
        const unsigned tb = WS_KV + (T0 >> 5) * TILE_B; const int s16 = hq & 1;
        if (!role) {
          const LAS f32x4* rp = (const LAS f32x4*)(lp + (h ? L_RED1 : L_RED)) + (h ? (g & 1) * 16 : g * 32) + l15;
          const int st = h ? 32 : 64, o2 = h ? 0 : 16;
          const f32x4 a = (rp[0] + rp[st]) + (rp[2 * st] + rp[3 * st]), b = (rp[o2] + rp[st + o2]) + (rp[2 * st + o2] + rp[3 * st + o2]);
          const u32x4 v = {pk(a[0], a[1]), pk(a[2], a[3]), pk(b[0], b[1]), pk(b[2], b[3])};
          if (lane < 32) ST16(v, tb + (g * 32 + s16 * 16 + l15) * 16);
          else if (lane < 48) ST8(((u32x2){v.x, v.y}), tb + 1024 + (s16 * 16 + l15) * 8);
          else { const float gv = 1.0f / (1.0f + __expf(-a[0])); __builtin_amdgcn_raw_buffer_store_b32(__float_as_uint(gv), wsr, WS_G + (T0 + l15) * 4, 0, 16); }
        } else if (lane < 44) {
          const int hh = lane >= 22, d = lane - hh * 22, dd = d < 20 ? d : 0, st = dd >= 16 ? 128 : 256;
          const LAS float* rp = (const LAS float*)(lp + (dd >= 16 ? L_RED1 : L_RED)) + (((dd & 15) >> 2) * 16 + 4 * hh) * 4 + (dd & 3);
          float e[8];
#pragma unroll
          for (int j = 0; j < 8; j++) { const int tk = (8 * (j >> 2) + (j & 3)) * 4; e[j] = (rp[tk] + rp[tk + st]) + (rp[tk + 2 * st] + rp[tk + 3 * st]); }
          u32x4 v = {pk(e[0], e[1]), pk(e[2], e[3]), pk(e[4], e[5]), pk(e[6], e[7])};
          if (d == 20) v = (u32x4){0x3F803F80u, 0x3F803F80u, 0x3F803F80u, 0x3F803F80u};
          if (d == 21) v = (u32x4){0u, 0u, 0u, 0u};
          ST16(v, tb + 1280 + (s16 * 2 + hh) * VB + d * 16);
        }
      }
      if (i == 2) { if (w3 == 0) set_flag(ws, WS_FKV + blockIdx.x * 8, tag, lane);
                    if (w3 >= 2) *(LAS u32x4*)(lp + L_ZERO + (tid - 384) * 16) = (u32x4){0u, 0u, 0u, 0u}; }
    }
    u64 fa, fb, fc, fd, fe;
    { gu64* f = (gu64*)(ws + WS_FKV) + lane;
      fa = __hip_atomic_load(f, RLX_AGENT); fb = __hip_atomic_load(f + 64, RLX_AGENT); fc = __hip_atomic_load(f + 128, RLX_AGENT); fd = __hip_atomic_load(f + 192, RLX_AGENT);
      fe = __hip_atomic_load((gu64*)(ws + WS_FQ) + (rt * 8 + hq) * 4 + p4, RLX_AGENT); }
    WAIT_BAR(14);
    asm volatile("" : "+v"(fa), "+v"(fb), "+v"(fc), "+v"(fd), "+v"(fe));
    const bool kvok = __all((fa == tag) & (fb == tag) & (fc == tag) & (fd == tag) & (fe == tag));
    asm volatile("" ::: "memory");
    BAR_L();
    if (!kvok) { wait_flags<256>(ws, WS_FKV, tag, lane); wait_flags<4>(ws, WS_FQ + (rt * 8 + hq) * 32, tag, lane); }
    KVL(0, 0);
    if (blockIdx.x == 0 && w3 == 0) set_flag(ws, WS_EPOCH, tag, lane);
    STAMP(3);
    BAR_L();
    const int ht = tid - 256;
    const u32x4 a = ldsr16(lp + L_HO + ht * 16), b = ldsr16(lp + L_HO + 4096 + ht * 16); const u32x2 c = ldsr8(lp + L_HO + 8192 + ht * 8);
    o0[0] = a.x; o0[1] = a.y; o0[2] = a.z; o0[3] = a.w; o0[4] = b.x; o0[5] = b.y; o0[6] = b.z; o0[7] = b.w; o0[8] = c.x; o0[9] = c.y;
  }
  STAMP(4);
  u32x4 ph[5];
  { const LAS char* pb = ((p4 & 1) == h) ? lp + L_WPHI + (2 * w2 + h) * 800 + q4 * 40 + (2 * (g & 1) + (p4 >> 1)) * 8 : lp + L_ZERO;
    const u32x4 b0 = {o0[0], o0[1], o0[2], o0[3]}, b1 = {o0[4], o0[5], o0[6], o0[7]}, b2 = {o0[8], o0[9], 0u, 0u};
    const u32x2 c0 = trd(pb + 640), c1 = trd(pb + 672);
    f32x16 P0 = {}, P1 = {};
    P0 = mfma32(trd2(pb, pb + 160), b0, P0); P1 = mfma32(trd2(pb + 32, pb + 192), b0, P1);
    P0 = mfma32(trd2(pb + 320, pb + 480), b1, P0); P1 = mfma32(trd2(pb + 352, pb + 512), b1, P1);
    P0 = mfma32((u32x4){c0.x, c0.y, 0u, 0u}, b2, P0); P1 = mfma32((u32x4){c1.x, c1.y, 0u, 0u}, b2, P1);
    KX(1, 1); KW(1, 1, 0); SBAR();
    f32x2 p[10];
#pragma unroll
    for (int f = 0; f < 10; f++) p[f] = f < 8 ? (f32x2){P0[(2 * f) & 15], P0[(2 * f + 1) & 15]} : (f32x2){P1[(2 * f) & 3], P1[(2 * f + 1) & 3]};
    float mx = fmaxf(p[0][0], p[0][1]), mn = fminf(p[0][0], p[0][1]);
#pragma unroll
    for (int f = 1; f < 10; f++) { mx = fmaxf(fmaxf(mx, p[f][0]), p[f][1]); mn = fminf(fminf(mn, p[f][0]), p[f][1]); }
    const f32x2 ma = {-mx * LOG2E, -mx * LOG2E}, mb = {mn * LOG2E, mn * LOG2E}, ca_ = {LOG2E, LOG2E}, cb_ = {-LOG2E, -LOG2E};
    f32x2 ea[10], eb[10], sa = {0.f, 0.f}, sb = {0.f, 0.f};
#pragma unroll
    for (int f = 0; f < 10; f++) { if (f % 2 == 0 && f < 8) { SBAR(); if (f == 0) KW(1, 1, 1); if (f == 2) KW(1, 1, 2); if (f == 4) KW(1, 1, 3); if (f == 6) KW(1, 1, 4); SBAR(); }
      const f32x2 xa = __builtin_elementwise_fma(p[f], ca_, ma), xb = __builtin_elementwise_fma(p[f], cb_, mb);
      ea[f] = (f32x2){ex2(xa[0]), ex2(xa[1])}; eb[f] = (f32x2){ex2(xb[0]), ex2(xb[1])}; sa += ea[f]; sb += eb[f]; }
    const float ia = __builtin_amdgcn_rcpf(sa[0] + sa[1]), ib = __builtin_amdgcn_rcpf(sb[0] + sb[1]);
    unsigned o[20];
#pragma unroll
    for (int f = 0; f < 10; f++) { const f32x2 ya = ea[f] * ia, yb = eb[f] * ib; o[f] = pk(ya[0], ya[1]); o[10 + f] = pk(yb[0], yb[1]); }
#pragma unroll
    for (int c = 0; c < 5; c++) ph[c] = (u32x4){o[4 * c], o[4 * c + 1], o[4 * c + 2], o[4 * c + 3]};
    asm volatile("" : "+v"(ph[0]), "+v"(ph[1]), "+v"(ph[2]), "+v"(ph[3]), "+v"(ph[4]));
  }
  u32x4 q1A, q1B; u32x2 q2A, q2B;
  if (role) {
    WAIT_BAR(0);
    STAMP(5);
    const LAS char* qa = lp + L_QL + w3 * 1280;
    q1A = ldsr16(qa + lane * 16); q1B = ldsr16(qa + 5120 + lane * 16); q2A = ldsr8(qa + 1024 + r * 8); q2B = ldsr8(qa + 6144 + r * 8);
    ARRIVE_NW(0);
  } else {
    const unsigned qb = WS_QX + (rt * 8 + hq) * QX_B + w3 * 1280;
    q1A = LD16(qb + lane * 16); q1B = LD16(qb + 5120 + lane * 16); q2A = LD8(qb + 1024 + r * 8); q2B = LD8(qb + 6144 + r * 8);
    { LAS char* d = lp + L_PHK + ((2 * w2 + h) * 128 + w3 * 32 + r) * 80;
#pragma unroll
      for (int c = 0; c < 5; c++) *(LAS u32x4*)(d + c * 16) = ph[c]; }
    WAIT_BAR(4);
    STAMP(5);
#pragma unroll 1
    for (unsigned spin = 0; spin < SPIN_CAP; ++spin) {
      if (!(missing(q1A) | missing(q1B) | missing((u32x4){q2A.x, q2A.y, q2B.x, q2B.y}))) break;
      __builtin_amdgcn_s_sleep(1);
      asm volatile("" ::: "memory");
      q1A = LD16(qb + lane * 16); q1B = LD16(qb + 5120 + lane * 16); q2A = LD8(qb + 1024 + r * 8); q2B = LD8(qb + 6144 + r * 8);
    }
  }
  STAMP(6);
  const int vr = r <= 20 ? r : r - 12;
  const int ok1 = lane * 16, ok2 = h ? (1280 + 20 * 16) : (1024 + r * 8), ov = 1280 + h * VB + vr * 16;
  const f32x16 Z = {};
  if (h) { q2A = (u32x2){0u, 0u}; q2B = (u32x2){0u, 0u}; }
  { const LAS char* t0 = lp;
    const u32x4 k1 = ldsr16(t0 + ok1); const u32x2 k2 = ldsr8(t0 + ok2);
    f32x16 sa = mfma32(k1, q1A, Z); sa = mfma32k8(k2, q2A, sa);
    f32x16 sb = mfma32(k1, q1B, Z); sb = mfma32k8(k2, q2B, sb);
    float ma = sa[0], mb = sb[0];
#pragma unroll
    for (int i = 1; i < 16; i++) { ma = fmaxf(ma, sa[i]); mb = fmaxf(mb, sb[i]); }
    ma = half_swap_max(ma); mb = half_swap_max(mb);
    const float mah = __uint_as_float(__float_as_uint(ma) & 0xFFFF0000u), mbh = __uint_as_float(__float_as_uint(mb) & 0xFFFF0000u);
    if (h) { q2A.x = pk(-mah, mah - ma); q2B.x = pk(-mbh, mbh - mb); } }
  f32x16 OA = {}, OB = {}, SA, SB;
  u32x4 k1, v1, v2, v2n, pA1, pA2, pB1, pB2; u32x2 k2;
  float e0, e1, e2, e3;
  const LAS char* c0p = lp + w2 * CH_B + ok1; const LAS char* c1p = lp + w2 * CH_B + ok2; const LAS char* c2p = lp + w2 * CH_B + ov;
  k1 = ldsr16(c0p); k2 = ldsr8(c1p);
  SA = mfma32(k1, q1A, Z); SA = mfma32k8(k2, q2A, SA);
  v1 = ldsr16(c2p); v2 = v1;
  e0 = e1 = e2 = e3 = 0.f; pB2 = (u32x4){0u, 0u, 0u, 0u};
#define G(MF, LD, C0_, C1_, X, S_, i) MF; LD; SBAR(); C0_; C1_; SBAR(); X##0 = ex2(S_[i]); X##1 = ex2(S_[i + 1]); X##2 = ex2(S_[i + 2]); X##3 = ex2(S_[i + 3]); asm volatile("" : "+v"(X##0), "+v"(X##1), "+v"(X##2), "+v"(X##3)); SBAR();
#define TILE(TB, TN, V2, H0, H1, H2) { float a0, a1, a2, a3; \
    G(SB = mfma32(k1, q1B, Z),    k1 = ldsr16(c0p + (TN)), pB2[2] = pk(e0, e1), pB2[3] = pk(e2, e3), a, SA, 0) \
    G(OB = mfma32(v2, pB2, OB),   V2,                      pA1[0] = pk(a0, a1), pA1[1] = pk(a2, a3), e, SA, 4) \
    G(SB = mfma32k8(k2, q2B, SB), k2 = ldsr8(c1p + (TN)),  pA1[2] = pk(e0, e1), pA1[3] = pk(e2, e3), a, SA, 8) \
    G(OA = mfma32(v1, pA1, OA),   H0,                      pA2[0] = pk(a0, a1), pA2[1] = pk(a2, a3), e, SA, 12) \
    G(SA = mfma32(k1, q1A, Z),    H1,                      pA2[2] = pk(e0, e1), pA2[3] = pk(e2, e3), a, SB, 0) \
    G(OA = mfma32(v2, pA2, OA),   H2,                      pB1[0] = pk(a0, a1), pB1[1] = pk(a2, a3), e, SB, 4) \
    G(SA = mfma32k8(k2, q2A, SA), (void)0,                 pB1[2] = pk(e0, e1), pB1[3] = pk(e2, e3), a, SB, 8) \
    G(OB = mfma32(v1, pB1, OB),   v1 = ldsr16(c2p + (TN)), pB2[0] = pk(a0, a1), pB2[1] = pk(a2, a3), e, SB, 12) }
#define NOP (void)0
#define TL(sl, t, H0, H1, H2) TILE((sl) * STEP_B + (t) * TILE_B, (sl) * STEP_B + ((t) + 1) * TILE_B, v2 = ldsr16(c2p + (sl) * STEP_B + (t) * TILE_B + 2 * VB), H0, H1, H2)
#define TL6(sl, H0, H1) TL(sl, 6, H0, H1, v2n = ldsr16(c2p + (sl) * STEP_B + 7 * TILE_B + 2 * VB))
#define TL7(sl, sn, H0, H1, H2) TILE((sl) * STEP_B + 7 * TILE_B, (sn) * STEP_B, v2 = v2n, H0, H1, H2)
  unsigned woff[5];
#pragma unroll
  for (int j = 0; j < 5; j++) { const int c = j * 64 + lane, rho = (c * 6554) >> 16, cc = c - rho * 10; const int k = (rho & ~12) | ((rho & 4) << 1) | ((rho & 8) >> 1); woff[j] = k * 1280 + cc * 16 + (4 - j) * 1024; }
#define WOG(wo, gr, j, dst) do { if ((j) == 4) WOP(wo, gr, 4, dst); else { if ((j) == 0) dmag_open(lds0 + (dst)); dmag<(j)>((wo) + C0 + (gr) * 32 * 640 - 2048, woff[j]); } } while (0)
#define WOP(wo, gr, j, dst) dma16s((wo) + C0 + (gr) * 32 * 640 - (4 - (j)) * 512, woff[j], lds0 + (dst) + (j) * 1024)
#define WA(gr, j) WOG(woA, gr, j, L_WO + (gr) * 5120)
  const u16* wxs = w < 4 ? woA + (16 + w3) * 32 * 640 : woB + (w < 7 ? 17 + w3 : 19) * 32 * 640;
  const int wxd = w < 4 ? L_WO + (16 + w3) * 5120 : 145408 + (w < 7 ? w3 : 2) * 5120;
#define WX(j) WOG(wxs, 0, j, wxd)
#define PRIO(n) __builtin_amdgcn_s_setprio(n)
  const unsigned fo = (rt * 8 + hq) * OT_B + lane * 16;
#define FILL(j) do { const int n_ = w * 3 + ((j) >> 1); ST16(ONES, (((j) & 1) ? WS_OB : WS_OA) + fo + (n_ < 20 ? n_ : 19) * 1024); } while (0)
#define CLR_VM() __builtin_amdgcn_s_waitcnt(0x0F70)
  const bool red = !role && w == 4;
  f32x4 part[16]; u64 fp0, fp1;
  auto red_load = [&]() {
    asm volatile("" : "+v"(fp0), "+v"(fp1));
    if (!__all((fp0 == tag) & (fp1 == tag))) wait_flags<128>(ws, WS_FPART + hq * 1024, tag, lane);
    asm volatile("" ::: "memory");
#pragma unroll
    for (int j = 0; j < 16; j++) part[j] = __builtin_bit_cast(f32x4, LD16(WS_PART + (hq * 16 + j) * PART_B + (rt * 60 + (lane < 60 ? lane : 59)) * 16));
  };
  auto red_store = [&]() {
    f32x4 a = ((part[0] + part[1]) + (part[2] + part[3])) + ((part[4] + part[5]) + (part[6] + part[7]));
    a += ((part[8] + part[9]) + (part[10] + part[11])) + ((part[12] + part[13]) + (part[14] + part[15]));
    const int hd = rt >> 2, u = (rt & 3) * 60 + lane, hi = u >= 160, fq = hi ? (u - 160) >> 3 : u >> 4, di = hi ? u & 7 : u & 15;
    if (lane < 60) ST8(((u32x2){pk(a[0], a[1]), pk(a[2], a[3])}), WS_KVR + hq * KVR_B + (((hd >> 1) * 2 + hi) * 5 + (fq >> 1)) * 512 + ((hd & 1) * 16 + di) * 16 + (fq & 1) * 8);
  };
  u32x4 ka[10]; u64 fkr; float gta;
  auto oa_load = [&]() {
    asm volatile("" : "+v"(fkr));
    if (!__all(fkr == tag)) wait_flags<16>(ws, WS_FKVR + hq * 128, tag, lane);
    asm volatile("" ::: "memory");
    const unsigned kb = (((r >> 2) & 1) == h) ? WS_KVR + hq * KVR_B + w2 * 5120 + (h * 16 + (r & 3) + 4 * (r >> 3)) * 16 : 0x7FFF0000u;
#pragma unroll
    for (int j = 0; j < 10; j++) ka[j] = LD16(kb + j * 512);
    gta = __uint_as_float(__builtin_amdgcn_raw_buffer_load_b32(wsr, WS_G + (rt * 128 + w3 * 32 + r) * 4, 0, 16));
  };
  u32x4 ja[2][3], jb[2]; f32x4 jc[3];
  const LAS char* jab = lp + L_PHK + ((w >> 1) * 128 + g * 8 + q4) * 80 + p4 * 8;
  const LAS char* jbb = lp + L_VIM + ((w >> 1) * 128 + g * 8 + q4) * 40 + (w & 1) * 32 + p4 * 8;
  auto kv_rd = [&](int k0) {
#pragma unroll
    for (int ks = 0; ks < 2; ks++) { jb[ks] = trd2(jbb + (k0 + ks) * 1280, jbb + (k0 + ks) * 1280 + 160);
#pragma unroll
      for (int ft = 0; ft < 3; ft++) ja[ks][ft] = trd2(jab + (k0 + ks) * 2560 + ft * 32, jab + (k0 + ks) * 2560 + 320 + ft * 32); } };
  auto kv_mm = [&](bool first) {
#pragma unroll
    for (int ks = 0; ks < 2; ks++) { if ((w & 1) && l15 == 4) jb[ks] = (u32x4){0x3F803F80u, 0x3F803F80u, 0x3F803F80u, 0x3F803F80u};
#pragma unroll
      for (int ft = 0; ft < 3; ft++) jc[ft] = mfma16(ja[ks][ft], jb[ks], (first && !ks) ? (f32x4){0.f, 0.f, 0.f, 0.f} : jc[ft]); } };
  auto kv_st = [&]() { const int dt = w & 1; const unsigned pb = WS_PART + (hq * 16 + rt) * PART_B + (w >> 1) * 3840 + (dt ? 2560 + g * 128 : g * 256) + l15 * 16;
#pragma unroll
    for (int ft = 0; ft < 3; ft++) if ((!dt || l15 < 8) && ft * 4 + g < 10) ST16(__builtin_bit_cast(u32x4, jc[ft]), pb + ft * (dt ? 512 : 1024)); };
  f32x16 oa0, oa1; u32x4 z0, z1, z2;
  LAS char* otb = lp + L_WO + (8 + w) * 5120;
  auto oa_mm = [&](int s) { oa0 = mfma32(ka[s], ph[s], s ? oa0 : Z); oa1 = mfma32(ka[5 + s], ph[s], s ? oa1 : Z); };
  auto oa_norm = [&]() { const float inv = __builtin_amdgcn_rcpf(oa1[4] + 1e-6f) * gta;
#pragma unroll
    for (int t = 0; t < 5; t++) { const int k0 = h * 20 + 4 * t;
      const u32x2 v = t < 4 ? (u32x2){nz(pk(oa0[(4 * t) & 15] * inv, oa0[(4 * t + 1) & 15] * inv)), nz(pk(oa0[(4 * t + 2) & 15] * inv, oa0[(4 * t + 3) & 15] * inv))} : (u32x2){nz(pk(oa1[0] * inv, oa1[1] * inv)), nz(pk(oa1[2] * inv, oa1[3] * inv))};
      *(LAS u32x2*)(otb + (r >> 4) * 1280 + (k0 >> 3) * 256 + (k0 & 4) * 2 + l15 * 16) = v; }
    z0 = ldsr16(otb + lane * 16); z1 = ldsr16(otb + 1280 + lane * 16); z2 = ldsr16(otb + (r >> 4) * 1280 + 1024 + l15 * 16); };
  auto oa_st = [&]() { const unsigned oa = WS_OA + (rt * 8 + w3 * 2) * OT_B + (hq * 2 + w2) * 1280;
    ST16(z0, oa + lane * 16); ST16(z1, oa + OT_B + lane * 16); if (!h) ST16(z2, oa + (r >> 4) * OT_B + 1024 + l15 * 16); };
  unsigned cpk;
  PRIO(3);
  TL(0, 0, if (!role) kv_rd(0); else FILL(0), NOP, NOP)
  TL(0, 1, if (!role) { kv_mm(true); kv_rd(2); ARRIVE_NW(0); } else FILL(1), NOP, NOP)
  TL(0, 2, if (!role) kv_mm(false); else FILL(2), NOP, if (!role) kv_st())
  TL(0, 3, NOP, NOP, cpk = __hip_atomic_load(cnt, __ATOMIC_RELAXED, __HIP_MEMORY_SCOPE_WORKGROUP))
  STAMP(14);
  if ((unsigned)__builtin_amdgcn_readfirstlane(cpk) < 8u) AWAIT(0, 8);
  asm volatile("" ::: "memory");
  TL(0, 4, NOP, NOP, if (!role) { WAIT_VM(0); set_flag(ws, WS_FPART + ((hq * 16 + rt) * 8 + w) * 8, tag, lane); } else FILL(3))
  PRIO(2);
  TL(0, 5, KX(2, 2), NOP, KW(2, 2, 0)) TL6(0, KW(2, 2, 1), KW(2, 2, 2))
  TL7(0, 1, KW(2, 2, 3), NOP, KW(2, 2, 4))
  STAMP(15);
  TL(1, 0, if (role) FILL(4), NOP, if (red) { fp0 = __hip_atomic_load((gu64*)(ws + WS_FPART) + hq * 128 + lane, RLX_AGENT); fp1 = __hip_atomic_load((gu64*)(ws + WS_FPART) + hq * 128 + 64 + lane, RLX_AGENT); })
  TL(1, 1, if (w < 4) { const int row = (w & 1) * 32 + (lane >> 1);
                      dma4s((w & 2 ? woB : woA) + C0 + ((rt & 7) * 2 + role) * 40 * 640, (row < 40 ? row : 39) * 1280 + (lane & 1) * 156, lds0 + 157696); }, NOP, if (role) FILL(5))
  TL(1, 2, NOP, NOP, if (red) red_load())
  PRIO(1);
  TL(1, 3, NOP, NOP, NOP)
  TL(1, 4, NOP, CLR_VM(), if (red) red_store())
  TL(1, 5, NOP, NOP, NOP)
  PRIO(0);
  TL6(1, if (red) { WAIT_VM(0); set_flag(ws, WS_FKVR + (hq * 16 + rt) * 8, tag, lane); }, NOP)
  if (red) WAIT_BAR(1); else WAIT_BAR(0);
  STAMP(12);
  PRIO(3);
  if (role) set_flag(ws, WS_FOA + ((rt * 8 + hq) * 8 + w) * 8, tag, lane);
  u64 foa;
  TL7(1, 2, KX(3, 0), KW(3, 0, 0), KW(3, 0, 1))
  TL(2, 0, KW(3, 0, 2), KW(3, 0, 3), KW(3, 0, 4)) TL(2, 1, WA(w, 0), WA(w, 1), WA(w, 2))
  TL(2, 2, WA(w, 3), WA(w, 4), (foa = __hip_atomic_load((gu64*)(ws + WS_FOA) + rt * 64 + lane, RLX_AGENT), fkr = __hip_atomic_load((gu64*)(ws + WS_FKVR) + hq * 16 + l15, RLX_AGENT)))
  PRIO(2);
  TL(2, 3, NOP, NOP, NOP)
  auto chk = [&]() { asm volatile("" : "+v"(foa), "+v"(fkr));
    if (!__all(foa == tag)) wait_flags<64>(ws, WS_FOA + rt * 512, tag, lane);
    asm volatile("" ::: "memory"); if (role) oa_load(); };
  TL(2, 4, chk(), NOP, NOP)
  PRIO(1);
  TL(2, 5, NOP, NOP, if (role) oa_mm(0))
  PRIO(0);
  TL6(2, if (role) oa_mm(1), if (role) oa_mm(2))
  BAR_L();
  STAMP(13);
  PRIO(3);
  TL7(2, 0, if (role) oa_mm(3), { if (role) oa_mm(4); CLR_VM(); }, WX(0))
  TL(0, 0, if (role) oa_norm(), WX(1), WX(2))
  TL(0, 1, if (role) oa_st(), WX(3), WX(4))
  TL(0, 2, WA(8 + w, 0), WA(8 + w, 1), WA(8 + w, 2))
  u32x4 wbr[11];
  const char* wbs = (const char*)(woB + C0 + w * 32 * 640) - 4096;
#define WB2(j) do { wbr[j] = *(const GAS u32x4*)(wbs + woff[j] + (j) * 1024); wbr[5 + (j)] = *(const GAS u32x4*)(wbs + 8 * 32 * 1280 + woff[j] + (j) * 1024); } while (0)
  TL(0, 3, WA(8 + w, 3), WA(8 + w, 4), WB2(0))
  PRIO(2);
  TL(0, 4, WB2(1), WB2(2), WB2(3))
  const int w5 = w < 5 ? w : 4;
  const unsigned wo16 = (w5 == 0 ? woff[0] : w5 == 1 ? woff[1] : w5 == 2 ? woff[2] : w5 == 3 ? woff[3] : woff[4]) + w5 * 1024;
  TL(0, 5, WB2(4), NOP, wbr[10] = *(const GAS u32x4*)((const char*)(woB + C0 + 16 * 32 * 640) - 4096 + wo16))
  PRIO(1);
  ARRIVE_NW(1);
  const int tp = w & 1, kq = w >> 1;
  const unsigned ofr = (rt * 8 + role * 4 + tp * 2) * OT_B + kq * 5120 + lane * 16;
  u32x4 of[2][5]; float gtb;
#define OFL(u) do { _Pragma("unroll") for (int i = 0; i < 5; i++) of[u][i] = LD16(WS_OA + ofr + (u) * OT_B + i * 1024); } while (0)
  TL6(0, OFL(0), gtb = __uint_as_float(__builtin_amdgcn_raw_buffer_load_b32(wsr, WS_G + (rt * 128 + w3 * 32 + w2 * 16 + l15) * 4, 0, 16)))
  PRIO(0);
  TL7(0, 0, OFL(1), NOP, NOP)
  pB2[2] = pk(e0, e1); pB2[3] = pk(e2, e3);
  OB = mfma32(v2, pB2, OB);
#undef TL
#undef TL6
#undef TL7
#undef TILE
#undef G
  STAMP(7);
  { LAS f32x4* xb = (LAS f32x4*)(lp + (w3 >> 1) * CH_B) + (w3 & 1) * (64 * 6);
#pragma unroll
    for (int i = 0; i < 12; i++) { auto rr = __builtin_amdgcn_permlane16_swap(__float_as_uint(OA[i]), __float_as_uint(OB[i]), false, false); OA[i] = __uint_as_float(rr[0]); OB[i] = __uint_as_float(rr[1]); }
    const f32x16 snd = w2 ? OA : OB;
    AWAIT(1, 8);
#pragma unroll
    for (int c = 0; c < 3; c++) xb[(w2 * 3 + c) * 64 + lane] = (f32x4){snd[4 * c], snd[4 * c + 1], snd[4 * c + 2], snd[4 * c + 3]};
    asm volatile("s_waitcnt vmcnt(22) lgkmcnt(0)\n\ts_barrier" ::: "memory");
    f32x16 O = w2 ? OB : OA;
#pragma unroll
    for (int c = 0; c < 3; c++) { const f32x4 t = xb[((w2 ^ 1) * 3 + c) * 64 + lane]; O[4 * c] += t[0]; O[4 * c + 1] += t[1]; O[4 * c + 2] += t[2]; O[4 * c + 3] += t[3]; }
    float l = from_upper(O[8]);
    float amax = 0.f;
#pragma unroll
    for (int i = 0; i < 12; i++) amax += fabsf(O[i]);
    const bool good = (l >= 0x1p-30f) && (l <= 0x1p100f) && (amax <= 0x1p126f);
    if (__builtin_expect(!__all(good), 0)) {
      u32x4 q1; u32x2 q2;
#pragma unroll
      for (int i = 0; i < 4; i++) { auto rr = __builtin_amdgcn_permlane16_swap(q1A[i], q1B[i], false, false); q1[i] = w2 ? rr[1] : rr[0]; }
#pragma unroll
      for (int i = 0; i < 2; i++) { auto rr = __builtin_amdgcn_permlane16_swap(q2A[i], q2B[i], false, false); q2[i] = h ? 0u : (w2 ? rr[1] : rr[0]); }
      float m = -INFINITY;
#pragma unroll 1
      for (int t = 0; t < 64; t++) {
        f32x16 s = mfma32(LD16(WS_KV + t * TILE_B + ok1), q1, Z); s = mfma32k8(LD8(WS_KV + t * TILE_B + ok2), q2, s);
#pragma unroll
        for (int i = 0; i < 16; i++) m = fmaxf(m, s[i]);
      }
      m = half_swap_max(m);
      O = Z;
#pragma unroll 1
      for (int t = 0; t < 64; t++) {
        f32x16 s = mfma32(LD16(WS_KV + t * TILE_B + ok1), q1, Z); s = mfma32k8(LD8(WS_KV + t * TILE_B + ok2), q2, s);
#pragma unroll
        for (int i = 0; i < 16; i++) s[i] = ex2(s[i] - m);
        O = mfma32(LD16(WS_KV + t * TILE_B + ov), (u32x4){pk(s[0], s[1]), pk(s[2], s[3]), pk(s[4], s[5]), pk(s[6], s[7])}, O);
        O = mfma32(LD16(WS_KV + t * TILE_B + ov + 2 * VB), (u32x4){pk(s[8], s[9]), pk(s[10], s[11]), pk(s[12], s[13]), pk(s[14], s[15])}, O);
      }
      l = from_upper(O[8]);
    }
    const float inv = __builtin_amdgcn_rcpf(l) * (1.0f - gtb);
    LAS char* tb = lp + w2 * CH_B + 12288 + w3 * 1536;
#pragma unroll
    for (int c = 0; c < 3; c++) { const int k0 = (g & 1) * 20 + 8 * c + 4 * h;
      *(LAS u32x2*)(tb + ((c == 2 && h) ? 1280 + (g & 1) * 8 : (k0 >> 3) * 256 + (k0 & 4) * 2) + l15 * 16) = (u32x2){nz(pk(O[4 * c] * inv, O[4 * c + 1] * inv)), nz(pk(O[4 * c + 2] * inv, O[4 * c + 3] * inv))}; }
    const unsigned ob = WS_OB + (rt * 8 + w3 * 2 + w2) * OT_B + hp * 1280 + lane * 16;
    const u32x4 z0 = ldsr16(tb + lane * 16), z1 = ldsr16(tb + 1024 + l15 * 16);
    ST16(z0, ob); ST16(z1, lane < 16 ? ob + 1024 : 0x7FFF0000u);
  }
  STAMP(8);
  auto of_missing = [&]() { u32x4 mm = of[0][0];
#pragma unroll
    for (int j = 1; j < 10; j++) mm = __builtin_elementwise_max(mm, of[j / 5][j % 5]);
    return missing(mm); };
  u32x4 wf[5][5];
#pragma unroll
  for (int i = 0; i < 5; i++) { const LAS char* wb = lp + L_WO + (kq * 5 + i) * 5120 + wlane;
#pragma unroll
    for (int t = 0; t < 5; t++) wf[i][t] = trd2(wb + t * 8, wb + 1280 + t * 8); }
  SBAR();
  if (__builtin_expect(of_missing(), 0)) {
#pragma unroll 1
    for (unsigned spin = 0; spin < SPIN_CAP; ++spin) {
      asm volatile("" ::: "memory");
      OFL(0); OFL(1);
      if (!of_missing()) break;
      __builtin_amdgcn_s_sleep(1);
    }
  }
  f32x4 c[2][5];
#pragma unroll
  for (int i = 0; i < 5; i++)
#pragma unroll
    for (int t = 0; t < 5; t++) c[0][t] = mfma16(wf[i][t], of[0][i], i ? c[0][t] : (f32x4){0.f, 0.f, 0.f, 0.f});
  BAR_L();
  SBAR();
  { LAS char* d = lp + L_WO + w * 5120 + lane * 16;
    *(LAS u32x4*)(lp + L_WO + 16 * 5120 + w5 * 1024 + lane * 16) = wbr[10];
#pragma unroll
    for (int i = 0; i < 5; i++) {
      *(LAS u32x4*)(d + i * 1024) = wbr[i]; *(LAS u32x4*)(d + 40960 + i * 1024) = wbr[5 + i]; of[0][i] = LD16(WS_OB + ofr + i * 1024); SBAR();
#pragma unroll
      for (int t = 0; t < 5; t++) c[1][t] = mfma16(wf[i][t], of[1][i], i ? c[1][t] : (f32x4){0.f, 0.f, 0.f, 0.f});
      SBAR();
    } }
  STAMP(9);
  BAR_L();
#pragma unroll
  for (int i = 0; i < 5; i++) { const int gr = kq * 5 + i; const LAS char* wb = lp + (gr < 17 ? L_WO + gr * 5120 : 145408 + (gr - 17) * 5120) + wlane;
#pragma unroll
    for (int t = 0; t < 5; t++) wf[i][t] = trd2(wb + t * 8, wb + 1280 + t * 8);
    SBAR(); of[1][i] = LD16(WS_OB + ofr + OT_B + i * 1024); SBAR(); }
  ARRIVE_NW(2);
  STAMP(10);
  LAS char* xw = lp + kq * 21504 + (tp * 32 + l15) * 336 + g * 80;
#pragma unroll
  for (int u = 0; u < 2; u++) {
#pragma unroll
    for (int i = 0; i < 5; i++) {
      if (__builtin_expect(missing(of[u][i]), 0)) {
#pragma unroll 1
        for (unsigned spin = 0; spin < SPIN_CAP; ++spin) {
          asm volatile("" ::: "memory");
#pragma unroll
          for (int j = u * 5 + i; j < 10; j++) of[j / 5][j % 5] = LD16(WS_OB + ofr + (j / 5) * OT_B + (j % 5) * 1024);
          if (!missing(of[u][i])) break;
          __builtin_amdgcn_s_sleep(1);
        }
      }
#pragma unroll
      for (int t = 0; t < 5; t++) c[u][t] = mfma16(wf[i][t], of[u][i], c[u][t]);
      if (u == 0 && i == 3) cpk = __hip_atomic_load(cnt + 2, __ATOMIC_RELAXED, __HIP_MEMORY_SCOPE_WORKGROUP);
      if (u == 1) { if (i == 0) { if (__builtin_expect((unsigned)__builtin_amdgcn_readfirstlane(cpk) < 8u, 0)) AWAIT(2, 8); asm volatile("" ::: "memory"); }
        *(LAS f32x4*)(xw + i * 16) = c[0][i]; }
      SBAR();
    }
  }
#pragma unroll
  for (int t = 0; t < 5; t++) *(LAS f32x4*)(xw + 16 * 336 + t * 16) = c[1][t];
  BAR_L();
  { const __amdgpu_buffer_rsrc_t orr = __builtin_amdgcn_make_buffer_rsrc(out, 0, 2048 * DM * 2, 0x00020000);
#pragma unroll
    for (int j = 0; j < 3; j++) { const int n = tid + 512 * j, nn = n < 1280 ? n : 0, row = (nn * 3277) >> 16, cq = nn - row * 20;
      const LAS f32x4* p = (const LAS f32x4*)(lp + row * 336 + cq * 16);
      const f32x4 z = (p[0] + p[1344]) + (p[2688] + p[4032]);
      __builtin_amdgcn_raw_buffer_store_b64((u32x2){pk(z[0], z[1]), pk(z[2], z[3])}, orr, n < 1280 ? ((rt * 128 + role * 64 + row) * DM + C0 + cq * 4) * 2 : 0x7FFF0000u, 0, 0); }
  }
  STAMP(11);
}
}

extern "C" void kernel_launch(void* const* d_in, const int* in_sizes, int n_in, void* d_out, int out_size, void* d_ws, size_t ws_size, hipStream_t stream) {
  using namespace gm;
  fused<<<256, 512, 0, stream>>>((const u16*)d_in[0], (const u16*)d_in[1], (const u16*)d_in[2], (const u16*)d_in[3], (const u16*)d_in[5], (const u16*)d_in[6],
                                 (const u16*)d_in[7], (const u16*)d_in[8], (const u16*)d_in[9], (u16*)d_out, (char*)d_ws);
}
```

```cpp
#include <hip/hip_runtime.h>
#include <hip/hip_bf16.h>
#include <stdint.h>

namespace gm {
typedef float f32x16 __attribute__((ext_vector_type(16)));
typedef float f32x4 __attribute__((ext_vector_type(4)));
typedef float f32x2 __attribute__((ext_vector_type(2)));
typedef short s16x4 __attribute__((ext_vector_type(4)));
typedef __bf16 b16x2 __attribute__((ext_vector_type(2)));
typedef __bf16 b16x8 __attribute__((ext_vector_type(8)));
typedef unsigned u32x4 __attribute__((ext_vector_type(4)));
typedef unsigned u32x2 __attribute__((ext_vector_type(2)));
typedef unsigned long long u64;
typedef unsigned short u16;
#define DI __device__ __forceinline__
#define LAS __attribute__((address_space(3)))
#define GAS __attribute__((address_space(1)))
typedef GAS u64 gu64;

constexpr int DM = 640;
constexpr unsigned WS_EPOCH = 0, WS_FKV = 2048, WS_FKVR = 4096, WS_FQ = 8192, WS_FPART = 16384, WS_FOA = 24576, WS_G = 57344;
constexpr unsigned WS_KVR = 8912896, KVR_B = 10240, WS_KV = 131072, WS_QX = 327680, QX_B = 10240, WS_PART = 1703936, PART_B = 15360;
constexpr unsigned WS_OA = 3670016, WS_OB = 6291456, WS_END = 8994816, OT_B = 20480;
constexpr int VB = 352, TILE_B = 1280 + 4 * VB, CH_B = TILE_B * 8, STEP_B = 2 * CH_B;
constexpr int STAGE_B = 36864, ST_W1 = 10240, ST_X = 20480;
constexpr int L_BUF = 43008, L_P0W = 0, L_P0X0 = 25600, L_P0X1 = 153600, L_RED = 158720, L_RED1 = 40960;
constexpr int L_HO = 86016, L_QL = 96256, L_VIM = 96256, L_PHK = 116736, L_WPHI = 158592, L_ZERO = 161792, L_CNT = 163712, L_TOTAL = 163840;
constexpr int L_WO = 43008;
constexpr float LOG2E = 1.4426950408889634f;
constexpr float QSCALE = 0.22360679774997896f * LOG2E;
constexpr unsigned SPIN_CAP = 20000, LSPIN_CAP = 400000;

DI unsigned pk(float a, float b) { f32x2 v = {a, b}; b16x2 r = __builtin_convertvector(v, b16x2); return __builtin_bit_cast(unsigned, r); }
DI f32x16 mfma32(u32x4 a, u32x4 b, f32x16 c) { return __builtin_amdgcn_mfma_f32_32x32x16_bf16(__builtin_bit_cast(b16x8, a), __builtin_bit_cast(b16x8, b), c, 0, 0, 0); }
DI f32x16 mfma32k8(u32x2 a, u32x2 b, f32x16 c) { return __builtin_amdgcn_mfma_f32_32x32x8bf16_1k(__builtin_bit_cast(s16x4, a), __builtin_bit_cast(s16x4, b), c, 0, 0, 0); }
DI f32x4 mfma16(u32x4 a, u32x4 b, f32x4 c) { return __builtin_amdgcn_mfma_f32_16x16x32_bf16(__builtin_bit_cast(b16x8, a), __builtin_bit_cast(b16x8, b), c, 0, 0, 0); }
DI u32x2 trd(const LAS char* p) { return __builtin_bit_cast(u32x2, __builtin_amdgcn_ds_read_tr16_b64_v4i16((LAS s16x4*)p)); }
DI u32x4 trd2(const LAS char* p0, const LAS char* p1) { u32x2 a = trd(p0), b = trd(p1); return (u32x4){a.x, a.y, b.x, b.y}; }
DI u32x4 ldsr16(const LAS char* p) { return *(const LAS u32x4*)p; }
DI u32x2 ldsr8(const LAS char* p) { return *(const LAS u32x2*)p; }
DI float ex2(float x) { return __builtin_amdgcn_exp2f(x); }
DI void dma16s(const void* base, unsigned voff, unsigned lds_base) {
  asm volatile("s_mov_b32 m0, %2\n\ts_nop 0\n\tglobal_load_lds_dwordx4 %0, %1" :: "v"(voff), "s"(base), "s"(lds_base) : "memory"); }
DI void dma16s_sc1(const void* base, unsigned voff, unsigned lds_base) {
  asm volatile("s_mov_b32 m0, %2\n\ts_nop 0\n\tglobal_load_lds_dwordx4 %0, %1 sc1" :: "v"(voff), "s"(base), "s"(lds_base) : "memory"); }
DI void dmag_open(unsigned lds_base) { asm volatile("s_mov_b32 m0, %0\n\ts_nop 0" :: "s"(lds_base) : "memory"); }
template <int J> DI void dmag(const void* base, unsigned voff) { asm volatile("global_load_lds_dwordx4 %0, %1 offset:%2" :: "v"(voff), "s"(base), "i"((J & 3) * 1024) : "memory"); }
template <int J> DI void dmag_sc1(const void* base, unsigned voff) { asm volatile("global_load_lds_dwordx4 %0, %1 offset:%2 sc1" :: "v"(voff), "s"(base), "i"((J & 3) * 1024) : "memory"); }
DI void dma4s(const void* base, unsigned voff, unsigned lds_base) {
  asm volatile("s_mov_b32 m0, %2\n\ts_nop 0\n\tglobal_load_lds_dword %0, %1" :: "v"(voff), "s"(base), "s"(lds_base) : "memory"); }
DI u32x4 ld16_async(const void* p) { u32x4 r; asm volatile("global_load_dwordx4 %0, %1, off" : "=v"(r) : "v"(p) : "memory"); return r; }
DI u32x2 ld8_sc1_async(const void* p) { u32x2 r; asm volatile("global_load_dwordx2 %0, %1, off sc1" : "=v"(r) : "v"(p) : "memory"); return r; }
#define BAR_L() asm volatile("s_waitcnt lgkmcnt(0)\n\ts_barrier" ::: "memory")
#define WAIT_BAR(N) asm volatile("s_waitcnt vmcnt(" #N ") lgkmcnt(0)\n\ts_barrier" ::: "memory")
#define WAIT_VM(N) asm volatile("s_waitcnt vmcnt(" #N ")" ::: "memory")
#define SBAR() __builtin_amdgcn_sched_barrier(0)
#define RLX_AGENT __ATOMIC_RELAXED, __HIP_MEMORY_SCOPE_AGENT
DI float half_swap_max(float m) { auto rr = __builtin_amdgcn_permlane32_swap(__float_as_uint(m), __float_as_uint(m), false, false); return fmaxf(__uint_as_float(rr[0]), __uint_as_float(rr[1])); }
DI float from_upper(float v) { auto rr = __builtin_amdgcn_permlane32_swap(__float_as_uint(v), __float_as_uint(v), false, false); return __uint_as_float(rr[1]); }

template <int NF>
DI bool flags_set(const char* ws, unsigned off, u64 tag, int lane) {
  gu64* f = (gu64*)(ws + off);
  bool ok = __hip_atomic_load(f + (NF < 64 ? (lane & (NF - 1)) : lane), RLX_AGENT) == tag;
  if (NF >= 128) ok &= __hip_atomic_load(f + 64 + lane, RLX_AGENT) == tag;
  if (NF >= 256) { ok &= __hip_atomic_load(f + 128 + lane, RLX_AGENT) == tag; ok &= __hip_atomic_load(f + 192 + lane, RLX_AGENT) == tag; }
  return ok;
}
template <int NF>
DI void wait_flags(const char* ws, unsigned off, u64 tag, int lane) {
#pragma unroll 1
  for (unsigned spin = 0; spin < SPIN_CAP; ++spin) {
    if (__all(flags_set<NF>(ws, off, tag, lane))) break;
    __builtin_amdgcn_s_sleep(2);
  }
  asm volatile("" ::: "memory");
}
DI void set_flag(char* ws, unsigned off, u64 tag, int lane) { if (lane == 0) __hip_atomic_store((gu64*)(ws + off), tag, RLX_AGENT); }
DI unsigned nz(unsigned v) { return v < 0xFFFFFFFEu ? v : 0xFFFFFFFEu; }
DI unsigned umax3(unsigned a, unsigned b, unsigned c) { unsigned m = a > b ? a : b; return m > c ? m : c; }
DI bool missing(u32x4 v) { return __any(umax3(umax3(v.x, v.y, v.z), v.w, 0u) == 0xFFFFFFFFu); }
#define ONES ((u32x4){0xFFFFFFFFu, 0xFFFFFFFFu, 0xFFFFFFFFu, 0xFFFFFFFFu})
#define ST16(v, off) __builtin_amdgcn_raw_buffer_store_b128((v), wsr, (off), 0, 16)
#define ST8(v, off) __builtin_amdgcn_raw_buffer_store_b64((v), wsr, (off), 0, 16)
#define LD16(off) __builtin_amdgcn_raw_buffer_load_b128(wsr, (off), 0, 16)
#define LD8(off) __builtin_amdgcn_raw_buffer_load_b64(wsr, (off), 0, 16)

#define STAMP(i) do {} while (0)

template <int role>
DI void body(const u16* __restrict__ x, const u16* __restrict__ wqkv, const u16* __restrict__ wphi, const u16* __restrict__ woA,
             const u16* __restrict__ wq, const u16* __restrict__ wkv, const u16* __restrict__ woB,
             const u16* __restrict__ gatew, u16* __restrict__ out, char* ws, LAS char* lp) {
  const int tid = threadIdx.x, lane = tid & 63;
  const int w = __builtin_amdgcn_readfirstlane(tid >> 6) & 7;
  const int l15 = lane & 15, g = lane >> 4, q4 = l15 >> 2, p4 = lane & 3, r = lane & 31, h = lane >> 5;
  const int bx = blockIdx.x & 7, bi = blockIdx.x >> 3;
  const int rt = (bx & 1) * 8 + (bi & 7), hp = (bx >> 1) * 4 + ((bi >> 3) & 3), hq = hp >> 1;
  const int w3 = w & 3, w2 = w >> 2;
  const unsigned lds0 = (unsigned)(uintptr_t)lp;
  const __amdgpu_buffer_rsrc_t wsr = __builtin_amdgcn_make_buffer_rsrc(ws, 0, WS_END, 0x00020000);
  LAS unsigned* cnt = (LAS unsigned*)(lp + L_CNT);
#define ARRIVE_NW(c) do { asm volatile("" ::: "memory"); if (lane == 0) __hip_atomic_fetch_add(cnt + (c), 1u, __ATOMIC_RELAXED, __HIP_MEMORY_SCOPE_WORKGROUP); asm volatile("" ::: "memory"); } while (0)
#define PEEK(c) ((unsigned)__builtin_amdgcn_readfirstlane(__hip_atomic_load(cnt + (c), __ATOMIC_RELAXED, __HIP_MEMORY_SCOPE_WORKGROUP)))
#define AWAIT(c, n) do { _Pragma("unroll 1") for (unsigned sp_ = 0; sp_ < LSPIN_CAP; ++sp_) { if (PEEK(c) >= (unsigned)(n)) break; __builtin_amdgcn_s_sleep(1); } asm volatile("" ::: "memory"); } while (0)
  STAMP(0);

  const int T0 = rt * 128 + hq * 16;
  const int C0 = hq * 80;
  const int wlane = ((g >> 1) * 16 + (g & 1) * 4 + q4) * 160 + p4 * 40;
  u64 tag;
  unsigned o0[10];
#define KVP(s, slot, p) dma16s_sc1(ws + WS_KV + (s) * STEP_B + (p) * 1024, lane * 16, lds0 + (slot) * STEP_B + (p) * 1024)
#define KVL(s, slot) do { _Pragma("unroll") for (int j = 0; j < 11; j++) if (j < 10 || w3 < 2) KVP(s, slot, w3 + 4 * j); } while (0)
#define KW(s, slot, j) do { if ((j) == 4) KVP(s, slot, w * 5 + 4); else { if ((j) == 0) dmag_open(lds0 + (slot) * STEP_B + w * 5120); dmag_sc1<(j)>(ws + WS_KV + (s) * STEP_B + w * 5120, lane * 16); } } while (0)
#define KX(s, slot) do { if (w < 2) KVP(s, slot, 40 + w); } while (0)
  if (w2 == 0) {
    u32x4 gf0, gf1, gf2, gf3, gf4, wpc;
    u32x2 ep = ld8_sc1_async(ws + WS_EPOCH);
    const int lx = w3 < 3 ? L_P0X0 + w3 * 5120 : L_P0X1;
    { const u16* wsrc = wkv + w3 * 3200;
#pragma unroll
      for (int j = 0; j < 5; j++) { const int n = j * 64 + lane, row = (n * 3277) >> 16, cc = n - row * 20; dma16s(x + T0 * DM + w3 * 160, row * 1280 + cc * 16, lds0 + lx + j * 1024); }
#pragma unroll
      for (int j = 0; j < 6; j++) dma16s(wsrc + j * 512, lane * 16, lds0 + L_P0W + w3 * 6400 + j * 1024);
      if (lane < 16) dma16s(wsrc + 6 * 512, lane * 16, lds0 + L_P0W + w3 * 6400 + 6 * 1024);
      const u16* gp = gatew + w3 * 160 + g * 8;
      gf0 = ld16_async(gp); gf1 = ld16_async(gp + 32); gf2 = ld16_async(gp + 64); gf3 = ld16_async(gp + 96); gf4 = ld16_async(gp + 128);
      wpc = ld16_async(wphi + hq * 1600 + (tid < 200 ? tid : 199) * 8);
    }
    asm volatile("s_waitcnt vmcnt(0)" : "+v"(gf0), "+v"(gf1), "+v"(gf2), "+v"(gf3), "+v"(gf4), "+v"(wpc), "+v"(ep) :: "memory");
    tag = (((u64)(unsigned)__builtin_amdgcn_readfirstlane(ep.y) << 32) | (unsigned)__builtin_amdgcn_readfirstlane(ep.x)) + 1ull;
    STAMP(1);
    { const LAS char* wb = lp + L_P0W + (w3 * 160 + g * 8 + q4) * 40;
      const LAS char* xb = lp + lx + l15 * 320 + g * 16;
      const bool gl = l15 == 4;
      f32x4 c0 = {}, c1 = {};
#define P0STEP(s, GF) { const u32x4 b = ldsr16(xb + (s) * 64); c0 = mfma16(trd2(wb + p4 * 8 + (s) * 1280, wb + p4 * 8 + (s) * 1280 + 160), b, c0); \
        u32x4 a = trd2(wb + 32 + (s) * 1280, wb + 32 + (s) * 1280 + 160); if (gl) a = GF; c1 = mfma16(a, b, c1); }
      P0STEP(0, gf0) P0STEP(1, gf1) P0STEP(2, gf2) P0STEP(3, gf3) P0STEP(4, gf4)
#undef P0STEP
      *(LAS f32x4*)(lp + L_RED + tid * 16) = c0;
      if (lane < 32) *(LAS f32x4*)(lp + L_RED1 + (w3 * 32 + lane) * 16) = c1; }
    STAMP(2);
    f32x16 acc[5];
#pragma unroll
    for (int b = 0; b < 5; b++) acc[b] = (f32x16){};
    const int wm = g & 1, whd = p4;
    const int wo0 = wm * ST_W1 + (8 * h + 4 * wm + q4) * 160 + whd * 40, wo1 = wm * ST_W1 + (8 * h + 4 * (wm ^ 1) + q4) * 160 + whd * 40;
    const int xo = ST_X + (w3 * 32 + r) * 128 + ((h ^ ((r >> 1) & 7)) * 16);
    u32x4 fa[5], fb[5], xa, xb;
#define KSTEP(FN, XN, s, FC, XC) { XN = ldsr16(sb + (xo ^ ((s) * 32))); \
      _Pragma("unroll") for (int j = 0; j < 5; j++) { if (i > 0 || (s) > 0) acc[j] = mfma32(FC[j], XC, acc[j]); FN[j] = trd2(sb + wo0 + (s) * 2560 + j * 8, sb + wo1 + (s) * 2560 + j * 8); SBAR(); } }
#pragma unroll
    for (int i = 0; i < 10; i++) {
      BAR_L();
      if (i == 1 && role) {
        const unsigned qb = WS_QX + (rt * 8 + hq) * QX_B + w3 * 1280 + lane * 16;
        ST16(ONES, qb); ST16(ONES, qb + 5120); if (lane < 32) ST16(ONES, qb + 1024 + (lane >> 4) * (5120 - 256)); }
      if (i == 4) { if (role) { WAIT_VM(0); set_flag(ws, WS_FQ + ((rt * 8 + hq) * 4 + w3) * 8, tag, lane); }
                    if (tid < 200) *(LAS u32x4*)(lp + L_WPHI + tid * 16) = wpc; }
      const LAS char* sb = lp + L_BUF + ((i + 2) % 3) * STAGE_B;
      KSTEP(fa, xa, 0, fb, xb) KSTEP(fb, xb, 1, fa, xa) KSTEP(fa, xa, 2, fb, xb)
      { xb = ldsr16(sb + (xo ^ 96));
        acc[0] = mfma32(fa[0], xa, acc[0]); fb[0] = trd2(sb + wo0 + 7680, sb + wo1 + 7680); fb[1] = trd2(sb + wo0 + 7688, sb + wo1 + 7688); SBAR();
        acc[1] = mfma32(fa[1], xa, acc[1]); fb[2] = trd2(sb + wo0 + 7696, sb + wo1 + 7696); fb[3] = trd2(sb + wo0 + 7704, sb + wo1 + 7704); SBAR();
        acc[2] = mfma32(fa[2], xa, acc[2]); fb[4] = trd2(sb + wo0 + 7712, sb + wo1 + 7712); SBAR();
        acc[3] = mfma32(fa[3], xa, acc[3]); acc[4] = mfma32(fa[4], xa, acc[4]); SBAR(); }
    }
#pragma unroll
    for (int j = 0; j < 5; j++) acc[j] = mfma32(fb[j], xb, acc[j]);
#undef KSTEP
    STAMP(3);
#define PK2(t, i, sc) pk(acc[t][i] * (sc), acc[t][(i) + 1] * (sc))
#define ROW(i, sc) const u32x4 ra = {PK2(0, i, sc), PK2(0, (i) + 2, sc), PK2(1, i, sc), PK2(1, (i) + 2, sc)}, rb = {PK2(2, i, sc), PK2(2, (i) + 2, sc), PK2(3, i, sc), PK2(3, (i) + 2, sc)}; const u32x2 rc = {PK2(4, i, sc), PK2(4, (i) + 2, sc)}
    { ROW(4, 1.0f);
      *(LAS u32x4*)(lp + L_HO + tid * 16) = ra; *(LAS u32x4*)(lp + L_HO + 4096 + tid * 16) = rb; *(LAS u32x2*)(lp + L_HO + 8192 + tid * 8) = rc; }
    BAR_L();
    const int qfo = (h * 4 + w3) * 1280;
    if (role) {
      { ROW(8, QSCALE); const unsigned qb = WS_QX + (rt * 8 + hq) * QX_B + qfo;
        ST16(((u32x4){nz(ra.x), nz(ra.y), nz(ra.z), nz(ra.w)}), qb + r * 16); ST16(((u32x4){nz(rb.x), nz(rb.y), nz(rb.z), nz(rb.w)}), qb + 512 + r * 16); ST8(((u32x2){nz(rc.x), nz(rc.y)}), qb + 1024 + r * 8); }
      { ROW(12, QSCALE); LAS char* d = lp + L_QL + qfo;
        *(LAS u32x4*)(d + r * 16) = ra; *(LAS u32x4*)(d + 512 + r * 16) = rb; *(LAS u32x2*)(d + 1024 + r * 8) = rc; }
    } else { LAS char* d = lp + L_VIM + (h * 128 + w3 * 32 + r) * 40;
#pragma unroll
      for (int t = 0; t < 5; t++) { *(LAS u32x2*)(d + t * 8) = (u32x2){PK2(t, 8, 1.0f), PK2(t, 10, 1.0f)}; *(LAS u32x2*)(d + 10240 + t * 8) = (u32x2){PK2(t, 12, 1.0f), PK2(t, 14, 1.0f)}; } }
#pragma unroll
    for (int t = 0; t < 5; t++) { o0[2 * t] = PK2(t, 0, 1.0f); o0[2 * t + 1] = PK2(t, 2, 1.0f); }
#undef ROW
#undef PK2
  } else {
    u32x2 ep = ld8_sc1_async(ws + WS_EPOCH);
    const bool second = w3 >> 1;
    const unsigned rs = (second && role) ? 1280u : 3840u;
    const char* wsrc = (second ? (role ? (const char*)(wq + C0) : (const char*)(wqkv + 1280 + C0)) : (const char*)(wqkv + (role ? 0 : 640) + C0)) + (w3 & 1) * 32 * rs;
    const char* xsrc = (const char*)(x + (rt * 128 + w3 * 32) * DM);
    int rot = (bi & 7) + 3 * ((bi >> 3) & 3); rot = rot >= 10 ? rot - 10 : rot;
    unsigned so[5], xoff[2];
#pragma unroll
    for (int j = 0; j < 5; j++) { const int c = j * 64 + lane, rho = (c * 6554) >> 16, cc = c - rho * 10; so[j] = (rho ^ (second ? 4 : 0)) * rs + cc * 16; }
#pragma unroll
    for (int j = 0; j < 2; j++) { const int row = j * 8 + (lane >> 3); xoff[j] = row * 1280 + (((lane & 7) ^ ((row >> 1) & 7)) * 16); }
    int bsel = 2, kc = rot;
    auto stage = [&]() {
      const unsigned lb = lds0 + L_BUF + bsel * STAGE_B;
      const char* bw = wsrc + kc * 64 * rs; const char* bxp = xsrc + kc * 128;
#pragma unroll
      for (int j = 0; j < 5; j++) { dma16s(bw, so[j], lb + (w3 * 5 + j) * 1024); if (j < 4) dma16s(bxp + (j >> 1) * 16 * 1280, xoff[j & 1], lb + ST_X + (w3 * 4 + j) * 1024); }
      bsel = bsel == 2 ? 0 : bsel + 1; kc = kc == 9 ? 0 : kc + 1;
    };
    stage(); stage();
    asm volatile("s_waitcnt vmcnt(9)" : "+v"(ep) :: "memory");
    tag = (((u64)(unsigned)__builtin_amdgcn_readfirstlane(ep.y) << 32) | (unsigned)__builtin_amdgcn_readfirstlane(ep.x)) + 1ull;
    STAMP(1); STAMP(2);
#pragma unroll 1
    for (int i = 0; i < 8; i++) {
      WAIT_BAR(9);
      stage();
      if (i == 0 && w3 == 0) {
        const unsigned tb = WS_KV + (T0 >> 5) * TILE_B; const int s16 = hq & 1;
        if (!role) {
          const LAS f32x4* rp = (const LAS f32x4*)(lp + (h ? L_RED1 : L_RED)) + (h ? (g & 1) * 16 : g * 32) + l15;
          const int st = h ? 32 : 64, o2 = h ? 0 : 16;
          const f32x4 a = (rp[0] + rp[st]) + (rp[2 * st] + rp[3 * st]), b = (rp[o2] + rp[st + o2]) + (rp[2 * st + o2] + rp[3 * st + o2]);
          const u32x4 v = {pk(a[0], a[1]), pk(a[2], a[3]), pk(b[0], b[1]), pk(b[2], b[3])};
          if (lane < 32) ST16(v, tb + (g * 32 + s16 * 16 + l15) * 16);
          else if (lane < 48) ST8(((u32x2){v.x, v.y}), tb + 1024 + (s16 * 16 + l15) * 8);
          else { const float gv = 1.0f / (1.0f + __expf(-a[0])); __builtin_amdgcn_raw_buffer_store_b32(__float_as_uint(gv), wsr, WS_G + (T0 + l15) * 4, 0, 16); }
        } else if (lane < 44) {
          const int hh = lane >= 22, d = lane - hh * 22, dd = d < 20 ? d : 0, st = dd >= 16 ? 128 : 256;
          const LAS float* rp = (const LAS float*)(lp + (dd >= 16 ? L_RED1 : L_RED)) + (((dd & 15) >> 2) * 16 + 4 * hh) * 4 + (dd & 3);
          float e[8];
#pragma unroll
          for (int j = 0; j < 8; j++) { const int tk = (8 * (j >> 2) + (j & 3)) * 4; e[j] = (rp[tk] + rp[tk + st]) + (rp[tk + 2 * st] + rp[tk + 3 * st]); }
          u32x4 v = {pk(e[0], e[1]), pk(e[2], e[3]), pk(e[4], e[5]), pk(e[6], e[7])};
          if (d == 20) v = (u32x4){0x3F803F80u, 0x3F803F80u, 0x3F803F80u, 0x3F803F80u};
          if (d == 21) v = (u32x4){0u, 0u, 0u, 0u};
          ST16(v, tb + 1280 + (s16 * 2 + hh) * VB + d * 16);
        }
      }
      if (i == 2) { if (w3 == 0) set_flag(ws, WS_FKV + blockIdx.x * 8, tag, lane);
                    if (w3 >= 2) *(LAS u32x4*)(lp + L_ZERO + (tid - 384) * 16) = (u32x4){0u, 0u, 0u, 0u}; }
    }
    u64 fa, fb, fc, fd, fe;
    { gu64* f = (gu64*)(ws + WS_FKV) + lane;
      fa = __hip_atomic_load(f, RLX_AGENT); fb = __hip_atomic_load(f + 64, RLX_AGENT); fc = __hip_atomic_load(f + 128, RLX_AGENT); fd = __hip_atomic_load(f + 192, RLX_AGENT);
      fe = __hip_atomic_load((gu64*)(ws + WS_FQ) + (rt * 8 + hq) * 4 + p4, RLX_AGENT); }
    WAIT_BAR(14);
    asm volatile("" : "+v"(fa), "+v"(fb), "+v"(fc), "+v"(fd), "+v"(fe));
    const bool kvok = __all((fa == tag) & (fb == tag) & (fc == tag) & (fd == tag) & (fe == tag));
    asm volatile("" ::: "memory");
    BAR_L();
    if (!kvok) { wait_flags<256>(ws, WS_FKV, tag, lane); wait_flags<4>(ws, WS_FQ + (rt * 8 + hq) * 32, tag, lane); }
    KVL(0, 0);
    if (blockIdx.x == 0 && w3 == 0) set_flag(ws, WS_EPOCH, tag, lane);
    STAMP(3);
    BAR_L();
    const int ht = tid - 256;
    const u32x4 a = ldsr16(lp + L_HO + ht * 16), b = ldsr16(lp + L_HO + 4096 + ht * 16); const u32x2 c = ldsr8(lp + L_HO + 8192 + ht * 8);
    o0[0] = a.x; o0[1] = a.y; o0[2] = a.z; o0[3] = a.w; o0[4] = b.x; o0[5] = b.y; o0[6] = b.z; o0[7] = b.w; o0[8] = c.x; o0[9] = c.y;
  }
  STAMP(4);
  u32x4 ph[5];
  { const LAS char* pb = ((p4 & 1) == h) ? lp + L_WPHI + (2 * w2 + h) * 800 + q4 * 40 + (2 * (g & 1) + (p4 >> 1)) * 8 : lp + L_ZERO;
    const u32x4 b0 = {o0[0], o0[1], o0[2], o0[3]}, b1 = {o0[4], o0[5], o0[6], o0[7]}, b2 = {o0[8], o0[9], 0u, 0u};
    const u32x2 c0 = trd(pb + 640), c1 = trd(pb + 672);
    f32x16 P0 = {}, P1 = {};
    P0 = mfma32(trd2(pb, pb + 160), b0, P0); P1 = mfma32(trd2(pb + 32, pb + 192), b0, P1);
    P0 = mfma32(trd2(pb + 320, pb + 480), b1, P0); P1 = mfma32(trd2(pb + 352, pb + 512), b1, P1);
    P0 = mfma32((u32x4){c0.x, c0.y, 0u, 0u}, b2, P0); P1 = mfma32((u32x4){c1.x, c1.y, 0u, 0u}, b2, P1);
    KX(1, 1); KW(1, 1, 0); SBAR();
    f32x2 p[10];
#pragma unroll
    for (int f = 0; f < 10; f++) p[f] = f < 8 ? (f32x2){P0[(2 * f) & 15], P0[(2 * f + 1) & 15]} : (f32x2){P1[(2 * f) & 3], P1[(2 * f + 1) & 3]};
    float mx = fmaxf(p[0][0], p[0][1]), mn = fminf(p[0][0], p[0][1]);
#pragma unroll
    for (int f = 1; f < 10; f++) { mx = fmaxf(fmaxf(mx, p[f][0]), p[f][1]); mn = fminf(fminf(mn, p[f][0]), p[f][1]); }
    const f32x2 ma = {-mx * LOG2E, -mx * LOG2E}, mb = {mn * LOG2E, mn * LOG2E}, ca_ = {LOG2E, LOG2E}, cb_ = {-LOG2E, -LOG2E};
    f32x2 ea[10], eb[10], sa = {0.f, 0.f}, sb = {0.f, 0.f};
#pragma unroll
    for (int f = 0; f < 10; f++) { if (f % 2 == 0 && f < 8) { SBAR(); if (f == 0) KW(1, 1, 1); if (f == 2) KW(1, 1, 2); if (f == 4) KW(1, 1, 3); if (f == 6) KW(1, 1, 4); SBAR(); }
      const f32x2 xa = __builtin_elementwise_fma(p[f], ca_, ma), xb = __builtin_elementwise_fma(p[f], cb_, mb);
      ea[f] = (f32x2){ex2(xa[0]), ex2(xa[1])}; eb[f] = (f32x2){ex2(xb[0]), ex2(xb[1])}; sa += ea[f]; sb += eb[f]; }
    const float ia = __builtin_amdgcn_rcpf(sa[0] + sa[1]), ib = __builtin_amdgcn_rcpf(sb[0] + sb[1]);
    unsigned o[20];
#pragma unroll
    for (int f = 0; f < 10; f++) { const f32x2 ya = ea[f] * ia, yb = eb[f] * ib; o[f] = pk(ya[0], ya[1]); o[10 + f] = pk(yb[0], yb[1]); }
#pragma unroll
    for (int c = 0; c < 5; c++) ph[c] = (u32x4){o[4 * c], o[4 * c + 1], o[4 * c + 2], o[4 * c + 3]};
    asm volatile("" : "+v"(ph[0]), "+v"(ph[1]), "+v"(ph[2]), "+v"(ph[3]), "+v"(ph[4]));
  }
  u32x4 q1A, q1B; u32x2 q2A, q2B;
  if (role) {
    WAIT_BAR(0);
    STAMP(5);
    const LAS char* qa = lp + L_QL + w3 * 1280;
    q1A = ldsr16(qa + lane * 16); q1B = ldsr16(qa + 5120 + lane * 16); q2A = ldsr8(qa + 1024 + r * 8); q2B = ldsr8(qa + 6144 + r * 8);
    ARRIVE_NW(0);
  } else {
    const unsigned qb = WS_QX + (rt * 8 + hq) * QX_B + w3 * 1280;
    q1A = LD16(qb + lane * 16); q1B = LD16(qb + 5120 + lane * 16); q2A = LD8(qb + 1024 + r * 8); q2B = LD8(qb + 6144 + r * 8);
    { LAS char* d = lp + L_PHK + ((2 * w2 + h) * 128 + w3 * 32 + r) * 80;
#pragma unroll
      for (int c = 0; c < 5; c++) *(LAS u32x4*)(d + c * 16) = ph[c]; }
    WAIT_BAR(4);
    STAMP(5);
#pragma unroll 1
    for (unsigned spin = 0; spin < SPIN_CAP; ++spin) {
      if (!(missing(q1A) | missing(q1B) | missing((u32x4){q2A.x, q2A.y, q2B.x, q2B.y}))) break;
      __builtin_amdgcn_s_sleep(1);
      asm volatile("" ::: "memory");
      q1A = LD16(qb + lane * 16); q1B = LD16(qb + 5120 + lane * 16); q2A = LD8(qb + 1024 + r * 8); q2B = LD8(qb + 6144 + r * 8);
    }
  }
  STAMP(6);
  const int vr = r <= 21 ? r : 21;
  const int ok1 = lane * 16, ok2 = h ? (1280 + 20 * 16) : (1024 + r * 8), ov = 1280 + h * VB + vr * 16;
  const f32x16 Z = {};
  if (h) { q2A = (u32x2){0u, 0u}; q2B = (u32x2){0u, 0u}; }
  { const LAS char* t0 = lp;
    const u32x4 k1 = ldsr16(t0 + ok1); const u32x2 k2 = ldsr8(t0 + ok2);
    f32x16 sa = mfma32(k1, q1A, Z); sa = mfma32k8(k2, q2A, sa);
    f32x16 sb = mfma32(k1, q1B, Z); sb = mfma32k8(k2, q2B, sb);
    float ma = sa[0], mb = sb[0];
#pragma unroll
    for (int i = 1; i < 16; i++) { ma = fmaxf(ma, sa[i]); mb = fmaxf(mb, sb[i]); }
    ma = half_swap_max(ma); mb = half_swap_max(mb);
    const float mah = __uint_as_float(__float_as_uint(ma) & 0xFFFF0000u), mbh = __uint_as_float(__float_as_uint(mb) & 0xFFFF0000u);
    if (h) { q2A.x = pk(-mah, mah - ma); q2B.x = pk(-mbh, mbh - mb); } }
  f32x16 OA = {}, OB = {}, SA, SB;
  u32x4 k1, v1, v2, v2n, pA1, pA2, pB1, pB2; u32x2 k2;
  float e0, e1, e2, e3;
  const LAS char* c0p = lp + w2 * CH_B + ok1; const LAS char* c1p = lp + w2 * CH_B + ok2; const LAS char* c2p = lp + w2 * CH_B + ov;
  k1 = ldsr16(c0p); k2 = ldsr8(c1p);
  SA = mfma32(k1, q1A, Z); SA = mfma32k8(k2, q2A, SA);
  v1 = ldsr16(c2p); v2 = v1;
  e0 = e1 = e2 = e3 = 0.f; pB2 = (u32x4){0u, 0u, 0u, 0u};
#define G(MF, LD, C0_, C1_, X, S_, i) MF; LD; SBAR(); C0_; C1_; SBAR(); X##0 = ex2(S_[i]); X##1 = ex2(S_[i + 1]); X##2 = ex2(S_[i + 2]); X##3 = ex2(S_[i + 3]); asm volatile("" : "+v"(X##0), "+v"(X##1), "+v"(X##2), "+v"(X##3)); SBAR();
#define TILE(TB, TN, V2, H0, H1, H2) { float a0, a1, a2, a3; \
    G(SB = mfma32(k1, q1B, Z),    k1 = ldsr16(c0p + (TN)), pB2[2] = pk(e0, e1), pB2[3] = pk(e2, e3), a, SA, 0) \
    G(OB = mfma32(v2, pB2, OB),   V2,                      pA1[0] = pk(a0, a1), pA1[1] = pk(a2, a3), e, SA, 4) \
    G(SB = mfma32k8(k2, q2B, SB), k2 = ldsr8(c1p + (TN)),  pA1[2] = pk(e0, e1), pA1[3] = pk(e2, e3), a, SA, 8) \
    G(OA = mfma32(v1, pA1, OA),   H0,                      pA2[0] = pk(a0, a1), pA2[1] = pk(a2, a3), e, SA, 12) \
    G(SA = mfma32(k1, q1A, Z),    H1,                      pA2[2] = pk(e0, e1), pA2[3] = pk(e2, e3), a, SB, 0) \
    G(OA = mfma32(v2, pA2, OA),   H2,                      pB1[0] = pk(a0, a1), pB1[1] = pk(a2, a3), e, SB, 4) \
    G(SA = mfma32k8(k2, q2A, SA), (void)0,                 pB1[2] = pk(e0, e1), pB1[3] = pk(e2, e3), a, SB, 8) \
    G(OB = mfma32(v1, pB1, OB),   v1 = ldsr16(c2p + (TN)), pB2[0] = pk(a0, a1), pB2[1] = pk(a2, a3), e, SB, 12) }
#define NOP (void)0
#define TL(sl, t, H0, H1, H2) TILE((sl) * STEP_B + (t) * TILE_B, (sl) * STEP_B + ((t) + 1) * TILE_B, v2 = ldsr16(c2p + (sl) * STEP_B + (t) * TILE_B + 2 * VB), H0, H1, H2)
#define TL6(sl, H0, H1) TL(sl, 6, H0, H1, v2n = ldsr16(c2p + (sl) * STEP_B + 7 * TILE_B + 2 * VB))
#define TL7(sl, sn, H0, H1, H2) TILE((sl) * STEP_B + 7 * TILE_B, (sn) * STEP_B, v2 = v2n, H0, H1, H2)
  unsigned woff[5];
#pragma unroll
  for (int j = 0; j < 5; j++) { const int c = j * 64 + lane, rho = (c * 6554) >> 16, cc = c - rho * 10; const int k = (rho & ~12) | ((rho & 4) << 1) | ((rho & 8) >> 1); woff[j] = k * 1280 + cc * 16 + (4 - j) * 1024; }
#define WOG(wo, gr, j, dst) do { if ((j) == 4) WOP(wo, gr, 4, dst); else { if ((j) == 0) dmag_open(lds0 + (dst)); dmag<(j)>((wo) + C0 + (gr) * 32 * 640 - 2048, woff[j]); } } while (0)
#define WOP(wo, gr, j, dst) dma16s((wo) + C0 + (gr) * 32 * 640 - (4 - (j)) * 512, woff[j], lds0 + (dst) + (j) * 1024)
#define WA(gr, j) WOG(woA, gr, j, L_WO + (gr) * 5120)
  const u16* wxs = w < 4 ? woA + (16 + w3) * 32 * 640 : woB + (w < 7 ? 17 + w3 : 19) * 32 * 640;
  const int wxd = w < 4 ? L_WO + (16 + w3) * 5120 : 145408 + (w < 7 ? w3 : 2) * 5120;
#define WX(j) WOG(wxs, 0, j, wxd)
#define PRIO(n) __builtin_amdgcn_s_setprio(n)
  const unsigned fo = (rt * 8 + hq) * OT_B + lane * 16;
#define FILL(j) do { const int n_ = w * 3 + ((j) >> 1); ST16(ONES, (((j) & 1) ? WS_OB : WS_OA) + fo + (n_ < 20 ? n_ : 19) * 1024); } while (0)
#define CLR_VM() __builtin_amdgcn_s_waitcnt(0x0F70)
  const bool red = !role && w == 4;
  f32x4 part[16]; u64 fp0, fp1;
  auto red_load = [&]() {
    asm volatile("" : "+v"(fp0), "+v"(fp1));
    if (!__all((fp0 == tag) & (fp1 == tag))) wait_flags<128>(ws, WS_FPART + hq * 1024, tag, lane);
    asm volatile("" ::: "memory");
#pragma unroll
    for (int j = 0; j < 16; j++) part[j] = __builtin_bit_cast(f32x4, LD16(WS_PART + (hq * 16 + j) * PART_B + (rt * 60 + (lane < 60 ? lane : 59)) * 16));
  };
  auto red_store = [&]() {
    f32x4 a = ((part[0] + part[1]) + (part[2] + part[3])) + ((part[4] + part[5]) + (part[6] + part[7]));
    a += ((part[8] + part[9]) + (part[10] + part[11])) + ((part[12] + part[13]) + (part[14] + part[15]));
    const int hd = rt >> 2, u = (rt & 3) * 60 + lane, hi = u >= 160, fq = hi ? (u - 160) >> 3 : u >> 4, di = hi ? u & 7 : u & 15;
    if (lane < 60) ST8(((u32x2){pk(a[0], a[1]), pk(a[2], a[3])}), WS_KVR + hq * KVR_B + (((hd >> 1) * 2 + hi) * 5 + (fq >> 1)) * 512 + ((hd & 1) * 16 + di) * 16 + (fq & 1) * 8);
  };
  u32x4 ka[10]; u64 fkr; float gta;
  auto oa_load = [&]() {
    asm volatile("" : "+v"(fkr));
    if (!__all(fkr == tag)) wait_flags<16>(ws, WS_FKVR + hq * 128, tag, lane);
    asm volatile("" ::: "memory");
    const unsigned kb = (((r >> 2) & 1) == h) ? WS_KVR + hq * KVR_B + w2 * 5120 + (h * 16 + (r & 3) + 4 * (r >> 3)) * 16 : 0x7FFF0000u;
#pragma unroll
    for (int j = 0; j < 10; j++) ka[j] = LD16(kb + j * 512);
    gta = __uint_as_float(__builtin_amdgcn_raw_buffer_load_b32(wsr, WS_G + (rt * 128 + w3 * 32 + r) * 4, 0, 16));
  };
  u32x4 ja[2][3], jb[2]; f32x4 jc[3];
  const LAS char* jab = lp + L_PHK + ((w >> 1) * 128 + g * 8 + q4) * 80 + p4 * 8;
  const LAS char* jbb = lp + L_VIM + ((w >> 1) * 128 + g * 8 + q4) * 40 + (w & 1) * 32 + p4 * 8;
  auto kv_rd = [&](int k0) {
#pragma unroll
    for (int ks = 0; ks < 2; ks++) { jb[ks] = trd2(jbb + (k0 + ks) * 1280, jbb + (k0 + ks) * 1280 + 160);
#pragma unroll
      for (int ft = 0; ft < 3; ft++) ja[ks][ft] = trd2(jab + (k0 + ks) * 2560 + ft * 32, jab + (k0 + ks) * 2560 + 320 + ft * 32); } };
  auto kv_mm = [&](bool first) {
#pragma unroll
    for (int ks = 0; ks < 2; ks++) { if ((w & 1) && l15 == 4) jb[ks] = (u32x4){0x3F803F80u, 0x3F803F80u, 0x3F803F80u, 0x3F803F80u};
#pragma unroll
      for (int ft = 0; ft < 3; ft++) jc[ft] = mfma16(ja[ks][ft], jb[ks], (first && !ks) ? (f32x4){0.f, 0.f, 0.f, 0.f} : jc[ft]); } };
  auto kv_st = [&]() { const int dt = w & 1; const unsigned pb = WS_PART + (hq * 16 + rt) * PART_B + (w >> 1) * 3840 + (dt ? 2560 + g * 128 : g * 256) + l15 * 16;
#pragma unroll
    for (int ft = 0; ft < 3; ft++) if ((!dt || l15 < 8) && ft * 4 + g < 10) ST16(__builtin_bit_cast(u32x4, jc[ft]), pb + ft * (dt ? 512 : 1024)); };
  f32x16 oa0, oa1; u32x4 z0, z1, z2;
  LAS char* otb = lp + L_WO + (8 + w) * 5120;
  auto oa_mm = [&](int s) { oa0 = mfma32(ka[s], ph[s], s ? oa0 : Z); oa1 = mfma32(ka[5 + s], ph[s], s ? oa1 : Z); };
  auto oa_norm = [&]() { const float inv = __builtin_amdgcn_rcpf(oa1[4] + 1e-6f) * gta;
#pragma unroll
    for (int t = 0; t < 5; t++) { const int k0 = h * 20 + 4 * t;
      const u32x2 v = t < 4 ? (u32x2){nz(pk(oa0[(4 * t) & 15] * inv, oa0[(4 * t + 1) & 15] * inv)), nz(pk(oa0[(4 * t + 2) & 15] * inv, oa0[(4 * t + 3) & 15] * inv))} : (u32x2){nz(pk(oa1[0] * inv, oa1[1] * inv)), nz(pk(oa1[2] * inv, oa1[3] * inv))};
      *(LAS u32x2*)(otb + (r >> 4) * 1280 + (k0 >> 3) * 256 + (k0 & 4) * 2 + l15 * 16) = v; }
    z0 = ldsr16(otb + lane * 16); z1 = ldsr16(otb + 1280 + lane * 16); z2 = ldsr16(otb + (r >> 4) * 1280 + 1024 + l15 * 16); };
  auto oa_st = [&]() { const unsigned oa = WS_OA + (rt * 8 + w3 * 2) * OT_B + (hq * 2 + w2) * 1280;
    ST16(z0, oa + lane * 16); ST16(z1, oa + OT_B + lane * 16); if (!h) ST16(z2, oa + (r >> 4) * OT_B + 1024 + l15 * 16); };
  unsigned cpk;
  PRIO(3);
  TL(0, 0, if (!role) kv_rd(0); else FILL(0), NOP, NOP)
  TL(0, 1, if (!role) { kv_mm(true); kv_rd(2); ARRIVE_NW(0); } else FILL(1), NOP, NOP)
  TL(0, 2, if (!role) kv_mm(false); else FILL(2), NOP, if (!role) kv_st())
  TL(0, 3, NOP, NOP, cpk = __hip_atomic_load(cnt, __ATOMIC_RELAXED, __HIP_MEMORY_SCOPE_WORKGROUP))
  STAMP(14);
  if ((unsigned)__builtin_amdgcn_readfirstlane(cpk) < 8u) AWAIT(0, 8);
  asm volatile("" ::: "memory");
  TL(0, 4, NOP, NOP, if (!role) { WAIT_VM(0); set_flag(ws, WS_FPART + ((hq * 16 + rt) * 8 + w) * 8, tag, lane); } else FILL(3))
  PRIO(2);
  TL(0, 5, KX(2, 2), NOP, KW(2, 2, 0)) TL6(0, KW(2, 2, 1), KW(2, 2, 2))
  TL7(0, 1, KW(2, 2, 3), NOP, KW(2, 2, 4))
  STAMP(15);
  TL(1, 0, if (role) FILL(4), NOP, if (red) { fp0 = __hip_atomic_load((gu64*)(ws + WS_FPART) + hq * 128 + lane, RLX_AGENT); fp1 = __hip_atomic_load((gu64*)(ws + WS_FPART) + hq * 128 + 64 + lane, RLX_AGENT); })
  TL(1, 1, if (w < 4) { const int row = (w & 1) * 32 + (lane >> 1);
                      dma4s((w & 2 ? woB : woA) + C0 + ((rt & 7) * 2 + role) * 40 * 640, (row < 40 ? row : 39) * 1280 + (lane & 1) * 156, lds0 + 157696); }, NOP, if (role) FILL(5))
  TL(1, 2, NOP, NOP, if (red) red_load())
  PRIO(1);
  TL(1, 3, NOP, NOP, NOP)
  TL(1, 4, NOP, CLR_VM(), if (red) red_store())
  TL(1, 5, NOP, NOP, NOP)
  PRIO(0);
  TL6(1, if (red) { WAIT_VM(0); set_flag(ws, WS_FKVR + (hq * 16 + rt) * 8, tag, lane); }, NOP)
  if (red) WAIT_BAR(1); else WAIT_BAR(0);
  STAMP(12);
  PRIO(3);
  if (role) set_flag(ws, WS_FOA + ((rt * 8 + hq) * 8 + w) * 8, tag, lane);
  u64 foa;
  TL7(1, 2, KX(3, 0), KW(3, 0, 0), KW(3, 0, 1))
  TL(2, 0, KW(3, 0, 2), KW(3, 0, 3), KW(3, 0, 4)) TL(2, 1, WA(w, 0), WA(w, 1), WA(w, 2))
  TL(2, 2, WA(w, 3), WA(w, 4), (foa = __hip_atomic_load((gu64*)(ws + WS_FOA) + rt * 64 + lane, RLX_AGENT), fkr = __hip_atomic_load((gu64*)(ws + WS_FKVR) + hq * 16 + l15, RLX_AGENT)))
  PRIO(2);
  TL(2, 3, NOP, NOP, NOP)
  auto chk = [&]() { asm volatile("" : "+v"(foa), "+v"(fkr));
    if (!__all(foa == tag)) wait_flags<64>(ws, WS_FOA + rt * 512, tag, lane);
    asm volatile("" ::: "memory"); if (role) oa_load(); };
  TL(2, 4, chk(), NOP, NOP)
  PRIO(1);
  TL(2, 5, NOP, NOP, if (role) oa_mm(0))
  PRIO(0);
  TL6(2, if (role) oa_mm(1), if (role) oa_mm(2))
  BAR_L();
  STAMP(13);
  PRIO(3);
  TL7(2, 0, if (role) oa_mm(3), { if (role) oa_mm(4); CLR_VM(); }, WX(0))
  TL(0, 0, if (role) oa_norm(), WX(1), WX(2))
  TL(0, 1, if (role) oa_st(), WX(3), WX(4))
  TL(0, 2, WA(8 + w, 0), WA(8 + w, 1), WA(8 + w, 2))
  u32x4 wbr[11];
  const char* wbs = (const char*)(woB + C0 + w * 32 * 640) - 4096;
#define WB2(j) do { wbr[j] = *(const GAS u32x4*)(wbs + woff[j] + (j) * 1024); wbr[5 + (j)] = *(const GAS u32x4*)(wbs + 8 * 32 * 1280 + woff[j] + (j) * 1024); } while (0)
  TL(0, 3, WA(8 + w, 3), WA(8 + w, 4), WB2(0))
  PRIO(2);
  TL(0, 4, WB2(1), WB2(2), WB2(3))
  const int w5 = w < 5 ? w : 4;
  const unsigned wo16 = (w5 == 0 ? woff[0] : w5 == 1 ? woff[1] : w5 == 2 ? woff[2] : w5 == 3 ? woff[3] : woff[4]) + w5 * 1024;
  TL(0, 5, WB2(4), NOP, wbr[10] = *(const GAS u32x4*)((const char*)(woB + C0 + 16 * 32 * 640) - 4096 + wo16))
  PRIO(1);
  ARRIVE_NW(1);
  const int tp = w & 1, kq = w >> 1;
  const unsigned ofr = (rt * 8 + role * 4 + tp * 2) * OT_B + kq * 5120 + lane * 16;
  u32x4 of[2][5]; float gtb;
#define OFL(u) do { _Pragma("unroll") for (int i = 0; i < 5; i++) of[u][i] = LD16(WS_OA + ofr + (u) * OT_B + i * 1024); } while (0)
  TL6(0, OFL(0), gtb = __uint_as_float(__builtin_amdgcn_raw_buffer_load_b32(wsr, WS_G + (rt * 128 + w3 * 32 + w2 * 16 + l15) * 4, 0, 16)))
  PRIO(0);
  TL7(0, 0, OFL(1), NOP, NOP)
  pB2[2] = pk(e0, e1); pB2[3] = pk(e2, e3);
  OB = mfma32(v2, pB2, OB);
#undef TL
#undef TL6
#undef TL7
#undef TILE
#undef G
  STAMP(7);
  { LAS f32x4* xb = (LAS f32x4*)(lp + (w3 >> 1) * CH_B) + (w3 & 1) * (64 * 6);
#pragma unroll
    for (int i = 0; i < 12; i++) { auto rr = __builtin_amdgcn_permlane16_swap(__float_as_uint(OA[i]), __float_as_uint(OB[i]), false, false); OA[i] = __uint_as_float(rr[0]); OB[i] = __uint_as_float(rr[1]); }
    const f32x16 snd = w2 ? OA : OB;
    AWAIT(1, 8);
#pragma unroll
    for (int c = 0; c < 3; c++) xb[(w2 * 3 + c) * 64 + lane] = (f32x4){snd[4 * c], snd[4 * c + 1], snd[4 * c + 2], snd[4 * c + 3]};
    asm volatile("s_waitcnt vmcnt(22) lgkmcnt(0)\n\ts_barrier" ::: "memory");
    f32x16 O = w2 ? OB : OA;
#pragma unroll
    for (int c = 0; c < 3; c++) { const f32x4 t = xb[((w2 ^ 1) * 3 + c) * 64 + lane]; O[4 * c] += t[0]; O[4 * c + 1] += t[1]; O[4 * c + 2] += t[2]; O[4 * c + 3] += t[3]; }
    float l = from_upper(O[8]);
    float amax = 0.f;
#pragma unroll
    for (int i = 0; i < 12; i++) amax += fabsf(O[i]);
    const bool good = (l >= 0x1p-30f) && (l <= 0x1p100f) && (amax <= 0x1p126f);
    if (__builtin_expect(!__all(good), 0)) {
      u32x4 q1; u32x2 q2;
#pragma unroll
      for (int i = 0; i < 4; i++) { auto rr = __builtin_amdgcn_permlane16_swap(q1A[i], q1B[i], false, false); q1[i] = w2 ? rr[1] : rr[0]; }
#pragma unroll
      for (int i = 0; i < 2; i++) { auto rr = __builtin_amdgcn_permlane16_swap(q2A[i], q2B[i], false, false); q2[i] = h ? 0u : (w2 ? rr[1] : rr[0]); }
      float m = -INFINITY;
#pragma unroll 1
      for (int t = 0; t < 64; t++) {
        f32x16 s = mfma32(LD16(WS_KV + t * TILE_B + ok1), q1, Z); s = mfma32k8(LD8(WS_KV + t * TILE_B + ok2), q2, s);
#pragma unroll
        for (int i = 0; i < 16; i++) m = fmaxf(m, s[i]);
      }
      m = half_swap_max(m);
      O = Z;
#pragma unroll 1
      for (int t = 0; t < 64; t++) {
        f32x16 s = mfma32(LD16(WS_KV + t * TILE_B + ok1), q1, Z); s = mfma32k8(LD8(WS_KV + t * TILE_B + ok2), q2, s);
#pragma unroll
        for (int i = 0; i < 16; i++) s[i] = ex2(s[i] - m);
        O = mfma32(LD16(WS_KV + t * TILE_B + ov), (u32x4){pk(s[0], s[1]), pk(s[2], s[3]), pk(s[4], s[5]), pk(s[6], s[7])}, O);
        O = mfma32(LD16(WS_KV + t * TILE_B + ov + 2 * VB), (u32x4){pk(s[8], s[9]), pk(s[10], s[11]), pk(s[12], s[13]), pk(s[14], s[15])}, O);
      }
      l = from_upper(O[8]);
    }
    const float inv = __builtin_amdgcn_rcpf(l) * (1.0f - gtb);
    LAS char* tb = lp + w2 * CH_B + 12288 + w3 * 1536;
#pragma unroll
    for (int c = 0; c < 3; c++) { const int k0 = (g & 1) * 20 + 8 * c + 4 * h;
      *(LAS u32x2*)(tb + ((c == 2 && h) ? 1280 + (g & 1) * 8 : (k0 >> 3) * 256 + (k0 & 4) * 2) + l15 * 16) = (u32x2){nz(pk(O[4 * c] * inv, O[4 * c + 1] * inv)), nz(pk(O[4 * c + 2] * inv, O[4 * c + 3] * inv))}; }
    const unsigned ob = WS_OB + (rt * 8 + w3 * 2 + w2) * OT_B + hp * 1280 + lane * 16;
    const u32x4 z0 = ldsr16(tb + lane * 16), z1 = ldsr16(tb + 1024 + l15 * 16);
    ST16(z0, ob); ST16(z1, lane < 16 ? ob + 1024 : 0x7FFF0000u);
  }
  STAMP(8);
  auto of_missing = [&]() { u32x4 mm = of[0][0];
#pragma unroll
    for (int j = 1; j < 10; j++) mm = __builtin_elementwise_max(mm, of[j / 5][j % 5]);
    return missing(mm); };
  u32x4 wf[5][5];
#pragma unroll
  for (int i = 0; i < 5; i++) { const LAS char* wb = lp + L_WO + (kq * 5 + i) * 5120 + wlane;
#pragma unroll
    for (int t = 0; t < 5; t++) wf[i][t] = trd2(wb + t * 8, wb + 1280 + t * 8); }
  SBAR();
  if (__builtin_expect(of_missing(), 0)) {
#pragma unroll 1
    for (unsigned spin = 0; spin < SPIN_CAP; ++spin) {
      asm volatile("" ::: "memory");
      OFL(0); OFL(1);
      if (!of_missing()) break;
      __builtin_amdgcn_s_sleep(1);
    }
  }
  f32x4 c[2][5];
#pragma unroll
  for (int i = 0; i < 5; i++)
#pragma unroll
    for (int t = 0; t < 5; t++) c[0][t] = mfma16(wf[i][t], of[0][i], i ? c[0][t] : (f32x4){0.f, 0.f, 0.f, 0.f});
  BAR_L();
  SBAR();
  { LAS char* d = lp + L_WO + w * 5120 + lane * 16;
    *(LAS u32x4*)(lp + L_WO + 16 * 5120 + w5 * 1024 + lane * 16) = wbr[10];
#pragma unroll
    for (int i = 0; i < 5; i++) {
      *(LAS u32x4*)(d + i * 1024) = wbr[i]; *(LAS u32x4*)(d + 40960 + i * 1024) = wbr[5 + i]; of[0][i] = LD16(WS_OB + ofr + i * 1024); SBAR();
#pragma unroll
      for (int t = 0; t < 5; t++) c[1][t] = mfma16(wf[i][t], of[1][i], i ? c[1][t] : (f32x4){0.f, 0.f, 0.f, 0.f});
      SBAR();
    } }
  STAMP(9);
  BAR_L();
#pragma unroll
  for (int i = 0; i < 5; i++) { const int gr = kq * 5 + i; const LAS char* wb = lp + (gr < 17 ? L_WO + gr * 5120 : 145408 + (gr - 17) * 5120) + wlane;
#pragma unroll
    for (int t = 0; t < 5; t++) wf[i][t] = trd2(wb + t * 8, wb + 1280 + t * 8);
    SBAR(); of[1][i] = LD16(WS_OB + ofr + OT_B + i * 1024); SBAR(); }
  ARRIVE_NW(2);
  STAMP(10);
  LAS char* xw = lp + kq * 21504 + (tp * 32 + l15) * 336 + g * 80;
#pragma unroll
  for (int u = 0; u < 2; u++) {
#pragma unroll
    for (int i = 0; i < 5; i++) {
      if (__builtin_expect(missing(of[u][i]), 0)) {
#pragma unroll 1
        for (unsigned spin = 0; spin < SPIN_CAP; ++spin) {
          asm volatile("" ::: "memory");
#pragma unroll
          for (int j = u * 5 + i; j < 10; j++) of[j / 5][j % 5] = LD16(WS_OB + ofr + (j / 5) * OT_B + (j % 5) * 1024);
          if (!missing(of[u][i])) break;
          __builtin_amdgcn_s_sleep(1);
        }
      }
#pragma unroll
      for (int t = 0; t < 5; t++) c[u][t] = mfma16(wf[i][t], of[u][i], c[u][t]);
      if (u == 0 && i == 3) cpk = __hip_atomic_load(cnt + 2, __ATOMIC_RELAXED, __HIP_MEMORY_SCOPE_WORKGROUP);
      if (u == 1) { if (i == 0) { if (__builtin_expect((unsigned)__builtin_amdgcn_readfirstlane(cpk) < 8u, 0)) AWAIT(2, 8); asm volatile("" ::: "memory"); }
        *(LAS f32x4*)(xw + i * 16) = c[0][i]; }
      SBAR();
    }
  }
#pragma unroll
  for (int t = 0; t < 5; t++) *(LAS f32x4*)(xw + 16 * 336 + t * 16) = c[1][t];
  BAR_L();
  { const __amdgpu_buffer_rsrc_t orr = __builtin_amdgcn_make_buffer_rsrc(out, 0, 2048 * DM * 2, 0x00020000);
#pragma unroll
    for (int j = 0; j < 3; j++) { const int n = tid + 512 * j, nn = n < 1280 ? n : 0, row = (nn * 3277) >> 16, cq = nn - row * 20;
      const LAS f32x4* p = (const LAS f32x4*)(lp + row * 336 + cq * 16);
      const f32x4 z = (p[0] + p[1344]) + (p[2688] + p[4032]);
      __builtin_amdgcn_raw_buffer_store_b64((u32x2){pk(z[0], z[1]), pk(z[2], z[3])}, orr, n < 1280 ? ((rt * 128 + role * 64 + row) * DM + C0 + cq * 4) * 2 : 0x7FFF0000u, 0, 0); }
  }
  STAMP(11);
}

__global__ __launch_bounds__(512, 2) void fused(const u16* __restrict__ x, const u16* __restrict__ wqkv, const u16* __restrict__ wphi, const u16* __restrict__ woA,
                                                const u16* __restrict__ wq, const u16* __restrict__ wk, const u16* __restrict__ wv, const u16* __restrict__ woB,
                                                const u16* __restrict__ gatew, u16* __restrict__ out, char* ws) {
  __shared__ __attribute__((aligned(16))) char lds[L_TOTAL];
  if ((blockIdx.x >> 6) & 1) body<1>(x, wqkv, wphi, woA, wq, wv, woB, gatew, out, ws, (LAS char*)lds);
  else body<0>(x, wqkv, wphi, woA, wq, wk, woB, gatew, out, ws, (LAS char*)lds);
}
}

extern "C" void kernel_launch(void* const* d_in, const int* in_sizes, int n_in, void* d_out, int out_size, void* d_ws, size_t ws_size, hipStream_t stream) {
  using namespace gm;
  fused<<<256, 512, 0, stream>>>((const u16*)d_in[0], (const u16*)d_in[1], (const u16*)d_in[2], (const u16*)d_in[3], (const u16*)d_in[5], (const u16*)d_in[6],
                                 (const u16*)d_in[7], (const u16*)d_in[8], (const u16*)d_in[9], (u16*)d_out, (char*)d_ws);
}
```

```cpp
#include <hip/hip_runtime.h>
#include <hip/hip_bf16.h>
#include <stdint.h>

namespace gm {
typedef float f32x16 __attribute__((ext_vector_type(16)));
typedef float f32x4 __attribute__((ext_vector_type(4)));
typedef float f32x2 __attribute__((ext_vector_type(2)));
typedef short s16x4 __attribute__((ext_vector_type(4)));
typedef __bf16 b16x2 __attribute__((ext_vector_type(2)));
typedef __bf16 b16x8 __attribute__((ext_vector_type(8)));
typedef unsigned u32x4 __attribute__((ext_vector_type(4)));
typedef unsigned u32x2 __attribute__((ext_vector_type(2)));
typedef unsigned long long u64;
typedef unsigned short u16;
#define DI __device__ __forceinline__
#define LAS __attribute__((address_space(3)))
#define GAS __attribute__((address_space(1)))
typedef GAS u64 gu64;

constexpr int DM = 640;
constexpr unsigned WS_EPOCH = 0, WS_FKV = 2048, WS_FKVR = 4096, WS_FQ = 8192, WS_FPART = 16384, WS_FOA = 24576, WS_G = 57344;
constexpr unsigned WS_KVR = 8912896, KVR_B = 10240, WS_KV = 131072, WS_QX = 327680, QX_B = 10240, WS_PART = 1703936, PART_B = 15360;
constexpr unsigned WS_OA = 3670016, WS_OB = 6291456, WS_END = 8994816, OT_B = 20480;
constexpr int VB = 352, TILE_B = 1280 + 4 * VB, CH_B = TILE_B * 8, STEP_B = 2 * CH_B;
constexpr int STAGE_B = 36864, ST_W1 = 10240, ST_X = 20480;
constexpr int L_BUF = 43008, L_P0W = 0, L_P0X0 = 25600, L_P0X1 = 153600, L_RED = 158720, L_RED1 = 40960;
constexpr int L_HO = 86016, L_QL = 96256, L_VIM = 96256, L_PHK = 116736, L_WPHI = 158592, L_ZERO = 161792, L_CNT = 163712, L_TOTAL = 163840;
constexpr int L_WO = 43008;
constexpr float LOG2E = 1.4426950408889634f;
constexpr float QSCALE = 0.22360679774997896f * LOG2E;
constexpr unsigned SPIN_CAP = 20000, LSPIN_CAP = 400000;

DI unsigned pk(float a, float b) { f32x2 v = {a, b}; b16x2 r = __builtin_convertvector(v, b16x2); return __builtin_bit_cast(unsigned, r); }
DI f32x16 mfma32(u32x4 a, u32x4 b, f32x16 c) { return __builtin_amdgcn_mfma_f32_32x32x16_bf16(__builtin_bit_cast(b16x8, a), __builtin_bit_cast(b16x8, b), c, 0, 0, 0); }
DI f32x16 mfma32k8(u32x2 a, u32x2 b, f32x16 c) { return __builtin_amdgcn_mfma_f32_32x32x8bf16_1k(__builtin_bit_cast(s16x4, a), __builtin_bit_cast(s16x4, b), c, 0, 0, 0); }
DI f32x4 mfma16(u32x4 a, u32x4 b, f32x4 c) { return __builtin_amdgcn_mfma_f32_16x16x32_bf16(__builtin_bit_cast(b16x8, a), __builtin_bit_cast(b16x8, b), c, 0, 0, 0); }
DI u32x2 trd(const LAS char* p) { return __builtin_bit_cast(u32x2, __builtin_amdgcn_ds_read_tr16_b64_v4i16((LAS s16x4*)p)); }
DI u32x4 trd2(const LAS char* p0, const LAS char* p1) { u32x2 a = trd(p0), b = trd(p1); return (u32x4){a.x, a.y, b.x, b.y}; }
DI u32x4 ldsr16(const LAS char* p) { return *(const LAS u32x4*)p; }
DI u32x2 ldsr8(const LAS char* p) { return *(const LAS u32x2*)p; }
DI float ex2(float x) { return __builtin_amdgcn_exp2f(x); }
DI void dma16s(const void* base, unsigned voff, unsigned lds_base) {
  asm volatile("s_mov_b32 m0, %2\n\ts_nop 0\n\tglobal_load_lds_dwordx4 %0, %1" :: "v"(voff), "s"(base), "s"(lds_base) : "memory"); }
DI void dma16s_sc1(const void* base, unsigned voff, unsigned lds_base) {
  asm volatile("s_mov_b32 m0, %2\n\ts_nop 0\n\tglobal_load_lds_dwordx4 %0, %1 sc1" :: "v"(voff), "s"(base), "s"(lds_base) : "memory"); }
DI void dmag_open(unsigned lds_base) { asm volatile("s_mov_b32 m0, %0\n\ts_nop 0" :: "s"(lds_base) : "memory"); }
template <int J> DI void dmag(const void* base, unsigned voff) { asm volatile("global_load_lds_dwordx4 %0, %1 offset:%2" :: "v"(voff), "s"(base), "i"((J & 3) * 1024) : "memory"); }
template <int J> DI void dmag_sc1(const void* base, unsigned voff) { asm volatile("global_load_lds_dwordx4 %0, %1 offset:%2 sc1" :: "v"(voff), "s"(base), "i"((J & 3) * 1024) : "memory"); }
DI void dma4s(const void* base, unsigned voff, unsigned lds_base) {
  asm volatile("s_mov_b32 m0, %2\n\ts_nop 0\n\tglobal_load_lds_dword %0, %1" :: "v"(voff), "s"(base), "s"(lds_base) : "memory"); }
DI u32x4 ld16_async(const void* p) { u32x4 r; asm volatile("global_load_dwordx4 %0, %1, off" : "=v"(r) : "v"(p) : "memory"); return r; }
DI u32x2 ld8_sc1_async(const void* p) { u32x2 r; asm volatile("global_load_dwordx2 %0, %1, off sc1" : "=v"(r) : "v"(p) : "memory"); return r; }
#define BAR_L() asm volatile("s_waitcnt lgkmcnt(0)\n\ts_barrier" ::: "memory")
#define WAIT_BAR(N) asm volatile("s_waitcnt vmcnt(" #N ") lgkmcnt(0)\n\ts_barrier" ::: "memory")
#define WAIT_VM(N) asm volatile("s_waitcnt vmcnt(" #N ")" ::: "memory")
#define SBAR() __builtin_amdgcn_sched_barrier(0)
#define RLX_AGENT __ATOMIC_RELAXED, __HIP_MEMORY_SCOPE_AGENT
DI float half_swap_max(float m) { auto rr = __builtin_amdgcn_permlane32_swap(__float_as_uint(m), __float_as_uint(m), false, false); return fmaxf(__uint_as_float(rr[0]), __uint_as_float(rr[1])); }
DI float from_upper(float v) { auto rr = __builtin_amdgcn_permlane32_swap(__float_as_uint(v), __float_as_uint(v), false, false); return __uint_as_float(rr[1]); }

template <int NF>
DI bool flags_set(const char* ws, unsigned off, u64 tag, int lane) {
  gu64* f = (gu64*)(ws + off);
  bool ok = __hip_atomic_load(f + (NF < 64 ? (lane & (NF - 1)) : lane), RLX_AGENT) == tag;
  if (NF >= 128) ok &= __hip_atomic_load(f + 64 + lane, RLX_AGENT) == tag;
  if (NF >= 256) { ok &= __hip_atomic_load(f + 128 + lane, RLX_AGENT) == tag; ok &= __hip_atomic_load(f + 192 + lane, RLX_AGENT) == tag; }
  return ok;
}
template <int NF>
DI void wait_flags(const char* ws, unsigned off, u64 tag, int lane) {
#pragma unroll 1
  for (unsigned spin = 0; spin < SPIN_CAP; ++spin) {
    if (__all(flags_set<NF>(ws, off, tag, lane))) break;
    __builtin_amdgcn_s_sleep(2);
  }
  asm volatile("" ::: "memory");
}
DI void set_flag(char* ws, unsigned off, u64 tag, int lane) { if (lane == 0) __hip_atomic_store((gu64*)(ws + off), tag, RLX_AGENT); }
DI unsigned nz(unsigned v) { return v < 0xFFFFFFFEu ? v : 0xFFFFFFFEu; }
DI unsigned umax3(unsigned a, unsigned b, unsigned c) { unsigned m = a > b ? a : b; return m > c ? m : c; }
DI bool missing(u32x4 v) { return __any(umax3(umax3(v.x, v.y, v.z), v.w, 0u) == 0xFFFFFFFFu); }
#define ONES ((u32x4){0xFFFFFFFFu, 0xFFFFFFFFu, 0xFFFFFFFFu, 0xFFFFFFFFu})
#define ST16(v, off) __builtin_amdgcn_raw_buffer_store_b128((v), wsr, (off), 0, 16)
#define ST8(v, off) __builtin_amdgcn_raw_buffer_store_b64((v), wsr, (off), 0, 16)
#define LD16(off) __builtin_amdgcn_raw_buffer_load_b128(wsr, (off), 0, 16)
#define LD8(off) __builtin_amdgcn_raw_buffer_load_b64(wsr, (off), 0, 16)

#define STAMP(i) do {} while (0)

template <int role>
DI void body(const u16* __restrict__ x, const u16* __restrict__ wqkv, const u16* __restrict__ wphi, const u16* __restrict__ woA,
             const u16* __restrict__ wq, const u16* __restrict__ wkv, const u16* __restrict__ woB,
             const u16* __restrict__ gatew, u16* __restrict__ out, char* ws, LAS char* lp) {
  const int tid = threadIdx.x, lane = tid & 63;
  const int w = __builtin_amdgcn_readfirstlane(tid >> 6) & 7;
  const int l15 = lane & 15, g = lane >> 4, q4 = l15 >> 2, p4 = lane & 3, r = lane & 31, h = lane >> 5;
  const int bx = blockIdx.x & 7, bi = blockIdx.x >> 3;
  const int rt = (bx & 1) * 8 + (bi & 7), hp = (bx >> 1) * 4 + ((bi >> 3) & 3), hq = hp >> 1;
  const int w3 = w & 3, w2 = w >> 2;
  const unsigned lds0 = (unsigned)(uintptr_t)lp;
  const __amdgpu_buffer_rsrc_t wsr = __builtin_amdgcn_make_buffer_rsrc(ws, 0, WS_END, 0x00020000);
  LAS unsigned* cnt = (LAS unsigned*)(lp + L_CNT);
#define ARRIVE_NW(c) do { asm volatile("" ::: "memory"); if (lane == 0) __hip_atomic_fetch_add(cnt + (c), 1u, __ATOMIC_RELAXED, __HIP_MEMORY_SCOPE_WORKGROUP); asm volatile("" ::: "memory"); } while (0)
#define PEEK(c) ((unsigned)__builtin_amdgcn_readfirstlane(__hip_atomic_load(cnt + (c), __ATOMIC_RELAXED, __HIP_MEMORY_SCOPE_WORKGROUP)))
#define AWAIT(c, n) do { _Pragma("unroll 1") for (unsigned sp_ = 0; sp_ < LSPIN_CAP; ++sp_) { if (PEEK(c) >= (unsigned)(n)) break; __builtin_amdgcn_s_sleep(1); } asm volatile("" ::: "memory"); } while (0)
  STAMP(0);

  const int T0 = rt * 128 + hq * 16;
  const int C0 = hq * 80;
  const int wlane = ((g >> 1) * 16 + (g & 1) * 4 + q4) * 160 + p4 * 40;
  u64 tag;
  unsigned o0[10];
#define KVP(s, slot, p) dma16s_sc1(ws + WS_KV + (s) * STEP_B + (p) * 1024, lane * 16, lds0 + (slot) * STEP_B + (p) * 1024)
#define KVL(s, slot) do { _Pragma("unroll") for (int j = 0; j < 11; j++) if (j < 10 || w3 < 2) KVP(s, slot, w3 + 4 * j); } while (0)
#define KW(s, slot, j) do { if ((j) == 4) KVP(s, slot, w * 5 + 4); else { if ((j) == 0) dmag_open(lds0 + (slot) * STEP_B + w * 5120); dmag_sc1<(j)>(ws + WS_KV + (s) * STEP_B + w * 5120, lane * 16); } } while (0)
#define KX(s, slot) do { if (w < 2) KVP(s, slot, 40 + w); } while (0)
  if (w2 == 0) {
    u32x4 gf0, gf1, gf2, gf3, gf4, wpc;
    u32x2 ep = ld8_sc1_async(ws + WS_EPOCH);
    const int lx = w3 < 3 ? L_P0X0 + w3 * 5120 : L_P0X1;
    { const u16* wsrc = wkv + w3 * 3200;
#pragma unroll
      for (int j = 0; j < 5; j++) { const int n = j * 64 + lane, row = (n * 3277) >> 16, cc = n - row * 20; dma16s(x + T0 * DM + w3 * 160, row * 1280 + cc * 16, lds0 + lx + j * 1024); }
#pragma unroll
      for (int j = 0; j < 6; j++) dma16s(wsrc + j * 512, lane * 16, lds0 + L_P0W + w3 * 6400 + j * 1024);
      if (lane < 16) dma16s(wsrc + 6 * 512, lane * 16, lds0 + L_P0W + w3 * 6400 + 6 * 1024);
      const u16* gp = gatew + w3 * 160 + g * 8;
      gf0 = ld16_async(gp); gf1 = ld16_async(gp + 32); gf2 = ld16_async(gp + 64); gf3 = ld16_async(gp + 96); gf4 = ld16_async(gp + 128);
      wpc = ld16_async(wphi + hq * 1600 + (tid < 200 ? tid : 199) * 8);
    }
    asm volatile("s_waitcnt vmcnt(0)" : "+v"(gf0), "+v"(gf1), "+v"(gf2), "+v"(gf3), "+v"(gf4), "+v"(wpc), "+v"(ep) :: "memory");
    tag = (((u64)(unsigned)__builtin_amdgcn_readfirstlane(ep.y) << 32) | (unsigned)__builtin_amdgcn_readfirstlane(ep.x)) + 1ull;
    STAMP(1);
    { const LAS char* wb = lp + L_P0W + (w3 * 160 + g * 8 + q4) * 40;
      const LAS char* xb = lp + lx + l15 * 320 + g * 16;
      const bool gl = l15 == 4;
      f32x4 c0 = {}, c1 = {};
#define P0STEP(s, GF) { const u32x4 b = ldsr16(xb + (s) * 64); c0 = mfma16(trd2(wb + p4 * 8 + (s) * 1280, wb + p4 * 8 + (s) * 1280 + 160), b, c0); \
        u32x4 a = trd2(wb + 32 + (s) * 1280, wb + 32 + (s) * 1280 + 160); if (gl) a = GF; c1 = mfma16(a, b, c1); }
      P0STEP(0, gf0) P0STEP(1, gf1) P0STEP(2, gf2) P0STEP(3, gf3) P0STEP(4, gf4)
#undef P0STEP
      *(LAS f32x4*)(lp + L_RED + tid * 16) = c0;
      if (lane < 32) *(LAS f32x4*)(lp + L_RED1 + (w3 * 32 + lane) * 16) = c1; }
    STAMP(2);
    f32x16 acc[5];
#pragma unroll
    for (int b = 0; b < 5; b++) acc[b] = (f32x16){};
    const int wm = g & 1, whd = p4;
    const int wo0 = wm * ST_W1 + (8 * h + 4 * wm + q4) * 160 + whd * 40, wo1 = wm * ST_W1 + (8 * h + 4 * (wm ^ 1) + q4) * 160 + whd * 40;
    const int xo = ST_X + (w3 * 32 + r) * 128 + ((h ^ ((r >> 1) & 7)) * 16);
    u32x4 fa[5], fb[5], xa, xb;
#define KSTEP(FN, XN, s, FC, XC) { XN = ldsr16(sb + (xo ^ ((s) * 32))); \
      _Pragma("unroll") for (int j = 0; j < 5; j++) { if (i > 0 || (s) > 0) acc[j] = mfma32(FC[j], XC, acc[j]); FN[j] = trd2(sb + wo0 + (s) * 2560 + j * 8, sb + wo1 + (s) * 2560 + j * 8); SBAR(); } }
#pragma unroll
    for (int i = 0; i < 10; i++) {
      BAR_L();
      if (i == 1 && role) {
        const unsigned qb = WS_QX + (rt * 8 + hq) * QX_B + w3 * 1280 + lane * 16;
        ST16(ONES, qb); ST16(ONES, qb + 5120); if (lane < 32) ST16(ONES, qb + 1024 + (lane >> 4) * (5120 - 256)); }
      if (i == 4) { if (role) { WAIT_VM(0); set_flag(ws, WS_FQ + ((rt * 8 + hq) * 4 + w3) * 8, tag, lane); }
                    if (tid < 200) *(LAS u32x4*)(lp + L_WPHI + tid * 16) = wpc; }
      const LAS char* sb = lp + L_BUF + ((i + 2) % 3) * STAGE_B;
      KSTEP(fa, xa, 0, fb, xb) KSTEP(fb, xb, 1, fa, xa) KSTEP(fa, xa, 2, fb, xb)
      { xb = ldsr16(sb + (xo ^ 96));
        acc[0] = mfma32(fa[0], xa, acc[0]); fb[0] = trd2(sb + wo0 + 7680, sb + wo1 + 7680); fb[1] = trd2(sb + wo0 + 7688, sb + wo1 + 7688); SBAR();
        acc[1] = mfma32(fa[1], xa, acc[1]); fb[2] = trd2(sb + wo0 + 7696, sb + wo1 + 7696); fb[3] = trd2(sb + wo0 + 7704, sb + wo1 + 7704); SBAR();
        acc[2] = mfma32(fa[2], xa, acc[2]); fb[4] = trd2(sb + wo0 + 7712, sb + wo1 + 7712); SBAR();
        acc[3] = mfma32(fa[3], xa, acc[3]); acc[4] = mfma32(fa[4], xa, acc[4]); SBAR(); }
    }
#pragma unroll
    for (int j = 0; j < 5; j++) acc[j] = mfma32(fb[j], xb, acc[j]);
#undef KSTEP
    STAMP(3);
#define PK2(t, i, sc) pk(acc[t][i] * (sc), acc[t][(i) + 1] * (sc))
#define ROW(i, sc) const u32x4 ra = {PK2(0, i, sc), PK2(0, (i) + 2, sc), PK2(1, i, sc), PK2(1, (i) + 2, sc)}, rb = {PK2(2, i, sc), PK2(2, (i) + 2, sc), PK2(3, i, sc), PK2(3, (i) + 2, sc)}; const u32x2 rc = {PK2(4, i, sc), PK2(4, (i) + 2, sc)}
    { ROW(4, 1.0f);
      *(LAS u32x4*)(lp + L_HO + tid * 16) = ra; *(LAS u32x4*)(lp + L_HO + 4096 + tid * 16) = rb; *(LAS u32x2*)(lp + L_HO + 8192 + tid * 8) = rc; }
    BAR_L();
    const int qfo = (h * 4 + w3) * 1280;
    if (role) {
      { ROW(8, QSCALE); const unsigned qb = WS_QX + (rt * 8 + hq) * QX_B + qfo;
        ST16(((u32x4){nz(ra.x), nz(ra.y), nz(ra.z), nz(ra.w)}), qb + r * 16); ST16(((u32x4){nz(rb.x), nz(rb.y), nz(rb.z), nz(rb.w)}), qb + 512 + r * 16); ST8(((u32x2){nz(rc.x), nz(rc.y)}), qb + 1024 + r * 8); }
      { ROW(12, QSCALE); LAS char* d = lp + L_QL + qfo;
        *(LAS u32x4*)(d + r * 16) = ra; *(LAS u32x4*)(d + 512 + r * 16) = rb; *(LAS u32x2*)(d + 1024 + r * 8) = rc; }
    } else { LAS char* d = lp + L_VIM + (h * 128 + w3 * 32 + r) * 40;
#pragma unroll
      for (int t = 0; t < 5; t++) { *(LAS u32x2*)(d + t * 8) = (u32x2){PK2(t, 8, 1.0f), PK2(t, 10, 1.0f)}; *(LAS u32x2*)(d + 10240 + t * 8) = (u32x2){PK2(t, 12, 1.0f), PK2(t, 14, 1.0f)}; } }
#pragma unroll
    for (int t = 0; t < 5; t++) { o0[2 * t] = PK2(t, 0, 1.0f); o0[2 * t + 1] = PK2(t, 2, 1.0f); }
#undef ROW
#undef PK2
  } else {
    u32x2 ep = ld8_sc1_async(ws + WS_EPOCH);
    const bool second = w3 >> 1;
    const unsigned rs = (second && role) ? 1280u : 3840u;
    const char* wsrc = (second ? (role ? (const char*)(wq + C0) : (const char*)(wqkv + 1280 + C0)) : (const char*)(wqkv + (role ? 0 : 640) + C0)) + (w3 & 1) * 32 * rs;
    const char* xsrc = (const char*)(x + (rt * 128 + w3 * 32) * DM);
    int rot = (bi & 7) + 3 * ((bi >> 3) & 3); rot = rot >= 10 ? rot - 10 : rot;
    unsigned so[5], xoff[2];
#pragma unroll
    for (int j = 0; j < 5; j++) { const int c = j * 64 + lane, rho = (c * 6554) >> 16, cc = c - rho * 10; so[j] = (rho ^ (second ? 4 : 0)) * rs + cc * 16; }
#pragma unroll
    for (int j = 0; j < 2; j++) { const int row = j * 8 + (lane >> 3); xoff[j] = row * 1280 + (((lane & 7) ^ ((row >> 1) & 7)) * 16); }
    int bsel = 2, kc = rot;
    auto stage = [&]() {
      const unsigned lb = lds0 + L_BUF + bsel * STAGE_B;
      const char* bw = wsrc + kc * 64 * rs; const char* bxp = xsrc + kc * 128;
#pragma unroll
      for (int j = 0; j < 5; j++) { dma16s(bw, so[j], lb + (w3 * 5 + j) * 1024); if (j < 4) dma16s(bxp + (j >> 1) * 16 * 1280, xoff[j & 1], lb + ST_X + (w3 * 4 + j) * 1024); }
      bsel = bsel == 2 ? 0 : bsel + 1; kc = kc == 9 ? 0 : kc + 1;
    };
    stage(); stage();
    asm volatile("s_waitcnt vmcnt(9)" : "+v"(ep) :: "memory");
    tag = (((u64)(unsigned)__builtin_amdgcn_readfirstlane(ep.y) << 32) | (unsigned)__builtin_amdgcn_readfirstlane(ep.x)) + 1ull;
    STAMP(1); STAMP(2);
#pragma unroll 1
    for (int i = 0; i < 8; i++) {
      WAIT_BAR(9);
      stage();
      if (i == 0 && w3 == 0) {
        const unsigned tb = WS_KV + (T0 >> 5) * TILE_B; const int s16 = hq & 1;
        if (!role) {
          const LAS f32x4* rp = (const LAS f32x4*)(lp + (h ? L_RED1 : L_RED)) + (h ? (g & 1) * 16 : g * 32) + l15;
          const int st = h ? 32 : 64, o2 = h ? 0 : 16;
          const f32x4 a = (rp[0] + rp[st]) + (rp[2 * st] + rp[3 * st]), b = (rp[o2] + rp[st + o2]) + (rp[2 * st + o2] + rp[3 * st + o2]);
          const u32x4 v = {pk(a[0], a[1]), pk(a[2], a[3]), pk(b[0], b[1]), pk(b[2], b[3])};
          if (lane < 32) ST16(v, tb + (g * 32 + s16 * 16 + l15) * 16);
          else if (lane < 48) ST8(((u32x2){v.x, v.y}), tb + 1024 + (s16 * 16 + l15) * 8);
          else { const float gv = 1.0f / (1.0f + __expf(-a[0])); __builtin_amdgcn_raw_buffer_store_b32(__float_as_uint(gv), wsr, WS_G + (T0 + l15) * 4, 0, 16); }
        } else if (lane < 44) {
          const int hh = lane >= 22, d = lane - hh * 22, dd = d < 20 ? d : 0, st = dd >= 16 ? 128 : 256;
          const LAS float* rp = (const LAS float*)(lp + (dd >= 16 ? L_RED1 : L_RED)) + (((dd & 15) >> 2) * 16 + 4 * hh) * 4 + (dd & 3);
          float e[8];
#pragma unroll
          for (int j = 0; j < 8; j++) { const int tk = (8 * (j >> 2) + (j & 3)) * 4; e[j] = (rp[tk] + rp[tk + st]) + (rp[tk + 2 * st] + rp[tk + 3 * st]); }
          u32x4 v = {pk(e[0], e[1]), pk(e[2], e[3]), pk(e[4], e[5]), pk(e[6], e[7])};
          if (d == 20) v = (u32x4){0x3F803F80u, 0x3F803F80u, 0x3F803F80u, 0x3F803F80u};
          if (d == 21) v = (u32x4){0u, 0u, 0u, 0u};
          ST16(v, tb + 1280 + (s16 * 2 + hh) * VB + d * 16);
        }
      }
      if (i == 2) { if (w3 == 0) set_flag(ws, WS_FKV + blockIdx.x * 8, tag, lane);
                    if (w3 >= 2) *(LAS u32x4*)(lp + L_ZERO + (tid - 384) * 16) = (u32x4){0u, 0u, 0u, 0u}; }
    }
    u64 fa, fb, fc, fd, fe;
    { gu64* f = (gu64*)(ws + WS_FKV) + lane;
      fa = __hip_atomic_load(f, RLX_AGENT); fb = __hip_atomic_load(f + 64, RLX_AGENT); fc = __hip_atomic_load(f + 128, RLX_AGENT); fd = __hip_atomic_load(f + 192, RLX_AGENT);
      fe = __hip_atomic_load((gu64*)(ws + WS_FQ) + (rt * 8 + hq) * 4 + p4, RLX_AGENT); }
    WAIT_BAR(14);
    asm volatile("" : "+v"(fa), "+v"(fb), "+v"(fc), "+v"(fd), "+v"(fe));
    const bool kvok = __all((fa == tag) & (fb == tag) & (fc == tag) & (fd == tag) & (fe == tag));
    asm volatile("" ::: "memory");
    BAR_L();
    if (!kvok) { wait_flags<256>(ws, WS_FKV, tag, lane); wait_flags<4>(ws, WS_FQ + (rt * 8 + hq) * 32, tag, lane); }
    KVL(0, 0);
    if (blockIdx.x == 0 && w3 == 0) set_flag(ws, WS_EPOCH, tag, lane);
    STAMP(3);
    BAR_L();
    const int ht = tid - 256;
    const u32x4 a = ldsr16(lp + L_HO + ht * 16), b = ldsr16(lp + L_HO + 4096 + ht * 16); const u32x2 c = ldsr8(lp + L_HO + 8192 + ht * 8);
    o0[0] = a.x; o0[1] = a.y; o0[2] = a.z; o0[3] = a.w; o0[4] = b.x; o0[5] = b.y; o0[6] = b.z; o0[7] = b.w; o0[8] = c.x; o0[9] = c.y;
  }
  STAMP(4);
  u32x4 ph[5];
  { const LAS char* pb = ((p4 & 1) == h) ? lp + L_WPHI + (2 * w2 + h) * 800 + q4 * 40 + (2 * (g & 1) + (p4 >> 1)) * 8 : lp + L_ZERO;
    const u32x4 b0 = {o0[0], o0[1], o0[2], o0[3]}, b1 = {o0[4], o0[5], o0[6], o0[7]}, b2 = {o0[8], o0[9], 0u, 0u};
    const u32x2 c0 = trd(pb + 640), c1 = trd(pb + 672);
    f32x16 P0 = {}, P1 = {};
    P0 = mfma32(trd2(pb, pb + 160), b0, P0); P1 = mfma32(trd2(pb + 32, pb + 192), b0, P1);
    P0 = mfma32(trd2(pb + 320, pb + 480), b1, P0); P1 = mfma32(trd2(pb + 352, pb + 512), b1, P1);
    P0 = mfma32((u32x4){c0.x, c0.y, 0u, 0u}, b2, P0); P1 = mfma32((u32x4){c1.x, c1.y, 0u, 0u}, b2, P1);
    KX(1, 1); KW(1, 1, 0); SBAR();
    f32x2 p[10];
#pragma unroll
    for (int f = 0; f < 10; f++) p[f] = f < 8 ? (f32x2){P0[(2 * f) & 15], P0[(2 * f + 1) & 15]} : (f32x2){P1[(2 * f) & 3], P1[(2 * f + 1) & 3]};
    float mx = fmaxf(p[0][0], p[0][1]), mn = fminf(p[0][0], p[0][1]);
#pragma unroll
    for (int f = 1; f < 10; f++) { mx = fmaxf(fmaxf(mx, p[f][0]), p[f][1]); mn = fminf(fminf(mn, p[f][0]), p[f][1]); }
    const f32x2 ma = {-mx * LOG2E, -mx * LOG2E}, mb = {mn * LOG2E, mn * LOG2E}, ca_ = {LOG2E, LOG2E}, cb_ = {-LOG2E, -LOG2E};
    f32x2 ea[10], eb[10], sa = {0.f, 0.f}, sb = {0.f, 0.f};
#pragma unroll
    for (int f = 0; f < 10; f++) { if (f % 2 == 0 && f < 8) { SBAR(); if (f == 0) KW(1, 1, 1); if (f == 2) KW(1, 1, 2); if (f == 4) KW(1, 1, 3); if (f == 6) KW(1, 1, 4); SBAR(); }
      const f32x2 xa = __builtin_elementwise_fma(p[f], ca_, ma), xb = __builtin_elementwise_fma(p[f], cb_, mb);
      ea[f] = (f32x2){ex2(xa[0]), ex2(xa[1])}; eb[f] = (f32x2){ex2(xb[0]), ex2(xb[1])}; sa += ea[f]; sb += eb[f]; }
    const float ia = __builtin_amdgcn_rcpf(sa[0] + sa[1]), ib = __builtin_amdgcn_rcpf(sb[0] + sb[1]);
    unsigned o[20];
#pragma unroll
    for (int f = 0; f < 10; f++) { const f32x2 ya = ea[f] * ia, yb = eb[f] * ib; o[f] = pk(ya[0], ya[1]); o[10 + f] = pk(yb[0], yb[1]); }
#pragma unroll
    for (int c = 0; c < 5; c++) ph[c] = (u32x4){o[4 * c], o[4 * c + 1], o[4 * c + 2], o[4 * c + 3]};
    asm volatile("" : "+v"(ph[0]), "+v"(ph[1]), "+v"(ph[2]), "+v"(ph[3]), "+v"(ph[4]));
  }
  u32x4 q1A, q1B; u32x2 q2A, q2B;
  if (role) {
    WAIT_BAR(0);
    STAMP(5);
    const LAS char* qa = lp + L_QL + w3 * 1280;
    q1A = ldsr16(qa + lane * 16); q1B = ldsr16(qa + 5120 + lane * 16); q2A = ldsr8(qa + 1024 + r * 8); q2B = ldsr8(qa + 6144 + r * 8);
    ARRIVE_NW(0);
  } else {
    const unsigned qb = WS_QX + (rt * 8 + hq) * QX_B + w3 * 1280;
    q1A = LD16(qb + lane * 16); q1B = LD16(qb + 5120 + lane * 16); q2A = LD8(qb + 1024 + r * 8); q2B = LD8(qb + 6144 + r * 8);
    { LAS char* d = lp + L_PHK + ((2 * w2 + h) * 128 + w3 * 32 + r) * 80;
#pragma unroll
      for (int c = 0; c < 5; c++) *(LAS u32x4*)(d + c * 16) = ph[c]; }
    WAIT_BAR(4);
    STAMP(5);
#pragma unroll 1
    for (unsigned spin = 0; spin < SPIN_CAP; ++spin) {
      if (!(missing(q1A) | missing(q1B) | missing((u32x4){q2A.x, q2A.y, q2B.x, q2B.y}))) break;
      __builtin_amdgcn_s_sleep(1);
      asm volatile("" ::: "memory");
      q1A = LD16(qb + lane * 16); q1B = LD16(qb + 5120 + lane * 16); q2A = LD8(qb + 1024 + r * 8); q2B = LD8(qb + 6144 + r * 8);
    }
  }
  STAMP(6);
  const int vr = r <= 21 ? r : 21;
  const int ok1 = lane * 16, ok2 = h ? (1280 + 20 * 16) : (1024 + r * 8), ov = 1280 + h * VB + vr * 16;
  const f32x16 Z = {};
  if (h) { q2A = (u32x2){0u, 0u}; q2B = (u32x2){0u, 0u}; }
  { const LAS char* t0 = lp;
    const u32x4 k1 = ldsr16(t0 + ok1); const u32x2 k2 = ldsr8(t0 + ok2);
    f32x16 sa = mfma32(k1, q1A, Z); sa = mfma32k8(k2, q2A, sa);
    f32x16 sb = mfma32(k1, q1B, Z); sb = mfma32k8(k2, q2B, sb);
    float ma = sa[0], mb = sb[0];
#pragma unroll
    for (int i = 1; i < 16; i++) { ma = fmaxf(ma, sa[i]); mb = fmaxf(mb, sb[i]); }
    ma = half_swap_max(ma); mb = half_swap_max(mb);
    const float mah = __uint_as_float(__float_as_uint(ma) & 0xFFFF0000u), mbh = __uint_as_float(__float_as_uint(mb) & 0xFFFF0000u);
    if (h) { q2A.x = pk(-mah, mah - ma); q2B.x = pk(-mbh, mbh - mb); } }
  f32x16 OA = {}, OB = {}, SA, SB;
  u32x4 k1, v1, v2, v2n, pA1, pA2, pB1, pB2; u32x2 k2;
  float e0, e1, e2, e3;
  const LAS char* c0p = lp + w2 * CH_B + ok1; const LAS char* c1p = lp + w2 * CH_B + ok2; const LAS char* c2p = lp + w2 * CH_B + ov;
  k1 = ldsr16(c0p); k2 = ldsr8(c1p);
  SA = mfma32(k1, q1A, Z); SA = mfma32k8(k2, q2A, SA);
  v1 = ldsr16(c2p); v2 = v1;
  e0 = e1 = e2 = e3 = 0.f; pB2 = (u32x4){0u, 0u, 0u, 0u};
#define G(MF, LD, C0_, C1_, X, S_, i) MF; LD; SBAR(); C0_; C1_; SBAR(); X##0 = ex2(S_[i]); X##1 = ex2(S_[i + 1]); X##2 = ex2(S_[i + 2]); X##3 = ex2(S_[i + 3]); asm volatile("" : "+v"(X##0), "+v"(X##1), "+v"(X##2), "+v"(X##3)); SBAR();
#define TILE(TB, TN, V2, H0, H1, H2) { float a0, a1, a2, a3; \
    G(SB = mfma32(k1, q1B, Z),    k1 = ldsr16(c0p + (TN)), pB2[2] = pk(e0, e1), pB2[3] = pk(e2, e3), a, SA, 0) \
    G(OB = mfma32(v2, pB2, OB),   V2,                      pA1[0] = pk(a0, a1), pA1[1] = pk(a2, a3), e, SA, 4) \
    G(SB = mfma32k8(k2, q2B, SB), k2 = ldsr8(c1p + (TN)),  pA1[2] = pk(e0, e1), pA1[3] = pk(e2, e3), a, SA, 8) \
    G(OA = mfma32(v1, pA1, OA),   H0,                      pA2[0] = pk(a0, a1), pA2[1] = pk(a2, a3), e, SA, 12) \
    G(SA = mfma32(k1, q1A, Z),    H1,                      pA2[2] = pk(e0, e1), pA2[3] = pk(e2, e3), a, SB, 0) \
    G(OA = mfma32(v2, pA2, OA),   H2,                      pB1[0] = pk(a0, a1), pB1[1] = pk(a2, a3), e, SB, 4) \
    G(SA = mfma32k8(k2, q2A, SA), (void)0,                 pB1[2] = pk(e0, e1), pB1[3] = pk(e2, e3), a, SB, 8) \
    G(OB = mfma32(v1, pB1, OB),   v1 = ldsr16(c2p + (TN)), pB2[0] = pk(a0, a1), pB2[1] = pk(a2, a3), e, SB, 12) }
#define NOP (void)0
#define TL(sl, t, H0, H1, H2) TILE((sl) * STEP_B + (t) * TILE_B, (sl) * STEP_B + ((t) + 1) * TILE_B, v2 = ldsr16(c2p + (sl) * STEP_B + (t) * TILE_B + 2 * VB), H0, H1, H2)
#define TL6(sl, H0, H1) TL(sl, 6, H0, H1, v2n = ldsr16(c2p + (sl) * STEP_B + 7 * TILE_B + 2 * VB))
#define TL7(sl, sn, H0, H1, H2) TILE((sl) * STEP_B + 7 * TILE_B, (sn) * STEP_B, v2 = v2n, H0, H1, H2)
  unsigned woff[5];
#pragma unroll
  for (int j = 0; j < 5; j++) { const int c = j * 64 + lane, rho = (c * 6554) >> 16, cc = c - rho * 10; const int k = (rho & ~12) | ((rho & 4) << 1) | ((rho & 8) >> 1); woff[j] = k * 1280 + cc * 16 + (4 - j) * 1024; }
#define WOG(wo, gr, j, dst) do { if ((j) == 4) WOP(wo, gr, 4, dst); else { if ((j) == 0) dmag_open(lds0 + (dst)); dmag<(j)>((wo) + C0 + (gr) * 32 * 640 - 2048, woff[j]); } } while (0)
#define WOP(wo, gr, j, dst) dma16s((wo) + C0 + (gr) * 32 * 640 - (4 - (j)) * 512, woff[j], lds0 + (dst) + (j) * 1024)
#define WA(gr, j) WOG(woA, gr, j, L_WO + (gr) * 5120)
  const u16* wxs = w < 4 ? woA + (16 + w3) * 32 * 640 : woB + (w < 7 ? 17 + w3 : 19) * 32 * 640;
  const int wxd = w < 4 ? L_WO + (16 + w3) * 5120 : 145408 + (w < 7 ? w3 : 2) * 5120;
#define WX(j) WOG(wxs, 0, j, wxd)
#define PRIO(n) __builtin_amdgcn_s_setprio(n)
  const unsigned fo = (rt * 8 + hq) * OT_B + lane * 16;
#define FILL(j) do { const int n_ = w * 3 + ((j) >> 1); ST16(ONES, (((j) & 1) ? WS_OB : WS_OA) + fo + (n_ < 20 ? n_ : 19) * 1024); } while (0)
#define CLR_VM() __builtin_amdgcn_s_waitcnt(0x0F70)
  const bool red = !role && w == 4;
  f32x4 part[16]; u64 fp0, fp1;
  auto red_load = [&]() {
    asm volatile("" : "+v"(fp0), "+v"(fp1));
    if (!__all((fp0 == tag) & (fp1 == tag))) wait_flags<128>(ws, WS_FPART + hq * 1024, tag, lane);
    asm volatile("" ::: "memory");
#pragma unroll
    for (int j = 0; j < 16; j++) part[j] = __builtin_bit_cast(f32x4, LD16(WS_PART + (hq * 16 + j) * PART_B + (rt * 60 + (lane < 60 ? lane : 59)) * 16));
  };
  auto red_store = [&]() {
    f32x4 a = ((part[0] + part[1]) + (part[2] + part[3])) + ((part[4] + part[5]) + (part[6] + part[7]));
    a += ((part[8] + part[9]) + (part[10] + part[11])) + ((part[12] + part[13]) + (part[14] + part[15]));
    const int hd = rt >> 2, u = (rt & 3) * 60 + lane, hi = u >= 160, fq = hi ? (u - 160) >> 3 : u >> 4, di = hi ? u & 7 : u & 15;
    if (lane < 60) ST8(((u32x2){pk(a[0], a[1]), pk(a[2], a[3])}), WS_KVR + hq * KVR_B + (((hd >> 1) * 2 + hi) * 5 + (fq >> 1)) * 512 + ((hd & 1) * 16 + di) * 16 + (fq & 1) * 8);
  };
  u32x4 ka[10]; u64 fkr; float gta; unsigned kb;
  auto oa_load = [&]() {
    asm volatile("" : "+v"(fkr));
    if (!__all(fkr == tag)) wait_flags<16>(ws, WS_FKVR + hq * 128, tag, lane);
    asm volatile("" ::: "memory");
    kb = (((r >> 2) & 1) == h) ? WS_KVR + hq * KVR_B + w2 * 5120 + (h * 16 + (r & 3) + 4 * (r >> 3)) * 16 : 0x7FFF0000u;
    ka[0] = LD16(kb); ka[5] = LD16(kb + 2560); ka[1] = LD16(kb + 512); ka[6] = LD16(kb + 3072);
    gta = __uint_as_float(__builtin_amdgcn_raw_buffer_load_b32(wsr, WS_G + (rt * 128 + w3 * 32 + r) * 4, 0, 16));
  };
  u32x4 ja[2][3], jb[2]; f32x4 jc[3];
  const LAS char* jab = lp + L_PHK + ((w >> 1) * 128 + g * 8 + q4) * 80 + p4 * 8;
  const LAS char* jbb = lp + L_VIM + ((w >> 1) * 128 + g * 8 + q4) * 40 + (w & 1) * 32 + p4 * 8;
  auto kv_rd = [&](int ks) { jb[ks & 1] = trd2(jbb + ks * 1280, jbb + ks * 1280 + 160);
#pragma unroll
    for (int ft = 0; ft < 3; ft++) ja[ks & 1][ft] = trd2(jab + ks * 2560 + ft * 32, jab + ks * 2560 + 320 + ft * 32); };
  auto kv_mm = [&](int ks) { if ((w & 1) && l15 == 4) jb[ks & 1] = (u32x4){0x3F803F80u, 0x3F803F80u, 0x3F803F80u, 0x3F803F80u};
#pragma unroll
    for (int ft = 0; ft < 3; ft++) jc[ft] = mfma16(ja[ks & 1][ft], jb[ks & 1], ks ? jc[ft] : (f32x4){0.f, 0.f, 0.f, 0.f}); };
  auto kv_st = [&]() { const int dt = w & 1; const unsigned pb = WS_PART + (hq * 16 + rt) * PART_B + (w >> 1) * 3840 + (dt ? 2560 + g * 128 : g * 256) + l15 * 16;
#pragma unroll
    for (int ft = 0; ft < 3; ft++) if ((!dt || l15 < 8) && ft * 4 + g < 10) ST16(__builtin_bit_cast(u32x4, jc[ft]), pb + ft * (dt ? 512 : 1024)); };
  f32x16 oa0, oa1; u32x4 z0, z1, z2;
  LAS char* otb = lp + L_WO + (8 + w) * 5120;
  auto oa_mm = [&](int s) { oa0 = mfma32(ka[s], ph[s], s ? oa0 : Z); oa1 = mfma32(ka[5 + s], ph[s], s ? oa1 : Z); };
  auto oa_norm = [&]() { const float inv = __builtin_amdgcn_rcpf(oa1[4] + 1e-6f) * gta;
#pragma unroll
    for (int t = 0; t < 5; t++) { const int k0 = h * 20 + 4 * t;
      const u32x2 v = t < 4 ? (u32x2){nz(pk(oa0[(4 * t) & 15] * inv, oa0[(4 * t + 1) & 15] * inv)), nz(pk(oa0[(4 * t + 2) & 15] * inv, oa0[(4 * t + 3) & 15] * inv))} : (u32x2){nz(pk(oa1[0] * inv, oa1[1] * inv)), nz(pk(oa1[2] * inv, oa1[3] * inv))};
      *(LAS u32x2*)(otb + (r >> 4) * 1280 + (k0 >> 3) * 256 + (k0 & 4) * 2 + l15 * 16) = v; }
    z0 = ldsr16(otb + lane * 16); z1 = ldsr16(otb + 1280 + lane * 16); z2 = ldsr16(otb + (r >> 4) * 1280 + 1024 + l15 * 16); };
  auto oa_st = [&]() { const unsigned oa = WS_OA + (rt * 8 + w3 * 2) * OT_B + (hq * 2 + w2) * 1280;
    ST16(z0, oa + lane * 16); ST16(z1, oa + OT_B + lane * 16); if (!h) ST16(z2, oa + (r >> 4) * OT_B + 1024 + l15 * 16); };
  unsigned cpk;
  PRIO(3);
  TL(0, 0, if (!role) kv_rd(0); else FILL(0), NOP, if (!role) kv_rd(1))
  TL(0, 1, if (!role) { kv_mm(0); kv_rd(2); } else FILL(1), NOP, if (!role) { kv_mm(1); kv_rd(3); ARRIVE_NW(0); })
  TL(0, 2, if (!role) kv_mm(2); else FILL(2), if (!role) kv_mm(3), if (!role) kv_st())
  TL(0, 3, NOP, NOP, cpk = __hip_atomic_load(cnt, __ATOMIC_RELAXED, __HIP_MEMORY_SCOPE_WORKGROUP))
  STAMP(14);
  if ((unsigned)__builtin_amdgcn_readfirstlane(cpk) < 8u) AWAIT(0, 8);
  asm volatile("" ::: "memory");
  TL(0, 4, NOP, NOP, if (!role) { WAIT_VM(0); set_flag(ws, WS_FPART + ((hq * 16 + rt) * 8 + w) * 8, tag, lane); } else FILL(3))
  PRIO(2);
  TL(0, 5, KX(2, 2), NOP, KW(2, 2, 0)) TL6(0, KW(2, 2, 1), KW(2, 2, 2))
  TL7(0, 1, KW(2, 2, 3), NOP, KW(2, 2, 4))
  STAMP(15);
  TL(1, 0, if (role) FILL(4), NOP, if (red) { fp0 = __hip_atomic_load((gu64*)(ws + WS_FPART) + hq * 128 + lane, RLX_AGENT); fp1 = __hip_atomic_load((gu64*)(ws + WS_FPART) + hq * 128 + 64 + lane, RLX_AGENT); })
  TL(1, 1, if (w < 4) { const int row = (w & 1) * 32 + (lane >> 1);
                      dma4s((w & 2 ? woB : woA) + C0 + ((rt & 7) * 2 + role) * 40 * 640, (row < 40 ? row : 39) * 1280 + (lane & 1) * 156, lds0 + 157696); }, NOP, if (role) FILL(5))
  TL(1, 2, NOP, NOP, if (red) red_load())
  PRIO(1);
  TL(1, 3, NOP, NOP, NOP)
  TL(1, 4, NOP, CLR_VM(), if (red) red_store())
  TL(1, 5, NOP, NOP, NOP)
  PRIO(0);
  TL6(1, if (red) { WAIT_VM(0); set_flag(ws, WS_FKVR + (hq * 16 + rt) * 8, tag, lane); }, NOP)
  if (red) WAIT_BAR(1); else WAIT_BAR(0);
  STAMP(12);
  PRIO(3);
  if (role) set_flag(ws, WS_FOA + ((rt * 8 + hq) * 8 + w) * 8, tag, lane);
  u64 foa;
  TL7(1, 2, KX(3, 0), KW(3, 0, 0), KW(3, 0, 1))
  TL(2, 0, KW(3, 0, 2), KW(3, 0, 3), KW(3, 0, 4)) TL(2, 1, WA(w, 0), WA(w, 1), WA(w, 2))
  TL(2, 2, WA(w, 3), WA(w, 4), (foa = __hip_atomic_load((gu64*)(ws + WS_FOA) + rt * 64 + lane, RLX_AGENT), fkr = __hip_atomic_load((gu64*)(ws + WS_FKVR) + hq * 16 + l15, RLX_AGENT)))
  PRIO(2);
  TL(2, 3, NOP, NOP, NOP)
  auto chk = [&]() { asm volatile("" : "+v"(foa), "+v"(fkr));
    if (!__all(foa == tag)) wait_flags<64>(ws, WS_FOA + rt * 512, tag, lane);
    asm volatile("" ::: "memory"); if (role) oa_load(); };
  TL(2, 4, chk(), if (role) { ka[2] = LD16(kb + 1024); ka[7] = LD16(kb + 3584); ka[3] = LD16(kb + 1536); }, if (role) { ka[8] = LD16(kb + 4096); ka[4] = LD16(kb + 2048); ka[9] = LD16(kb + 4608); })
  PRIO(1);
  TL(2, 5, NOP, NOP, if (role) oa_mm(0))
  PRIO(0);
  TL6(2, if (role) oa_mm(1), if (role) oa_mm(2))
  BAR_L();
  STAMP(13);
  PRIO(3);
  TL7(2, 0, if (role) oa_mm(3), { if (role) oa_mm(4); CLR_VM(); }, WX(0))
  TL(0, 0, if (role) oa_norm(), WX(1), WX(2))
  TL(0, 1, if (role) oa_st(), WX(3), WX(4))
  TL(0, 2, WA(8 + w, 0), WA(8 + w, 1), WA(8 + w, 2))
  u32x4 wbr[11];
  const char* wbs = (const char*)(woB + C0 + w * 32 * 640) - 4096;
#define WB2(j) do { wbr[j] = *(const GAS u32x4*)(wbs + woff[j] + (j) * 1024); wbr[5 + (j)] = *(const GAS u32x4*)(wbs + 8 * 32 * 1280 + woff[j] + (j) * 1024); } while (0)
  TL(0, 3, WA(8 + w, 3), WA(8 + w, 4), WB2(0))
  PRIO(2);
  TL(0, 4, WB2(1), WB2(2), WB2(3))
  const int w5 = w < 5 ? w : 4;
  const unsigned wo16 = (w5 == 0 ? woff[0] : w5 == 1 ? woff[1] : w5 == 2 ? woff[2] : w5 == 3 ? woff[3] : woff[4]) + w5 * 1024;
  ARRIVE_NW(1);
  TL(0, 5, WB2(4), NOP, wbr[10] = *(const GAS u32x4*)((const char*)(woB + C0 + 16 * 32 * 640) - 4096 + wo16))
  PRIO(1);
  const int tp = w & 1, kq = w >> 1;
  const unsigned ofr = (rt * 8 + role * 4 + tp * 2) * OT_B + kq * 5120 + lane * 16;
  u32x4 of[2][5]; float gtb;
#define OFL(u) do { _Pragma("unroll") for (int i = 0; i < 5; i++) of[u][i] = LD16(WS_OA + ofr + (u) * OT_B + i * 1024); } while (0)
  TL6(0, OFL(0), gtb = __uint_as_float(__builtin_amdgcn_raw_buffer_load_b32(wsr, WS_G + (rt * 128 + w3 * 32 + w2 * 16 + l15) * 4, 0, 16)))
  PRIO(0);
  TL7(0, 0, OFL(1), NOP, cpk = __hip_atomic_load(cnt + 1, __ATOMIC_RELAXED, __HIP_MEMORY_SCOPE_WORKGROUP))
  pB2[2] = pk(e0, e1); pB2[3] = pk(e2, e3);
  OB = mfma32(v2, pB2, OB);
#undef TL
#undef TL6
#undef TL7
#undef TILE
#undef G
  STAMP(7);
  { LAS f32x4* xb = (LAS f32x4*)(lp + (w3 >> 1) * CH_B) + (w3 & 1) * (64 * 6);
#pragma unroll
    for (int i = 0; i < 12; i++) { auto rr = __builtin_amdgcn_permlane16_swap(__float_as_uint(OA[i]), __float_as_uint(OB[i]), false, false); OA[i] = __uint_as_float(rr[0]); OB[i] = __uint_as_float(rr[1]); }
    const f32x16 snd = w2 ? OA : OB;
    if (__builtin_expect((unsigned)__builtin_amdgcn_readfirstlane(cpk) < 8u, 0)) AWAIT(1, 8);
    asm volatile("" ::: "memory");
#pragma unroll
    for (int c = 0; c < 3; c++) xb[(w2 * 3 + c) * 64 + lane] = (f32x4){snd[4 * c], snd[4 * c + 1], snd[4 * c + 2], snd[4 * c + 3]};
    asm volatile("s_waitcnt vmcnt(22) lgkmcnt(0)\n\ts_barrier" ::: "memory");
    f32x16 O = w2 ? OB : OA;
#pragma unroll
    for (int c = 0; c < 3; c++) { const f32x4 t = xb[((w2 ^ 1) * 3 + c) * 64 + lane]; O[4 * c] += t[0]; O[4 * c + 1] += t[1]; O[4 * c + 2] += t[2]; O[4 * c + 3] += t[3]; }
    float l = from_upper(O[8]);
    float amax = 0.f;
#pragma unroll
    for (int i = 0; i < 12; i++) amax += fabsf(O[i]);
    const bool good = (l >= 0x1p-30f) && (l <= 0x1p100f) && (amax <= 0x1p126f);
    if (__builtin_expect(!__all(good), 0)) {
      u32x4 q1; u32x2 q2;
#pragma unroll
      for (int i = 0; i < 4; i++) { auto rr = __builtin_amdgcn_permlane16_swap(q1A[i], q1B[i], false, false); q1[i] = w2 ? rr[1] : rr[0]; }
#pragma unroll
      for (int i = 0; i < 2; i++) { auto rr = __builtin_amdgcn_permlane16_swap(q2A[i], q2B[i], false, false); q2[i] = h ? 0u : (w2 ? rr[1] : rr[0]); }
      float m = -INFINITY;
#pragma unroll 1
      for (int t = 0; t < 64; t++) {
        f32x16 s = mfma32(LD16(WS_KV + t * TILE_B + ok1), q1, Z); s = mfma32k8(LD8(WS_KV + t * TILE_B + ok2), q2, s);
#pragma unroll
        for (int i = 0; i < 16; i++) m = fmaxf(m, s[i]);
      }
      m = half_swap_max(m);
      O = Z;
#pragma unroll 1
      for (int t = 0; t < 64; t++) {
        f32x16 s = mfma32(LD16(WS_KV + t * TILE_B + ok1), q1, Z); s = mfma32k8(LD8(WS_KV + t * TILE_B + ok2), q2, s);
#pragma unroll
        for (int i = 0; i < 16; i++) s[i] = ex2(s[i] - m);
        O = mfma32(LD16(WS_KV + t * TILE_B + ov), (u32x4){pk(s[0], s[1]), pk(s[2], s[3]), pk(s[4], s[5]), pk(s[6], s[7])}, O);
        O = mfma32(LD16(WS_KV + t * TILE_B + ov + 2 * VB), (u32x4){pk(s[8], s[9]), pk(s[10], s[11]), pk(s[12], s[13]), pk(s[14], s[15])}, O);
      }
      l = from_upper(O[8]);
    }
    const float inv = __builtin_amdgcn_rcpf(l) * (1.0f - gtb);
    LAS char* tb = lp + w2 * CH_B + 12288 + w3 * 1536;
#pragma unroll
    for (int c = 0; c < 3; c++) { const int k0 = (g & 1) * 20 + 8 * c + 4 * h;
      *(LAS u32x2*)(tb + ((c == 2 && h) ? 1280 + (g & 1) * 8 : (k0 >> 3) * 256 + (k0 & 4) * 2) + l15 * 16) = (u32x2){nz(pk(O[4 * c] * inv, O[4 * c + 1] * inv)), nz(pk(O[4 * c + 2] * inv, O[4 * c + 3] * inv))}; }
    const unsigned ob = WS_OB + (rt * 8 + w3 * 2 + w2) * OT_B + hp * 1280 + lane * 16;
    const u32x4 z0 = ldsr16(tb + lane * 16), z1 = ldsr16(tb + 1024 + l15 * 16);
    ST16(z0, ob); ST16(z1, lane < 16 ? ob + 1024 : 0x7FFF0000u);
  }
  STAMP(8);
  auto of_missing = [&]() { u32x4 mm = of[0][0];
#pragma unroll
    for (int j = 1; j < 10; j++) mm = __builtin_elementwise_max(mm, of[j / 5][j % 5]);
    return missing(mm); };
  u32x4 wf[5][5];
#pragma unroll
  for (int i = 0; i < 5; i++) { const LAS char* wb = lp + L_WO + (kq * 5 + i) * 5120 + wlane;
#pragma unroll
    for (int t = 0; t < 5; t++) wf[i][t] = trd2(wb + t * 8, wb + 1280 + t * 8); }
  SBAR();
  if (__builtin_expect(of_missing(), 0)) {
#pragma unroll 1
    for (unsigned spin = 0; spin < SPIN_CAP; ++spin) {
      asm volatile("" ::: "memory");
      OFL(0); OFL(1);
      if (!of_missing()) break;
      __builtin_amdgcn_s_sleep(1);
    }
  }
  f32x4 c[2][5];
#pragma unroll
  for (int i = 0; i < 5; i++)
#pragma unroll
    for (int t = 0; t < 5; t++) c[0][t] = mfma16(wf[i][t], of[0][i], i ? c[0][t] : (f32x4){0.f, 0.f, 0.f, 0.f});
  BAR_L();
  SBAR();
  { LAS char* d = lp + L_WO + w * 5120 + lane * 16;
    *(LAS u32x4*)(lp + L_WO + 16 * 5120 + w5 * 1024 + lane * 16) = wbr[10];
#pragma unroll
    for (int i = 0; i < 5; i++) {
      *(LAS u32x4*)(d + i * 1024) = wbr[i]; *(LAS u32x4*)(d + 40960 + i * 1024) = wbr[5 + i]; of[0][i] = LD16(WS_OB + ofr + i * 1024); SBAR();
#pragma unroll
      for (int t = 0; t < 5; t++) c[1][t] = mfma16(wf[i][t], of[1][i], i ? c[1][t] : (f32x4){0.f, 0.f, 0.f, 0.f});
      SBAR();
    } }
  STAMP(9);
  BAR_L();
#pragma unroll
  for (int i = 0; i < 5; i++) { const int gr = kq * 5 + i; const LAS char* wb = lp + (gr < 17 ? L_WO + gr * 5120 : 145408 + (gr - 17) * 5120) + wlane;
#pragma unroll
    for (int t = 0; t < 5; t++) wf[i][t] = trd2(wb + t * 8, wb + 1280 + t * 8);
    SBAR(); of[1][i] = LD16(WS_OB + ofr + OT_B + i * 1024); SBAR(); }
  ARRIVE_NW(2);
  STAMP(10);
  LAS char* xw = lp + kq * 21504 + (tp * 32 + l15) * 336 + g * 80;
#pragma unroll
  for (int u = 0; u < 2; u++) {
#pragma unroll
    for (int i = 0; i < 5; i++) {
      if (__builtin_expect(missing(of[u][i]), 0)) {
#pragma unroll 1
        for (unsigned spin = 0; spin < SPIN_CAP; ++spin) {
          asm volatile("" ::: "memory");
#pragma unroll
          for (int j = u * 5 + i; j < 10; j++) of[j / 5][j % 5] = LD16(WS_OB + ofr + (j / 5) * OT_B + (j % 5) * 1024);
          if (!missing(of[u][i])) break;
          __builtin_amdgcn_s_sleep(1);
        }
      }
#pragma unroll
      for (int t = 0; t < 5; t++) c[u][t] = mfma16(wf[i][t], of[u][i], c[u][t]);
      if (u == 0 && i == 3) cpk = __hip_atomic_load(cnt + 2, __ATOMIC_RELAXED, __HIP_MEMORY_SCOPE_WORKGROUP);
      if (u == 1) { if (i == 0) { if (__builtin_expect((unsigned)__builtin_amdgcn_readfirstlane(cpk) < 8u, 0)) AWAIT(2, 8); asm volatile("" ::: "memory"); }
        *(LAS f32x4*)(xw + i * 16) = c[0][i]; }
      SBAR();
    }
  }
#pragma unroll
  for (int t = 0; t < 5; t++) *(LAS f32x4*)(xw + 16 * 336 + t * 16) = c[1][t];
  BAR_L();
  { const __amdgpu_buffer_rsrc_t orr = __builtin_amdgcn_make_buffer_rsrc(out, 0, 2048 * DM * 2, 0x00020000);
#pragma unroll
    for (int j = 0; j < 3; j++) { const int n = tid + 512 * j, nn = n < 1280 ? n : 0, row = (nn * 3277) >> 16, cq = nn - row * 20;
      const LAS f32x4* p = (const LAS f32x4*)(lp + row * 336 + cq * 16);
      const f32x4 z = (p[0] + p[1344]) + (p[2688] + p[4032]);
      __builtin_amdgcn_raw_buffer_store_b64((u32x2){pk(z[0], z[1]), pk(z[2], z[3])}, orr, n < 1280 ? ((rt * 128 + role * 64 + row) * DM + C0 + cq * 4) * 2 : 0x7FFF0000u, 0, 0); }
  }
  STAMP(11);
}

__global__ __launch_bounds__(512, 2) void fused(const u16* __restrict__ x, const u16* __restrict__ wqkv, const u16* __restrict__ wphi, const u16* __restrict__ woA,
                                                const u16* __restrict__ wq, const u16* __restrict__ wk, const u16* __restrict__ wv, const u16* __restrict__ woB,
                                                const u16* __restrict__ gatew, u16* __restrict__ out, char* ws) {
  __shared__ __attribute__((aligned(16))) char lds[L_TOTAL];
  if ((blockIdx.x >> 6) & 1) body<1>(x, wqkv, wphi, woA, wq, wv, woB, gatew, out, ws, (LAS char*)lds);
  else body<0>(x, wqkv, wphi, woA, wq, wk, woB, gatew, out, ws, (LAS char*)lds);
}
}

extern "C" void kernel_launch(void* const* d_in, const int* in_sizes, int n_in, void* d_out, int out_size, void* d_ws, size_t ws_size, hipStream_t stream) {
  using namespace gm;
  fused<<<256, 512, 0, stream>>>((const u16*)d_in[0], (const u16*)d_in[1], (const u16*)d_in[2], (const u16*)d_in[3], (const u16*)d_in[5], (const u16*)d_in[6],
                                 (const u16*)d_in[7], (const u16*)d_in[8], (const u16*)d_in[9], (u16*)d_out, (char*)d_ws);
}
```

```cpp
#include <hip/hip_runtime.h>
#include <hip/hip_bf16.h>
#include <stdint.h>

namespace gm {
typedef float f32x16 __attribute__((ext_vector_type(16)));
typedef float f32x4 __attribute__((ext_vector_type(4)));
typedef float f32x2 __attribute__((ext_vector_type(2)));
typedef short s16x4 __attribute__((ext_vector_type(4)));
typedef __bf16 b16x2 __attribute__((ext_vector_type(2)));
typedef __bf16 b16x8 __attribute__((ext_vector_type(8)));
typedef unsigned u32x4 __attribute__((ext_vector_type(4)));
typedef unsigned u32x2 __attribute__((ext_vector_type(2)));
typedef unsigned long long u64;
typedef unsigned short u16;
#define DI __device__ __forceinline__
#define LAS __attribute__((address_space(3)))
#define GAS __attribute__((address_space(1)))
typedef GAS u64 gu64;

constexpr int DM = 640;
constexpr unsigned WS_EPOCH = 0, WS_FKV = 2048, WS_FKVR = 4096, WS_FQ = 8192, WS_FPART = 16384, WS_FOA = 24576, WS_G = 57344;
constexpr unsigned WS_KVR = 8912896, KVR_B = 10240, WS_KV = 131072, WS_QX = 327680, QX_B = 10240, WS_PART = 1703936, PART_B = 15360;
constexpr unsigned WS_OA = 3670016, WS_OB = 6291456, WS_END = 8994816, OT_B = 20480;
constexpr int VB = 352, TILE_B = 1280 + 4 * VB, CH_B = TILE_B * 8, STEP_B = 2 * CH_B;
constexpr int STAGE_B = 36864, ST_W1 = 10240, ST_X = 20480;
constexpr int L_BUF = 43008, L_P0W = 0, L_P0X0 = 25600, L_P0X1 = 153600, L_RED = 158720, L_RED1 = 40960;
constexpr int L_HO = 86016, L_QL = 96256, L_VIM = 96256, L_PHK = 116736, L_WPHI = 158592, L_ZERO = 161792, L_CNT = 163712, L_TOTAL = 163840;
constexpr int L_WO = 43008;
constexpr float LOG2E = 1.4426950408889634f;
constexpr float QSCALE = 0.22360679774997896f * LOG2E;
constexpr unsigned SPIN_CAP = 20000, LSPIN_CAP = 400000;

DI unsigned pk(float a, float b) { f32x2 v = {a, b}; b16x2 r = __builtin_convertvector(v, b16x2); return __builtin_bit_cast(unsigned, r); }
DI f32x16 mfma32(u32x4 a, u32x4 b, f32x16 c) { return __builtin_amdgcn_mfma_f32_32x32x16_bf16(__builtin_bit_cast(b16x8, a), __builtin_bit_cast(b16x8, b), c, 0, 0, 0); }
DI f32x16 mfma32k8(u32x2 a, u32x2 b, f32x16 c) { return __builtin_amdgcn_mfma_f32_32x32x8bf16_1k(__builtin_bit_cast(s16x4, a), __builtin_bit_cast(s16x4, b), c, 0, 0, 0); }
DI f32x4 mfma16(u32x4 a, u32x4 b, f32x4 c) { return __builtin_amdgcn_mfma_f32_16x16x32_bf16(__builtin_bit_cast(b16x8, a), __builtin_bit_cast(b16x8, b), c, 0, 0, 0); }
DI u32x2 trd(const LAS char* p) { return __builtin_bit_cast(u32x2, __builtin_amdgcn_ds_read_tr16_b64_v4i16((LAS s16x4*)p)); }
DI u32x4 trd2(const LAS char* p0, const LAS char* p1) { u32x2 a = trd(p0), b = trd(p1); return (u32x4){a.x, a.y, b.x, b.y}; }
DI u32x4 ldsr16(const LAS char* p) { return *(const LAS u32x4*)p; }
DI u32x2 ldsr8(const LAS char* p) { return *(const LAS u32x2*)p; }
DI float ex2(float x) { return __builtin_amdgcn_exp2f(x); }
DI void dma16s(const void* base, unsigned voff, unsigned lds_base) {
  asm volatile("s_mov_b32 m0, %2\n\ts_nop 0\n\tglobal_load_lds_dwordx4 %0, %1" :: "v"(voff), "s"(base), "s"(lds_base) : "memory"); }
DI void dma16s_sc1(const void* base, unsigned voff, unsigned lds_base) {
  asm volatile("s_mov_b32 m0, %2\n\ts_nop 0\n\tglobal_load_lds_dwordx4 %0, %1 sc1" :: "v"(voff), "s"(base), "s"(lds_base) : "memory"); }
DI void dmag_open(unsigned lds_base) { asm volatile("s_mov_b32 m0, %0\n\ts_nop 0" :: "s"(lds_base) : "memory"); }
template <int J> DI void dmag(const void* base, unsigned voff) { asm volatile("global_load_lds_dwordx4 %0, %1 offset:%2" :: "v"(voff), "s"(base), "i"((J & 3) * 1024) : "memory"); }
template <int J> DI void dmag_sc1(const void* base, unsigned voff) { asm volatile("global_load_lds_dwordx4 %0, %1 offset:%2 sc1" :: "v"(voff), "s"(base), "i"((J & 3) * 1024) : "memory"); }
DI void dma4s(const void* base, unsigned voff, unsigned lds_base) {
  asm volatile("s_mov_b32 m0, %2\n\ts_nop 0\n\tglobal_load_lds_dword %0, %1" :: "v"(voff), "s"(base), "s"(lds_base) : "memory"); }
DI u32x4 ld16_async(const void* p) { u32x4 r; asm volatile("global_load_dwordx4 %0, %1, off" : "=v"(r) : "v"(p) : "memory"); return r; }
DI u32x2 ld8_sc1_async(const void* p) { u32x2 r; asm volatile("global_load_dwordx2 %0, %1, off sc1" : "=v"(r) : "v"(p) : "memory"); return r; }
#define BAR_L() asm volatile("s_waitcnt lgkmcnt(0)\n\ts_barrier" ::: "memory")
#define WAIT_BAR(N) asm volatile("s_waitcnt vmcnt(" #N ") lgkmcnt(0)\n\ts_barrier" ::: "memory")
#define WAIT_VM(N) asm volatile("s_waitcnt vmcnt(" #N ")" ::: "memory")
#define SBAR() __builtin_amdgcn_sched_barrier(0)
#define RLX_AGENT __ATOMIC_RELAXED, __HIP_MEMORY_SCOPE_AGENT
DI float half_swap_max(float m) { auto rr = __builtin_amdgcn_permlane32_swap(__float_as_uint(m), __float_as_uint(m), false, false); return fmaxf(__uint_as_float(rr[0]), __uint_as_float(rr[1])); }
DI float from_upper(float v) { auto rr = __builtin_amdgcn_permlane32_swap(__float_as_uint(v), __float_as_uint(v), false, false); return __uint_as_float(rr[1]); }

template <int NF>
DI bool flags_set(const char* ws, unsigned off, u64 tag, int lane) {
  gu64* f = (gu64*)(ws + off);
  bool ok = __hip_atomic_load(f + (NF < 64 ? (lane & (NF - 1)) : lane), RLX_AGENT) == tag;
  if (NF >= 128) ok &= __hip_atomic_load(f + 64 + lane, RLX_AGENT) == tag;
  if (NF >= 256) { ok &= __hip_atomic_load(f + 128 + lane, RLX_AGENT) == tag; ok &= __hip_atomic_load(f + 192 + lane, RLX_AGENT) == tag; }
  return ok;
}
template <int NF>
DI void wait_flags(const char* ws, unsigned off, u64 tag, int lane) {
#pragma unroll 1
  for (unsigned spin = 0; spin < SPIN_CAP; ++spin) {
    if (__all(flags_set<NF>(ws, off, tag, lane))) break;
    __builtin_amdgcn_s_sleep(2);
  }
  asm volatile("" ::: "memory");
}
DI void set_flag(char* ws, unsigned off, u64 tag, int lane) { if (lane == 0) __hip_atomic_store((gu64*)(ws + off), tag, RLX_AGENT); }
DI unsigned nz(unsigned v) { return v < 0xFFFFFFFEu ? v : 0xFFFFFFFEu; }
DI unsigned umax3(unsigned a, unsigned b, unsigned c) { unsigned m = a > b ? a : b; return m > c ? m : c; }
DI bool missing(u32x4 v) { return __any(umax3(umax3(v.x, v.y, v.z), v.w, 0u) == 0xFFFFFFFFu); }
#define ONES ((u32x4){0xFFFFFFFFu, 0xFFFFFFFFu, 0xFFFFFFFFu, 0xFFFFFFFFu})
#define ST16(v, off) __builtin_amdgcn_raw_buffer_store_b128((v), wsr, (off), 0, 16)
#define ST8(v, off) __builtin_amdgcn_raw_buffer_store_b64((v), wsr, (off), 0, 16)
#define LD16(off) __builtin_amdgcn_raw_buffer_load_b128(wsr, (off), 0, 16)
#define LD8(off) __builtin_amdgcn_raw_buffer_load_b64(wsr, (off), 0, 16)

#define STAMP(i) do { if ((i) == 0) pa_ = __builtin_amdgcn_s_memrealtime(); if ((i) == 0) pb_ = __builtin_amdgcn_s_memrealtime(); if ((i) == 11) { u64 x_ = (blockIdx.x == 37 && w == 5) ? (u64)((unsigned)(pb_ - pa_) / 1u) : 0ull; \
    while (__builtin_amdgcn_s_memrealtime() - tstart < 5000ull + x_) __builtin_amdgcn_s_sleep(2); } } while (0)

__global__ __launch_bounds__(512, 2) void fused(const u16* __restrict__ x, const u16* __restrict__ wqkv, const u16* __restrict__ wphi, const u16* __restrict__ woA,
                                                const u16* __restrict__ wq, const u16* __restrict__ wk, const u16* __restrict__ wv, const u16* __restrict__ woB,
                                                const u16* __restrict__ gatew, u16* __restrict__ out, char* ws) {
  __shared__ __attribute__((aligned(16))) char lds[L_TOTAL];
  const int tid = threadIdx.x, lane = tid & 63;
  const int w = __builtin_amdgcn_readfirstlane(tid >> 6) & 7;
  const int l15 = lane & 15, g = lane >> 4, q4 = l15 >> 2, p4 = lane & 3, r = lane & 31, h = lane >> 5;
  const int bx = blockIdx.x & 7, bi = blockIdx.x >> 3;
  const int rt = (bx & 1) * 8 + (bi & 7), hp = (bx >> 1) * 4 + ((bi >> 3) & 3), hq = hp >> 1, role = hp & 1;
  const int w3 = w & 3, w2 = w >> 2;
  LAS char* lp = (LAS char*)lds;
  const unsigned lds0 = (unsigned)(uintptr_t)lds;
  const __amdgpu_buffer_rsrc_t wsr = __builtin_amdgcn_make_buffer_rsrc(ws, 0, WS_END, 0x00020000);
  LAS unsigned* cnt = (LAS unsigned*)(lp + L_CNT);
#define ARRIVE_NW(c) do { asm volatile("" ::: "memory"); if (lane == 0) __hip_atomic_fetch_add(cnt + (c), 1u, __ATOMIC_RELAXED, __HIP_MEMORY_SCOPE_WORKGROUP); asm volatile("" ::: "memory"); } while (0)
#define PEEK(c) ((unsigned)__builtin_amdgcn_readfirstlane(__hip_atomic_load(cnt + (c), __ATOMIC_RELAXED, __HIP_MEMORY_SCOPE_WORKGROUP)))
#define AWAIT(c, n) do { _Pragma("unroll 1") for (unsigned sp_ = 0; sp_ < LSPIN_CAP; ++sp_) { if (PEEK(c) >= (unsigned)(n)) break; __builtin_amdgcn_s_sleep(1); } asm volatile("" ::: "memory"); } while (0)
  const u64 tstart = __builtin_amdgcn_s_memrealtime(); u64 pa_ = 0, pb_ = 0;
  STAMP(0);
  asm volatile("" :: "s"(ws), "s"(x) : "memory");
  STAMP(4);

  const int T0 = rt * 128 + hq * 16;
  const int C0 = hq * 80;
  const int wlane = ((g >> 1) * 16 + (g & 1) * 4 + q4) * 160 + p4 * 40;
  u64 tag;
  unsigned o0[10];
#define KVP(s, slot, p) dma16s_sc1(ws + WS_KV + (s) * STEP_B + (p) * 1024, lane * 16, lds0 + (slot) * STEP_B + (p) * 1024)
#define KVL(s, slot) do { _Pragma("unroll") for (int j = 0; j < 11; j++) if (j < 10 || w3 < 2) KVP(s, slot, w3 + 4 * j); } while (0)
#define KW(s, slot, j) do { if ((j) == 4) KVP(s, slot, w * 5 + 4); else { if ((j) == 0) dmag_open(lds0 + (slot) * STEP_B + w * 5120); dmag_sc1<(j)>(ws + WS_KV + (s) * STEP_B + w * 5120, lane * 16); } } while (0)
#define KX(s, slot) do { if (w < 2) KVP(s, slot, 40 + w); } while (0)
  if (w2 == 0) {
    u32x4 gf0, gf1, gf2, gf3, gf4, wpc;
    u32x2 ep = ld8_sc1_async(ws + WS_EPOCH);
    const int lx = w3 < 3 ? L_P0X0 + w3 * 5120 : L_P0X1;
    { const u16* wsrc = (role ? wv : wk) + w3 * 3200;
#pragma unroll
      for (int j = 0; j < 5; j++) { const int n = j * 64 + lane, row = (n * 3277) >> 16, cc = n - row * 20; dma16s(x + T0 * DM + w3 * 160, row * 1280 + cc * 16, lds0 + lx + j * 1024); }
#pragma unroll
      for (int j = 0; j < 6; j++) dma16s(wsrc + j * 512, lane * 16, lds0 + L_P0W + w3 * 6400 + j * 1024);
      if (lane < 16) dma16s(wsrc + 6 * 512, lane * 16, lds0 + L_P0W + w3 * 6400 + 6 * 1024);
      const u16* gp = gatew + w3 * 160 + g * 8;
      gf0 = ld16_async(gp); gf1 = ld16_async(gp + 32); gf2 = ld16_async(gp + 64); gf3 = ld16_async(gp + 96); gf4 = ld16_async(gp + 128);
      wpc = ld16_async(wphi + hq * 1600 + (tid < 200 ? tid : 199) * 8);
    }
    asm volatile("s_waitcnt vmcnt(0)" : "+v"(gf0), "+v"(gf1), "+v"(gf2), "+v"(gf3), "+v"(gf4), "+v"(wpc), "+v"(ep) :: "memory");
    tag = (((u64)(unsigned)__builtin_amdgcn_readfirstlane(ep.y) << 32) | (unsigned)__builtin_amdgcn_readfirstlane(ep.x)) + 1ull;
    STAMP(1);
    { const LAS char* wb = lp + L_P0W + (w3 * 160 + g * 8 + q4) * 40;
      const LAS char* xb = lp + lx + l15 * 320 + g * 16;
      const bool gl = l15 == 4;
      f32x4 c0 = {}, c1 = {};
#define P0STEP(s, GF) { const u32x4 b = ldsr16(xb + (s) * 64); c0 = mfma16(trd2(wb + p4 * 8 + (s) * 1280, wb + p4 * 8 + (s) * 1280 + 160), b, c0); \
        u32x4 a = trd2(wb + 32 + (s) * 1280, wb + 32 + (s) * 1280 + 160); if (gl) a = GF; c1 = mfma16(a, b, c1); }
      P0STEP(0, gf0) P0STEP(1, gf1) P0STEP(2, gf2) P0STEP(3, gf3) P0STEP(4, gf4)
#undef P0STEP
      *(LAS f32x4*)(lp + L_RED + tid * 16) = c0;
      if (lane < 32) *(LAS f32x4*)(lp + L_RED1 + (w3 * 32 + lane) * 16) = c1; }
    STAMP(2);
    f32x16 acc[5];
#pragma unroll
    for (int b = 0; b < 5; b++) acc[b] = (f32x16){};
    const int wm = g & 1, whd = p4;
    const int wo0 = wm * ST_W1 + (8 * h + 4 * wm + q4) * 160 + whd * 40, wo1 = wm * ST_W1 + (8 * h + 4 * (wm ^ 1) + q4) * 160 + whd * 40;
    const int xo = ST_X + (w3 * 32 + r) * 128 + ((h ^ ((r >> 1) & 7)) * 16);
    u32x4 fa[5], fb[5], xa, xb;
#define KSTEP(FN, XN, s, FC, XC) { XN = ldsr16(sb + (xo ^ ((s) * 32))); \
      _Pragma("unroll") for (int j = 0; j < 5; j++) { if (i > 0 || (s) > 0) acc[j] = mfma32(FC[j], XC, acc[j]); FN[j] = trd2(sb + wo0 + (s) * 2560 + j * 8, sb + wo1 + (s) * 2560 + j * 8); SBAR(); } }
#pragma unroll
    for (int i = 0; i < 10; i++) {
      BAR_L();
      if (i == 1 && role) {
        const unsigned qb = WS_QX + (rt * 8 + hq) * QX_B + w3 * 1280 + lane * 16;
        ST16(ONES, qb); ST16(ONES, qb + 5120); if (lane < 32) ST16(ONES, qb + 1024 + (lane >> 4) * (5120 - 256)); }
      if (i == 4) { if (role) { WAIT_VM(0); set_flag(ws, WS_FQ + ((rt * 8 + hq) * 4 + w3) * 8, tag, lane); }
                    if (tid < 200) *(LAS u32x4*)(lp + L_WPHI + tid * 16) = wpc; }
      const LAS char* sb = lp + L_BUF + ((i + 2) % 3) * STAGE_B;
      KSTEP(fa, xa, 0, fb, xb) KSTEP(fb, xb, 1, fa, xa) KSTEP(fa, xa, 2, fb, xb)
      { xb = ldsr16(sb + (xo ^ 96));
        acc[0] = mfma32(fa[0], xa, acc[0]); fb[0] = trd2(sb + wo0 + 7680, sb + wo1 + 7680); fb[1] = trd2(sb + wo0 + 7688, sb + wo1 + 7688); SBAR();
        acc[1] = mfma32(fa[1], xa, acc[1]); fb[2] = trd2(sb + wo0 + 7696, sb + wo1 + 7696); fb[3] = trd2(sb + wo0 + 7704, sb + wo1 + 7704); SBAR();
        acc[2] = mfma32(fa[2], xa, acc[2]); fb[4] = trd2(sb + wo0 + 7712, sb + wo1 + 7712); SBAR();
        acc[3] = mfma32(fa[3], xa, acc[3]); acc[4] = mfma32(fa[4], xa, acc[4]); SBAR(); }
    }
#pragma unroll
    for (int j = 0; j < 5; j++) acc[j] = mfma32(fb[j], xb, acc[j]);
#undef KSTEP
    STAMP(3);
#define PK2(t, i, sc) pk(acc[t][i] * (sc), acc[t][(i) + 1] * (sc))
#define ROW(i, sc) const u32x4 ra = {PK2(0, i, sc), PK2(0, (i) + 2, sc), PK2(1, i, sc), PK2(1, (i) + 2, sc)}, rb = {PK2(2, i, sc), PK2(2, (i) + 2, sc), PK2(3, i, sc), PK2(3, (i) + 2, sc)}; const u32x2 rc = {PK2(4, i, sc), PK2(4, (i) + 2, sc)}
    { ROW(4, 1.0f);
      *(LAS u32x4*)(lp + L_HO + tid * 16) = ra; *(LAS u32x4*)(lp + L_HO + 4096 + tid * 16) = rb; *(LAS u32x2*)(lp + L_HO + 8192 + tid * 8) = rc; }
    BAR_L();
    const int qfo = (h * 4 + w3) * 1280;
    if (role) {
      { ROW(8, QSCALE); const unsigned qb = WS_QX + (rt * 8 + hq) * QX_B + qfo;
        ST16(((u32x4){nz(ra.x), nz(ra.y), nz(ra.z), nz(ra.w)}), qb + r * 16); ST16(((u32x4){nz(rb.x), nz(rb.y), nz(rb.z), nz(rb.w)}), qb + 512 + r * 16); ST8(((u32x2){nz(rc.x), nz(rc.y)}), qb + 1024 + r * 8); }
      { ROW(12, QSCALE); LAS char* d = lp + L_QL + qfo;
        *(LAS u32x4*)(d + r * 16) = ra; *(LAS u32x4*)(d + 512 + r * 16) = rb; *(LAS u32x2*)(d + 1024 + r * 8) = rc; }
    } else { LAS char* d = lp + L_VIM + (h * 128 + w3 * 32 + r) * 40;
#pragma unroll
      for (int t = 0; t < 5; t++) { *(LAS u32x2*)(d + t * 8) = (u32x2){PK2(t, 8, 1.0f), PK2(t, 10, 1.0f)}; *(LAS u32x2*)(d + 10240 + t * 8) = (u32x2){PK2(t, 12, 1.0f), PK2(t, 14, 1.0f)}; } }
#pragma unroll
    for (int t = 0; t < 5; t++) { o0[2 * t] = PK2(t, 0, 1.0f); o0[2 * t + 1] = PK2(t, 2, 1.0f); }
#undef ROW
#undef PK2
  } else {
    u32x2 ep = ld8_sc1_async(ws + WS_EPOCH);
    const bool second = w3 >> 1;
    const unsigned rs = (second && role) ? 1280u : 3840u;
    const char* wsrc = (second ? (role ? (const char*)(wq + C0) : (const char*)(wqkv + 1280 + C0)) : (const char*)(wqkv + (role ? 0 : 640) + C0)) + (w3 & 1) * 32 * rs;
    const char* xsrc = (const char*)(x + (rt * 128 + w3 * 32) * DM);
    int rot = (bi & 7) + 3 * ((bi >> 3) & 3); rot = rot >= 10 ? rot - 10 : rot;
    unsigned so[5], xoff[2];
#pragma unroll
    for (int j = 0; j < 5; j++) { const int c = j * 64 + lane, rho = (c * 6554) >> 16, cc = c - rho * 10; so[j] = (rho ^ (second ? 4 : 0)) * rs + cc * 16; }
#pragma unroll
    for (int j = 0; j < 2; j++) { const int row = j * 8 + (lane >> 3); xoff[j] = row * 1280 + (((lane & 7) ^ ((row >> 1) & 7)) * 16); }
    int bsel = 2, kc = rot;
    auto stage = [&]() {
      const unsigned lb = lds0 + L_BUF + bsel * STAGE_B;
      const char* bw = wsrc + kc * 64 * rs; const char* bxp = xsrc + kc * 128;
#pragma unroll
      for (int j = 0; j < 5; j++) { dma16s(bw, so[j], lb + (w3 * 5 + j) * 1024); if (j < 4) dma16s(bxp + (j >> 1) * 16 * 1280, xoff[j & 1], lb + ST_X + (w3 * 4 + j) * 1024); }
      bsel = bsel == 2 ? 0 : bsel + 1; kc = kc == 9 ? 0 : kc + 1;
    };
    STAMP(14); stage(); STAMP(15); stage();
    asm volatile("s_waitcnt vmcnt(9)" : "+v"(ep) :: "memory");
    tag = (((u64)(unsigned)__builtin_amdgcn_readfirstlane(ep.y) << 32) | (unsigned)__builtin_amdgcn_readfirstlane(ep.x)) + 1ull;
    STAMP(1); STAMP(2);
#pragma unroll 1
    for (int i = 0; i < 8; i++) {
      WAIT_BAR(9);
      stage();
      if (i == 0 && w3 == 0) {
        const unsigned tb = WS_KV + (T0 >> 5) * TILE_B; const int s16 = hq & 1;
        if (!role) {
          const LAS f32x4* rp = (const LAS f32x4*)(lp + (h ? L_RED1 : L_RED)) + (h ? (g & 1) * 16 : g * 32) + l15;
          const int st = h ? 32 : 64, o2 = h ? 0 : 16;
          const f32x4 a = (rp[0] + rp[st]) + (rp[2 * st] + rp[3 * st]), b = (rp[o2] + rp[st + o2]) + (rp[2 * st + o2] + rp[3 * st + o2]);
          const u32x4 v = {pk(a[0], a[1]), pk(a[2], a[3]), pk(b[0], b[1]), pk(b[2], b[3])};
          if (lane < 32) ST16(v, tb + (g * 32 + s16 * 16 + l15) * 16);
          else if (lane < 48) ST8(((u32x2){v.x, v.y}), tb + 1024 + (s16 * 16 + l15) * 8);
          else { const float gv = 1.0f / (1.0f + __expf(-a[0])); __builtin_amdgcn_raw_buffer_store_b32(__float_as_uint(gv), wsr, WS_G + (T0 + l15) * 4, 0, 16); }
        } else if (lane < 44) {
          const int hh = lane >= 22, d = lane - hh * 22, dd = d < 20 ? d : 0, st = dd >= 16 ? 128 : 256;
          const LAS float* rp = (const LAS float*)(lp + (dd >= 16 ? L_RED1 : L_RED)) + (((dd & 15) >> 2) * 16 + 4 * hh) * 4 + (dd & 3);
          float e[8];
#pragma unroll
          for (int j = 0; j < 8; j++) { const int tk = (8 * (j >> 2) + (j & 3)) * 4; e[j] = (rp[tk] + rp[tk + st]) + (rp[tk + 2 * st] + rp[tk + 3 * st]); }
          u32x4 v = {pk(e[0], e[1]), pk(e[2], e[3]), pk(e[4], e[5]), pk(e[6], e[7])};
          if (d == 20) v = (u32x4){0x3F803F80u, 0x3F803F80u, 0x3F803F80u, 0x3F803F80u};
          if (d == 21) v = (u32x4){0u, 0u, 0u, 0u};
          ST16(v, tb + 1280 + (s16 * 2 + hh) * VB + d * 16);
        }
      }
      if (i == 2) { if (w3 == 0) set_flag(ws, WS_FKV + blockIdx.x * 8, tag, lane);
                    if (w3 >= 2) *(LAS u32x4*)(lp + L_ZERO + (tid - 384) * 16) = (u32x4){0u, 0u, 0u, 0u}; }
    }
    u64 fa, fb, fc, fd, fe;
    { gu64* f = (gu64*)(ws + WS_FKV) + lane;
      fa = __hip_atomic_load(f, RLX_AGENT); fb = __hip_atomic_load(f + 64, RLX_AGENT); fc = __hip_atomic_load(f + 128, RLX_AGENT); fd = __hip_atomic_load(f + 192, RLX_AGENT);
      fe = __hip_atomic_load((gu64*)(ws + WS_FQ) + (rt * 8 + hq) * 4 + p4, RLX_AGENT); }
    WAIT_BAR(14);
    asm volatile("" : "+v"(fa), "+v"(fb), "+v"(fc), "+v"(fd), "+v"(fe));
    const bool kvok = __all((fa == tag) & (fb == tag) & (fc == tag) & (fd == tag) & (fe == tag));
    asm volatile("" ::: "memory");
    BAR_L();
    if (!kvok) { wait_flags<256>(ws, WS_FKV, tag, lane); wait_flags<4>(ws, WS_FQ + (rt * 8 + hq) * 32, tag, lane); }
    KVL(0, 0);
    if (blockIdx.x == 0 && w3 == 0) set_flag(ws, WS_EPOCH, tag, lane);
    STAMP(3);
    BAR_L();
    const int ht = tid - 256;
    const u32x4 a = ldsr16(lp + L_HO + ht * 16), b = ldsr16(lp + L_HO + 4096 + ht * 16); const u32x2 c = ldsr8(lp + L_HO + 8192 + ht * 8);
    o0[0] = a.x; o0[1] = a.y; o0[2] = a.z; o0[3] = a.w; o0[4] = b.x; o0[5] = b.y; o0[6] = b.z; o0[7] = b.w; o0[8] = c.x; o0[9] = c.y;
  }
  u32x4 ph[5];
  { const LAS char* pb = ((p4 & 1) == h) ? lp + L_WPHI + (2 * w2 + h) * 800 + q4 * 40 + (2 * (g & 1) + (p4 >> 1)) * 8 : lp + L_ZERO;
    const u32x4 b0 = {o0[0], o0[1], o0[2], o0[3]}, b1 = {o0[4], o0[5], o0[6], o0[7]}, b2 = {o0[8], o0[9], 0u, 0u};
    const u32x2 c0 = trd(pb + 640), c1 = trd(pb + 672);
    f32x16 P0 = {}, P1 = {};
    P0 = mfma32(trd2(pb, pb + 160), b0, P0); P1 = mfma32(trd2(pb + 32, pb + 192), b0, P1);
    P0 = mfma32(trd2(pb + 320, pb + 480), b1, P0); P1 = mfma32(trd2(pb + 352, pb + 512), b1, P1);
    P0 = mfma32((u32x4){c0.x, c0.y, 0u, 0u}, b2, P0); P1 = mfma32((u32x4){c1.x, c1.y, 0u, 0u}, b2, P1);
    KX(1, 1); KW(1, 1, 0); SBAR();
    f32x2 p[10];
#pragma unroll
    for (int f = 0; f < 10; f++) p[f] = f < 8 ? (f32x2){P0[(2 * f) & 15], P0[(2 * f + 1) & 15]} : (f32x2){P1[(2 * f) & 3], P1[(2 * f + 1) & 3]};
    float mx = fmaxf(p[0][0], p[0][1]), mn = fminf(p[0][0], p[0][1]);
#pragma unroll
    for (int f = 1; f < 10; f++) { mx = fmaxf(fmaxf(mx, p[f][0]), p[f][1]); mn = fminf(fminf(mn, p[f][0]), p[f][1]); }
    const f32x2 ma = {-mx * LOG2E, -mx * LOG2E}, mb = {mn * LOG2E, mn * LOG2E}, ca_ = {LOG2E, LOG2E}, cb_ = {-LOG2E, -LOG2E};
    f32x2 ea[10], eb[10], sa = {0.f, 0.f}, sb = {0.f, 0.f};
#pragma unroll
    for (int f = 0; f < 10; f++) { if (f % 2 == 0 && f < 8) { SBAR(); if (f == 0) KW(1, 1, 1); if (f == 2) KW(1, 1, 2); if (f == 4) KW(1, 1, 3); if (f == 6) KW(1, 1, 4); SBAR(); }
      const f32x2 xa = __builtin_elementwise_fma(p[f], ca_, ma), xb = __builtin_elementwise_fma(p[f], cb_, mb);
      ea[f] = (f32x2){ex2(xa[0]), ex2(xa[1])}; eb[f] = (f32x2){ex2(xb[0]), ex2(xb[1])}; sa += ea[f]; sb += eb[f]; }
    const float ia = __builtin_amdgcn_rcpf(sa[0] + sa[1]), ib = __builtin_amdgcn_rcpf(sb[0] + sb[1]);
    unsigned o[20];
#pragma unroll
    for (int f = 0; f < 10; f++) { const f32x2 ya = ea[f] * ia, yb = eb[f] * ib; o[f] = pk(ya[0], ya[1]); o[10 + f] = pk(yb[0], yb[1]); }
#pragma unroll
    for (int c = 0; c < 5; c++) ph[c] = (u32x4){o[4 * c], o[4 * c + 1], o[4 * c + 2], o[4 * c + 3]};
    asm volatile("" : "+v"(ph[0]), "+v"(ph[1]), "+v"(ph[2]), "+v"(ph[3]), "+v"(ph[4]));
  }
  u32x4 q1A, q1B; u32x2 q2A, q2B;
  if (role) {
    WAIT_BAR(0);
    STAMP(5);
    const LAS char* qa = lp + L_QL + w3 * 1280;
    q1A = ldsr16(qa + lane * 16); q1B = ldsr16(qa + 5120 + lane * 16); q2A = ldsr8(qa + 1024 + r * 8); q2B = ldsr8(qa + 6144 + r * 8);
    ARRIVE_NW(0);
  } else {
    const unsigned qb = WS_QX + (rt * 8 + hq) * QX_B + w3 * 1280;
    q1A = LD16(qb + lane * 16); q1B = LD16(qb + 5120 + lane * 16); q2A = LD8(qb + 1024 + r * 8); q2B = LD8(qb + 6144 + r * 8);
    { LAS char* d = lp + L_PHK + ((2 * w2 + h) * 128 + w3 * 32 + r) * 80;
#pragma unroll
      for (int c = 0; c < 5; c++) *(LAS u32x4*)(d + c * 16) = ph[c]; }
    WAIT_BAR(4);
    STAMP(5);
#pragma unroll 1
    for (unsigned spin = 0; spin < SPIN_CAP; ++spin) {
      if (!(missing(q1A) | missing(q1B) | missing((u32x4){q2A.x, q2A.y, q2B.x, q2B.y}))) break;
      __builtin_amdgcn_s_sleep(1);
      asm volatile("" ::: "memory");
      q1A = LD16(qb + lane * 16); q1B = LD16(qb + 5120 + lane * 16); q2A = LD8(qb + 1024 + r * 8); q2B = LD8(qb + 6144 + r * 8);
    }
  }
  STAMP(6);
  const int vr = r <= 21 ? r : 21;
  const int ok1 = lane * 16, ok2 = h ? (1280 + 20 * 16) : (1024 + r * 8), ov = 1280 + h * VB + vr * 16;
  const f32x16 Z = {};
  if (h) { q2A = (u32x2){0u, 0u}; q2B = (u32x2){0u, 0u}; }
  { const LAS char* t0 = lp;
    const u32x4 k1 = ldsr16(t0 + ok1); const u32x2 k2 = ldsr8(t0 + ok2);
    f32x16 sa = mfma32(k1, q1A, Z); sa = mfma32k8(k2, q2A, sa);
    f32x16 sb = mfma32(k1, q1B, Z); sb = mfma32k8(k2, q2B, sb);
    float ma = sa[0], mb = sb[0];
#pragma unroll
    for (int i = 1; i < 16; i++) { ma = fmaxf(ma, sa[i]); mb = fmaxf(mb, sb[i]); }
    ma = half_swap_max(ma); mb = half_swap_max(mb);
    const float mah = __uint_as_float(__float_as_uint(ma) & 0xFFFF0000u), mbh = __uint_as_float(__float_as_uint(mb) & 0xFFFF0000u);
    if (h) { q2A.x = pk(-mah, mah - ma); q2B.x = pk(-mbh, mbh - mb); } }
  f32x16 OA = {}, OB = {}, SA, SB;
  u32x4 k1, v1, v2, v2n, pA1, pA2, pB1, pB2; u32x2 k2;
  float e0, e1, e2, e3;
  const LAS char* c0p = lp + w2 * CH_B + ok1; const LAS char* c1p = lp + w2 * CH_B + ok2; const LAS char* c2p = lp + w2 * CH_B + ov;
  k1 = ldsr16(c0p); k2 = ldsr8(c1p);
  SA = mfma32(k1, q1A, Z); SA = mfma32k8(k2, q2A, SA);
  v1 = ldsr16(c2p); v2 = v1;
  e0 = e1 = e2 = e3 = 0.f; pB2 = (u32x4){0u, 0u, 0u, 0u};
#define G(MF, LD, C0_, C1_, X, S_, i) MF; LD; SBAR(); C0_; C1_; SBAR(); X##0 = ex2(S_[i]); X##1 = ex2(S_[i + 1]); X##2 = ex2(S_[i + 2]); X##3 = ex2(S_[i + 3]); asm volatile("" : "+v"(X##0), "+v"(X##1), "+v"(X##2), "+v"(X##3)); SBAR();
#define TILE(TB, TN, V2, H0, H1, H2) { float a0, a1, a2, a3; \
    G(SB = mfma32(k1, q1B, Z),    k1 = ldsr16(c0p + (TN)), pB2[2] = pk(e0, e1), pB2[3] = pk(e2, e3), a, SA, 0) \
    G(OB = mfma32(v2, pB2, OB),   V2,                      pA1[0] = pk(a0, a1), pA1[1] = pk(a2, a3), e, SA, 4) \
    G(SB = mfma32k8(k2, q2B, SB), k2 = ldsr8(c1p + (TN)),  pA1[2] = pk(e0, e1), pA1[3] = pk(e2, e3), a, SA, 8) \
    G(OA = mfma32(v1, pA1, OA),   H0,                      pA2[0] = pk(a0, a1), pA2[1] = pk(a2, a3), e, SA, 12) \
    G(SA = mfma32(k1, q1A, Z),    H1,                      pA2[2] = pk(e0, e1), pA2[3] = pk(e2, e3), a, SB, 0) \
    G(OA = mfma32(v2, pA2, OA),   H2,                      pB1[0] = pk(a0, a1), pB1[1] = pk(a2, a3), e, SB, 4) \
    G(SA = mfma32k8(k2, q2A, SA), (void)0,                 pB1[2] = pk(e0, e1), pB1[3] = pk(e2, e3), a, SB, 8) \
    G(OB = mfma32(v1, pB1, OB),   v1 = ldsr16(c2p + (TN)), pB2[0] = pk(a0, a1), pB2[1] = pk(a2, a3), e, SB, 12) }
#define NOP (void)0
#define TL(sl, t, H0, H1, H2) TILE((sl) * STEP_B + (t) * TILE_B, (sl) * STEP_B + ((t) + 1) * TILE_B, v2 = ldsr16(c2p + (sl) * STEP_B + (t) * TILE_B + 2 * VB), H0, H1, H2)
#define TL6(sl, H0, H1) TL(sl, 6, H0, H1, v2n = ldsr16(c2p + (sl) * STEP_B + 7 * TILE_B + 2 * VB))
#define TL7(sl, sn, H0, H1, H2) TILE((sl) * STEP_B + 7 * TILE_B, (sn) * STEP_B, v2 = v2n, H0, H1, H2)
  unsigned woff[5];
#pragma unroll
  for (int j = 0; j < 5; j++) { const int c = j * 64 + lane, rho = (c * 6554) >> 16, cc = c - rho * 10; const int k = (rho & ~12) | ((rho & 4) << 1) | ((rho & 8) >> 1); woff[j] = k * 1280 + cc * 16 + (4 - j) * 1024; }
#define WOG(wo, gr, j, dst) do { if ((j) == 4) WOP(wo, gr, 4, dst); else { if ((j) == 0) dmag_open(lds0 + (dst)); dmag<(j)>((wo) + C0 + (gr) * 32 * 640 - 2048, woff[j]); } } while (0)
#define WOP(wo, gr, j, dst) dma16s((wo) + C0 + (gr) * 32 * 640 - (4 - (j)) * 512, woff[j], lds0 + (dst) + (j) * 1024)
#define WA(gr, j) WOG(woA, gr, j, L_WO + (gr) * 5120)
  const u16* wxs = w < 4 ? woA + (16 + w3) * 32 * 640 : woB + (w < 7 ? 17 + w3 : 19) * 32 * 640;
  const int wxd = w < 4 ? L_WO + (16 + w3) * 5120 : 145408 + (w < 7 ? w3 : 2) * 5120;
#define WX(j) WOG(wxs, 0, j, wxd)
#define PRIO(n) __builtin_amdgcn_s_setprio(n)
  const unsigned fo = (rt * 8 + hq) * OT_B + lane * 16;
#define FILL(j) do { const int n_ = w * 3 + ((j) >> 1); ST16(ONES, (((j) & 1) ? WS_OB : WS_OA) + fo + (n_ < 20 ? n_ : 19) * 1024); } while (0)
#define CLR_VM() __builtin_amdgcn_s_waitcnt(0x0F70)
  const bool red = !role && w == 4;
  f32x4 part[16]; u64 fp0, fp1;
  auto red_load = [&]() {
    asm volatile("" : "+v"(fp0), "+v"(fp1));
    if (!__all((fp0 == tag) & (fp1 == tag))) wait_flags<128>(ws, WS_FPART + hq * 1024, tag, lane);
    asm volatile("" ::: "memory");
#pragma unroll
    for (int j = 0; j < 16; j++) part[j] = __builtin_bit_cast(f32x4, LD16(WS_PART + (hq * 16 + j) * PART_B + (rt * 60 + (lane < 60 ? lane : 59)) * 16));
  };
  auto red_store = [&]() {
    f32x4 a = ((part[0] + part[1]) + (part[2] + part[3])) + ((part[4] + part[5]) + (part[6] + part[7]));
    a += ((part[8] + part[9]) + (part[10] + part[11])) + ((part[12] + part[13]) + (part[14] + part[15]));
    const int hd = rt >> 2, u = (rt & 3) * 60 + lane, hi = u >= 160, fq = hi ? (u - 160) >> 3 : u >> 4, di = hi ? u & 7 : u & 15;
    if (lane < 60) ST8(((u32x2){pk(a[0], a[1]), pk(a[2], a[3])}), WS_KVR + hq * KVR_B + (((hd >> 1) * 2 + hi) * 5 + (fq >> 1)) * 512 + ((hd & 1) * 16 + di) * 16 + (fq & 1) * 8);
  };
  u32x4 ka[10]; u64 fkr; float gta;
  auto oa_load = [&]() {
    asm volatile("" : "+v"(fkr));
    if (!__all(fkr == tag)) wait_flags<16>(ws, WS_FKVR + hq * 128, tag, lane);
    asm volatile("" ::: "memory");
    const unsigned kb = (((r >> 2) & 1) == h) ? WS_KVR + hq * KVR_B + w2 * 5120 + (h * 16 + (r & 3) + 4 * (r >> 3)) * 16 : 0x7FFF0000u;
#pragma unroll
    for (int j = 0; j < 10; j++) ka[j] = LD16(kb + j * 512);
    gta = __uint_as_float(__builtin_amdgcn_raw_buffer_load_b32(wsr, WS_G + (rt * 128 + w3 * 32 + r) * 4, 0, 16));
  };
  u32x4 ja[2][3], jb[2]; f32x4 jc[3];
  const LAS char* jab = lp + L_PHK + ((w >> 1) * 128 + g * 8 + q4) * 80 + p4 * 8;
  const LAS char* jbb = lp + L_VIM + ((w >> 1) * 128 + g * 8 + q4) * 40 + (w & 1) * 32 + p4 * 8;
  auto kv_rd = [&](int k0) {
#pragma unroll
    for (int ks = 0; ks < 2; ks++) { jb[ks] = trd2(jbb + (k0 + ks) * 1280, jbb + (k0 + ks) * 1280 + 160);
#pragma unroll
      for (int ft = 0; ft < 3; ft++) ja[ks][ft] = trd2(jab + (k0 + ks) * 2560 + ft * 32, jab + (k0 + ks) * 2560 + 320 + ft * 32); } };
  auto kv_mm = [&](bool first) {
#pragma unroll
    for (int ks = 0; ks < 2; ks++) { if ((w & 1) && l15 == 4) jb[ks] = (u32x4){0x3F803F80u, 0x3F803F80u, 0x3F803F80u, 0x3F803F80u};
#pragma unroll
      for (int ft = 0; ft < 3; ft++) jc[ft] = mfma16(ja[ks][ft], jb[ks], (first && !ks) ? (f32x4){0.f, 0.f, 0.f, 0.f} : jc[ft]); } };
  auto kv_st = [&]() { const int dt = w & 1; const unsigned pb = WS_PART + (hq * 16 + rt) * PART_B + (w >> 1) * 3840 + (dt ? 2560 + g * 128 : g * 256) + l15 * 16;
#pragma unroll
    for (int ft = 0; ft < 3; ft++) if ((!dt || l15 < 8) && ft * 4 + g < 10) ST16(__builtin_bit_cast(u32x4, jc[ft]), pb + ft * (dt ? 512 : 1024)); };
  f32x16 oa0, oa1; u32x4 z0, z1, z2;
  LAS char* otb = lp + L_WO + (8 + w) * 5120;
  auto oa_mm = [&](int s) { oa0 = mfma32(ka[s], ph[s], s ? oa0 : Z); oa1 = mfma32(ka[5 + s], ph[s], s ? oa1 : Z); };
  auto oa_norm = [&]() { const float inv = __builtin_amdgcn_rcpf(oa1[4] + 1e-6f) * gta;
#pragma unroll
    for (int t = 0; t < 5; t++) { const int k0 = h * 20 + 4 * t;
      const u32x2 v = t < 4 ? (u32x2){nz(pk(oa0[(4 * t) & 15] * inv, oa0[(4 * t + 1) & 15] * inv)), nz(pk(oa0[(4 * t + 2) & 15] * inv, oa0[(4 * t + 3) & 15] * inv))} : (u32x2){nz(pk(oa1[0] * inv, oa1[1] * inv)), nz(pk(oa1[2] * inv, oa1[3] * inv))};
      *(LAS u32x2*)(otb + (r >> 4) * 1280 + (k0 >> 3) * 256 + (k0 & 4) * 2 + l15 * 16) = v; }
    z0 = ldsr16(otb + lane * 16); z1 = ldsr16(otb + 1280 + lane * 16); z2 = ldsr16(otb + (r >> 4) * 1280 + 1024 + l15 * 16); };
  auto oa_st = [&]() { const unsigned oa = WS_OA + (rt * 8 + w3 * 2) * OT_B + (hq * 2 + w2) * 1280;
    ST16(z0, oa + lane * 16); ST16(z1, oa + OT_B + lane * 16); if (!h) ST16(z2, oa + (r >> 4) * OT_B + 1024 + l15 * 16); };
  unsigned cpk;
  PRIO(3);
  TL(0, 0, if (!role) kv_rd(0); else FILL(0), NOP, NOP)
  TL(0, 1, if (!role) { kv_mm(true); kv_rd(2); ARRIVE_NW(0); } else FILL(1), NOP, NOP)
  TL(0, 2, if (!role) kv_mm(false); else FILL(2), NOP, if (!role) kv_st())
  TL(0, 3, NOP, NOP, cpk = __hip_atomic_load(cnt, __ATOMIC_RELAXED, __HIP_MEMORY_SCOPE_WORKGROUP))
  if ((unsigned)__builtin_amdgcn_readfirstlane(cpk) < 8u) AWAIT(0, 8);
  asm volatile("" ::: "memory");
  TL(0, 4, NOP, NOP, if (!role) { WAIT_VM(0); set_flag(ws, WS_FPART + ((hq * 16 + rt) * 8 + w) * 8, tag, lane); } else FILL(3))
  PRIO(2);
  TL(0, 5, KX(2, 2), NOP, KW(2, 2, 0)) TL6(0, KW(2, 2, 1), KW(2, 2, 2))
  TL7(0, 1, KW(2, 2, 3), NOP, KW(2, 2, 4))
  TL(1, 0, if (role) FILL(4), NOP, if (red) { fp0 = __hip_atomic_load((gu64*)(ws + WS_FPART) + hq * 128 + lane, RLX_AGENT); fp1 = __hip_atomic_load((gu64*)(ws + WS_FPART) + hq * 128 + 64 + lane, RLX_AGENT); })
  TL(1, 1, if (w < 4) { const int row = (w & 1) * 32 + (lane >> 1);
                      dma4s((w & 2 ? woB : woA) + C0 + ((rt & 7) * 2 + role) * 40 * 640, (row < 40 ? row : 39) * 1280 + (lane & 1) * 156, lds0 + 157696); }, NOP, if (role) FILL(5))
  TL(1, 2, NOP, NOP, if (red) red_load())
  PRIO(1);
  TL(1, 3, NOP, NOP, NOP)
  TL(1, 4, NOP, CLR_VM(), if (red) red_store())
  TL(1, 5, NOP, NOP, NOP)
  PRIO(0);
  TL6(1, if (red) { WAIT_VM(0); set_flag(ws, WS_FKVR + (hq * 16 + rt) * 8, tag, lane); }, NOP)
  if (red) WAIT_BAR(1); else WAIT_BAR(0);
  STAMP(12);
  PRIO(3);
  if (role) set_flag(ws, WS_FOA + ((rt * 8 + hq) * 8 + w) * 8, tag, lane);
  u64 foa;
  TL7(1, 2, KX(3, 0), KW(3, 0, 0), KW(3, 0, 1))
  TL(2, 0, KW(3, 0, 2), KW(3, 0, 3), KW(3, 0, 4)) TL(2, 1, WA(w, 0), WA(w, 1), WA(w, 2))
  TL(2, 2, WA(w, 3), WA(w, 4), (foa = __hip_atomic_load((gu64*)(ws + WS_FOA) + rt * 64 + lane, RLX_AGENT), fkr = __hip_atomic_load((gu64*)(ws + WS_FKVR) + hq * 16 + l15, RLX_AGENT)))
  PRIO(2);
  TL(2, 3, NOP, NOP, NOP)
  auto chk = [&]() { asm volatile("" : "+v"(foa), "+v"(fkr));
    if (!__all(foa == tag)) wait_flags<64>(ws, WS_FOA + rt * 512, tag, lane);
    asm volatile("" ::: "memory"); if (role) oa_load(); };
  TL(2, 4, chk(), NOP, NOP)
  PRIO(1);
  TL(2, 5, NOP, NOP, if (role) oa_mm(0))
  PRIO(0);
  TL6(2, if (role) oa_mm(1), if (role) oa_mm(2))
  BAR_L();
  STAMP(13);
  PRIO(3);
  TL7(2, 0, if (role) oa_mm(3), { if (role) oa_mm(4); CLR_VM(); }, WX(0))
  TL(0, 0, if (role) oa_norm(), WX(1), WX(2))
  TL(0, 1, if (role) oa_st(), WX(3), WX(4))
  TL(0, 2, WA(8 + w, 0), WA(8 + w, 1), WA(8 + w, 2))
  u32x4 wbr[11];
  const char* wbs = (const char*)(woB + C0 + w * 32 * 640) - 4096;
#define WB2(j) do { wbr[j] = *(const GAS u32x4*)(wbs + woff[j] + (j) * 1024); wbr[5 + (j)] = *(const GAS u32x4*)(wbs + 8 * 32 * 1280 + woff[j] + (j) * 1024); } while (0)
  TL(0, 3, WA(8 + w, 3), WA(8 + w, 4), WB2(0))
  PRIO(2);
  TL(0, 4, WB2(1), WB2(2), WB2(3))
  const int w5 = w < 5 ? w : 4;
  const unsigned wo16 = (w5 == 0 ? woff[0] : w5 == 1 ? woff[1] : w5 == 2 ? woff[2] : w5 == 3 ? woff[3] : woff[4]) + w5 * 1024;
  TL(0, 5, WB2(4), NOP, wbr[10] = *(const GAS u32x4*)((const char*)(woB + C0 + 16 * 32 * 640) - 4096 + wo16))
  PRIO(1);
  ARRIVE_NW(1);
  const int tp = w & 1, kq = w >> 1;
  const unsigned ofr = (rt * 8 + role * 4 + tp * 2) * OT_B + kq * 5120 + lane * 16;
  u32x4 of[2][5]; float gtb;
#define OFL(u) do { _Pragma("unroll") for (int i = 0; i < 5; i++) of[u][i] = LD16(WS_OA + ofr + (u) * OT_B + i * 1024); } while (0)
  TL6(0, OFL(0), gtb = __uint_as_float(__builtin_amdgcn_raw_buffer_load_b32(wsr, WS_G + (rt * 128 + w3 * 32 + w2 * 16 + l15) * 4, 0, 16)))
  PRIO(0);
  TL7(0, 0, OFL(1), NOP, NOP)
  pB2[2] = pk(e0, e1); pB2[3] = pk(e2, e3);
  OB = mfma32(v2, pB2, OB);
#undef TL
#undef TL6
#undef TL7
#undef TILE
#undef G
  STAMP(7);
  { LAS f32x4* xb = (LAS f32x4*)(lp + (w3 >> 1) * CH_B) + (w3 & 1) * (64 * 6);
#pragma unroll
    for (int i = 0; i < 12; i++) { auto rr = __builtin_amdgcn_permlane16_swap(__float_as_uint(OA[i]), __float_as_uint(OB[i]), false, false); OA[i] = __uint_as_float(rr[0]); OB[i] = __uint_as_float(rr[1]); }
    const f32x16 snd = w2 ? OA : OB;
    AWAIT(1, 8);
#pragma unroll
    for (int c = 0; c < 3; c++) xb[(w2 * 3 + c) * 64 + lane] = (f32x4){snd[4 * c], snd[4 * c + 1], snd[4 * c + 2], snd[4 * c + 3]};
    asm volatile("s_waitcnt vmcnt(22) lgkmcnt(0)\n\ts_barrier" ::: "memory");
    f32x16 O = w2 ? OB : OA;
#pragma unroll
    for (int c = 0; c < 3; c++) { const f32x4 t = xb[((w2 ^ 1) * 3 + c) * 64 + lane]; O[4 * c] += t[0]; O[4 * c + 1] += t[1]; O[4 * c + 2] += t[2]; O[4 * c + 3] += t[3]; }
    float l = from_upper(O[8]);
    float amax = 0.f;
#pragma unroll
    for (int i = 0; i < 12; i++) amax += fabsf(O[i]);
    const bool good = (l >= 0x1p-30f) && (l <= 0x1p100f) && (amax <= 0x1p126f);
    if (__builtin_expect(!__all(good), 0)) {
      u32x4 q1; u32x2 q2;
#pragma unroll
      for (int i = 0; i < 4; i++) { auto rr = __builtin_amdgcn_permlane16_swap(q1A[i], q1B[i], false, false); q1[i] = w2 ? rr[1] : rr[0]; }
#pragma unroll
      for (int i = 0; i < 2; i++) { auto rr = __builtin_amdgcn_permlane16_swap(q2A[i], q2B[i], false, false); q2[i] = h ? 0u : (w2 ? rr[1] : rr[0]); }
      float m = -INFINITY;
#pragma unroll 1
      for (int t = 0; t < 64; t++) {
        f32x16 s = mfma32(LD16(WS_KV + t * TILE_B + ok1), q1, Z); s = mfma32k8(LD8(WS_KV + t * TILE_B + ok2), q2, s);
#pragma unroll
        for (int i = 0; i < 16; i++) m = fmaxf(m, s[i]);
      }
      m = half_swap_max(m);
      O = Z;
#pragma unroll 1
      for (int t = 0; t < 64; t++) {
        f32x16 s = mfma32(LD16(WS_KV + t * TILE_B + ok1), q1, Z); s = mfma32k8(LD8(WS_KV + t * TILE_B + ok2), q2, s);
#pragma unroll
        for (int i = 0; i < 16; i++) s[i] = ex2(s[i] - m);
        O = mfma32(LD16(WS_KV + t * TILE_B + ov), (u32x4){pk(s[0], s[1]), pk(s[2], s[3]), pk(s[4], s[5]), pk(s[6], s[7])}, O);
        O = mfma32(LD16(WS_KV + t * TILE_B + ov + 2 * VB), (u32x4){pk(s[8], s[9]), pk(s[10], s[11]), pk(s[12], s[13]), pk(s[14], s[15])}, O);
      }
      l = from_upper(O[8]);
    }
    const float inv = __builtin_amdgcn_rcpf(l) * (1.0f - gtb);
    LAS char* tb = lp + w2 * CH_B + 12288 + w3 * 1536;
#pragma unroll
    for (int c = 0; c < 3; c++) { const int k0 = (g & 1) * 20 + 8 * c + 4 * h;
      *(LAS u32x2*)(tb + ((c == 2 && h) ? 1280 + (g & 1) * 8 : (k0 >> 3) * 256 + (k0 & 4) * 2) + l15 * 16) = (u32x2){nz(pk(O[4 * c] * inv, O[4 * c + 1] * inv)), nz(pk(O[4 * c + 2] * inv, O[4 * c + 3] * inv))}; }
    const unsigned ob = WS_OB + (rt * 8 + w3 * 2 + w2) * OT_B + hp * 1280 + lane * 16;
    const u32x4 z0 = ldsr16(tb + lane * 16), z1 = ldsr16(tb + 1024 + l15 * 16);
    ST16(z0, ob); ST16(z1, lane < 16 ? ob + 1024 : 0x7FFF0000u);
  }
  STAMP(8);
  auto of_missing = [&]() { u32x4 mm = of[0][0];
#pragma unroll
    for (int j = 1; j < 10; j++) mm = __builtin_elementwise_max(mm, of[j / 5][j % 5]);
    return missing(mm); };
  u32x4 wf[5][5];
#pragma unroll
  for (int i = 0; i < 5; i++) { const LAS char* wb = lp + L_WO + (kq * 5 + i) * 5120 + wlane;
#pragma unroll
    for (int t = 0; t < 5; t++) wf[i][t] = trd2(wb + t * 8, wb + 1280 + t * 8); }
  SBAR();
  if (__builtin_expect(of_missing(), 0)) {
#pragma unroll 1
    for (unsigned spin = 0; spin < SPIN_CAP; ++spin) {
      asm volatile("" ::: "memory");
      OFL(0); OFL(1);
      if (!of_missing()) break;
      __builtin_amdgcn_s_sleep(1);
    }
  }
  f32x4 c[2][5];
#pragma unroll
  for (int i = 0; i < 5; i++)
#pragma unroll
    for (int t = 0; t < 5; t++) c[0][t] = mfma16(wf[i][t], of[0][i], i ? c[0][t] : (f32x4){0.f, 0.f, 0.f, 0.f});
  BAR_L();
  SBAR();
  { LAS char* d = lp + L_WO + w * 5120 + lane * 16;
    *(LAS u32x4*)(lp + L_WO + 16 * 5120 + w5 * 1024 + lane * 16) = wbr[10];
#pragma unroll
    for (int i = 0; i < 5; i++) {
      *(LAS u32x4*)(d + i * 1024) = wbr[i]; *(LAS u32x4*)(d + 40960 + i * 1024) = wbr[5 + i]; of[0][i] = LD16(WS_OB + ofr + i * 1024); SBAR();
#pragma unroll
      for (int t = 0; t < 5; t++) c[1][t] = mfma16(wf[i][t], of[1][i], i ? c[1][t] : (f32x4){0.f, 0.f, 0.f, 0.f});
      SBAR();
    } }
  STAMP(9);
  BAR_L();
#pragma unroll
  for (int i = 0; i < 5; i++) { const int gr = kq * 5 + i; const LAS char* wb = lp + (gr < 17 ? L_WO + gr * 5120 : 145408 + (gr - 17) * 5120) + wlane;
#pragma unroll
    for (int t = 0; t < 5; t++) wf[i][t] = trd2(wb + t * 8, wb + 1280 + t * 8);
    SBAR(); of[1][i] = LD16(WS_OB + ofr + OT_B + i * 1024); SBAR(); }
  ARRIVE_NW(2);
  STAMP(10);
  LAS char* xw = lp + kq * 21504 + (tp * 32 + l15) * 336 + g * 80;
#pragma unroll
  for (int u = 0; u < 2; u++) {
#pragma unroll
    for (int i = 0; i < 5; i++) {
      if (__builtin_expect(missing(of[u][i]), 0)) {
#pragma unroll 1
        for (unsigned spin = 0; spin < SPIN_CAP; ++spin) {
          asm volatile("" ::: "memory");
#pragma unroll
          for (int j = u * 5 + i; j < 10; j++) of[j / 5][j % 5] = LD16(WS_OB + ofr + (j / 5) * OT_B + (j % 5) * 1024);
          if (!missing(of[u][i])) break;
          __builtin_amdgcn_s_sleep(1);
        }
      }
#pragma unroll
      for (int t = 0; t < 5; t++) c[u][t] = mfma16(wf[i][t], of[u][i], c[u][t]);
      if (u == 0 && i == 3) cpk = __hip_atomic_load(cnt + 2, __ATOMIC_RELAXED, __HIP_MEMORY_SCOPE_WORKGROUP);
      if (u == 1) { if (i == 0) { if (__builtin_expect((unsigned)__builtin_amdgcn_readfirstlane(cpk) < 8u, 0)) AWAIT(2, 8); asm volatile("" ::: "memory"); }
        *(LAS f32x4*)(xw + i * 16) = c[0][i]; }
      SBAR();
    }
  }
#pragma unroll
  for (int t = 0; t < 5; t++) *(LAS f32x4*)(xw + 16 * 336 + t * 16) = c[1][t];
  BAR_L();
  { const __amdgpu_buffer_rsrc_t orr = __builtin_amdgcn_make_buffer_rsrc(out, 0, 2048 * DM * 2, 0x00020000);
#pragma unroll
    for (int j = 0; j < 3; j++) { const int n = tid + 512 * j, nn = n < 1280 ? n : 0, row = (nn * 3277) >> 16, cq = nn - row * 20;
      const LAS f32x4* p = (const LAS f32x4*)(lp + row * 336 + cq * 16);
      const f32x4 z = (p[0] + p[1344]) + (p[2688] + p[4032]);
      __builtin_amdgcn_raw_buffer_store_b64((u32x2){pk(z[0], z[1]), pk(z[2], z[3])}, orr, n < 1280 ? ((rt * 128 + role * 64 + row) * DM + C0 + cq * 4) * 2 : 0x7FFF0000u, 0, 0); }
  }
  STAMP(11);
}
}

extern "C" void kernel_launch(void* const* d_in, const int* in_sizes, int n_in, void* d_out, int out_size, void* d_ws, size_t ws_size, hipStream_t stream) {
  using namespace gm;
  fused<<<256, 512, 0, stream>>>((const u16*)d_in[0], (const u16*)d_in[1], (const u16*)d_in[2], (const u16*)d_in[3], (const u16*)d_in[5], (const u16*)d_in[6],
                                 (const u16*)d_in[7], (const u16*)d_in[8], (const u16*)d_in[9], (u16*)d_out, (char*)d_ws);
}
```

```cpp
#include <hip/hip_runtime.h>
#include <hip/hip_bf16.h>
#include <stdint.h>

namespace gm {
typedef float f32x16 __attribute__((ext_vector_type(16)));
typedef float f32x4 __attribute__((ext_vector_type(4)));
typedef float f32x2 __attribute__((ext_vector_type(2)));
typedef short s16x4 __attribute__((ext_vector_type(4)));
typedef __bf16 b16x2 __attribute__((ext_vector_type(2)));
typedef __bf16 b16x8 __attribute__((ext_vector_type(8)));
typedef unsigned u32x4 __attribute__((ext_vector_type(4)));
typedef unsigned u32x2 __attribute__((ext_vector_type(2)));
typedef unsigned long long u64;
typedef unsigned short u16;
#define DI __device__ __forceinline__
#define LAS __attribute__((address_space(3)))
#define GAS __attribute__((address_space(1)))
typedef GAS u64 gu64;

constexpr int DM = 640;
constexpr unsigned WS_EPOCH = 0, WS_FKV = 2048, WS_FKVR = 4096, WS_FQ = 8192, WS_FPART = 16384, WS_FOA = 24576, WS_G = 57344;
constexpr unsigned WS_KVR = 8912896, KVR_B = 10240, WS_KV = 131072, WS_QX = 327680, QX_B = 10240, WS_PART = 1703936, PART_B = 15360;
constexpr unsigned WS_OA = 3670016, WS_OB = 6291456, WS_END = 8994816, OT_B = 20480;
constexpr int VB = 352, TILE_B = 1280 + 4 * VB, CH_B = TILE_B * 8, STEP_B = 2 * CH_B;
constexpr int STAGE_B = 36864, ST_W1 = 10240, ST_X = 20480;
constexpr int L_BUF = 43008, L_P0W = 0, L_P0X0 = 25600, L_P0X1 = 153600, L_RED = 158720, L_RED1 = 40960;
constexpr int L_HO = 86016, L_QL = 96256, L_VIM = 96256, L_PHK = 116736, L_WPHI = 158592, L_ZERO = 161792, L_CNT = 163712, L_TOTAL = 163840;
constexpr int L_WO = 43008;
constexpr float LOG2E = 1.4426950408889634f;
constexpr float QSCALE = 0.22360679774997896f * LOG2E;
constexpr unsigned SPIN_CAP = 20000, LSPIN_CAP = 400000;

DI unsigned pk(float a, float b) { f32x2 v = {a, b}; b16x2 r = __builtin_convertvector(v, b16x2); return __builtin_bit_cast(unsigned, r); }
DI f32x16 mfma32(u32x4 a, u32x4 b, f32x16 c) { return __builtin_amdgcn_mfma_f32_32x32x16_bf16(__builtin_bit_cast(b16x8, a), __builtin_bit_cast(b16x8, b), c, 0, 0, 0); }
DI f32x16 mfma32k8(u32x2 a, u32x2 b, f32x16 c) { return __builtin_amdgcn_mfma_f32_32x32x8bf16_1k(__builtin_bit_cast(s16x4, a), __builtin_bit_cast(s16x4, b), c, 0, 0, 0); }
DI f32x4 mfma16(u32x4 a, u32x4 b, f32x4 c) { return __builtin_amdgcn_mfma_f32_16x16x32_bf16(__builtin_bit_cast(b16x8, a), __builtin_bit_cast(b16x8, b), c, 0, 0, 0); }
DI u32x2 trd(const LAS char* p) { return __builtin_bit_cast(u32x2, __builtin_amdgcn_ds_read_tr16_b64_v4i16((LAS s16x4*)p)); }
DI u32x4 trd2(const LAS char* p0, const LAS char* p1) { u32x2 a = trd(p0), b = trd(p1); return (u32x4){a.x, a.y, b.x, b.y}; }
DI u32x4 ldsr16(const LAS char* p) { return *(const LAS u32x4*)p; }
DI u32x2 ldsr8(const LAS char* p) { return *(const LAS u32x2*)p; }
DI float ex2(float x) { return __builtin_amdgcn_exp2f(x); }
DI void dma16s(const void* base, unsigned voff, unsigned lds_base) {
  asm volatile("s_mov_b32 m0, %2\n\ts_nop 0\n\tglobal_load_lds_dwordx4 %0, %1" :: "v"(voff), "s"(base), "s"(lds_base) : "memory"); }
DI void dma16s_sc1(const void* base, unsigned voff, unsigned lds_base) {
  asm volatile("s_mov_b32 m0, %2\n\ts_nop 0\n\tglobal_load_lds_dwordx4 %0, %1 sc1" :: "v"(voff), "s"(base), "s"(lds_base) : "memory"); }
DI void dmag_open(unsigned lds_base) { asm volatile("s_mov_b32 m0, %0\n\ts_nop 0" :: "s"(lds_base) : "memory"); }
template <int J> DI void dmag(const void* base, unsigned voff) { asm volatile("global_load_lds_dwordx4 %0, %1 offset:%2" :: "v"(voff), "s"(base), "i"((J & 3) * 1024) : "memory"); }
template <int J> DI void dmag_sc1(const void* base, unsigned voff) { asm volatile("global_load_lds_dwordx4 %0, %1 offset:%2 sc1" :: "v"(voff), "s"(base), "i"((J & 3) * 1024) : "memory"); }
DI void dma4s(const void* base, unsigned voff, unsigned lds_base) {
  asm volatile("s_mov_b32 m0, %2\n\ts_nop 0\n\tglobal_load_lds_dword %0, %1" :: "v"(voff), "s"(base), "s"(lds_base) : "memory"); }
DI u32x4 ld16_async(const void* p) { u32x4 r; asm volatile("global_load_dwordx4 %0, %1, off" : "=v"(r) : "v"(p) : "memory"); return r; }
DI u32x2 ld8_sc1_async(const void* p) { u32x2 r; asm volatile("global_load_dwordx2 %0, %1, off sc1" : "=v"(r) : "v"(p) : "memory"); return r; }
#define BAR_L() asm volatile("s_waitcnt lgkmcnt(0)\n\ts_barrier" ::: "memory")
#define WAIT_BAR(N) asm volatile("s_waitcnt vmcnt(" #N ") lgkmcnt(0)\n\ts_barrier" ::: "memory")
#define WAIT_VM(N) asm volatile("s_waitcnt vmcnt(" #N ")" ::: "memory")
#define SBAR() __builtin_amdgcn_sched_barrier(0)
#define RLX_AGENT __ATOMIC_RELAXED, __HIP_MEMORY_SCOPE_AGENT
DI float half_swap_max(float m) { auto rr = __builtin_amdgcn_permlane32_swap(__float_as_uint(m), __float_as_uint(m), false, false); return fmaxf(__uint_as_float(rr[0]), __uint_as_float(rr[1])); }
DI float from_upper(float v) { auto rr = __builtin_amdgcn_permlane32_swap(__float_as_uint(v), __float_as_uint(v), false, false); return __uint_as_float(rr[1]); }

template <int NF>
DI bool flags_set(const char* ws, unsigned off, u64 tag, int lane) {
  gu64* f = (gu64*)(ws + off);
  bool ok = __hip_atomic_load(f + (NF < 64 ? (lane & (NF - 1)) : lane), RLX_AGENT) == tag;
  if (NF >= 128) ok &= __hip_atomic_load(f + 64 + lane, RLX_AGENT) == tag;
  if (NF >= 256) { ok &= __hip_atomic_load(f + 128 + lane, RLX_AGENT) == tag; ok &= __hip_atomic_load(f + 192 + lane, RLX_AGENT) == tag; }
  return ok;
}
template <int NF>
DI void wait_flags(const char* ws, unsigned off, u64 tag, int lane) {
#pragma unroll 1
  for (unsigned spin = 0; spin < SPIN_CAP; ++spin) {
    if (__all(flags_set<NF>(ws, off, tag, lane))) break;
    __builtin_amdgcn_s_sleep(2);
  }
  asm volatile("" ::: "memory");
}
DI void set_flag(char* ws, unsigned off, u64 tag, int lane) { if (lane == 0) __hip_atomic_store((gu64*)(ws + off), tag, RLX_AGENT); }
DI unsigned nz(unsigned v) { return v < 0xFFFFFFFEu ? v : 0xFFFFFFFEu; }
DI unsigned umax3(unsigned a, unsigned b, unsigned c) { unsigned m = a > b ? a : b; return m > c ? m : c; }
DI bool missing(u32x4 v) { return __any(umax3(umax3(v.x, v.y, v.z), v.w, 0u) == 0xFFFFFFFFu); }
#define ONES ((u32x4){0xFFFFFFFFu, 0xFFFFFFFFu, 0xFFFFFFFFu, 0xFFFFFFFFu})
#define ST16(v, off) __builtin_amdgcn_raw_buffer_store_b128((v), wsr, (off), 0, 16)
#define ST8(v, off) __builtin_amdgcn_raw_buffer_store_b64((v), wsr, (off), 0, 16)
#define LD16(off) __builtin_amdgcn_raw_buffer_load_b128(wsr, (off), 0, 16)
#define LD8(off) __builtin_amdgcn_raw_buffer_load_b64(wsr, (off), 0, 16)

#define STAMP(i) do { if ((i) == 6) pa_ = __builtin_amdgcn_s_memrealtime(); if ((i) == 7) pb_ = __builtin_amdgcn_s_memrealtime(); if ((i) == 11) { u64 x_ = (((unsigned)__builtin_amdgcn_s_getreg(20 | (3 << 11)) & 0xfu) == 0) ? (u64)((unsigned)(pb_ - pa_) / 1u) : 0ull; \
    while (__builtin_amdgcn_s_memrealtime() - tstart < 5000ull + x_) __builtin_amdgcn_s_sleep(2); } } while (0)

__global__ __launch_bounds__(512, 2) void fused(const u16* __restrict__ x, const u16* __restrict__ wqkv, const u16* __restrict__ wphi, const u16* __restrict__ woA,
                                                const u16* __restrict__ wq, const u16* __restrict__ wk, const u16* __restrict__ wv, const u16* __restrict__ woB,
                                                const u16* __restrict__ gatew, u16* __restrict__ out, char* ws) {
  __shared__ __attribute__((aligned(16))) char lds[L_TOTAL];
  const int tid = threadIdx.x, lane = tid & 63;
  const int w = __builtin_amdgcn_readfirstlane(tid >> 6) & 7;
  const int l15 = lane & 15, g = lane >> 4, q4 = l15 >> 2, p4 = lane & 3, r = lane & 31, h = lane >> 5;
  const int bx = blockIdx.x & 7, bi = blockIdx.x >> 3;
  const int rt = (bx & 1) * 8 + (bi & 7), hp = (bx >> 1) * 4 + ((bi >> 3) & 3), hq = hp >> 1, role = hp & 1;
  const int w3 = w & 3, w2 = w >> 2;
  LAS char* lp = (LAS char*)lds;
  const unsigned lds0 = (unsigned)(uintptr_t)lds;
  const __amdgpu_buffer_rsrc_t wsr = __builtin_amdgcn_make_buffer_rsrc(ws, 0, WS_END, 0x00020000);
  LAS unsigned* cnt = (LAS unsigned*)(lp + L_CNT);
#define ARRIVE_NW(c) do { asm volatile("" ::: "memory"); if (lane == 0) __hip_atomic_fetch_add(cnt + (c), 1u, __ATOMIC_RELAXED, __HIP_MEMORY_SCOPE_WORKGROUP); asm volatile("" ::: "memory"); } while (0)
#define PEEK(c) ((unsigned)__builtin_amdgcn_readfirstlane(__hip_atomic_load(cnt + (c), __ATOMIC_RELAXED, __HIP_MEMORY_SCOPE_WORKGROUP)))
#define AWAIT(c, n) do { _Pragma("unroll 1") for (unsigned sp_ = 0; sp_ < LSPIN_CAP; ++sp_) { if (PEEK(c) >= (unsigned)(n)) break; __builtin_amdgcn_s_sleep(1); } asm volatile("" ::: "memory"); } while (0)
  const u64 tstart = __builtin_amdgcn_s_memrealtime(); u64 pa_ = 0, pb_ = 0;
  STAMP(0);

  const int T0 = rt * 128 + hq * 16;
  const int C0 = hq * 80;
  const int wlane = ((g >> 1) * 16 + (g & 1) * 4 + q4) * 160 + p4 * 40;
  u64 tag;
  unsigned o0[10];
#define KVP(s, slot, p) dma16s_sc1(ws + WS_KV + (s) * STEP_B + (p) * 1024, lane * 16, lds0 + (slot) * STEP_B + (p) * 1024)
#define KVL(s, slot) do { _Pragma("unroll") for (int j = 0; j < 11; j++) if (j < 10 || w3 < 2) KVP(s, slot, w3 + 4 * j); } while (0)
#define KW(s, slot, j) do { if ((j) == 4) KVP(s, slot, w * 5 + 4); else { if ((j) == 0) dmag_open(lds0 + (slot) * STEP_B + w * 5120); dmag_sc1<(j)>(ws + WS_KV + (s) * STEP_B + w * 5120, lane * 16); } } while (0)
#define KX(s, slot) do { if (w < 2) KVP(s, slot, 40 + w); } while (0)
  if (w2 == 0) {
    u32x4 gf0, gf1, gf2, gf3, gf4, wpc;
    u32x2 ep = ld8_sc1_async(ws + WS_EPOCH);
    const int lx = w3 < 3 ? L_P0X0 + w3 * 5120 : L_P0X1;
    { const u16* wsrc = (role ? wv : wk) + w3 * 3200;
#pragma unroll
      for (int j = 0; j < 5; j++) { const int n = j * 64 + lane, row = (n * 3277) >> 16, cc = n - row * 20; dma16s(x + T0 * DM + w3 * 160, row * 1280 + cc * 16, lds0 + lx + j * 1024); }
#pragma unroll
      for (int j = 0; j < 6; j++) dma16s(wsrc + j * 512, lane * 16, lds0 + L_P0W + w3 * 6400 + j * 1024);
      if (lane < 16) dma16s(wsrc + 6 * 512, lane * 16, lds0 + L_P0W + w3 * 6400 + 6 * 1024);
      const u16* gp = gatew + w3 * 160 + g * 8;
      gf0 = ld16_async(gp); gf1 = ld16_async(gp + 32); gf2 = ld16_async(gp + 64); gf3 = ld16_async(gp + 96); gf4 = ld16_async(gp + 128);
      wpc = ld16_async(wphi + hq * 1600 + (tid < 200 ? tid : 199) * 8);
    }
    asm volatile("s_waitcnt vmcnt(0)" : "+v"(gf0), "+v"(gf1), "+v"(gf2), "+v"(gf3), "+v"(gf4), "+v"(wpc), "+v"(ep) :: "memory");
    tag = (((u64)(unsigned)__builtin_amdgcn_readfirstlane(ep.y) << 32) | (unsigned)__builtin_amdgcn_readfirstlane(ep.x)) + 1ull;
    STAMP(1);
    { const LAS char* wb = lp + L_P0W + (w3 * 160 + g * 8 + q4) * 40;
      const LAS char* xb = lp + lx + l15 * 320 + g * 16;
      const bool gl = l15 == 4;
      f32x4 c0 = {}, c1 = {};
#define P0STEP(s, GF) { const u32x4 b = ldsr16(xb + (s) * 64); c0 = mfma16(trd2(wb + p4 * 8 + (s) * 1280, wb + p4 * 8 + (s) * 1280 + 160), b, c0); \
        u32x4 a = trd2(wb + 32 + (s) * 1280, wb + 32 + (s) * 1280 + 160); if (gl) a = GF; c1 = mfma16(a, b, c1); }
      P0STEP(0, gf0) P0STEP(1, gf1) P0STEP(2, gf2) P0STEP(3, gf3) P0STEP(4, gf4)
#undef P0STEP
      *(LAS f32x4*)(lp + L_RED + tid * 16) = c0;
      if (lane < 32) *(LAS f32x4*)(lp + L_RED1 + (w3 * 32 + lane) * 16) = c1; }
    STAMP(2);
    f32x16 acc[5];
#pragma unroll
    for (int b = 0; b < 5; b++) acc[b] = (f32x16){};
    const int wm = g & 1, whd = p4;
    const int wo0 = wm * ST_W1 + (8 * h + 4 * wm + q4) * 160 + whd * 40, wo1 = wm * ST_W1 + (8 * h + 4 * (wm ^ 1) + q4) * 160 + whd * 40;
    const int xo = ST_X + (w3 * 32 + r) * 128 + ((h ^ ((r >> 1) & 7)) * 16);
    u32x4 fa[5], fb[5], xa, xb;
#define KSTEP(FN, XN, s, FC, XC) { XN = ldsr16(sb + (xo ^ ((s) * 32))); \
      _Pragma("unroll") for (int j = 0; j < 5; j++) { if (i > 0 || (s) > 0) acc[j] = mfma32(FC[j], XC, acc[j]); FN[j] = trd2(sb + wo0 + (s) * 2560 + j * 8, sb + wo1 + (s) * 2560 + j * 8); SBAR(); } }
#pragma unroll
    for (int i = 0; i < 10; i++) {
      BAR_L();
      if (i == 1 && role) {
        const unsigned qb = WS_QX + (rt * 8 + hq) * QX_B + w3 * 1280 + lane * 16;
        ST16(ONES, qb); ST16(ONES, qb + 5120); if (lane < 32) ST16(ONES, qb + 1024 + (lane >> 4) * (5120 - 256)); }
      if (i == 4) { if (role) { WAIT_VM(0); set_flag(ws, WS_FQ + ((rt * 8 + hq) * 4 + w3) * 8, tag, lane); }
                    if (tid < 200) *(LAS u32x4*)(lp + L_WPHI + tid * 16) = wpc; }
      const LAS char* sb = lp + L_BUF + ((i + 2) % 3) * STAGE_B;
      KSTEP(fa, xa, 0, fb, xb) KSTEP(fb, xb, 1, fa, xa) KSTEP(fa, xa, 2, fb, xb)
      { xb = ldsr16(sb + (xo ^ 96));
        acc[0] = mfma32(fa[0], xa, acc[0]); fb[0] = trd2(sb + wo0 + 7680, sb + wo1 + 7680); fb[1] = trd2(sb + wo0 + 7688, sb + wo1 + 7688); SBAR();
        acc[1] = mfma32(fa[1], xa, acc[1]); fb[2] = trd2(sb + wo0 + 7696, sb + wo1 + 7696); fb[3] = trd2(sb + wo0 + 7704, sb + wo1 + 7704); SBAR();
        acc[2] = mfma32(fa[2], xa, acc[2]); fb[4] = trd2(sb + wo0 + 7712, sb + wo1 + 7712); SBAR();
        acc[3] = mfma32(fa[3], xa, acc[3]); acc[4] = mfma32(fa[4], xa, acc[4]); SBAR(); }
    }
#pragma unroll
    for (int j = 0; j < 5; j++) acc[j] = mfma32(fb[j], xb, acc[j]);
#undef KSTEP
    STAMP(3);
#define PK2(t, i, sc) pk(acc[t][i] * (sc), acc[t][(i) + 1] * (sc))
#define ROW(i, sc) const u32x4 ra = {PK2(0, i, sc), PK2(0, (i) + 2, sc), PK2(1, i, sc), PK2(1, (i) + 2, sc)}, rb = {PK2(2, i, sc), PK2(2, (i) + 2, sc), PK2(3, i, sc), PK2(3, (i) + 2, sc)}; const u32x2 rc = {PK2(4, i, sc), PK2(4, (i) + 2, sc)}
    { ROW(4, 1.0f);
      *(LAS u32x4*)(lp + L_HO + tid * 16) = ra; *(LAS u32x4*)(lp + L_HO + 4096 + tid * 16) = rb; *(LAS u32x2*)(lp + L_HO + 8192 + tid * 8) = rc; }
    BAR_L();
    const int qfo = (h * 4 + w3) * 1280;
    if (role) {
      { ROW(8, QSCALE); const unsigned qb = WS_QX + (rt * 8 + hq) * QX_B + qfo;
        ST16(((u32x4){nz(ra.x), nz(ra.y), nz(ra.z), nz(ra.w)}), qb + r * 16); ST16(((u32x4){nz(rb.x), nz(rb.y), nz(rb.z), nz(rb.w)}), qb + 512 + r * 16); ST8(((u32x2){nz(rc.x), nz(rc.y)}), qb + 1024 + r * 8); }
      { ROW(12, QSCALE); LAS char* d = lp + L_QL + qfo;
        *(LAS u32x4*)(d + r * 16) = ra; *(LAS u32x4*)(d + 512 + r * 16) = rb; *(LAS u32x2*)(d + 1024 + r * 8) = rc; }
    } else { LAS char* d = lp + L_VIM + (h * 128 + w3 * 32 + r) * 40;
#pragma unroll
      for (int t = 0; t < 5; t++) { *(LAS u32x2*)(d + t * 8) = (u32x2){PK2(t, 8, 1.0f), PK2(t, 10, 1.0f)}; *(LAS u32x2*)(d + 10240 + t * 8) = (u32x2){PK2(t, 12, 1.0f), PK2(t, 14, 1.0f)}; } }
#pragma unroll
    for (int t = 0; t < 5; t++) { o0[2 * t] = PK2(t, 0, 1.0f); o0[2 * t + 1] = PK2(t, 2, 1.0f); }
#undef ROW
#undef PK2
  } else {
    u32x2 ep = ld8_sc1_async(ws + WS_EPOCH);
    const bool second = w3 >> 1;
    const unsigned rs = (second && role) ? 1280u : 3840u;
    const char* wsrc = (second ? (role ? (const char*)(wq + C0) : (const char*)(wqkv + 1280 + C0)) : (const char*)(wqkv + (role ? 0 : 640) + C0)) + (w3 & 1) * 32 * rs;
    const char* xsrc = (const char*)(x + (rt * 128 + w3 * 32) * DM);
    int rot = (bi & 7) + 3 * ((bi >> 3) & 3); rot = rot >= 10 ? rot - 10 : rot;
    unsigned so[5], xoff[2];
#pragma unroll
    for (int j = 0; j < 5; j++) { const int c = j * 64 + lane, rho = (c * 6554) >> 16, cc = c - rho * 10; so[j] = (rho ^ (second ? 4 : 0)) * rs + cc * 16; }
#pragma unroll
    for (int j = 0; j < 2; j++) { const int row = j * 8 + (lane >> 3); xoff[j] = row * 1280 + (((lane & 7) ^ ((row >> 1) & 7)) * 16); }
    int bsel = 2, kc = rot;
    auto stage = [&]() {
      const unsigned lb = lds0 + L_BUF + bsel * STAGE_B;
      const char* bw = wsrc + kc * 64 * rs; const char* bxp = xsrc + kc * 128;
#pragma unroll
      for (int j = 0; j < 5; j++) { dma16s(bw, so[j], lb + (w3 * 5 + j) * 1024); if (j < 4) dma16s(bxp + (j >> 1) * 16 * 1280, xoff[j & 1], lb + ST_X + (w3 * 4 + j) * 1024); }
      bsel = bsel == 2 ? 0 : bsel + 1; kc = kc == 9 ? 0 : kc + 1;
    };
    stage(); stage();
    asm volatile("s_waitcnt vmcnt(9)" : "+v"(ep) :: "memory");
    tag = (((u64)(unsigned)__builtin_amdgcn_readfirstlane(ep.y) << 32) | (unsigned)__builtin_amdgcn_readfirstlane(ep.x)) + 1ull;
    STAMP(1); STAMP(2);
#pragma unroll 1
    for (int i = 0; i < 8; i++) {
      WAIT_BAR(9);
      stage();
      if (i == 0 && w3 == 0) {
        const unsigned tb = WS_KV + (T0 >> 5) * TILE_B; const int s16 = hq & 1;
        if (!role) {
          const LAS f32x4* rp = (const LAS f32x4*)(lp + (h ? L_RED1 : L_RED)) + (h ? (g & 1) * 16 : g * 32) + l15;
          const int st = h ? 32 : 64, o2 = h ? 0 : 16;
          const f32x4 a = (rp[0] + rp[st]) + (rp[2 * st] + rp[3 * st]), b = (rp[o2] + rp[st + o2]) + (rp[2 * st + o2] + rp[3 * st + o2]);
          const u32x4 v = {pk(a[0], a[1]), pk(a[2], a[3]), pk(b[0], b[1]), pk(b[2], b[3])};
          if (lane < 32) ST16(v, tb + (g * 32 + s16 * 16 + l15) * 16);
          else if (lane < 48) ST8(((u32x2){v.x, v.y}), tb + 1024 + (s16 * 16 + l15) * 8);
          else { const float gv = 1.0f / (1.0f + __expf(-a[0])); __builtin_amdgcn_raw_buffer_store_b32(__float_as_uint(gv), wsr, WS_G + (T0 + l15) * 4, 0, 16); }
        } else if (lane < 44) {
          const int hh = lane >= 22, d = lane - hh * 22, dd = d < 20 ? d : 0, st = dd >= 16 ? 128 : 256;
          const LAS float* rp = (const LAS float*)(lp + (dd >= 16 ? L_RED1 : L_RED)) + (((dd & 15) >> 2) * 16 + 4 * hh) * 4 + (dd & 3);
          float e[8];
#pragma unroll
          for (int j = 0; j < 8; j++) { const int tk = (8 * (j >> 2) + (j & 3)) * 4; e[j] = (rp[tk] + rp[tk + st]) + (rp[tk + 2 * st] + rp[tk + 3 * st]); }
          u32x4 v = {pk(e[0], e[1]), pk(e[2], e[3]), pk(e[4], e[5]), pk(e[6], e[7])};
          if (d == 20) v = (u32x4){0x3F803F80u, 0x3F803F80u, 0x3F803F80u, 0x3F803F80u};
          if (d == 21) v = (u32x4){0u, 0u, 0u, 0u};
          ST16(v, tb + 1280 + (s16 * 2 + hh) * VB + d * 16);
        }
      }
      if (i == 2) { if (w3 == 0) set_flag(ws, WS_FKV + blockIdx.x * 8, tag, lane);
                    if (w3 >= 2) *(LAS u32x4*)(lp + L_ZERO + (tid - 384) * 16) = (u32x4){0u, 0u, 0u, 0u}; }
    }
    u64 fa, fb, fc, fd, fe;
    { gu64* f = (gu64*)(ws + WS_FKV) + lane;
      fa = __hip_atomic_load(f, RLX_AGENT); fb = __hip_atomic_load(f + 64, RLX_AGENT); fc = __hip_atomic_load(f + 128, RLX_AGENT); fd = __hip_atomic_load(f + 192, RLX_AGENT);
      fe = __hip_atomic_load((gu64*)(ws + WS_FQ) + (rt * 8 + hq) * 4 + p4, RLX_AGENT); }
    WAIT_BAR(14);
    asm volatile("" : "+v"(fa), "+v"(fb), "+v"(fc), "+v"(fd), "+v"(fe));
    const bool kvok = __all((fa == tag) & (fb == tag) & (fc == tag) & (fd == tag) & (fe == tag));
    asm volatile("" ::: "memory");
    BAR_L();
    if (!kvok) { wait_flags<256>(ws, WS_FKV, tag, lane); wait_flags<4>(ws, WS_FQ + (rt * 8 + hq) * 32, tag, lane); }
    KVL(0, 0);
    if (blockIdx.x == 0 && w3 == 0) set_flag(ws, WS_EPOCH, tag, lane);
    STAMP(3);
    BAR_L();
    const int ht = tid - 256;
    const u32x4 a = ldsr16(lp + L_HO + ht * 16), b = ldsr16(lp + L_HO + 4096 + ht * 16); const u32x2 c = ldsr8(lp + L_HO + 8192 + ht * 8);
    o0[0] = a.x; o0[1] = a.y; o0[2] = a.z; o0[3] = a.w; o0[4] = b.x; o0[5] = b.y; o0[6] = b.z; o0[7] = b.w; o0[8] = c.x; o0[9] = c.y;
  }
  STAMP(4);
  u32x4 ph[5];
  { const LAS char* pb = ((p4 & 1) == h) ? lp + L_WPHI + (2 * w2 + h) * 800 + q4 * 40 + (2 * (g & 1) + (p4 >> 1)) * 8 : lp + L_ZERO;
    const u32x4 b0 = {o0[0], o0[1], o0[2], o0[3]}, b1 = {o0[4], o0[5], o0[6], o0[7]}, b2 = {o0[8], o0[9], 0u, 0u};
    const u32x2 c0 = trd(pb + 640), c1 = trd(pb + 672);
    f32x16 P0 = {}, P1 = {};
    P0 = mfma32(trd2(pb, pb + 160), b0, P0); P1 = mfma32(trd2(pb + 32, pb + 192), b0, P1);
    P0 = mfma32(trd2(pb + 320, pb + 480), b1, P0); P1 = mfma32(trd2(pb + 352, pb + 512), b1, P1);
    P0 = mfma32((u32x4){c0.x, c0.y, 0u, 0u}, b2, P0); P1 = mfma32((u32x4){c1.x, c1.y, 0u, 0u}, b2, P1);
    KX(1, 1); KW(1, 1, 0); SBAR();
    f32x2 p[10];
#pragma unroll
    for (int f = 0; f < 10; f++) p[f] = f < 8 ? (f32x2){P0[(2 * f) & 15], P0[(2 * f + 1) & 15]} : (f32x2){P1[(2 * f) & 3], P1[(2 * f + 1) & 3]};
    float mx = fmaxf(p[0][0], p[0][1]), mn = fminf(p[0][0], p[0][1]);
#pragma unroll
    for (int f = 1; f < 10; f++) { mx = fmaxf(fmaxf(mx, p[f][0]), p[f][1]); mn = fminf(fminf(mn, p[f][0]), p[f][1]); }
    const f32x2 ma = {-mx * LOG2E, -mx * LOG2E}, mb = {mn * LOG2E, mn * LOG2E}, ca_ = {LOG2E, LOG2E}, cb_ = {-LOG2E, -LOG2E};
    f32x2 ea[10], eb[10], sa = {0.f, 0.f}, sb = {0.f, 0.f};
#pragma unroll
    for (int f = 0; f < 10; f++) { if (f % 2 == 0 && f < 8) { SBAR(); if (f == 0) KW(1, 1, 1); if (f == 2) KW(1, 1, 2); if (f == 4) KW(1, 1, 3); if (f == 6) KW(1, 1, 4); SBAR(); }
      const f32x2 xa = __builtin_elementwise_fma(p[f], ca_, ma), xb = __builtin_elementwise_fma(p[f], cb_, mb);
      ea[f] = (f32x2){ex2(xa[0]), ex2(xa[1])}; eb[f] = (f32x2){ex2(xb[0]), ex2(xb[1])}; sa += ea[f]; sb += eb[f]; }
    const float ia = __builtin_amdgcn_rcpf(sa[0] + sa[1]), ib = __builtin_amdgcn_rcpf(sb[0] + sb[1]);
    unsigned o[20];
#pragma unroll
    for (int f = 0; f < 10; f++) { const f32x2 ya = ea[f] * ia, yb = eb[f] * ib; o[f] = pk(ya[0], ya[1]); o[10 + f] = pk(yb[0], yb[1]); }
#pragma unroll
    for (int c = 0; c < 5; c++) ph[c] = (u32x4){o[4 * c], o[4 * c + 1], o[4 * c + 2], o[4 * c + 3]};
    asm volatile("" : "+v"(ph[0]), "+v"(ph[1]), "+v"(ph[2]), "+v"(ph[3]), "+v"(ph[4]));
  }
  u32x4 q1A, q1B; u32x2 q2A, q2B;
  if (role) {
    WAIT_BAR(0);
    STAMP(5);
    const LAS char* qa = lp + L_QL + w3 * 1280;
    q1A = ldsr16(qa + lane * 16); q1B = ldsr16(qa + 5120 + lane * 16); q2A = ldsr8(qa + 1024 + r * 8); q2B = ldsr8(qa + 6144 + r * 8);
    ARRIVE_NW(0);
  } else {
    const unsigned qb = WS_QX + (rt * 8 + hq) * QX_B + w3 * 1280;
    q1A = LD16(qb + lane * 16); q1B = LD16(qb + 5120 + lane * 16); q2A = LD8(qb + 1024 + r * 8); q2B = LD8(qb + 6144 + r * 8);
    { LAS char* d = lp + L_PHK + ((2 * w2 + h) * 128 + w3 * 32 + r) * 80;
#pragma unroll
      for (int c = 0; c < 5; c++) *(LAS u32x4*)(d + c * 16) = ph[c]; }
    WAIT_BAR(4);
    STAMP(5);
#pragma unroll 1
    for (unsigned spin = 0; spin < SPIN_CAP; ++spin) {
      if (!(missing(q1A) | missing(q1B) | missing((u32x4){q2A.x, q2A.y, q2B.x, q2B.y}))) break;
      __builtin_amdgcn_s_sleep(1);
      asm volatile("" ::: "memory");
      q1A = LD16(qb + lane * 16); q1B = LD16(qb + 5120 + lane * 16); q2A = LD8(qb + 1024 + r * 8); q2B = LD8(qb + 6144 + r * 8);
    }
  }
  STAMP(6);
  const int vr = r <= 21 ? r : 21;
  const int ok1 = lane * 16, ok2 = h ? (1280 + 20 * 16) : (1024 + r * 8), ov = 1280 + h * VB + vr * 16;
  const f32x16 Z = {};
  if (h) { q2A = (u32x2){0u, 0u}; q2B = (u32x2){0u, 0u}; }
  { const LAS char* t0 = lp;
    const u32x4 k1 = ldsr16(t0 + ok1); const u32x2 k2 = ldsr8(t0 + ok2);
    f32x16 sa = mfma32(k1, q1A, Z); sa = mfma32k8(k2, q2A, sa);
    f32x16 sb = mfma32(k1, q1B, Z); sb = mfma32k8(k2, q2B, sb);
    float ma = sa[0], mb = sb[0];
#pragma unroll
    for (int i = 1; i < 16; i++) { ma = fmaxf(ma, sa[i]); mb = fmaxf(mb, sb[i]); }
    ma = half_swap_max(ma); mb = half_swap_max(mb);
    const float mah = __uint_as_float(__float_as_uint(ma) & 0xFFFF0000u), mbh = __uint_as_float(__float_as_uint(mb) & 0xFFFF0000u);
    if (h) { q2A.x = pk(-mah, mah - ma); q2B.x = pk(-mbh, mbh - mb); } }
  f32x16 OA = {}, OB = {}, SA, SB;
  u32x4 k1, v1, v2, v2n, pA1, pA2, pB1, pB2; u32x2 k2;
  float e0, e1, e2, e3;
  const LAS char* c0p = lp + w2 * CH_B + ok1; const LAS char* c1p = lp + w2 * CH_B + ok2; const LAS char* c2p = lp + w2 * CH_B + ov;
  k1 = ldsr16(c0p); k2 = ldsr8(c1p);
  SA = mfma32(k1, q1A, Z); SA = mfma32k8(k2, q2A, SA);
  v1 = ldsr16(c2p); v2 = v1;
  e0 = e1 = e2 = e3 = 0.f; pB2 = (u32x4){0u, 0u, 0u, 0u};
#define G(MF, LD, C0_, C1_, X, S_, i) MF; LD; SBAR(); C0_; C1_; SBAR(); X##0 = ex2(S_[i]); X##1 = ex2(S_[i + 1]); X##2 = ex2(S_[i + 2]); X##3 = ex2(S_[i + 3]); asm volatile("" : "+v"(X##0), "+v"(X##1), "+v"(X##2), "+v"(X##3)); SBAR();
#define TILE(TB, TN, V2, H0, H1, H2) { float a0, a1, a2, a3; \
    G(SB = mfma32(k1, q1B, Z),    k1 = ldsr16(c0p + (TN)), pB2[2] = pk(e0, e1), pB2[3] = pk(e2, e3), a, SA, 0) \
    G(OB = mfma32(v2, pB2, OB),   V2,                      pA1[0] = pk(a0, a1), pA1[1] = pk(a2, a3), e, SA, 4) \
    G(SB = mfma32k8(k2, q2B, SB), k2 = ldsr8(c1p + (TN)),  pA1[2] = pk(e0, e1), pA1[3] = pk(e2, e3), a, SA, 8) \
    G(OA = mfma32(v1, pA1, OA),   H0,                      pA2[0] = pk(a0, a1), pA2[1] = pk(a2, a3), e, SA, 12) \
    G(SA = mfma32(k1, q1A, Z),    H1,                      pA2[2] = pk(e0, e1), pA2[3] = pk(e2, e3), a, SB, 0) \
    G(OA = mfma32(v2, pA2, OA),   H2,                      pB1[0] = pk(a0, a1), pB1[1] = pk(a2, a3), e, SB, 4) \
    G(SA = mfma32k8(k2, q2A, SA), (void)0,                 pB1[2] = pk(e0, e1), pB1[3] = pk(e2, e3), a, SB, 8) \
    G(OB = mfma32(v1, pB1, OB),   v1 = ldsr16(c2p + (TN)), pB2[0] = pk(a0, a1), pB2[1] = pk(a2, a3), e, SB, 12) }
#define NOP (void)0
#define TL(sl, t, H0, H1, H2) TILE((sl) * STEP_B + (t) * TILE_B, (sl) * STEP_B + ((t) + 1) * TILE_B, v2 = ldsr16(c2p + (sl) * STEP_B + (t) * TILE_B + 2 * VB), H0, H1, H2)
#define TL6(sl, H0, H1) TL(sl, 6, H0, H1, v2n = ldsr16(c2p + (sl) * STEP_B + 7 * TILE_B + 2 * VB))
#define TL7(sl, sn, H0, H1, H2) TILE((sl) * STEP_B + 7 * TILE_B, (sn) * STEP_B, v2 = v2n, H0, H1, H2)
  unsigned woff[5];
#pragma unroll
  for (int j = 0; j < 5; j++) { const int c = j * 64 + lane, rho = (c * 6554) >> 16, cc = c - rho * 10; const int k = (rho & ~12) | ((rho & 4) << 1) | ((rho & 8) >> 1); woff[j] = k * 1280 + cc * 16 + (4 - j) * 1024; }
#define WOG(wo, gr, j, dst) do { if ((j) == 4) WOP(wo, gr, 4, dst); else { if ((j) == 0) dmag_open(lds0 + (dst)); dmag<(j)>((wo) + C0 + (gr) * 32 * 640 - 2048, woff[j]); } } while (0)
#define WOP(wo, gr, j, dst) dma16s((wo) + C0 + (gr) * 32 * 640 - (4 - (j)) * 512, woff[j], lds0 + (dst) + (j) * 1024)
#define WA(gr, j) WOG(woA, gr, j, L_WO + (gr) * 5120)
  const u16* wxs = w < 4 ? woA + (16 + w3) * 32 * 640 : woB + (w < 7 ? 17 + w3 : 19) * 32 * 640;
  const int wxd = w < 4 ? L_WO + (16 + w3) * 5120 : 145408 + (w < 7 ? w3 : 2) * 5120;
#define WX(j) WOG(wxs, 0, j, wxd)
#define PRIO(n) __builtin_amdgcn_s_setprio(n)
  const unsigned fo = (rt * 8 + hq) * OT_B + lane * 16;
#define FILL(j) do { const int n_ = w * 3 + ((j) >> 1); ST16(ONES, (((j) & 1) ? WS_OB : WS_OA) + fo + (n_ < 20 ? n_ : 19) * 1024); } while (0)
#define CLR_VM() __builtin_amdgcn_s_waitcnt(0x0F70)
  const bool red = !role && w == 4;
  f32x4 part[16]; u64 fp0, fp1;
  auto red_load = [&]() {
    asm volatile("" : "+v"(fp0), "+v"(fp1));
    if (!__all((fp0 == tag) & (fp1 == tag))) wait_flags<128>(ws, WS_FPART + hq * 1024, tag, lane);
    asm volatile("" ::: "memory");
#pragma unroll
    for (int j = 0; j < 16; j++) part[j] = __builtin_bit_cast(f32x4, LD16(WS_PART + (hq * 16 + j) * PART_B + (rt * 60 + (lane < 60 ? lane : 59)) * 16));
  };
  auto red_store = [&]() {
    f32x4 a = ((part[0] + part[1]) + (part[2] + part[3])) + ((part[4] + part[5]) + (part[6] + part[7]));
    a += ((part[8] + part[9]) + (part[10] + part[11])) + ((part[12] + part[13]) + (part[14] + part[15]));
    const int hd = rt >> 2, u = (rt & 3) * 60 + lane, hi = u >= 160, fq = hi ? (u - 160) >> 3 : u >> 4, di = hi ? u & 7 : u & 15;
    if (lane < 60) ST8(((u32x2){pk(a[0], a[1]), pk(a[2], a[3])}), WS_KVR + hq * KVR_B + (((hd >> 1) * 2 + hi) * 5 + (fq >> 1)) * 512 + ((hd & 1) * 16 + di) * 16 + (fq & 1) * 8);
  };
  u32x4 ka[10]; u64 fkr; float gta;
  auto oa_load = [&]() {
    asm volatile("" : "+v"(fkr));
    if (!__all(fkr == tag)) wait_flags<16>(ws, WS_FKVR + hq * 128, tag, lane);
    asm volatile("" ::: "memory");
    const unsigned kb = (((r >> 2) & 1) == h) ? WS_KVR + hq * KVR_B + w2 * 5120 + (h * 16 + (r & 3) + 4 * (r >> 3)) * 16 : 0x7FFF0000u;
#pragma unroll
    for (int j = 0; j < 10; j++) ka[j] = LD16(kb + j * 512);
    gta = __uint_as_float(__builtin_amdgcn_raw_buffer_load_b32(wsr, WS_G + (rt * 128 + w3 * 32 + r) * 4, 0, 16));
  };
  u32x4 ja[2][3], jb[2]; f32x4 jc[3];
  const LAS char* jab = lp + L_PHK + ((w >> 1) * 128 + g * 8 + q4) * 80 + p4 * 8;
  const LAS char* jbb = lp + L_VIM + ((w >> 1) * 128 + g * 8 + q4) * 40 + (w & 1) * 32 + p4 * 8;
  auto kv_rd = [&](int k0) {
#pragma unroll
    for (int ks = 0; ks < 2; ks++) { jb[ks] = trd2(jbb + (k0 + ks) * 1280, jbb + (k0 + ks) * 1280 + 160);
#pragma unroll
      for (int ft = 0; ft < 3; ft++) ja[ks][ft] = trd2(jab + (k0 + ks) * 2560 + ft * 32, jab + (k0 + ks) * 2560 + 320 + ft * 32); } };
  auto kv_mm = [&](bool first) {
#pragma unroll
    for (int ks = 0; ks < 2; ks++) { if ((w & 1) && l15 == 4) jb[ks] = (u32x4){0x3F803F80u, 0x3F803F80u, 0x3F803F80u, 0x3F803F80u};
#pragma unroll
      for (int ft = 0; ft < 3; ft++) jc[ft] = mfma16(ja[ks][ft], jb[ks], (first && !ks) ? (f32x4){0.f, 0.f, 0.f, 0.f} : jc[ft]); } };
  auto kv_st = [&]() { const int dt = w & 1; const unsigned pb = WS_PART + (hq * 16 + rt) * PART_B + (w >> 1) * 3840 + (dt ? 2560 + g * 128 : g * 256) + l15 * 16;
#pragma unroll
    for (int ft = 0; ft < 3; ft++) if ((!dt || l15 < 8) && ft * 4 + g < 10) ST16(__builtin_bit_cast(u32x4, jc[ft]), pb + ft * (dt ? 512 : 1024)); };
  f32x16 oa0, oa1; u32x4 z0, z1, z2;
  LAS char* otb = lp + L_WO + (8 + w) * 5120;
  auto oa_mm = [&](int s) { oa0 = mfma32(ka[s], ph[s], s ? oa0 : Z); oa1 = mfma32(ka[5 + s], ph[s], s ? oa1 : Z); };
  auto oa_norm = [&]() { const float inv = __builtin_amdgcn_rcpf(oa1[4] + 1e-6f) * gta;
#pragma unroll
    for (int t = 0; t < 5; t++) { const int k0 = h * 20 + 4 * t;
      const u32x2 v = t < 4 ? (u32x2){nz(pk(oa0[(4 * t) & 15] * inv, oa0[(4 * t + 1) & 15] * inv)), nz(pk(oa0[(4 * t + 2) & 15] * inv, oa0[(4 * t + 3) & 15] * inv))} : (u32x2){nz(pk(oa1[0] * inv, oa1[1] * inv)), nz(pk(oa1[2] * inv, oa1[3] * inv))};
      *(LAS u32x2*)(otb + (r >> 4) * 1280 + (k0 >> 3) * 256 + (k0 & 4) * 2 + l15 * 16) = v; }
    z0 = ldsr16(otb + lane * 16); z1 = ldsr16(otb + 1280 + lane * 16); z2 = ldsr16(otb + (r >> 4) * 1280 + 1024 + l15 * 16); };
  auto oa_st = [&]() { const unsigned oa = WS_OA + (rt * 8 + w3 * 2) * OT_B + (hq * 2 + w2) * 1280;
    ST16(z0, oa + lane * 16); ST16(z1, oa + OT_B + lane * 16); if (!h) ST16(z2, oa + (r >> 4) * OT_B + 1024 + l15 * 16); };
  unsigned cpk;
  PRIO(3);
  TL(0, 0, if (!role) kv_rd(0); else FILL(0), NOP, NOP)
  TL(0, 1, if (!role) { kv_mm(true); kv_rd(2); ARRIVE_NW(0); } else FILL(1), NOP, NOP)
  TL(0, 2, if (!role) kv_mm(false); else FILL(2), NOP, if (!role) kv_st())
  TL(0, 3, NOP, NOP, cpk = __hip_atomic_load(cnt, __ATOMIC_RELAXED, __HIP_MEMORY_SCOPE_WORKGROUP))
  STAMP(14);
  if ((unsigned)__builtin_amdgcn_readfirstlane(cpk) < 8u) AWAIT(0, 8);
  asm volatile("" ::: "memory");
  TL(0, 4, NOP, NOP, if (!role) { WAIT_VM(0); set_flag(ws, WS_FPART + ((hq * 16 + rt) * 8 + w) * 8, tag, lane); } else FILL(3))
  PRIO(2);
  TL(0, 5, KX(2, 2), NOP, KW(2, 2, 0)) TL6(0, KW(2, 2, 1), KW(2, 2, 2))
  TL7(0, 1, KW(2, 2, 3), NOP, KW(2, 2, 4))
  STAMP(15);
  TL(1, 0, if (role) FILL(4), NOP, if (red) { fp0 = __hip_atomic_load((gu64*)(ws + WS_FPART) + hq * 128 + lane, RLX_AGENT); fp1 = __hip_atomic_load((gu64*)(ws + WS_FPART) + hq * 128 + 64 + lane, RLX_AGENT); })
  TL(1, 1, if (w < 4) { const int row = (w & 1) * 32 + (lane >> 1);
                      dma4s((w & 2 ? woB : woA) + C0 + ((rt & 7) * 2 + role) * 40 * 640, (row < 40 ? row : 39) * 1280 + (lane & 1) * 156, lds0 + 157696); }, NOP, if (role) FILL(5))
  TL(1, 2, NOP, NOP, if (red) red_load())
  PRIO(1);
  TL(1, 3, NOP, NOP, NOP)
  TL(1, 4, NOP, CLR_VM(), if (red) red_store())
  TL(1, 5, NOP, NOP, NOP)
  PRIO(0);
  TL6(1, if (red) { WAIT_VM(0); set_flag(ws, WS_FKVR + (hq * 16 + rt) * 8, tag, lane); }, NOP)
  if (red) WAIT_BAR(1); else WAIT_BAR(0);
  STAMP(12);
  PRIO(3);
  if (role) set_flag(ws, WS_FOA + ((rt * 8 + hq) * 8 + w) * 8, tag, lane);
  u64 foa;
  TL7(1, 2, KX(3, 0), KW(3, 0, 0), KW(3, 0, 1))
  TL(2, 0, KW(3, 0, 2), KW(3, 0, 3), KW(3, 0, 4)) TL(2, 1, WA(w, 0), WA(w, 1), WA(w, 2))
  TL(2, 2, WA(w, 3), WA(w, 4), (foa = __hip_atomic_load((gu64*)(ws + WS_FOA) + rt * 64 + lane, RLX_AGENT), fkr = __hip_atomic_load((gu64*)(ws + WS_FKVR) + hq * 16 + l15, RLX_AGENT)))
  PRIO(2);
  TL(2, 3, NOP, NOP, NOP)
  auto chk = [&]() { asm volatile("" : "+v"(foa), "+v"(fkr));
    if (!__all(foa == tag)) wait_flags<64>(ws, WS_FOA + rt * 512, tag, lane);
    asm volatile("" ::: "memory"); if (role) oa_load(); };
  TL(2, 4, chk(), NOP, NOP)
  PRIO(1);
  TL(2, 5, NOP, NOP, if (role) oa_mm(0))
  PRIO(0);
  TL6(2, if (role) oa_mm(1), if (role) oa_mm(2))
  BAR_L();
  STAMP(13);
  PRIO(3);
  TL7(2, 0, if (role) oa_mm(3), { if (role) oa_mm(4); CLR_VM(); }, WX(0))
  TL(0, 0, if (role) oa_norm(), WX(1), WX(2))
  TL(0, 1, if (role) oa_st(), WX(3), WX(4))
  TL(0, 2, WA(8 + w, 0), WA(8 + w, 1), WA(8 + w, 2))
  u32x4 wbr[11];
  const char* wbs = (const char*)(woB + C0 + w * 32 * 640) - 4096;
#define WB2(j) do { wbr[j] = *(const GAS u32x4*)(wbs + woff[j] + (j) * 1024); wbr[5 + (j)] = *(const GAS u32x4*)(wbs + 8 * 32 * 1280 + woff[j] + (j) * 1024); } while (0)
  TL(0, 3, WA(8 + w, 3), WA(8 + w, 4), WB2(0))
  PRIO(2);
  TL(0, 4, WB2(1), WB2(2), WB2(3))
  const int w5 = w < 5 ? w : 4;
  const unsigned wo16 = (w5 == 0 ? woff[0] : w5 == 1 ? woff[1] : w5 == 2 ? woff[2] : w5 == 3 ? woff[3] : woff[4]) + w5 * 1024;
  TL(0, 5, WB2(4), NOP, wbr[10] = *(const GAS u32x4*)((const char*)(woB + C0 + 16 * 32 * 640) - 4096 + wo16))
  PRIO(1);
  ARRIVE_NW(1);
  const int tp = w & 1, kq = w >> 1;
  const unsigned ofr = (rt * 8 + role * 4 + tp * 2) * OT_B + kq * 5120 + lane * 16;
  u32x4 of[2][5]; float gtb;
#define OFL(u) do { _Pragma("unroll") for (int i = 0; i < 5; i++) of[u][i] = LD16(WS_OA + ofr + (u) * OT_B + i * 1024); } while (0)
  TL6(0, OFL(0), gtb = __uint_as_float(__builtin_amdgcn_raw_buffer_load_b32(wsr, WS_G + (rt * 128 + w3 * 32 + w2 * 16 + l15) * 4, 0, 16)))
  PRIO(0);
  TL7(0, 0, OFL(1), NOP, NOP)
  pB2[2] = pk(e0, e1); pB2[3] = pk(e2, e3);
  OB = mfma32(v2, pB2, OB);
#undef TL
#undef TL6
#undef TL7
#undef TILE
#undef G
  STAMP(7);
  { LAS f32x4* xb = (LAS f32x4*)(lp + (w3 >> 1) * CH_B) + (w3 & 1) * (64 * 6);
#pragma unroll
    for (int i = 0; i < 12; i++) { auto rr = __builtin_amdgcn_permlane16_swap(__float_as_uint(OA[i]), __float_as_uint(OB[i]), false, false); OA[i] = __uint_as_float(rr[0]); OB[i] = __uint_as_float(rr[1]); }
    const f32x16 snd = w2 ? OA : OB;
    AWAIT(1, 8);
#pragma unroll
    for (int c = 0; c < 3; c++) xb[(w2 * 3 + c) * 64 + lane] = (f32x4){snd[4 * c], snd[4 * c + 1], snd[4 * c + 2], snd[4 * c + 3]};
    asm volatile("s_waitcnt vmcnt(22) lgkmcnt(0)\n\ts_barrier" ::: "memory");
    f32x16 O = w2 ? OB : OA;
#pragma unroll
    for (int c = 0; c < 3; c++) { const f32x4 t = xb[((w2 ^ 1) * 3 + c) * 64 + lane]; O[4 * c] += t[0]; O[4 * c + 1] += t[1]; O[4 * c + 2] += t[2]; O[4 * c + 3] += t[3]; }
    float l = from_upper(O[8]);
    float amax = 0.f;
#pragma unroll
    for (int i = 0; i < 12; i++) amax += fabsf(O[i]);
    const bool good = (l >= 0x1p-30f) && (l <= 0x1p100f) && (amax <= 0x1p126f);
    if (__builtin_expect(!__all(good), 0)) {
      u32x4 q1; u32x2 q2;
#pragma unroll
      for (int i = 0; i < 4; i++) { auto rr = __builtin_amdgcn_permlane16_swap(q1A[i], q1B[i], false, false); q1[i] = w2 ? rr[1] : rr[0]; }
#pragma unroll
      for (int i = 0; i < 2; i++) { auto rr = __builtin_amdgcn_permlane16_swap(q2A[i], q2B[i], false, false); q2[i] = h ? 0u : (w2 ? rr[1] : rr[0]); }
      float m = -INFINITY;
#pragma unroll 1
      for (int t = 0; t < 64; t++) {
        f32x16 s = mfma32(LD16(WS_KV + t * TILE_B + ok1), q1, Z); s = mfma32k8(LD8(WS_KV + t * TILE_B + ok2), q2, s);
#pragma unroll
        for (int i = 0; i < 16; i++) m = fmaxf(m, s[i]);
      }
      m = half_swap_max(m);
      O = Z;
#pragma unroll 1
      for (int t = 0; t < 64; t++) {
        f32x16 s = mfma32(LD16(WS_KV + t * TILE_B + ok1), q1, Z); s = mfma32k8(LD8(WS_KV + t * TILE_B + ok2), q2, s);
#pragma unroll
        for (int i = 0; i < 16; i++) s[i] = ex2(s[i] - m);
        O = mfma32(LD16(WS_KV + t * TILE_B + ov), (u32x4){pk(s[0], s[1]), pk(s[2], s[3]), pk(s[4], s[5]), pk(s[6], s[7])}, O);
        O = mfma32(LD16(WS_KV + t * TILE_B + ov + 2 * VB), (u32x4){pk(s[8], s[9]), pk(s[10], s[11]), pk(s[12], s[13]), pk(s[14], s[15])}, O);
      }
      l = from_upper(O[8]);
    }
    const float inv = __builtin_amdgcn_rcpf(l) * (1.0f - gtb);
    LAS char* tb = lp + w2 * CH_B + 12288 + w3 * 1536;
#pragma unroll
    for (int c = 0; c < 3; c++) { const int k0 = (g & 1) * 20 + 8 * c + 4 * h;
      *(LAS u32x2*)(tb + ((c == 2 && h) ? 1280 + (g & 1) * 8 : (k0 >> 3) * 256 + (k0 & 4) * 2) + l15 * 16) = (u32x2){nz(pk(O[4 * c] * inv, O[4 * c + 1] * inv)), nz(pk(O[4 * c + 2] * inv, O[4 * c + 3] * inv))}; }
    const unsigned ob = WS_OB + (rt * 8 + w3 * 2 + w2) * OT_B + hp * 1280 + lane * 16;
    const u32x4 z0 = ldsr16(tb + lane * 16), z1 = ldsr16(tb + 1024 + l15 * 16);
    ST16(z0, ob); ST16(z1, lane < 16 ? ob + 1024 : 0x7FFF0000u);
  }
  STAMP(8);
  auto of_missing = [&]() { u32x4 mm = of[0][0];
#pragma unroll
    for (int j = 1; j < 10; j++) mm = __builtin_elementwise_max(mm, of[j / 5][j % 5]);
    return missing(mm); };
  u32x4 wf[5][5];
#pragma unroll
  for (int i = 0; i < 5; i++) { const LAS char* wb = lp + L_WO + (kq * 5 + i) * 5120 + wlane;
#pragma unroll
    for (int t = 0; t < 5; t++) wf[i][t] = trd2(wb + t * 8, wb + 1280 + t * 8); }
  SBAR();
  if (__builtin_expect(of_missing(), 0)) {
#pragma unroll 1
    for (unsigned spin = 0; spin < SPIN_CAP; ++spin) {
      asm volatile("" ::: "memory");
      OFL(0); OFL(1);
      if (!of_missing()) break;
      __builtin_amdgcn_s_sleep(1);
    }
  }
  f32x4 c[2][5];
#pragma unroll
  for (int i = 0; i < 5; i++)
#pragma unroll
    for (int t = 0; t < 5; t++) c[0][t] = mfma16(wf[i][t], of[0][i], i ? c[0][t] : (f32x4){0.f, 0.f, 0.f, 0.f});
  BAR_L();
  SBAR();
  { LAS char* d = lp + L_WO + w * 5120 + lane * 16;
    *(LAS u32x4*)(lp + L_WO + 16 * 5120 + w5 * 1024 + lane * 16) = wbr[10];
#pragma unroll
    for (int i = 0; i < 5; i++) {
      *(LAS u32x4*)(d + i * 1024) = wbr[i]; *(LAS u32x4*)(d + 40960 + i * 1024) = wbr[5 + i]; of[0][i] = LD16(WS_OB + ofr + i * 1024); SBAR();
#pragma unroll
      for (int t = 0; t < 5; t++) c[1][t] = mfma16(wf[i][t], of[1][i], i ? c[1][t] : (f32x4){0.f, 0.f, 0.f, 0.f});
      SBAR();
    } }
  STAMP(9);
  BAR_L();
#pragma unroll
  for (int i = 0; i < 5; i++) { const int gr = kq * 5 + i; const LAS char* wb = lp + (gr < 17 ? L_WO + gr * 5120 : 145408 + (gr - 17) * 5120) + wlane;
#pragma unroll
    for (int t = 0; t < 5; t++) wf[i][t] = trd2(wb + t * 8, wb + 1280 + t * 8);
    SBAR(); of[1][i] = LD16(WS_OB + ofr + OT_B + i * 1024); SBAR(); }
  ARRIVE_NW(2);
  STAMP(10);
  LAS char* xw = lp + kq * 21504 + (tp * 32 + l15) * 336 + g * 80;
#pragma unroll
  for (int u = 0; u < 2; u++) {
#pragma unroll
    for (int i = 0; i < 5; i++) {
      if (__builtin_expect(missing(of[u][i]), 0)) {
#pragma unroll 1
        for (unsigned spin = 0; spin < SPIN_CAP; ++spin) {
          asm volatile("" ::: "memory");
#pragma unroll
          for (int j = u * 5 + i; j < 10; j++) of[j / 5][j % 5] = LD16(WS_OB + ofr + (j / 5) * OT_B + (j % 5) * 1024);
          if (!missing(of[u][i])) break;
          __builtin_amdgcn_s_sleep(1);
        }
      }
#pragma unroll
      for (int t = 0; t < 5; t++) c[u][t] = mfma16(wf[i][t], of[u][i], c[u][t]);
      if (u == 0 && i == 3) cpk = __hip_atomic_load(cnt + 2, __ATOMIC_RELAXED, __HIP_MEMORY_SCOPE_WORKGROUP);
      if (u == 1) { if (i == 0) { if (__builtin_expect((unsigned)__builtin_amdgcn_readfirstlane(cpk) < 8u, 0)) AWAIT(2, 8); asm volatile("" ::: "memory"); }
        *(LAS f32x4*)(xw + i * 16) = c[0][i]; }
      SBAR();
    }
  }
#pragma unroll
  for (int t = 0; t < 5; t++) *(LAS f32x4*)(xw + 16 * 336 + t * 16) = c[1][t];
  BAR_L();
  { const __amdgpu_buffer_rsrc_t orr = __builtin_amdgcn_make_buffer_rsrc(out, 0, 2048 * DM * 2, 0x00020000);
#pragma unroll
    for (int j = 0; j < 3; j++) { const int n = tid + 512 * j, nn = n < 1280 ? n : 0, row = (nn * 3277) >> 16, cq = nn - row * 20;
      const LAS f32x4* p = (const LAS f32x4*)(lp + row * 336 + cq * 16);
      const f32x4 z = (p[0] + p[1344]) + (p[2688] + p[4032]);
      __builtin_amdgcn_raw_buffer_store_b64((u32x2){pk(z[0], z[1]), pk(z[2], z[3])}, orr, n < 1280 ? ((rt * 128 + role * 64 + row) * DM + C0 + cq * 4) * 2 : 0x7FFF0000u, 0, 0); }
  }
  STAMP(11);
}
}

extern "C" void kernel_launch(void* const* d_in, const int* in_sizes, int n_in, void* d_out, int out_size, void* d_ws, size_t ws_size, hipStream_t stream) {
  using namespace gm;
  fused<<<256, 512, 0, stream>>>((const u16*)d_in[0], (const u16*)d_in[1], (const u16*)d_in[2], (const u16*)d_in[3], (const u16*)d_in[5], (const u16*)d_in[6],
                                 (const u16*)d_in[7], (const u16*)d_in[8], (const u16*)d_in[9], (u16*)d_out, (char*)d_ws);
}
```

```cpp
#include <hip/hip_runtime.h>
#include <hip/hip_bf16.h>
#include <stdint.h>

namespace gm {
typedef float f32x16 __attribute__((ext_vector_type(16)));
typedef float f32x4 __attribute__((ext_vector_type(4)));
typedef float f32x2 __attribute__((ext_vector_type(2)));
typedef short s16x4 __attribute__((ext_vector_type(4)));
typedef __bf16 b16x2 __attribute__((ext_vector_type(2)));
typedef __bf16 b16x8 __attribute__((ext_vector_type(8)));
typedef unsigned u32x4 __attribute__((ext_vector_type(4)));
typedef unsigned u32x2 __attribute__((ext_vector_type(2)));
typedef unsigned long long u64;
typedef unsigned short u16;
#define DI __device__ __forceinline__
#define LAS __attribute__((address_space(3)))
#define GAS __attribute__((address_space(1)))
typedef GAS u64 gu64;

constexpr int DM = 640;
constexpr unsigned WS_EPOCH = 0, WS_FKV = 2048, WS_FKVR = 4096, WS_FQ = 8192, WS_FPART = 16384, WS_FOA = 24576, WS_G = 57344;
constexpr unsigned WS_KVR = 8912896, KVR_B = 10240, WS_KV = 131072, WS_QX = 327680, QX_B = 10240, WS_PART = 1703936, PART_B = 15360;
constexpr unsigned WS_OA = 3670016, WS_OB = 6291456, WS_END = 8994816, OT_B = 20480;
constexpr int VB = 352, TILE_B = 1280 + 4 * VB, CH_B = TILE_B * 8, STEP_B = 2 * CH_B;
constexpr int STAGE_B = 36864, ST_W1 = 10240, ST_X = 20480;
constexpr int L_BUF = 43008, L_P0W = 0, L_P0X0 = 25600, L_P0X1 = 153600, L_RED = 158720, L_RED1 = 40960;
constexpr int L_HO = 86016, L_QL = 96256, L_VIM = 96256, L_PHK = 116736, L_WPHI = 158592, L_ZERO = 161792, L_CNT = 163712, L_TOTAL = 163840;
constexpr int L_WO = 43008;
constexpr float LOG2E = 1.4426950408889634f;
constexpr float QSCALE = 0.22360679774997896f * LOG2E;
constexpr unsigned SPIN_CAP = 20000, LSPIN_CAP = 400000;

DI unsigned pk(float a, float b) { f32x2 v = {a, b}; b16x2 r = __builtin_convertvector(v, b16x2); return __builtin_bit_cast(unsigned, r); }
DI f32x16 mfma32(u32x4 a, u32x4 b, f32x16 c) { return __builtin_amdgcn_mfma_f32_32x32x16_bf16(__builtin_bit_cast(b16x8, a), __builtin_bit_cast(b16x8, b), c, 0, 0, 0); }
DI f32x16 mfma32k8(u32x2 a, u32x2 b, f32x16 c) { return __builtin_amdgcn_mfma_f32_32x32x8bf16_1k(__builtin_bit_cast(s16x4, a), __builtin_bit_cast(s16x4, b), c, 0, 0, 0); }
DI f32x4 mfma16(u32x4 a, u32x4 b, f32x4 c) { return __builtin_amdgcn_mfma_f32_16x16x32_bf16(__builtin_bit_cast(b16x8, a), __builtin_bit_cast(b16x8, b), c, 0, 0, 0); }
DI u32x2 trd(const LAS char* p) { return __builtin_bit_cast(u32x2, __builtin_amdgcn_ds_read_tr16_b64_v4i16((LAS s16x4*)p)); }
DI u32x4 trd2(const LAS char* p0, const LAS char* p1) { u32x2 a = trd(p0), b = trd(p1); return (u32x4){a.x, a.y, b.x, b.y}; }
DI u32x4 ldsr16(const LAS char* p) { return *(const LAS u32x4*)p; }
DI u32x2 ldsr8(const LAS char* p) { return *(const LAS u32x2*)p; }
DI float ex2(float x) { return __builtin_amdgcn_exp2f(x); }
DI void dma16s(const void* base, unsigned voff, unsigned lds_base) {
  asm volatile("s_mov_b32 m0, %2\n\ts_nop 0\n\tglobal_load_lds_dwordx4 %0, %1" :: "v"(voff), "s"(base), "s"(lds_base) : "memory"); }
DI void dma16s_sc1(const void* base, unsigned voff, unsigned lds_base) {
  asm volatile("s_mov_b32 m0, %2\n\ts_nop 0\n\tglobal_load_lds_dwordx4 %0, %1 sc1" :: "v"(voff), "s"(base), "s"(lds_base) : "memory"); }
DI void dmag_open(unsigned lds_base) { asm volatile("s_mov_b32 m0, %0\n\ts_nop 0" :: "s"(lds_base) : "memory"); }
template <int J> DI void dmag(const void* base, unsigned voff) { asm volatile("global_load_lds_dwordx4 %0, %1 offset:%2" :: "v"(voff), "s"(base), "i"((J & 3) * 1024) : "memory"); }
template <int J> DI void dmag_sc1(const void* base, unsigned voff) { asm volatile("global_load_lds_dwordx4 %0, %1 offset:%2 sc1" :: "v"(voff), "s"(base), "i"((J & 3) * 1024) : "memory"); }
DI void dma4s(const void* base, unsigned voff, unsigned lds_base) {
  asm volatile("s_mov_b32 m0, %2\n\ts_nop 0\n\tglobal_load_lds_dword %0, %1" :: "v"(voff), "s"(base), "s"(lds_base) : "memory"); }
DI u32x4 ld16_async(const void* p) { u32x4 r; asm volatile("global_load_dwordx4 %0, %1, off" : "=v"(r) : "v"(p) : "memory"); return r; }
DI u32x2 ld8_sc1_async(const void* p) { u32x2 r; asm volatile("global_load_dwordx2 %0, %1, off sc1" : "=v"(r) : "v"(p) : "memory"); return r; }
#define BAR_L() asm volatile("s_waitcnt lgkmcnt(0)\n\ts_barrier" ::: "memory")
#define WAIT_BAR(N) asm volatile("s_waitcnt vmcnt(" #N ") lgkmcnt(0)\n\ts_barrier" ::: "memory")
#define WAIT_VM(N) asm volatile("s_waitcnt vmcnt(" #N ")" ::: "memory")
#define SBAR() __builtin_amdgcn_sched_barrier(0)
#define RLX_AGENT __ATOMIC_RELAXED, __HIP_MEMORY_SCOPE_AGENT
DI float half_swap_max(float m) { auto rr = __builtin_amdgcn_permlane32_swap(__float_as_uint(m), __float_as_uint(m), false, false); return fmaxf(__uint_as_float(rr[0]), __uint_as_float(rr[1])); }
DI float from_upper(float v) { auto rr = __builtin_amdgcn_permlane32_swap(__float_as_uint(v), __float_as_uint(v), false, false); return __uint_as_float(rr[1]); }

template <int NF>
DI bool flags_set(const char* ws, unsigned off, u64 tag, int lane) {
  gu64* f = (gu64*)(ws + off);
  bool ok = __hip_atomic_load(f + (NF < 64 ? (lane & (NF - 1)) : lane), RLX_AGENT) == tag;
  if (NF >= 128) ok &= __hip_atomic_load(f + 64 + lane, RLX_AGENT) == tag;
  if (NF >= 256) { ok &= __hip_atomic_load(f + 128 + lane, RLX_AGENT) == tag; ok &= __hip_atomic_load(f + 192 + lane, RLX_AGENT) == tag; }
  return ok;
}
template <int NF>
DI void wait_flags(const char* ws, unsigned off, u64 tag, int lane) {
#pragma unroll 1
  for (unsigned spin = 0; spin < SPIN_CAP; ++spin) {
    if (__all(flags_set<NF>(ws, off, tag, lane))) break;
    __builtin_amdgcn_s_sleep(2);
  }
  asm volatile("" ::: "memory");
}
DI void set_flag(char* ws, unsigned off, u64 tag, int lane) { if (lane == 0) __hip_atomic_store((gu64*)(ws + off), tag, RLX_AGENT); }
DI unsigned nz(unsigned v) { return v < 0xFFFFFFFEu ? v : 0xFFFFFFFEu; }
DI unsigned umax3(unsigned a, unsigned b, unsigned c) { unsigned m = a > b ? a : b; return m > c ? m : c; }
DI bool missing(u32x4 v) { return __any(umax3(umax3(v.x, v.y, v.z), v.w, 0u) == 0xFFFFFFFFu); }
#define ONES ((u32x4){0xFFFFFFFFu, 0xFFFFFFFFu, 0xFFFFFFFFu, 0xFFFFFFFFu})
#define ST16(v, off) __builtin_amdgcn_raw_buffer_store_b128((v), wsr, (off), 0, 16)
#define ST8(v, off) __builtin_amdgcn_raw_buffer_store_b64((v), wsr, (off), 0, 16)
#define LD16(off) __builtin_amdgcn_raw_buffer_load_b128(wsr, (off), 0, 16)
#define LD8(off) __builtin_amdgcn_raw_buffer_load_b64(wsr, (off), 0, 16)

#define STAMP(i) do {} while (0)

template <int role>
DI void body(const u16* __restrict__ x, const u16* __restrict__ wqkv, const u16* __restrict__ wphi, const u16* __restrict__ woA,
             const u16* __restrict__ wq, const u16* __restrict__ wkv, const u16* __restrict__ woB,
             const u16* __restrict__ gatew, u16* __restrict__ out, char* ws, LAS char* lp) {
  const int tid = threadIdx.x, lane = tid & 63;
  const int w = __builtin_amdgcn_readfirstlane(tid >> 6) & 7;
  const int l15 = lane & 15, g = lane >> 4, q4 = l15 >> 2, p4 = lane & 3, r = lane & 31, h = lane >> 5;
  const int bx = blockIdx.x & 7, bi = blockIdx.x >> 3;
  const int rt = (bx & 1) * 8 + (bi & 7), hp = (bx >> 1) * 4 + ((bi >> 3) & 3), hq = hp >> 1;
  const int w3 = w & 3, w2 = w >> 2;
  const unsigned lds0 = (unsigned)(uintptr_t)lp;
  const __amdgpu_buffer_rsrc_t wsr = __builtin_amdgcn_make_buffer_rsrc(ws, 0, WS_END, 0x00020000);
  LAS unsigned* cnt = (LAS unsigned*)(lp + L_CNT);
#define ARRIVE_NW(c) do { asm volatile("" ::: "memory"); if (lane == 0) __hip_atomic_fetch_add(cnt + (c), 1u, __ATOMIC_RELAXED, __HIP_MEMORY_SCOPE_WORKGROUP); asm volatile("" ::: "memory"); } while (0)
#define PEEK(c) ((unsigned)__builtin_amdgcn_readfirstlane(__hip_atomic_load(cnt + (c), __ATOMIC_RELAXED, __HIP_MEMORY_SCOPE_WORKGROUP)))
#define AWAIT(c, n) do { _Pragma("unroll 1") for (unsigned sp_ = 0; sp_ < LSPIN_CAP; ++sp_) { if (PEEK(c) >= (unsigned)(n)) break; __builtin_amdgcn_s_sleep(1); } asm volatile("" ::: "memory"); } while (0)
  STAMP(0);

  const int T0 = rt * 128 + hq * 16;
  const int C0 = hq * 80;
  const int wlane = ((g >> 1) * 16 + (g & 1) * 4 + q4) * 160 + p4 * 40;
  u64 tag;
  unsigned o0[10];
#define KVP(s, slot, p) dma16s_sc1(ws + WS_KV + (s) * STEP_B + (p) * 1024, lane * 16, lds0 + (slot) * STEP_B + (p) * 1024)
#define KVL(s, slot) do { _Pragma("unroll") for (int j = 0; j < 11; j++) if (j < 10 || w3 < 2) KVP(s, slot, w3 + 4 * j); } while (0)
#define KW(s, slot, j) do { if ((j) == 4) KVP(s, slot, w * 5 + 4); else { if ((j) == 0) dmag_open(lds0 + (slot) * STEP_B + w * 5120); dmag_sc1<(j)>(ws + WS_KV + (s) * STEP_B + w * 5120, lane * 16); } } while (0)
#define KX(s, slot) do { if (w < 2) KVP(s, slot, 40 + w); } while (0)
  unsigned woff[5];
  auto woff_init = [&]() { int ln = lane; asm volatile("" : "+v"(ln));
#pragma unroll
    for (int j = 0; j < 5; j++) { const int c = j * 64 + ln, rho = (c * 6554) >> 16, cc = c - rho * 10; const int k = (rho & ~12) | ((rho & 4) << 1) | ((rho & 8) >> 1); woff[j] = k * 1280 + cc * 16 + (4 - j) * 1024; }
    asm volatile("" : "+v"(woff[0]), "+v"(woff[1]), "+v"(woff[2]), "+v"(woff[3]), "+v"(woff[4])); };
  if (w2 == 0) {
    u32x4 gf0, gf1, gf2, gf3, gf4, wpc;
    u32x2 ep = ld8_sc1_async(ws + WS_EPOCH);
    const int lx = w3 < 3 ? L_P0X0 + w3 * 5120 : L_P0X1;
    { const u16* wsrc = wkv + w3 * 3200;
#pragma unroll
      for (int j = 0; j < 5; j++) { const int n = j * 64 + lane, row = (n * 3277) >> 16, cc = n - row * 20; dma16s(x + T0 * DM + w3 * 160, row * 1280 + cc * 16, lds0 + lx + j * 1024); }
#pragma unroll
      for (int j = 0; j < 6; j++) dma16s(wsrc + j * 512, lane * 16, lds0 + L_P0W + w3 * 6400 + j * 1024);
      if (lane < 16) dma16s(wsrc + 6 * 512, lane * 16, lds0 + L_P0W + w3 * 6400 + 6 * 1024);
      const u16* gp = gatew + w3 * 160 + g * 8;
      gf0 = ld16_async(gp); gf1 = ld16_async(gp + 32); gf2 = ld16_async(gp + 64); gf3 = ld16_async(gp + 96); gf4 = ld16_async(gp + 128);
      wpc = ld16_async(wphi + hq * 1600 + (tid < 200 ? tid : 199) * 8);
    }
    woff_init();
    asm volatile("s_waitcnt vmcnt(0)" : "+v"(gf0), "+v"(gf1), "+v"(gf2), "+v"(gf3), "+v"(gf4), "+v"(wpc), "+v"(ep) :: "memory");
    tag = (((u64)(unsigned)__builtin_amdgcn_readfirstlane(ep.y) << 32) | (unsigned)__builtin_amdgcn_readfirstlane(ep.x)) + 1ull;
    STAMP(1);
    { const LAS char* wb = lp + L_P0W + (w3 * 160 + g * 8 + q4) * 40;
      const LAS char* xb = lp + lx + l15 * 320 + g * 16;
      const bool gl = l15 == 4;
      f32x4 c0 = {}, c1 = {};
#define P0STEP(s, GF) { const u32x4 b = ldsr16(xb + (s) * 64); c0 = mfma16(trd2(wb + p4 * 8 + (s) * 1280, wb + p4 * 8 + (s) * 1280 + 160), b, c0); \
        u32x4 a = trd2(wb + 32 + (s) * 1280, wb + 32 + (s) * 1280 + 160); if (gl) a = GF; c1 = mfma16(a, b, c1); }
      P0STEP(0, gf0) P0STEP(1, gf1) P0STEP(2, gf2) P0STEP(3, gf3) P0STEP(4, gf4)
#undef P0STEP
      *(LAS f32x4*)(lp + L_RED + tid * 16) = c0;
      if (lane < 32) *(LAS f32x4*)(lp + L_RED1 + (w3 * 32 + lane) * 16) = c1; }
    STAMP(2);
    f32x16 acc[5];
#pragma unroll
    for (int b = 0; b < 5; b++) acc[b] = (f32x16){};
    const int wm = g & 1, whd = p4;
    const int wo0 = wm * ST_W1 + (8 * h + 4 * wm + q4) * 160 + whd * 40, wo1 = wm * ST_W1 + (8 * h + 4 * (wm ^ 1) + q4) * 160 + whd * 40;
    const int xo = ST_X + (w3 * 32 + r) * 128 + ((h ^ ((r >> 1) & 7)) * 16);
    u32x4 fa[5], fb[5], xa, xb;
#define KSTEP(FN, XN, s, FC, XC) { XN = ldsr16(sb + (xo ^ ((s) * 32))); \
      _Pragma("unroll") for (int j = 0; j < 5; j++) { if (i > 0 || (s) > 0) acc[j] = mfma32(FC[j], XC, acc[j]); FN[j] = trd2(sb + wo0 + (s) * 2560 + j * 8, sb + wo1 + (s) * 2560 + j * 8); SBAR(); } }
#pragma unroll
    for (int i = 0; i < 10; i++) {
      BAR_L();
      if (i == 1 && role) {
        const unsigned qb = WS_QX + (rt * 8 + hq) * QX_B + w3 * 1280 + lane * 16;
        ST16(ONES, qb); ST16(ONES, qb + 5120); if (lane < 32) ST16(ONES, qb + 1024 + (lane >> 4) * (5120 - 256)); }
      if (i == 4) { if (role) { WAIT_VM(0); set_flag(ws, WS_FQ + ((rt * 8 + hq) * 4 + w3) * 8, tag, lane); }
                    if (tid < 200) *(LAS u32x4*)(lp + L_WPHI + tid * 16) = wpc; }
      const LAS char* sb = lp + L_BUF + ((i + 2) % 3) * STAGE_B;
      KSTEP(fa, xa, 0, fb, xb) KSTEP(fb, xb, 1, fa, xa) KSTEP(fa, xa, 2, fb, xb)
      { xb = ldsr16(sb + (xo ^ 96));
        acc[0] = mfma32(fa[0], xa, acc[0]); fb[0] = trd2(sb + wo0 + 7680, sb + wo1 + 7680); fb[1] = trd2(sb + wo0 + 7688, sb + wo1 + 7688); SBAR();
        acc[1] = mfma32(fa[1], xa, acc[1]); fb[2] = trd2(sb + wo0 + 7696, sb + wo1 + 7696); fb[3] = trd2(sb + wo0 + 7704, sb + wo1 + 7704); SBAR();
        acc[2] = mfma32(fa[2], xa, acc[2]); fb[4] = trd2(sb + wo0 + 7712, sb + wo1 + 7712); SBAR();
        acc[3] = mfma32(fa[3], xa, acc[3]); acc[4] = mfma32(fa[4], xa, acc[4]); SBAR(); }
    }
#pragma unroll
    for (int j = 0; j < 5; j++) acc[j] = mfma32(fb[j], xb, acc[j]);
#undef KSTEP
    STAMP(3);
#define PK2(t, i, sc) pk(acc[t][i] * (sc), acc[t][(i) + 1] * (sc))
#define ROW(i, sc) const u32x4 ra = {PK2(0, i, sc), PK2(0, (i) + 2, sc), PK2(1, i, sc), PK2(1, (i) + 2, sc)}, rb = {PK2(2, i, sc), PK2(2, (i) + 2, sc), PK2(3, i, sc), PK2(3, (i) + 2, sc)}; const u32x2 rc = {PK2(4, i, sc), PK2(4, (i) + 2, sc)}
    { ROW(4, 1.0f);
      *(LAS u32x4*)(lp + L_HO + tid * 16) = ra; *(LAS u32x4*)(lp + L_HO + 4096 + tid * 16) = rb; *(LAS u32x2*)(lp + L_HO + 8192 + tid * 8) = rc; }
    BAR_L();
    const int qfo = (h * 4 + w3) * 1280;
    if (role) {
      { ROW(8, QSCALE); const unsigned qb = WS_QX + (rt * 8 + hq) * QX_B + qfo;
        ST16(((u32x4){nz(ra.x), nz(ra.y), nz(ra.z), nz(ra.w)}), qb + r * 16); ST16(((u32x4){nz(rb.x), nz(rb.y), nz(rb.z), nz(rb.w)}), qb + 512 + r * 16); ST8(((u32x2){nz(rc.x), nz(rc.y)}), qb + 1024 + r * 8); }
      { ROW(12, QSCALE); LAS char* d = lp + L_QL + qfo;
        *(LAS u32x4*)(d + r * 16) = ra; *(LAS u32x4*)(d + 512 + r * 16) = rb; *(LAS u32x2*)(d + 1024 + r * 8) = rc; }
    } else { LAS char* d = lp + L_VIM + (h * 128 + w3 * 32 + r) * 40;
#pragma unroll
      for (int t = 0; t < 5; t++) { *(LAS u32x2*)(d + t * 8) = (u32x2){PK2(t, 8, 1.0f), PK2(t, 10, 1.0f)}; *(LAS u32x2*)(d + 10240 + t * 8) = (u32x2){PK2(t, 12, 1.0f), PK2(t, 14, 1.0f)}; } }
#pragma unroll
    for (int t = 0; t < 5; t++) { o0[2 * t] = PK2(t, 0, 1.0f); o0[2 * t + 1] = PK2(t, 2, 1.0f); }
#undef ROW
#undef PK2
  } else {
    u32x2 ep = ld8_sc1_async(ws + WS_EPOCH);
    const bool second = w3 >> 1;
    const unsigned rs = (second && role) ? 1280u : 3840u;
    const char* wsrc = (second ? (role ? (const char*)(wq + C0) : (const char*)(wqkv + 1280 + C0)) : (const char*)(wqkv + (role ? 0 : 640) + C0)) + (w3 & 1) * 32 * rs;
    const char* xsrc = (const char*)(x + (rt * 128 + w3 * 32) * DM);
    int rot = (bi & 7) + 3 * ((bi >> 3) & 3); rot = rot >= 10 ? rot - 10 : rot;
    unsigned so[5], xoff[2];
#pragma unroll
    for (int j = 0; j < 5; j++) { const int c = j * 64 + lane, rho = (c * 6554) >> 16, cc = c - rho * 10; so[j] = (rho ^ (second ? 4 : 0)) * rs + cc * 16; }
#pragma unroll
    for (int j = 0; j < 2; j++) { const int row = j * 8 + (lane >> 3); xoff[j] = row * 1280 + (((lane & 7) ^ ((row >> 1) & 7)) * 16); }
    int bsel = 2, kc = rot;
    auto stage = [&]() {
      const unsigned lb = lds0 + L_BUF + bsel * STAGE_B;
      const char* bw = wsrc + kc * 64 * rs; const char* bxp = xsrc + kc * 128;
#pragma unroll
      for (int j = 0; j < 5; j++) { dma16s(bw, so[j], lb + (w3 * 5 + j) * 1024); if (j < 4) dma16s(bxp + (j >> 1) * 16 * 1280, xoff[j & 1], lb + ST_X + (w3 * 4 + j) * 1024); }
      bsel = bsel == 2 ? 0 : bsel + 1; kc = kc == 9 ? 0 : kc + 1;
    };
    stage(); stage();
    woff_init();
    asm volatile("s_waitcnt vmcnt(9)" : "+v"(ep) :: "memory");
    tag = (((u64)(unsigned)__builtin_amdgcn_readfirstlane(ep.y) << 32) | (unsigned)__builtin_amdgcn_readfirstlane(ep.x)) + 1ull;
    STAMP(1); STAMP(2);
#pragma unroll 1
    for (int i = 0; i < 8; i++) {
      WAIT_BAR(9);
      stage();
      if (i == 0 && w3 == 0) {
        const unsigned tb = WS_KV + (T0 >> 5) * TILE_B; const int s16 = hq & 1;
        if (!role) {
          const LAS f32x4* rp = (const LAS f32x4*)(lp + (h ? L_RED1 : L_RED)) + (h ? (g & 1) * 16 : g * 32) + l15;
          const int st = h ? 32 : 64, o2 = h ? 0 : 16;
          const f32x4 a = (rp[0] + rp[st]) + (rp[2 * st] + rp[3 * st]), b = (rp[o2] + rp[st + o2]) + (rp[2 * st + o2] + rp[3 * st + o2]);
          const u32x4 v = {pk(a[0], a[1]), pk(a[2], a[3]), pk(b[0], b[1]), pk(b[2], b[3])};
          if (lane < 32) ST16(v, tb + (g * 32 + s16 * 16 + l15) * 16);
          else if (lane < 48) ST8(((u32x2){v.x, v.y}), tb + 1024 + (s16 * 16 + l15) * 8);
          else { const float gv = 1.0f / (1.0f + __expf(-a[0])); __builtin_amdgcn_raw_buffer_store_b32(__float_as_uint(gv), wsr, WS_G + (T0 + l15) * 4, 0, 16); }
        } else if (lane < 44) {
          const int hh = lane >= 22, d = lane - hh * 22, dd = d < 20 ? d : 0, st = dd >= 16 ? 128 : 256;
          const LAS float* rp = (const LAS float*)(lp + (dd >= 16 ? L_RED1 : L_RED)) + (((dd & 15) >> 2) * 16 + 4 * hh) * 4 + (dd & 3);
          float e[8];
#pragma unroll
          for (int j = 0; j < 8; j++) { const int tk = (8 * (j >> 2) + (j & 3)) * 4; e[j] = (rp[tk] + rp[tk + st]) + (rp[tk + 2 * st] + rp[tk + 3 * st]); }
          u32x4 v = {pk(e[0], e[1]), pk(e[2], e[3]), pk(e[4], e[5]), pk(e[6], e[7])};
          if (d == 20) v = (u32x4){0x3F803F80u, 0x3F803F80u, 0x3F803F80u, 0x3F803F80u};
          if (d == 21) v = (u32x4){0u, 0u, 0u, 0u};
          ST16(v, tb + 1280 + (s16 * 2 + hh) * VB + d * 16);
        }
      }
      if (i == 2) { if (w3 == 0) set_flag(ws, WS_FKV + blockIdx.x * 8, tag, lane);
                    if (w3 >= 2) *(LAS u32x4*)(lp + L_ZERO + (tid - 384) * 16) = (u32x4){0u, 0u, 0u, 0u}; }
    }
    u64 fa, fb, fc, fd, fe;
    { gu64* f = (gu64*)(ws + WS_FKV) + lane;
      fa = __hip_atomic_load(f, RLX_AGENT); fb = __hip_atomic_load(f + 64, RLX_AGENT); fc = __hip_atomic_load(f + 128, RLX_AGENT); fd = __hip_atomic_load(f + 192, RLX_AGENT);
      fe = __hip_atomic_load((gu64*)(ws + WS_FQ) + (rt * 8 + hq) * 4 + p4, RLX_AGENT); }
    WAIT_BAR(14);
    asm volatile("" : "+v"(fa), "+v"(fb), "+v"(fc), "+v"(fd), "+v"(fe));
    const bool kvok = __all((fa == tag) & (fb == tag) & (fc == tag) & (fd == tag) & (fe == tag));
    asm volatile("" ::: "memory");
    BAR_L();
    if (!kvok) { wait_flags<256>(ws, WS_FKV, tag, lane); wait_flags<4>(ws, WS_FQ + (rt * 8 + hq) * 32, tag, lane); }
    KVL(0, 0);
    if (blockIdx.x == 0 && w3 == 0) set_flag(ws, WS_EPOCH, tag, lane);
    STAMP(3);
    BAR_L();
    const int ht = tid - 256;
    const u32x4 a = ldsr16(lp + L_HO + ht * 16), b = ldsr16(lp + L_HO + 4096 + ht * 16); const u32x2 c = ldsr8(lp + L_HO + 8192 + ht * 8);
    o0[0] = a.x; o0[1] = a.y; o0[2] = a.z; o0[3] = a.w; o0[4] = b.x; o0[5] = b.y; o0[6] = b.z; o0[7] = b.w; o0[8] = c.x; o0[9] = c.y;
  }
  STAMP(4);
  u32x4 ph[5];
  { const LAS char* pb = ((p4 & 1) == h) ? lp + L_WPHI + (2 * w2 + h) * 800 + q4 * 40 + (2 * (g & 1) + (p4 >> 1)) * 8 : lp + L_ZERO;
    const u32x4 b0 = {o0[0], o0[1], o0[2], o0[3]}, b1 = {o0[4], o0[5], o0[6], o0[7]}, b2 = {o0[8], o0[9], 0u, 0u};
    const u32x2 c0 = trd(pb + 640), c1 = trd(pb + 672);
    f32x16 P0 = {}, P1 = {};
    P0 = mfma32(trd2(pb, pb + 160), b0, P0); P1 = mfma32(trd2(pb + 32, pb + 192), b0, P1);
    P0 = mfma32(trd2(pb + 320, pb + 480), b1, P0); P1 = mfma32(trd2(pb + 352, pb + 512), b1, P1);
    P0 = mfma32((u32x4){c0.x, c0.y, 0u, 0u}, b2, P0); P1 = mfma32((u32x4){c1.x, c1.y, 0u, 0u}, b2, P1);
    KX(1, 1); KW(1, 1, 0); SBAR();
    f32x2 p[10];
#pragma unroll
    for (int f = 0; f < 10; f++) p[f] = f < 8 ? (f32x2){P0[(2 * f) & 15], P0[(2 * f + 1) & 15]} : (f32x2){P1[(2 * f) & 3], P1[(2 * f + 1) & 3]};
    float mx = fmaxf(p[0][0], p[0][1]), mn = fminf(p[0][0], p[0][1]);
#pragma unroll
    for (int f = 1; f < 10; f++) { mx = fmaxf(fmaxf(mx, p[f][0]), p[f][1]); mn = fminf(fminf(mn, p[f][0]), p[f][1]); }
    const f32x2 ma = {-mx * LOG2E, -mx * LOG2E}, mb = {mn * LOG2E, mn * LOG2E}, ca_ = {LOG2E, LOG2E}, cb_ = {-LOG2E, -LOG2E};
    f32x2 ea[10], eb[10], sa = {0.f, 0.f}, sb = {0.f, 0.f};
#pragma unroll
    for (int f = 0; f < 10; f++) { if (f % 2 == 0 && f < 8) { SBAR(); if (f == 0) KW(1, 1, 1); if (f == 2) KW(1, 1, 2); if (f == 4) KW(1, 1, 3); if (f == 6) KW(1, 1, 4); SBAR(); }
      const f32x2 xa = __builtin_elementwise_fma(p[f], ca_, ma), xb = __builtin_elementwise_fma(p[f], cb_, mb);
      ea[f] = (f32x2){ex2(xa[0]), ex2(xa[1])}; eb[f] = (f32x2){ex2(xb[0]), ex2(xb[1])}; sa += ea[f]; sb += eb[f]; }
    const float ia = __builtin_amdgcn_rcpf(sa[0] + sa[1]), ib = __builtin_amdgcn_rcpf(sb[0] + sb[1]);
    unsigned o[20];
#pragma unroll
    for (int f = 0; f < 10; f++) { const f32x2 ya = ea[f] * ia, yb = eb[f] * ib; o[f] = pk(ya[0], ya[1]); o[10 + f] = pk(yb[0], yb[1]); }
#pragma unroll
    for (int c = 0; c < 5; c++) ph[c] = (u32x4){o[4 * c], o[4 * c + 1], o[4 * c + 2], o[4 * c + 3]};
    asm volatile("" : "+v"(ph[0]), "+v"(ph[1]), "+v"(ph[2]), "+v"(ph[3]), "+v"(ph[4]));
  }
  u32x4 q1A, q1B; u32x2 q2A, q2B;
  if (role) {
    WAIT_BAR(0);
    STAMP(5);
    const LAS char* qa = lp + L_QL + w3 * 1280;
    q1A = ldsr16(qa + lane * 16); q1B = ldsr16(qa + 5120 + lane * 16); q2A = ldsr8(qa + 1024 + r * 8); q2B = ldsr8(qa + 6144 + r * 8);
    ARRIVE_NW(0);
  } else {
    const unsigned qb = WS_QX + (rt * 8 + hq) * QX_B + w3 * 1280;
    q1A = LD16(qb + lane * 16); q1B = LD16(qb + 5120 + lane * 16); q2A = LD8(qb + 1024 + r * 8); q2B = LD8(qb + 6144 + r * 8);
    { LAS char* d = lp + L_PHK + ((2 * w2 + h) * 128 + w3 * 32 + r) * 80;
#pragma unroll
      for (int c = 0; c < 5; c++) *(LAS u32x4*)(d + c * 16) = ph[c]; }
    WAIT_BAR(4);
    STAMP(5);
#pragma unroll 1
    for (unsigned spin = 0; spin < SPIN_CAP; ++spin) {
      if (!(missing(q1A) | missing(q1B) | missing((u32x4){q2A.x, q2A.y, q2B.x, q2B.y}))) break;
      __builtin_amdgcn_s_sleep(1);
      asm volatile("" ::: "memory");
      q1A = LD16(qb + lane * 16); q1B = LD16(qb + 5120 + lane * 16); q2A = LD8(qb + 1024 + r * 8); q2B = LD8(qb + 6144 + r * 8);
    }
  }
  STAMP(6);
  const int vr = r <= 21 ? r : 21;
  const int ok1 = lane * 16, ok2 = h ? (1280 + 20 * 16) : (1024 + r * 8), ov = 1280 + h * VB + vr * 16;
  const f32x16 Z = {};
  if (h) { q2A = (u32x2){0u, 0u}; q2B = (u32x2){0u, 0u}; }
  { const LAS char* t0 = lp;
    const u32x4 k1 = ldsr16(t0 + ok1); const u32x2 k2 = ldsr8(t0 + ok2);
    f32x16 sa = mfma32(k1, q1A, Z); sa = mfma32k8(k2, q2A, sa);
    f32x16 sb = mfma32(k1, q1B, Z); sb = mfma32k8(k2, q2B, sb);
    float ma = sa[0], mb = sb[0];
#pragma unroll
    for (int i = 1; i < 16; i++) { ma = fmaxf(ma, sa[i]); mb = fmaxf(mb, sb[i]); }
    ma = half_swap_max(ma); mb = half_swap_max(mb);
    const float mah = __uint_as_float(__float_as_uint(ma) & 0xFFFF0000u), mbh = __uint_as_float(__float_as_uint(mb) & 0xFFFF0000u);
    if (h) { q2A.x = pk(-mah, mah - ma); q2B.x = pk(-mbh, mbh - mb); } }
  f32x16 OA = {}, OB = {}, SA, SB;
  u32x4 k1, v1, v2, v2n, pA1, pA2, pB1, pB2; u32x2 k2;
  float e0, e1, e2, e3;
  const LAS char* c0p = lp + w2 * CH_B + ok1; const LAS char* c1p = lp + w2 * CH_B + ok2; const LAS char* c2p = lp + w2 * CH_B + ov;
  k1 = ldsr16(c0p); k2 = ldsr8(c1p);
  SA = mfma32(k1, q1A, Z); SA = mfma32k8(k2, q2A, SA);
  v1 = ldsr16(c2p); v2 = v1;
  e0 = e1 = e2 = e3 = 0.f; pB2 = (u32x4){0u, 0u, 0u, 0u};
#define G(MF, LD, C0_, C1_, X, S_, i) MF; LD; SBAR(); C0_; C1_; SBAR(); X##0 = ex2(S_[i]); X##1 = ex2(S_[i + 1]); X##2 = ex2(S_[i + 2]); X##3 = ex2(S_[i + 3]); asm volatile("" : "+v"(X##0), "+v"(X##1), "+v"(X##2), "+v"(X##3)); SBAR();
#define TILE(TB, TN, V2, H0, H1, H2) { float a0, a1, a2, a3; \
    G(SB = mfma32(k1, q1B, Z),    k1 = ldsr16(d0p + (TN)), pB2[2] = pk(e0, e1), pB2[3] = pk(e2, e3), a, SA, 0) \
    G(OB = mfma32(v2, pB2, OB),   V2,                      pA1[0] = pk(a0, a1), pA1[1] = pk(a2, a3), e, SA, 4) \
    G(SB = mfma32k8(k2, q2B, SB), k2 = ldsr8(d1p + (TN)),  pA1[2] = pk(e0, e1), pA1[3] = pk(e2, e3), a, SA, 8) \
    G(OA = mfma32(v1, pA1, OA),   H0,                      pA2[0] = pk(a0, a1), pA2[1] = pk(a2, a3), e, SA, 12) \
    G(SA = mfma32(k1, q1A, Z),    H1,                      pA2[2] = pk(e0, e1), pA2[3] = pk(e2, e3), a, SB, 0) \
    G(OA = mfma32(v2, pA2, OA),   H2,                      pB1[0] = pk(a0, a1), pB1[1] = pk(a2, a3), e, SB, 4) \
    G(SA = mfma32k8(k2, q2A, SA), (void)0,                 pB1[2] = pk(e0, e1), pB1[3] = pk(e2, e3), a, SB, 8) \
    G(OB = mfma32(v1, pB1, OB),   v1 = ldsr16(d2p + (TN)), pB2[0] = pk(a0, a1), pB2[1] = pk(a2, a3), e, SB, 12) }
#define NOP (void)0
  const LAS char *d0p = c0p, *d1p = c1p, *d2p = c2p;
#define TL(sl, t, H0, H1, H2) TILE(0, ((t) + 1) * TILE_B, v2 = ldsr16(d2p + (t) * TILE_B + 2 * VB), H0, H1, H2)
#define TL6(sl, H0, H1) TL(sl, 6, H0, H1, v2n = ldsr16(d2p + 7 * TILE_B + 2 * VB))
#define TL7(sl, sn, H0, H1, H2) d0p += ((sn) - (sl)) * STEP_B; d1p += ((sn) - (sl)) * STEP_B; d2p += ((sn) - (sl)) * STEP_B; asm volatile("" : "+v"(d0p), "+v"(d1p), "+v"(d2p)); TILE(0, 0, v2 = v2n, H0, H1, H2)
#define WOG(wo, gr, j, dst) do { if ((j) == 4) WOP(wo, gr, 4, dst); else { if ((j) == 0) dmag_open(lds0 + (dst)); dmag<(j)>((wo) + C0 + (gr) * 32 * 640 - 2048, woff[j]); } } while (0)
#define WOP(wo, gr, j, dst) dma16s((wo) + C0 + (gr) * 32 * 640 - (4 - (j)) * 512, woff[j], lds0 + (dst) + (j) * 1024)
#define WA(gr, j) WOG(woA, gr, j, L_WO + (gr) * 5120)
  const u16* wxs = (w2 ? woB : woA) + (16 + w3) * 32 * 640;
  const int wxd = w2 ? 145408 + (w3 - 1) * 5120 : L_WO + (16 + w3) * 5120;
#define WX(j) WOG(wxs, 0, j, wxd)
#define PRIO(n) __builtin_amdgcn_s_setprio(n)
  const unsigned fo = (rt * 8 + hq) * OT_B + lane * 16;
#define FILL(j) do { const int n_ = w * 3 + ((j) >> 1); ST16(ONES, (((j) & 1) ? WS_OB : WS_OA) + fo + (n_ < 20 ? n_ : 19) * 1024); } while (0)
#define CLR_VM() __builtin_amdgcn_s_waitcnt(0x0F70)
  const bool red = !role && w == 4;
  f32x4 part[16]; u64 fp0, fp1;
  auto red_load = [&]() {
    asm volatile("" : "+v"(fp0), "+v"(fp1));
    if (!__all((fp0 == tag) & (fp1 == tag))) wait_flags<128>(ws, WS_FPART + hq * 1024, tag, lane);
    asm volatile("" ::: "memory");
#pragma unroll
    for (int j = 0; j < 16; j++) part[j] = __builtin_bit_cast(f32x4, LD16(WS_PART + (hq * 16 + j) * PART_B + (rt * 60 + (lane < 60 ? lane : 59)) * 16));
  };
  auto red_store = [&]() {
    f32x4 a = ((part[0] + part[1]) + (part[2] + part[3])) + ((part[4] + part[5]) + (part[6] + part[7]));
    a += ((part[8] + part[9]) + (part[10] + part[11])) + ((part[12] + part[13]) + (part[14] + part[15]));
    const int hd = rt >> 2, u = (rt & 3) * 60 + lane, hi = u >= 160, fq = hi ? (u - 160) >> 3 : u >> 4, di = hi ? u & 7 : u & 15;
    if (lane < 60) ST8(((u32x2){pk(a[0], a[1]), pk(a[2], a[3])}), WS_KVR + hq * KVR_B + (((hd >> 1) * 2 + hi) * 5 + (fq >> 1)) * 512 + ((hd & 1) * 16 + di) * 16 + (fq & 1) * 8);
  };
  u32x4 ka[10]; u64 fkr; float gta; unsigned kb;
  auto oa_load = [&]() {
    asm volatile("" : "+v"(fkr));
    if (!__all(fkr == tag)) wait_flags<16>(ws, WS_FKVR + hq * 128, tag, lane);
    asm volatile("" ::: "memory");
    kb = (((r >> 2) & 1) == h) ? WS_KVR + hq * KVR_B + w2 * 5120 + (h * 16 + (r & 3) + 4 * (r >> 3)) * 16 : 0x7FFF0000u;
    ka[0] = LD16(kb); ka[5] = LD16(kb + 2560); ka[1] = LD16(kb + 512); ka[6] = LD16(kb + 3072);
    gta = __uint_as_float(__builtin_amdgcn_raw_buffer_load_b32(wsr, WS_G + (rt * 128 + w3 * 32 + r) * 4, 0, 16));
  };
  u32x4 ja[2][3], jb[2]; f32x4 jc[3];
  const LAS char* jab = lp + L_PHK + ((w >> 1) * 128 + g * 8 + q4) * 80 + p4 * 8;
  const LAS char* jbb = lp + L_VIM + ((w >> 1) * 128 + g * 8 + q4) * 40 + (w & 1) * 32 + p4 * 8;
  auto kv_rd = [&](int ks) { jb[ks & 1] = trd2(jbb + ks * 1280, jbb + ks * 1280 + 160);
#pragma unroll
    for (int ft = 0; ft < 3; ft++) ja[ks & 1][ft] = trd2(jab + ks * 2560 + ft * 32, jab + ks * 2560 + 320 + ft * 32); };
  auto kv_mm = [&](int ks) { if ((w & 1) && l15 == 4) jb[ks & 1] = (u32x4){0x3F803F80u, 0x3F803F80u, 0x3F803F80u, 0x3F803F80u};
#pragma unroll
    for (int ft = 0; ft < 3; ft++) jc[ft] = mfma16(ja[ks & 1][ft], jb[ks & 1], ks ? jc[ft] : (f32x4){0.f, 0.f, 0.f, 0.f}); };
  auto kv_st = [&]() { const int dt = w & 1; const unsigned pb = WS_PART + (hq * 16 + rt) * PART_B + (w >> 1) * 3840 + (dt ? 2560 + g * 128 : g * 256) + l15 * 16;
#pragma unroll
    for (int ft = 0; ft < 3; ft++) if ((!dt || l15 < 8) && ft * 4 + g < 10) ST16(__builtin_bit_cast(u32x4, jc[ft]), pb + ft * (dt ? 512 : 1024)); };
  f32x16 oa0, oa1; u32x4 z0, z1, z2;
  LAS char* otb = lp + L_WO + (8 + w) * 5120;
  auto oa_mm = [&](int s) { oa0 = mfma32(ka[s], ph[s], s ? oa0 : Z); oa1 = mfma32(ka[5 + s], ph[s], s ? oa1 : Z); };
  auto oa_norm = [&]() { const float inv = __builtin_amdgcn_rcpf(oa1[4] + 1e-6f) * gta;
#pragma unroll
    for (int t = 0; t < 5; t++) { const int k0 = h * 20 + 4 * t;
      const u32x2 v = t < 4 ? (u32x2){nz(pk(oa0[(4 * t) & 15] * inv, oa0[(4 * t + 1) & 15] * inv)), nz(pk(oa0[(4 * t + 2) & 15] * inv, oa0[(4 * t + 3) & 15] * inv))} : (u32x2){nz(pk(oa1[0] * inv, oa1[1] * inv)), nz(pk(oa1[2] * inv, oa1[3] * inv))};
      *(LAS u32x2*)(otb + (r >> 4) * 1280 + (k0 >> 3) * 256 + (k0 & 4) * 2 + l15 * 16) = v; }
    z0 = ldsr16(otb + lane * 16); z1 = ldsr16(otb + 1280 + lane * 16); z2 = ldsr16(otb + (r >> 4) * 1280 + 1024 + l15 * 16); };
  auto oa_st = [&]() { const unsigned oa = WS_OA + (rt * 8 + w3 * 2) * OT_B + (hq * 2 + w2) * 1280;
    ST16(z0, oa + lane * 16); ST16(z1, oa + OT_B + lane * 16); if (!h) ST16(z2, oa + (r >> 4) * OT_B + 1024 + l15 * 16); };
  unsigned cpk;
  PRIO(3);
  TL(0, 0, if (!role) kv_rd(0); else FILL(0), NOP, if (!role) kv_rd(1))
  TL(0, 1, if (!role) { kv_mm(0); kv_rd(2); } else FILL(1), NOP, if (!role) { kv_mm(1); kv_rd(3); ARRIVE_NW(0); })
  TL(0, 2, if (!role) kv_mm(2); else FILL(2), if (!role) kv_mm(3), if (!role) kv_st())

  TL(0, 3, if (w < 4) { const int row = (w & 1) * 32 + (lane >> 1);
                      dma4s((w & 2 ? woB : woA) + C0 + ((rt & 7) * 2 + role) * 40 * 640, (row < 40 ? row : 39) * 1280 + (lane & 1) * 156, lds0 + 160768); }, NOP, NOP)
  TL(0, 4, NOP, NOP, if (role) FILL(3))
  PRIO(2);
  TL(0, 5, if (!role) { WAIT_VM(0); set_flag(ws, WS_FPART + ((hq * 16 + rt) * 8 + w) * 8, tag, lane); }, NOP, cpk = __hip_atomic_load(cnt, __ATOMIC_RELAXED, __HIP_MEMORY_SCOPE_WORKGROUP))
  if (__builtin_expect((unsigned)__builtin_amdgcn_readfirstlane(cpk) < 8u, 0)) AWAIT(0, 8);
  asm volatile("" ::: "memory");
  TL6(0, KX(2, 2), KW(2, 2, 0))
  TL7(0, 1, KW(2, 2, 1), KW(2, 2, 2), KW(2, 2, 3))
  TL(1, 0, KW(2, 2, 4), if (role) FILL(4), if (red) { fp0 = __hip_atomic_load((gu64*)(ws + WS_FPART) + hq * 128 + lane, RLX_AGENT); fp1 = __hip_atomic_load((gu64*)(ws + WS_FPART) + hq * 128 + 64 + lane, RLX_AGENT); })
  TL(1, 1, if (w3) { WX(0); WX(1); }, if (role) FILL(5), if (w3) { WX(2); WX(3); })
  TL(1, 2, if (w3) WX(4), NOP, if (red) red_load())
  PRIO(1);
  TL(1, 3, NOP, NOP, NOP)
  TL(1, 4, NOP, if (!role) CLR_VM(), if (red) red_store())
  TL(1, 5, NOP, NOP, NOP)
  PRIO(0);
  TL6(1, if (red) { WAIT_VM(0); set_flag(ws, WS_FKVR + (hq * 16 + rt) * 8, tag, lane); }, NOP)
  if (red) WAIT_BAR(1); else WAIT_BAR(0);
  STAMP(12);
  PRIO(3);
  if (role) set_flag(ws, WS_FOA + ((rt * 8 + hq) * 8 + w) * 8, tag, lane);
  u64 foa;
  TL7(1, 2, KX(3, 0), KW(3, 0, 0), KW(3, 0, 1))
  TL(2, 0, KW(3, 0, 2), KW(3, 0, 3), KW(3, 0, 4)) TL(2, 1, WA(w, 0), WA(w, 1), WA(w, 2))
  TL(2, 2, WA(w, 3), WA(w, 4), (foa = __hip_atomic_load((gu64*)(ws + WS_FOA) + rt * 64 + lane, RLX_AGENT), fkr = __hip_atomic_load((gu64*)(ws + WS_FKVR) + hq * 16 + l15, RLX_AGENT)))
  PRIO(2);
  TL(2, 3, NOP, NOP, NOP)
  auto chk = [&]() { asm volatile("" : "+v"(foa), "+v"(fkr));
    if (!__all(foa == tag)) wait_flags<64>(ws, WS_FOA + rt * 512, tag, lane);
    asm volatile("" ::: "memory"); if (role) oa_load(); };
  TL(2, 4, chk(), if (role) { ka[2] = LD16(kb + 1024); ka[7] = LD16(kb + 3584); ka[3] = LD16(kb + 1536); }, if (role) { ka[8] = LD16(kb + 4096); ka[4] = LD16(kb + 2048); ka[9] = LD16(kb + 4608); })
  PRIO(1);
  TL(2, 5, NOP, NOP, if (role) oa_mm(0))
  PRIO(0);
  TL6(2, if (role) oa_mm(1), if (role) oa_mm(2))
  BAR_L();
  STAMP(13);
  PRIO(3);
  const int w5 = w < 5 ? w : 4;
  const unsigned wo16 = (w5 == 0 ? woff[0] : w5 == 1 ? woff[1] : w5 == 2 ? woff[2] : w5 == 3 ? woff[3] : woff[4]) + w5 * 1024;
  u32x4 wbr[11];
  const char* wbs = (const char*)(woB + C0 + w * 32 * 640) - 4096;
#define WB1(n) wbr[n] = *(const GAS u32x4*)(wbs + ((n) / 5) * (8 * 32 * 1280) + woff[(n) % 5] + ((n) % 5) * 1024)
#define WB2(j) do { WB1(j); WB1(5 + (j)); } while (0)
#define WA16() dma16s(woA + C0 + 16 * 32 * 640 - 2048, wo16, lds0 + L_WO + 16 * 5120 + w5 * 1024)
#define WB16() wbr[10] = *(const GAS u32x4*)((const char*)(woB + C0 + 16 * 32 * 640) - 4096 + wo16)
  if (role) {
    TL7(2, 0, oa_mm(3), { oa_mm(4); CLR_VM(); }, WA16())
    TL(0, 0, oa_norm(), NOP, oa_st())
    TL(0, 1, WA(8 + w, 0), WA(8 + w, 1), WA(8 + w, 2))
    TL(0, 2, WA(8 + w, 3), WA(8 + w, 4), WB2(0))
    TL(0, 3, WB2(1), NOP, WB2(2))
    PRIO(2);
    TL(0, 4, WB2(3), NOP, WB2(4))
  } else {
    TL7(2, 0, WA16(), WA(8 + w, 0), WA(8 + w, 1))
    TL(0, 0, WA(8 + w, 2), WA(8 + w, 3), WA(8 + w, 4))
    TL(0, 1, WB1(0), WB1(5), WB1(1)) TL(0, 2, WB1(6), WB1(2), WB1(7)) TL(0, 3, WB1(3), NOP, WB1(8))
    PRIO(2);
    TL(0, 4, WB1(4), NOP, WB1(9))
  }
  ARRIVE_NW(1);
  TL(0, 5, WB16(), NOP, NOP)
  PRIO(1);
  const int tp = w & 1, kq = w >> 1;
  const unsigned ofr = (rt * 8 + role * 4 + tp * 2) * OT_B + kq * 5120 + lane * 16;
  u32x4 of[2][5]; float gtb;
#define OFL(u) do { _Pragma("unroll") for (int i = 0; i < 5; i++) of[u][i] = LD16(WS_OA + ofr + (u) * OT_B + i * 1024); } while (0)
  TL6(0, OFL(0), gtb = __uint_as_float(__builtin_amdgcn_raw_buffer_load_b32(wsr, WS_G + (rt * 128 + w3 * 32 + w2 * 16 + l15) * 4, 0, 16)))
  PRIO(0);
  TL7(0, 0, OFL(1), NOP, cpk = __hip_atomic_load(cnt + 1, __ATOMIC_RELAXED, __HIP_MEMORY_SCOPE_WORKGROUP))
  pB2[2] = pk(e0, e1); pB2[3] = pk(e2, e3);
  OB = mfma32(v2, pB2, OB);
#undef TL
#undef TL6
#undef TL7
#undef TILE
#undef G
  STAMP(7);
  { LAS f32x4* xb = (LAS f32x4*)(lp + (w3 >> 1) * CH_B) + (w3 & 1) * (64 * 6);
#pragma unroll
    for (int i = 0; i < 12; i++) { auto rr = __builtin_amdgcn_permlane16_swap(__float_as_uint(OA[i]), __float_as_uint(OB[i]), false, false); OA[i] = __uint_as_float(rr[0]); OB[i] = __uint_as_float(rr[1]); }
    const f32x16 snd = w2 ? OA : OB;
    if (__builtin_expect((unsigned)__builtin_amdgcn_readfirstlane(cpk) < 8u, 0)) AWAIT(1, 8);
    asm volatile("" ::: "memory");
#pragma unroll
    for (int c = 0; c < 3; c++) xb[(w2 * 3 + c) * 64 + lane] = (f32x4){snd[4 * c], snd[4 * c + 1], snd[4 * c + 2], snd[4 * c + 3]};
    asm volatile("s_waitcnt vmcnt(22) lgkmcnt(0)\n\ts_barrier" ::: "memory");
    f32x16 O = w2 ? OB : OA;
#pragma unroll
    for (int c = 0; c < 3; c++) { const f32x4 t = xb[((w2 ^ 1) * 3 + c) * 64 + lane]; O[4 * c] += t[0]; O[4 * c + 1] += t[1]; O[4 * c + 2] += t[2]; O[4 * c + 3] += t[3]; }
    float l = from_upper(O[8]);
    float amax = 0.f;
#pragma unroll
    for (int i = 0; i < 12; i++) amax += fabsf(O[i]);
    const bool good = (l >= 0x1p-30f) && (l <= 0x1p100f) && (amax <= 0x1p126f);
    if (__builtin_expect(!__all(good), 0)) {
      u32x4 q1; u32x2 q2;
#pragma unroll
      for (int i = 0; i < 4; i++) { auto rr = __builtin_amdgcn_permlane16_swap(q1A[i], q1B[i], false, false); q1[i] = w2 ? rr[1] : rr[0]; }
#pragma unroll
      for (int i = 0; i < 2; i++) { auto rr = __builtin_amdgcn_permlane16_swap(q2A[i], q2B[i], false, false); q2[i] = h ? 0u : (w2 ? rr[1] : rr[0]); }
      float m = -INFINITY;
#pragma unroll 1
      for (int t = 0; t < 64; t++) {
        f32x16 s = mfma32(LD16(WS_KV + t * TILE_B + ok1), q1, Z); s = mfma32k8(LD8(WS_KV + t * TILE_B + ok2), q2, s);
#pragma unroll
        for (int i = 0; i < 16; i++) m = fmaxf(m, s[i]);
      }
      m = half_swap_max(m);
      O = Z;
#pragma unroll 1
      for (int t = 0; t < 64; t++) {
        f32x16 s = mfma32(LD16(WS_KV + t * TILE_B + ok1), q1, Z); s = mfma32k8(LD8(WS_KV + t * TILE_B + ok2), q2, s);
#pragma unroll
        for (int i = 0; i < 16; i++) s[i] = ex2(s[i] - m);
        O = mfma32(LD16(WS_KV + t * TILE_B + ov), (u32x4){pk(s[0], s[1]), pk(s[2], s[3]), pk(s[4], s[5]), pk(s[6], s[7])}, O);
        O = mfma32(LD16(WS_KV + t * TILE_B + ov + 2 * VB), (u32x4){pk(s[8], s[9]), pk(s[10], s[11]), pk(s[12], s[13]), pk(s[14], s[15])}, O);
      }
      l = from_upper(O[8]);
    }
    const float inv = __builtin_amdgcn_rcpf(l) * (1.0f - gtb);
    LAS char* tb = lp + w2 * CH_B + 12288 + w3 * 1536;
#pragma unroll
    for (int c = 0; c < 3; c++) { const int k0 = (g & 1) * 20 + 8 * c + 4 * h;
      *(LAS u32x2*)(tb + ((c == 2 && h) ? 1280 + (g & 1) * 8 : (k0 >> 3) * 256 + (k0 & 4) * 2) + l15 * 16) = (u32x2){nz(pk(O[4 * c] * inv, O[4 * c + 1] * inv)), nz(pk(O[4 * c + 2] * inv, O[4 * c + 3] * inv))}; }
    const unsigned ob = WS_OB + (rt * 8 + w3 * 2 + w2) * OT_B + hp * 1280 + lane * 16;
    const u32x4 z0 = ldsr16(tb + lane * 16), z1 = ldsr16(tb + 1024 + l15 * 16);
    ST16(z0, ob); ST16(z1, lane < 16 ? ob + 1024 : 0x7FFF0000u);
  }
  STAMP(8);
  auto of_missing = [&]() { u32x4 mm = of[0][0];
#pragma unroll
    for (int j = 1; j < 10; j++) mm = __builtin_elementwise_max(mm, of[j / 5][j % 5]);
    return missing(mm); };
  u32x4 wf[5][5];
#pragma unroll
  for (int i = 0; i < 5; i++) { const LAS char* wb = lp + L_WO + (kq * 5 + i) * 5120 + wlane;
#pragma unroll
    for (int t = 0; t < 5; t++) wf[i][t] = trd2(wb + t * 8, wb + 1280 + t * 8); }
  SBAR();
  if (__builtin_expect(of_missing(), 0)) {
#pragma unroll 1
    for (unsigned spin = 0; spin < SPIN_CAP; ++spin) {
      asm volatile("" ::: "memory");
      OFL(0); OFL(1);
      if (!of_missing()) break;
      __builtin_amdgcn_s_sleep(1);
    }
  }
  f32x4 c[2][5];
#pragma unroll
  for (int i = 0; i < 5; i++)
#pragma unroll
    for (int t = 0; t < 5; t++) c[0][t] = mfma16(wf[i][t], of[0][i], i ? c[0][t] : (f32x4){0.f, 0.f, 0.f, 0.f});
  BAR_L();
  SBAR();
  { LAS char* d = lp + L_WO + w * 5120 + lane * 16;
    *(LAS u32x4*)(lp + L_WO + 16 * 5120 + w5 * 1024 + lane * 16) = wbr[10];
#pragma unroll
    for (int i = 0; i < 5; i++) {
      *(LAS u32x4*)(d + i * 1024) = wbr[i]; *(LAS u32x4*)(d + 40960 + i * 1024) = wbr[5 + i]; of[0][i] = LD16(WS_OB + ofr + i * 1024); SBAR();
#pragma unroll
      for (int t = 0; t < 5; t++) c[1][t] = mfma16(wf[i][t], of[1][i], i ? c[1][t] : (f32x4){0.f, 0.f, 0.f, 0.f});
      SBAR();
    } }
  STAMP(9);
  BAR_L();
#pragma unroll
  for (int i = 0; i < 5; i++) { const int gr = kq * 5 + i; const LAS char* wb = lp + (gr < 17 ? L_WO + gr * 5120 : 145408 + (gr - 17) * 5120) + wlane;
#pragma unroll
    for (int t = 0; t < 5; t++) wf[i][t] = trd2(wb + t * 8, wb + 1280 + t * 8);
    SBAR(); of[1][i] = LD16(WS_OB + ofr + OT_B + i * 1024); SBAR(); }
  ARRIVE_NW(2);
  STAMP(10);
  LAS char* xw = lp + kq * 21504 + (tp * 32 + l15) * 336 + g * 80;
#pragma unroll
  for (int u = 0; u < 2; u++) {
#pragma unroll
    for (int i = 0; i < 5; i++) {
      if (__builtin_expect(missing(of[u][i]), 0)) {
#pragma unroll 1
        for (unsigned spin = 0; spin < SPIN_CAP; ++spin) {
          asm volatile("" ::: "memory");
#pragma unroll
          for (int j = u * 5 + i; j < 10; j++) of[j / 5][j % 5] = LD16(WS_OB + ofr + (j / 5) * OT_B + (j % 5) * 1024);
          if (!missing(of[u][i])) break;
          __builtin_amdgcn_s_sleep(1);
        }
      }
#pragma unroll
      for (int t = 0; t < 5; t++) c[u][t] = mfma16(wf[i][t], of[u][i], c[u][t]);
      if (u == 0 && i == 3) cpk = __hip_atomic_load(cnt + 2, __ATOMIC_RELAXED, __HIP_MEMORY_SCOPE_WORKGROUP);
      if (u == 1) { if (i == 0) { if (__builtin_expect((unsigned)__builtin_amdgcn_readfirstlane(cpk) < 8u, 0)) AWAIT(2, 8); asm volatile("" ::: "memory"); }
        *(LAS f32x4*)(xw + i * 16) = c[0][i]; }
      SBAR();
    }
  }
#pragma unroll
  for (int t = 0; t < 5; t++) *(LAS f32x4*)(xw + 16 * 336 + t * 16) = c[1][t];
  BAR_L();
  { const __amdgpu_buffer_rsrc_t orr = __builtin_amdgcn_make_buffer_rsrc(out, 0, 2048 * DM * 2, 0x00020000);
#pragma unroll
    for (int j = 0; j < 3; j++) { const int n = tid + 512 * j, nn = n < 1280 ? n : 0, row = (nn * 3277) >> 16, cq = nn - row * 20;
      const LAS f32x4* p = (const LAS f32x4*)(lp + row * 336 + cq * 16);
      const f32x4 z = (p[0] + p[1344]) + (p[2688] + p[4032]);
      __builtin_amdgcn_raw_buffer_store_b64((u32x2){pk(z[0], z[1]), pk(z[2], z[3])}, orr, n < 1280 ? ((rt * 128 + role * 64 + row) * DM + C0 + cq * 4) * 2 : 0x7FFF0000u, 0, 0); }
  }
  STAMP(11);
}

__global__ __launch_bounds__(512, 2) void fused(const u16* __restrict__ x, const u16* __restrict__ wqkv, const u16* __restrict__ wphi, const u16* __restrict__ woA,
                                                const u16* __restrict__ wq, const u16* __restrict__ wk, const u16* __restrict__ wv, const u16* __restrict__ woB,
                                                const u16* __restrict__ gatew, u16* __restrict__ out, char* ws) {
  __shared__ __attribute__((aligned(16))) char lds[L_TOTAL];
  if ((blockIdx.x >> 6) & 1) body<1>(x, wqkv, wphi, woA, wq, wv, woB, gatew, out, ws, (LAS char*)lds);
  else body<0>(x, wqkv, wphi, woA, wq, wk, woB, gatew, out, ws, (LAS char*)lds);
}
}

extern "C" void kernel_launch(void* const* d_in, const int* in_sizes, int n_in, void* d_out, int out_size, void* d_ws, size_t ws_size, hipStream_t stream) {
  using namespace gm;
  fused<<<256, 512, 0, stream>>>((const u16*)d_in[0], (const u16*)d_in[1], (const u16*)d_in[2], (const u16*)d_in[3], (const u16*)d_in[5], (const u16*)d_in[6],
                                 (const u16*)d_in[7], (const u16*)d_in[8], (const u16*)d_in[9], (u16*)d_out, (char*)d_ws);
}
```

```cpp
#include <hip/hip_runtime.h>
#include <hip/hip_bf16.h>
#include <stdint.h>

namespace gm {
typedef float f32x16 __attribute__((ext_vector_type(16)));
typedef float f32x4 __attribute__((ext_vector_type(4)));
typedef float f32x2 __attribute__((ext_vector_type(2)));
typedef short s16x4 __attribute__((ext_vector_type(4)));
typedef __bf16 b16x2 __attribute__((ext_vector_type(2)));
typedef __bf16 b16x8 __attribute__((ext_vector_type(8)));
typedef unsigned u32x4 __attribute__((ext_vector_type(4)));
typedef unsigned u32x2 __attribute__((ext_vector_type(2)));
typedef unsigned long long u64;
typedef unsigned short u16;
#define DI __device__ __forceinline__
#define LAS __attribute__((address_space(3)))
#define GAS __attribute__((address_space(1)))
typedef GAS u64 gu64;

constexpr int DM = 640;
constexpr unsigned WS_EPOCH = 0, WS_FKV = 2048, WS_FKVR = 4096, WS_FQ = 8192, WS_FPART = 16384, WS_FOA = 24576, WS_SPARE = 40960  , WS_G = 57344;
constexpr unsigned WS_KVR = 8912896, KVR_B = 10240, WS_KV = 131072, WS_QX = 327680, QX_B = 10240, WS_PART = 1703936, PART_B = 15360;
constexpr unsigned WS_OA = 3670016, WS_OB = 6291456, WS_END = 8994816, OT_B = 20480;
constexpr int VB = 352, TILE_B = 1280 + 4 * VB, CH_B = TILE_B * 8, STEP_B = 2 * CH_B;
constexpr int STAGE_B = 36864, ST_W1 = 10240, ST_X = 20480;
constexpr int L_BUF = 43008, L_P0W = 0, L_P0X0 = 25600, L_P0X1 = 153600, L_RED = 158720, L_RED1 = 40960;
constexpr int L_HO = 86016, L_QL = 96256, L_VIM = 96256, L_PHK = 116736, L_WPHI = 158592, L_ZERO = 161792, L_CNT = 163712, L_TOTAL = 163840;
constexpr int L_WO = 43008;
constexpr float LOG2E = 1.4426950408889634f;
constexpr float QSCALE = 0.22360679774997896f * LOG2E;
constexpr unsigned SPIN_CAP = 20000, LSPIN_CAP = 400000;

DI unsigned pk(float a, float b) { f32x2 v = {a, b}; b16x2 r = __builtin_convertvector(v, b16x2); return __builtin_bit_cast(unsigned, r); }
DI f32x16 mfma32(u32x4 a, u32x4 b, f32x16 c) { return __builtin_amdgcn_mfma_f32_32x32x16_bf16(__builtin_bit_cast(b16x8, a), __builtin_bit_cast(b16x8, b), c, 0, 0, 0); }
DI f32x16 mfma32k8(u32x2 a, u32x2 b, f32x16 c) { return __builtin_amdgcn_mfma_f32_32x32x8bf16_1k(__builtin_bit_cast(s16x4, a), __builtin_bit_cast(s16x4, b), c, 0, 0, 0); }
DI f32x4 mfma16(u32x4 a, u32x4 b, f32x4 c) { return __builtin_amdgcn_mfma_f32_16x16x32_bf16(__builtin_bit_cast(b16x8, a), __builtin_bit_cast(b16x8, b), c, 0, 0, 0); }
DI u32x2 trd(const LAS char* p) { return __builtin_bit_cast(u32x2, __builtin_amdgcn_ds_read_tr16_b64_v4i16((LAS s16x4*)p)); }
DI u32x4 trd2(const LAS char* p0, const LAS char* p1) { u32x2 a = trd(p0), b = trd(p1); return (u32x4){a.x, a.y, b.x, b.y}; }
DI u32x4 ldsr16(const LAS char* p) { return *(const LAS u32x4*)p; }
DI u32x2 ldsr8(const LAS char* p) { return *(const LAS u32x2*)p; }
DI float ex2(float x) { return __builtin_amdgcn_exp2f(x); }
DI void dma16s(const void* base, unsigned voff, unsigned lds_base) {
  asm volatile("s_mov_b32 m0, %2\n\ts_nop 0\n\tglobal_load_lds_dwordx4 %0, %1" :: "v"(voff), "s"(base), "s"(lds_base) : "memory"); }
DI void dma16s_sc1(const void* base, unsigned voff, unsigned lds_base) {
  asm volatile("s_mov_b32 m0, %2\n\ts_nop 0\n\tglobal_load_lds_dwordx4 %0, %1 sc1" :: "v"(voff), "s"(base), "s"(lds_base) : "memory"); }
DI void dmag_open(unsigned lds_base) { asm volatile("s_mov_b32 m0, %0\n\ts_nop 0" :: "s"(lds_base) : "memory"); }
template <int J> DI void dmag(const void* base, unsigned voff) { asm volatile("global_load_lds_dwordx4 %0, %1 offset:%2" :: "v"(voff), "s"(base), "i"((J & 3) * 1024) : "memory"); }
template <int J> DI void dmag_sc1(const void* base, unsigned voff) { asm volatile("global_load_lds_dwordx4 %0, %1 offset:%2 sc1" :: "v"(voff), "s"(base), "i"((J & 3) * 1024) : "memory"); }
DI void dma4s(const void* base, unsigned voff, unsigned lds_base) {
  asm volatile("s_mov_b32 m0, %2\n\ts_nop 0\n\tglobal_load_lds_dword %0, %1" :: "v"(voff), "s"(base), "s"(lds_base) : "memory"); }
DI u32x4 ld16_async(const void* p) { u32x4 r; asm volatile("global_load_dwordx4 %0, %1, off" : "=v"(r) : "v"(p) : "memory"); return r; }
DI u32x2 ld8_sc1_async(const void* p) { u32x2 r; asm volatile("global_load_dwordx2 %0, %1, off sc1" : "=v"(r) : "v"(p) : "memory"); return r; }
#define BAR_L() asm volatile("s_waitcnt lgkmcnt(0)\n\ts_barrier" ::: "memory")
#define WAIT_BAR(N) asm volatile("s_waitcnt vmcnt(" #N ") lgkmcnt(0)\n\ts_barrier" ::: "memory")
#define WAIT_VM(N) asm volatile("s_waitcnt vmcnt(" #N ")" ::: "memory")
#define SBAR() __builtin_amdgcn_sched_barrier(0)
#define RLX_AGENT __ATOMIC_RELAXED, __HIP_MEMORY_SCOPE_AGENT
DI float half_swap_max(float m) { auto rr = __builtin_amdgcn_permlane32_swap(__float_as_uint(m), __float_as_uint(m), false, false); return fmaxf(__uint_as_float(rr[0]), __uint_as_float(rr[1])); }
DI float from_upper(float v) { auto rr = __builtin_amdgcn_permlane32_swap(__float_as_uint(v), __float_as_uint(v), false, false); return __uint_as_float(rr[1]); }

template <int NF>
DI bool flags_set(const char* ws, unsigned off, u64 tag, int lane) {
  gu64* f = (gu64*)(ws + off);
  bool ok = __hip_atomic_load(f + (NF < 64 ? (lane & (NF - 1)) : lane), RLX_AGENT) == tag;
  if (NF >= 128) ok &= __hip_atomic_load(f + 64 + lane, RLX_AGENT) == tag;
  if (NF >= 256) { ok &= __hip_atomic_load(f + 128 + lane, RLX_AGENT) == tag; ok &= __hip_atomic_load(f + 192 + lane, RLX_AGENT) == tag; }
  return ok;
}
template <int NF>
DI void wait_flags(const char* ws, unsigned off, u64 tag, int lane) {
#pragma unroll 1
  for (unsigned spin = 0; spin < SPIN_CAP; ++spin) {
    if (__all(flags_set<NF>(ws, off, tag, lane))) break;
    __builtin_amdgcn_s_sleep(2);
  }
  asm volatile("" ::: "memory");
}
DI unsigned nz(unsigned v) { return v < 0xFFFFFFFEu ? v : 0xFFFFFFFEu; }
DI unsigned umax3(unsigned a, unsigned b, unsigned c) { unsigned m = a > b ? a : b; return m > c ? m : c; }
DI bool missing(u32x4 v) { return __any(umax3(umax3(v.x, v.y, v.z), v.w, 0u) == 0xFFFFFFFFu); }
#define ONES ((u32x4){0xFFFFFFFFu, 0xFFFFFFFFu, 0xFFFFFFFFu, 0xFFFFFFFFu})
#define ST16(v, off) __builtin_amdgcn_raw_buffer_store_b128((v), wsr, (off), 0, 16)
#define ST8(v, off) __builtin_amdgcn_raw_buffer_store_b64((v), wsr, (off), 0, 16)
#define LD16(off) __builtin_amdgcn_raw_buffer_load_b128(wsr, (off), 0, 16)
#define ST16S(v, voff, soff) __builtin_amdgcn_raw_buffer_store_b128((v), wsr, (voff), (soff), 16)
#define LD16S(voff, soff) __builtin_amdgcn_raw_buffer_load_b128(wsr, (voff), (soff), 16)
#define LD8(off) __builtin_amdgcn_raw_buffer_load_b64(wsr, (off), 0, 16)

#define STAMP(i) do {} while (0)

template <int role>
DI void body(const u16* __restrict__ x, const u16* __restrict__ wqkv, const u16* __restrict__ wphi, const u16* __restrict__ woA,
             const u16* __restrict__ wq, const u16* __restrict__ wkv, const u16* __restrict__ woB,
             const u16* __restrict__ gatew, u16* __restrict__ out, char* ws, LAS char* lp) {
  const int tid = threadIdx.x, lane = tid & 63;
  const int w = __builtin_amdgcn_readfirstlane(tid >> 6) & 7;
  const int l15 = lane & 15, g = lane >> 4, q4 = l15 >> 2, p4 = lane & 3, r = lane & 31, h = lane >> 5;
  const int bx = blockIdx.x & 7, bi = blockIdx.x >> 3;
  const int rt = (bx & 1) * 8 + (bi & 7), hp = (bx >> 1) * 4 + ((bi >> 3) & 3), hq = hp >> 1;
  const int w3 = w & 3, w2 = w >> 2;
  const unsigned lds0 = (unsigned)(uintptr_t)lp;
  const __amdgpu_buffer_rsrc_t wsr = __builtin_amdgcn_make_buffer_rsrc(ws, 0, WS_END, 0x00020000);
  LAS unsigned* cnt = (LAS unsigned*)(lp + L_CNT);
#define ARRIVE_NW(c) do { asm volatile("" ::: "memory"); __hip_atomic_store(cnt + (c) * 8 + w, 1u, __ATOMIC_RELAXED, __HIP_MEMORY_SCOPE_WORKGROUP); asm volatile("" ::: "memory"); } while (0)
#define PEEK(c) __hip_atomic_load(cnt + (c) * 8 + (lane & 7), __ATOMIC_RELAXED, __HIP_MEMORY_SCOPE_WORKGROUP)
#define AWAIT(c, v) do { if (__builtin_expect(!__all((v) != 0u), 0)) { _Pragma("unroll 1") for (unsigned sp_ = 0; sp_ < LSPIN_CAP; ++sp_) { if (__all(PEEK(c) != 0u)) break; __builtin_amdgcn_s_sleep(1); } } asm volatile("" ::: "memory"); } while (0)
#define POLL(off) __builtin_bit_cast(u64, __builtin_amdgcn_raw_buffer_load_b64(wsr, lane * 8, (off), 16))
#define FLAG(off) __builtin_amdgcn_raw_buffer_store_b64(__builtin_bit_cast(u32x2, tag), wsr, lane ? 0x7FFF0000u : 0u, (off), 16)
  STAMP(0);

  const int T0 = rt * 128 + hq * 16;
  const int C0 = hq * 80;
  const int wlane = ((g >> 1) * 16 + (g & 1) * 4 + q4) * 160 + p4 * 40;
  u64 tag;
  unsigned o0[10];
#define KVP(s, slot, p) dma16s_sc1(ws + WS_KV + (s) * STEP_B + (p) * 1024, lane * 16, lds0 + (slot) * STEP_B + (p) * 1024)
#define KVL(s, slot) do { _Pragma("unroll") for (int j = 0; j < 11; j++) if (j < 10 || w3 < 2) KVP(s, slot, w3 + 4 * j); } while (0)
#define KW(s, slot, j) do { if ((j) == 4) KVP(s, slot, w * 5 + 4); else { if ((j) == 0) dmag_open(lds0 + (slot) * STEP_B + w * 5120); dmag_sc1<(j)>(ws + WS_KV + (s) * STEP_B + w * 5120, lane * 16); } } while (0)
#define KX(s, slot) KVP(s, slot, 40 + (w & 1))
  const int w5 = w < 5 ? w : 4;
  unsigned wo16;
  unsigned woff[5];
  auto woff_init = [&]() { int ln = lane; asm volatile("" : "+v"(ln));
#pragma unroll
    for (int j = 0; j < 5; j++) { const int c = j * 64 + ln, rho = (c * 6554) >> 16, cc = c - rho * 10; const int k = (rho & ~12) | ((rho & 4) << 1) | ((rho & 8) >> 1); woff[j] = k * 1280 + cc * 16 + (4 - j) * 1024; }
    { const int c = w5 * 64 + ln, rho = (c * 6554) >> 16, cc = c - rho * 10; const int k = (rho & ~12) | ((rho & 4) << 1) | ((rho & 8) >> 1); wo16 = k * 1280 + cc * 16 + 4096; }
    asm volatile("" : "+v"(woff[0]), "+v"(woff[1]), "+v"(woff[2]), "+v"(woff[3]), "+v"(woff[4]), "+v"(wo16)); };
  if (w2 == 0) {
    u32x4 gf0, gf1, gf2, gf3, gf4, wpc;
    u32x2 ep = ld8_sc1_async(ws + WS_EPOCH);
    const int lx = w3 < 3 ? L_P0X0 + w3 * 5120 : L_P0X1;
    { const u16* wsrc = wkv + w3 * 3200;
#pragma unroll
      for (int j = 0; j < 5; j++) { const int n = j * 64 + lane, row = (n * 3277) >> 16, cc = n - row * 20; dma16s(x + T0 * DM + w3 * 160, row * 1280 + cc * 16, lds0 + lx + j * 1024); }
#pragma unroll
      for (int j = 0; j < 6; j++) dma16s(wsrc + j * 512, lane * 16, lds0 + L_P0W + w3 * 6400 + j * 1024);
      if (lane < 16) dma16s(wsrc + 6 * 512, lane * 16, lds0 + L_P0W + w3 * 6400 + 6 * 1024);
      const u16* gp = gatew + w3 * 160 + g * 8;
      gf0 = ld16_async(gp); gf1 = ld16_async(gp + 32); gf2 = ld16_async(gp + 64); gf3 = ld16_async(gp + 96); gf4 = ld16_async(gp + 128);
      wpc = ld16_async(wphi + hq * 1600 + (tid < 200 ? tid : 199) * 8);
    }
    woff_init();
    asm volatile("s_waitcnt vmcnt(0)" : "+v"(gf0), "+v"(gf1), "+v"(gf2), "+v"(gf3), "+v"(gf4), "+v"(wpc), "+v"(ep) :: "memory");
    tag = (((u64)(unsigned)__builtin_amdgcn_readfirstlane(ep.y) << 32) | (unsigned)__builtin_amdgcn_readfirstlane(ep.x)) + 1ull;
    STAMP(1);
    { const LAS char* wb = lp + L_P0W + (w3 * 160 + g * 8 + q4) * 40;
      const LAS char* xb = lp + lx + l15 * 320 + g * 16;
      const bool gl = l15 == 4;
      f32x4 c0 = {}, c1 = {};
#define P0STEP(s, GF) { const u32x4 b = ldsr16(xb + (s) * 64); c0 = mfma16(trd2(wb + p4 * 8 + (s) * 1280, wb + p4 * 8 + (s) * 1280 + 160), b, c0); \
        u32x4 a = trd2(wb + 32 + (s) * 1280, wb + 32 + (s) * 1280 + 160); if (gl) a = GF; c1 = mfma16(a, b, c1); }
      P0STEP(0, gf0) P0STEP(1, gf1) P0STEP(2, gf2) P0STEP(3, gf3) P0STEP(4, gf4)
#undef P0STEP
      *(LAS f32x4*)(lp + L_RED + tid * 16) = c0;
      if (lane < 32) *(LAS f32x4*)(lp + L_RED1 + (w3 * 32 + lane) * 16) = c1; }
    STAMP(2);
    f32x16 acc[5];
#pragma unroll
    for (int b = 0; b < 5; b++) acc[b] = (f32x16){};
    const int wm = g & 1, whd = p4;
    const int wo0 = wm * ST_W1 + (8 * h + 4 * wm + q4) * 160 + whd * 40, wo1 = wm * ST_W1 + (8 * h + 4 * (wm ^ 1) + q4) * 160 + whd * 40;
    const int xo = ST_X + (w3 * 32 + r) * 128 + ((h ^ ((r >> 1) & 7)) * 16);
    u32x4 fa[5], fb[5], xa, xb;
#define KSTEP(FN, XN, s, FC, XC) { XN = ldsr16(sb + (xo ^ ((s) * 32))); \
      _Pragma("unroll") for (int j = 0; j < 5; j++) { if (i > 0 || (s) > 0) acc[j] = mfma32(FC[j], XC, acc[j]); FN[j] = trd2(sb + wo0 + (s) * 2560 + j * 8, sb + wo1 + (s) * 2560 + j * 8); SBAR(); } }
#pragma unroll
    for (int i = 0; i < 10; i++) {
      BAR_L();
      if (i == 1 && role) {
        const unsigned qb = WS_QX + (rt * 8 + hq) * QX_B + w3 * 1280 + lane * 16;
        ST16(ONES, qb); ST16(ONES, qb + 5120); if (lane < 32) ST16(ONES, qb + 1024 + (lane >> 4) * (5120 - 256)); }
      if (i == 4) { if (role) { WAIT_VM(0); FLAG(WS_FQ + ((rt * 8 + hq) * 4 + w3) * 8); }
                    if (tid < 200) *(LAS u32x4*)(lp + L_WPHI + tid * 16) = wpc; }
      const LAS char* sb = lp + L_BUF + ((i + 2) % 3) * STAGE_B;
      KSTEP(fa, xa, 0, fb, xb) KSTEP(fb, xb, 1, fa, xa) KSTEP(fa, xa, 2, fb, xb)
      { xb = ldsr16(sb + (xo ^ 96));
        acc[0] = mfma32(fa[0], xa, acc[0]); fb[0] = trd2(sb + wo0 + 7680, sb + wo1 + 7680); fb[1] = trd2(sb + wo0 + 7688, sb + wo1 + 7688); SBAR();
        acc[1] = mfma32(fa[1], xa, acc[1]); fb[2] = trd2(sb + wo0 + 7696, sb + wo1 + 7696); fb[3] = trd2(sb + wo0 + 7704, sb + wo1 + 7704); SBAR();
        acc[2] = mfma32(fa[2], xa, acc[2]); fb[4] = trd2(sb + wo0 + 7712, sb + wo1 + 7712); SBAR();
        acc[3] = mfma32(fa[3], xa, acc[3]); acc[4] = mfma32(fa[4], xa, acc[4]); SBAR(); }
    }
#pragma unroll
    for (int j = 0; j < 5; j++) acc[j] = mfma32(fb[j], xb, acc[j]);
#undef KSTEP
    STAMP(3);
#define PK2(t, i, sc) pk(acc[t][i] * (sc), acc[t][(i) + 1] * (sc))
#define ROW(i, sc) const u32x4 ra = {PK2(0, i, sc), PK2(0, (i) + 2, sc), PK2(1, i, sc), PK2(1, (i) + 2, sc)}, rb = {PK2(2, i, sc), PK2(2, (i) + 2, sc), PK2(3, i, sc), PK2(3, (i) + 2, sc)}; const u32x2 rc = {PK2(4, i, sc), PK2(4, (i) + 2, sc)}
    { ROW(4, 1.0f);
      *(LAS u32x4*)(lp + L_HO + tid * 16) = ra; *(LAS u32x4*)(lp + L_HO + 4096 + tid * 16) = rb; *(LAS u32x2*)(lp + L_HO + 8192 + tid * 8) = rc; }
    BAR_L();
    const int qfo = (h * 4 + w3) * 1280;
    if (role) {
      { ROW(8, QSCALE); const unsigned qb = WS_QX + (rt * 8 + hq) * QX_B + qfo;
        ST16(((u32x4){nz(ra.x), nz(ra.y), nz(ra.z), nz(ra.w)}), qb + r * 16); ST16(((u32x4){nz(rb.x), nz(rb.y), nz(rb.z), nz(rb.w)}), qb + 512 + r * 16); ST8(((u32x2){nz(rc.x), nz(rc.y)}), qb + 1024 + r * 8); }
      { ROW(12, QSCALE); LAS char* d = lp + L_QL + qfo;
        *(LAS u32x4*)(d + r * 16) = ra; *(LAS u32x4*)(d + 512 + r * 16) = rb; *(LAS u32x2*)(d + 1024 + r * 8) = rc; }
    } else { LAS char* d = lp + L_VIM + (h * 128 + w3 * 32 + r) * 40;
#pragma unroll
      for (int t = 0; t < 5; t++) { *(LAS u32x2*)(d + t * 8) = (u32x2){PK2(t, 8, 1.0f), PK2(t, 10, 1.0f)}; *(LAS u32x2*)(d + 10240 + t * 8) = (u32x2){PK2(t, 12, 1.0f), PK2(t, 14, 1.0f)}; } }
#pragma unroll
    for (int t = 0; t < 5; t++) { o0[2 * t] = PK2(t, 0, 1.0f); o0[2 * t + 1] = PK2(t, 2, 1.0f); }
#undef ROW
#undef PK2
  } else {
    u32x2 ep = ld8_sc1_async(ws + WS_EPOCH);
    const bool second = w3 >> 1;
    const unsigned rs = (second && role) ? 1280u : 3840u;
    const char* wsrc = (second ? (role ? (const char*)(wq + C0) : (const char*)(wqkv + 1280 + C0)) : (const char*)(wqkv + (role ? 0 : 640) + C0)) + (w3 & 1) * 32 * rs;
    const char* xsrc = (const char*)(x + (rt * 128 + w3 * 32) * DM);
    int rot = (bi & 7) + 3 * ((bi >> 3) & 3); rot = rot >= 10 ? rot - 10 : rot;
    unsigned so[5], xoff[2];
#pragma unroll
    for (int j = 0; j < 5; j++) { const int c = j * 64 + lane, rho = (c * 6554) >> 16, cc = c - rho * 10; so[j] = (rho ^ (second ? 4 : 0)) * rs + cc * 16; }
#pragma unroll
    for (int j = 0; j < 2; j++) { const int row = j * 8 + (lane >> 3); xoff[j] = row * 1280 + (((lane & 7) ^ ((row >> 1) & 7)) * 16); }
    int bsel = 2, kc = rot;
    auto stage = [&]() {
      const unsigned lb = lds0 + L_BUF + bsel * STAGE_B;
      const char* bw = wsrc + kc * 64 * rs; const char* bxp = xsrc + kc * 128;
#pragma unroll
      for (int j = 0; j < 5; j++) { dma16s(bw, so[j], lb + (w3 * 5 + j) * 1024); if (j < 4) dma16s(bxp + (j >> 1) * 16 * 1280, xoff[j & 1], lb + ST_X + (w3 * 4 + j) * 1024); }
      bsel = bsel == 2 ? 0 : bsel + 1; kc = kc == 9 ? 0 : kc + 1;
    };
    stage(); stage();
    woff_init();
    asm volatile("s_waitcnt vmcnt(9)" : "+v"(ep) :: "memory");
    tag = (((u64)(unsigned)__builtin_amdgcn_readfirstlane(ep.y) << 32) | (unsigned)__builtin_amdgcn_readfirstlane(ep.x)) + 1ull;
    STAMP(1); STAMP(2);
#pragma unroll 1
    for (int i = 0; i < 8; i++) {
      WAIT_BAR(9);
      stage();
      if (i == 0 && w3 == 0) {
        const unsigned tb = WS_KV + (T0 >> 5) * TILE_B; const int s16 = hq & 1;
        if (!role) {
          const LAS f32x4* rp = (const LAS f32x4*)(lp + (h ? L_RED1 : L_RED)) + (h ? (g & 1) * 16 : g * 32) + l15;
          const int st = h ? 32 : 64, o2 = h ? 0 : 16;
          const f32x4 a = (rp[0] + rp[st]) + (rp[2 * st] + rp[3 * st]), b = (rp[o2] + rp[st + o2]) + (rp[2 * st + o2] + rp[3 * st + o2]);
          const u32x4 v = {pk(a[0], a[1]), pk(a[2], a[3]), pk(b[0], b[1]), pk(b[2], b[3])};
          if (lane < 32) ST16(v, tb + (g * 32 + s16 * 16 + l15) * 16);
          else if (lane < 48) ST8(((u32x2){v.x, v.y}), tb + 1024 + (s16 * 16 + l15) * 8);
          else { const float gv = 1.0f / (1.0f + __expf(-a[0])); __builtin_amdgcn_raw_buffer_store_b32(__float_as_uint(gv), wsr, WS_G + (T0 + l15) * 4, 0, 16); }
        } else if (lane < 44) {
          const int hh = lane >= 22, d = lane - hh * 22, dd = d < 20 ? d : 0, st = dd >= 16 ? 128 : 256;
          const LAS float* rp = (const LAS float*)(lp + (dd >= 16 ? L_RED1 : L_RED)) + (((dd & 15) >> 2) * 16 + 4 * hh) * 4 + (dd & 3);
          float e[8];
#pragma unroll
          for (int j = 0; j < 8; j++) { const int tk = (8 * (j >> 2) + (j & 3)) * 4; e[j] = (rp[tk] + rp[tk + st]) + (rp[tk + 2 * st] + rp[tk + 3 * st]); }
          u32x4 v = {pk(e[0], e[1]), pk(e[2], e[3]), pk(e[4], e[5]), pk(e[6], e[7])};
          if (d == 20) v = (u32x4){0x3F803F80u, 0x3F803F80u, 0x3F803F80u, 0x3F803F80u};
          if (d == 21) v = (u32x4){0u, 0u, 0u, 0u};
          ST16(v, tb + 1280 + (s16 * 2 + hh) * VB + d * 16);
        }
      }
      if (i == 2) { if (w3 == 0) FLAG(WS_FKV + blockIdx.x * 8);
                    if (w3 >= 2) *(LAS u32x4*)(lp + L_ZERO + (tid - 384) * 16) = (u32x4){0u, 0u, 0u, 0u}; }
    }
    u64 fa, fb, fc, fd, fe;
    { gu64* f = (gu64*)(ws + WS_FKV) + lane;
      fa = __hip_atomic_load(f, RLX_AGENT); fb = __hip_atomic_load(f + 64, RLX_AGENT); fc = __hip_atomic_load(f + 128, RLX_AGENT); fd = __hip_atomic_load(f + 192, RLX_AGENT);
      fe = __hip_atomic_load((gu64*)(ws + WS_FQ) + (rt * 8 + hq) * 4 + p4, RLX_AGENT); }
    WAIT_BAR(14);
    asm volatile("" : "+v"(fa), "+v"(fb), "+v"(fc), "+v"(fd), "+v"(fe));
    const bool kvok = __all((fa == tag) & (fb == tag) & (fc == tag) & (fd == tag) & (fe == tag));
    asm volatile("" ::: "memory");
    BAR_L();
    if (!kvok) { wait_flags<256>(ws, WS_FKV, tag, lane); wait_flags<4>(ws, WS_FQ + (rt * 8 + hq) * 32, tag, lane); }
    KVL(0, 0);
    if (blockIdx.x == 0 && w3 == 0) FLAG(WS_EPOCH);
    STAMP(3);
    BAR_L();
    const int ht = tid - 256;
    const u32x4 a = ldsr16(lp + L_HO + ht * 16), b = ldsr16(lp + L_HO + 4096 + ht * 16); const u32x2 c = ldsr8(lp + L_HO + 8192 + ht * 8);
    o0[0] = a.x; o0[1] = a.y; o0[2] = a.z; o0[3] = a.w; o0[4] = b.x; o0[5] = b.y; o0[6] = b.z; o0[7] = b.w; o0[8] = c.x; o0[9] = c.y;
  }
  STAMP(4);
  u32x4 ph[5];
  { const LAS char* pb = ((p4 & 1) == h) ? lp + L_WPHI + (2 * w2 + h) * 800 + q4 * 40 + (2 * (g & 1) + (p4 >> 1)) * 8 : lp + L_ZERO;
    const u32x4 b0 = {o0[0], o0[1], o0[2], o0[3]}, b1 = {o0[4], o0[5], o0[6], o0[7]}, b2 = {o0[8], o0[9], 0u, 0u};
    const u32x2 c0 = trd(pb + 640), c1 = trd(pb + 672);
    f32x16 P0 = {}, P1 = {};
    P0 = mfma32(trd2(pb, pb + 160), b0, P0); P1 = mfma32(trd2(pb + 32, pb + 192), b0, P1);
    P0 = mfma32(trd2(pb + 320, pb + 480), b1, P0); P1 = mfma32(trd2(pb + 352, pb + 512), b1, P1);
    P0 = mfma32((u32x4){c0.x, c0.y, 0u, 0u}, b2, P0); P1 = mfma32((u32x4){c1.x, c1.y, 0u, 0u}, b2, P1);
    KX(1, 1); KW(1, 1, 0); SBAR();
    f32x2 p[10];
#pragma unroll
    for (int f = 0; f < 10; f++) p[f] = f < 8 ? (f32x2){P0[(2 * f) & 15], P0[(2 * f + 1) & 15]} : (f32x2){P1[(2 * f) & 3], P1[(2 * f + 1) & 3]};
    float mx = fmaxf(p[0][0], p[0][1]), mn = fminf(p[0][0], p[0][1]);
#pragma unroll
    for (int f = 1; f < 10; f++) { mx = fmaxf(fmaxf(mx, p[f][0]), p[f][1]); mn = fminf(fminf(mn, p[f][0]), p[f][1]); }
    const f32x2 ma = {-mx * LOG2E, -mx * LOG2E}, mb = {mn * LOG2E, mn * LOG2E}, ca_ = {LOG2E, LOG2E}, cb_ = {-LOG2E, -LOG2E};
    f32x2 ea[10], eb[10], sa = {0.f, 0.f}, sb = {0.f, 0.f};
#pragma unroll
    for (int f = 0; f < 10; f++) { if (f % 2 == 0 && f < 8) { SBAR(); if (f == 0) KW(1, 1, 1); if (f == 2) KW(1, 1, 2); if (f == 4) KW(1, 1, 3); if (f == 6) KW(1, 1, 4); SBAR(); }
      const f32x2 xa = __builtin_elementwise_fma(p[f], ca_, ma), xb = __builtin_elementwise_fma(p[f], cb_, mb);
      ea[f] = (f32x2){ex2(xa[0]), ex2(xa[1])}; eb[f] = (f32x2){ex2(xb[0]), ex2(xb[1])}; sa += ea[f]; sb += eb[f]; }
    const float ia = __builtin_amdgcn_rcpf(sa[0] + sa[1]), ib = __builtin_amdgcn_rcpf(sb[0] + sb[1]);
    unsigned o[20];
#pragma unroll
    for (int f = 0; f < 10; f++) { const f32x2 ya = ea[f] * ia, yb = eb[f] * ib; o[f] = pk(ya[0], ya[1]); o[10 + f] = pk(yb[0], yb[1]); }
#pragma unroll
    for (int c = 0; c < 5; c++) ph[c] = (u32x4){o[4 * c], o[4 * c + 1], o[4 * c + 2], o[4 * c + 3]};
    asm volatile("" : "+v"(ph[0]), "+v"(ph[1]), "+v"(ph[2]), "+v"(ph[3]), "+v"(ph[4]));
  }
  u32x4 q1A, q1B; u32x2 q2A, q2B;
  if (role) {
    WAIT_BAR(0);
    STAMP(5);
    const LAS char* qa = lp + L_QL + w3 * 1280;
    q1A = ldsr16(qa + lane * 16); q1B = ldsr16(qa + 5120 + lane * 16); q2A = ldsr8(qa + 1024 + r * 8); q2B = ldsr8(qa + 6144 + r * 8);
    ARRIVE_NW(0);
  } else {
    const unsigned qb = WS_QX + (rt * 8 + hq) * QX_B + w3 * 1280;
    q1A = LD16(qb + lane * 16); q1B = LD16(qb + 5120 + lane * 16); q2A = LD8(qb + 1024 + r * 8); q2B = LD8(qb + 6144 + r * 8);
    { LAS char* d = lp + L_PHK + ((2 * w2 + h) * 128 + w3 * 32 + r) * 80;
#pragma unroll
      for (int c = 0; c < 5; c++) *(LAS u32x4*)(d + c * 16) = ph[c]; }
    WAIT_BAR(4);
    STAMP(5);
#pragma unroll 1
    for (unsigned spin = 0; spin < SPIN_CAP; ++spin) {
      if (!(missing(q1A) | missing(q1B) | missing((u32x4){q2A.x, q2A.y, q2B.x, q2B.y}))) break;
      __builtin_amdgcn_s_sleep(1);
      asm volatile("" ::: "memory");
      q1A = LD16(qb + lane * 16); q1B = LD16(qb + 5120 + lane * 16); q2A = LD8(qb + 1024 + r * 8); q2B = LD8(qb + 6144 + r * 8);
    }
  }
  STAMP(6);
  const int vr = r <= 21 ? r : 21;
  const int ok1 = lane * 16, ok2 = h ? (1280 + 20 * 16) : (1024 + r * 8), ov = 1280 + h * VB + vr * 16;
  const f32x16 Z = {};
  if (h) { q2A = (u32x2){0u, 0u}; q2B = (u32x2){0u, 0u}; }
  { const LAS char* t0 = lp;
    const u32x4 k1 = ldsr16(t0 + ok1); const u32x2 k2 = ldsr8(t0 + ok2);
    f32x16 sa = mfma32(k1, q1A, Z); sa = mfma32k8(k2, q2A, sa);
    f32x16 sb = mfma32(k1, q1B, Z); sb = mfma32k8(k2, q2B, sb);
    float ma = sa[0], mb = sb[0];
#pragma unroll
    for (int i = 1; i < 16; i++) { ma = fmaxf(ma, sa[i]); mb = fmaxf(mb, sb[i]); }
    ma = half_swap_max(ma); mb = half_swap_max(mb);
    const float mah = __uint_as_float(__float_as_uint(ma) & 0xFFFF0000u), mbh = __uint_as_float(__float_as_uint(mb) & 0xFFFF0000u);
    if (h) { q2A.x = pk(-mah, mah - ma); q2B.x = pk(-mbh, mbh - mb); } }
  f32x16 OA = {}, OB = {}, SA, SB;
  u32x4 k1, v1, v2, v2n, pA1, pA2, pB1, pB2; u32x2 k2;
  float e0, e1, e2, e3;
  const LAS char* c0p = lp + w2 * CH_B + ok1; const LAS char* c1p = lp + w2 * CH_B + ok2; const LAS char* c2p = lp + w2 * CH_B + ov;
  k1 = ldsr16(c0p); k2 = ldsr8(c1p);
  SA = mfma32(k1, q1A, Z); SA = mfma32k8(k2, q2A, SA);
  v1 = ldsr16(c2p); v2 = v1;
  e0 = e1 = e2 = e3 = 0.f; pB2 = (u32x4){0u, 0u, 0u, 0u};
#define G(MF, LD, H, M, C0_, C1_, X, S_, i) MF; LD; H; SBAR(); C0_; C1_; SBAR(); X##0 = ex2(S_[i]); X##1 = ex2(S_[i + 1]); SBAR(); M; SBAR(); X##2 = ex2(S_[i + 2]); X##3 = ex2(S_[i + 3]); \
    asm volatile("" : "+v"(X##0), "+v"(X##1), "+v"(X##2), "+v"(X##3)); SBAR();
#define TILEF(TN, V2, F) { float a0, a1, a2, a3; \
    G(SB = mfma32(k1, q1B, Z),    k1 = ldsr16(d0p + (TN)), F(0), F(8),  pB2[2] = pk(e0, e1), pB2[3] = pk(e2, e3), a, SA, 0) \
    G(OB = mfma32(v2, pB2, OB),   V2,                      F(1), F(9),  pA1[0] = pk(a0, a1), pA1[1] = pk(a2, a3), e, SA, 4) \
    G(SB = mfma32k8(k2, q2B, SB), k2 = ldsr8(d1p + (TN)),  F(2), F(10), pA1[2] = pk(e0, e1), pA1[3] = pk(e2, e3), a, SA, 8) \
    G(OA = mfma32(v1, pA1, OA),   (void)0,                 F(3), F(11), pA2[0] = pk(a0, a1), pA2[1] = pk(a2, a3), e, SA, 12) \
    G(SA = mfma32(k1, q1A, Z),    (void)0,                 F(4), F(12), pA2[2] = pk(e0, e1), pA2[3] = pk(e2, e3), a, SB, 0) \
    G(OA = mfma32(v2, pA2, OA),   (void)0,                 F(5), F(13), pB1[0] = pk(a0, a1), pB1[1] = pk(a2, a3), e, SB, 4) \
    G(SA = mfma32k8(k2, q2A, SA), (void)0,                 F(6), F(14), pB1[2] = pk(e0, e1), pB1[3] = pk(e2, e3), a, SB, 8) \
    G(OB = mfma32(v1, pB1, OB),   v1 = ldsr16(d2p + (TN)), F(7), F(15), pB2[0] = pk(a0, a1), pB2[1] = pk(a2, a3), e, SB, 12) }
#define TILE(TB, TN, V2, H0, H1, H2) { auto f_ = [&](int n) { if (n == 3) { H0; } if (n == 4) { H1; } if (n == 5) { H2; } }; TILEF(TN, V2, f_) }
#define TLF(t, F) TILEF(((t) + 1) * TILE_B, v2 = ldsr16(d2p + (t) * TILE_B + 2 * VB), F)
#define NOP (void)0
  const LAS char *d0p = c0p, *d1p = c1p, *d2p = c2p;
#define TL(sl, t, H0, H1, H2) TILE(0, ((t) + 1) * TILE_B, v2 = ldsr16(d2p + (t) * TILE_B + 2 * VB), H0, H1, H2)
#define TL6(sl, H0, H1) TL(sl, 6, H0, H1, v2n = ldsr16(d2p + 7 * TILE_B + 2 * VB))
#define TL7(sl, sn, H0, H1, H2) d0p += ((sn) - (sl)) * STEP_B; d1p += ((sn) - (sl)) * STEP_B; d2p += ((sn) - (sl)) * STEP_B; asm volatile("" : "+v"(d0p), "+v"(d1p), "+v"(d2p)); TILE(0, 0, v2 = v2n, H0, H1, H2)
#define WOG(wo, gr, j, dst) do { if ((j) == 4) WOP(wo, gr, 4, dst); else { if ((j) == 0) dmag_open(lds0 + (dst)); dmag<(j)>((wo) + C0 + (gr) * 32 * 640 - 2048, woff[j]); } } while (0)
#define WOP(wo, gr, j, dst) dma16s((wo) + C0 + (gr) * 32 * 640 - (4 - (j)) * 512, woff[j], lds0 + (dst) + (j) * 1024)
#define WA(gr, j) WOG(woA, gr, j, L_WO + (gr) * 5120)
  const int wx3 = w3 ? w3 : 1;
  const u16* wxs = (w2 ? woB : woA) + (16 + wx3) * 32 * 640;
  const int wxd = w2 ? 145408 + (wx3 - 1) * 5120 : L_WO + (16 + wx3) * 5120;
#define WX(j) WOG(wxs, 0, j, wxd)
#define PRIO(n) __builtin_amdgcn_s_setprio(n)
  u32x4 ones = ONES; if (role) asm volatile("" : "+v"(ones));
#define FILL(j) do { const int n_ = w * 3 + ((j) >> 1); ST16S(ones, ok1, (((j) & 1) ? WS_OB : WS_OA) + (rt * 8 + hq) * OT_B + (n_ < 20 ? n_ : 19) * 1024); } while (0)
#define CLR_VM() __builtin_amdgcn_s_waitcnt(0x0F70)
  const bool red = !role && w2;
  f32x4 part[4]; u64 fp0, fp1;
  auto red_load = [&]() {
    asm volatile("" : "+v"(fp0), "+v"(fp1));
    if (__builtin_expect(!__all((fp0 == tag) & (fp1 == tag)), 0)) wait_flags<128>(ws, WS_FPART + hq * 1024, tag, lane);
    asm volatile("" ::: "memory");
#pragma unroll
    for (int j = 0; j < 4; j++) part[j] = __builtin_bit_cast(f32x4, LD16S(g * (4 * PART_B) + l15 * 16, WS_PART + (hq * 16 + j) * PART_B + rt * 960 + w3 * 240));
  };
  auto red_store = [&]() {
    f32x4 a = (part[0] + part[1]) + (part[2] + part[3]);
#pragma unroll
    for (int i = 0; i < 4; i++) { auto r16 = __builtin_amdgcn_permlane16_swap(__float_as_uint(a[i]), __float_as_uint(a[i]), false, false); const float t = __uint_as_float(r16[0]) + __uint_as_float(r16[1]);
      auto r32 = __builtin_amdgcn_permlane32_swap(__float_as_uint(t), __float_as_uint(t), false, false); a[i] = __uint_as_float(r32[0]) + __uint_as_float(r32[1]); }
    const int hd = rt >> 2, u = (rt & 3) * 60 + w3 * 15 + lane, hi = u >= 160, fq = hi ? (u - 160) >> 3 : u >> 4, di = hi ? u & 7 : u & 15;
    __builtin_amdgcn_raw_buffer_store_b64((u32x2){pk(a[0], a[1]), pk(a[2], a[3])}, wsr, lane < 15 ? (hi * 5 + (fq >> 1)) * 512 + di * 16 + (fq & 1) * 8 : 0x7FFF0000u, WS_KVR + hq * KVR_B + (hd >> 1) * 5120 + (hd & 1) * 256, 16);
  };
  u32x4 ka[10]; u64 fkr; float gta; unsigned kb; const unsigned kbs = WS_KVR + hq * KVR_B + w2 * 5120;
  auto oa_load = [&]() {
    asm volatile("" : "+v"(fkr));
    if (__builtin_expect(!__all(fkr == tag), 0)) wait_flags<64>(ws, WS_FKVR + hq * 512, tag, lane);
    asm volatile("" ::: "memory");
    kb = (((r >> 2) & 1) == h) ? (h * 16 + (r & 3) + 4 * (r >> 3)) * 16 : 0x7FFF0000u;
    ka[0] = LD16S(kb, kbs); ka[5] = LD16S(kb, kbs + 2560); ka[1] = LD16S(kb, kbs + 512); ka[6] = LD16S(kb, kbs + 3072);
    gta = __uint_as_float(__builtin_amdgcn_raw_buffer_load_b32(wsr, WS_G + (rt * 128 + w3 * 32 + r) * 4, 0, 16));
  };
  u32x4 jf[16]; f32x4 jc[3];
  const LAS char* jab = lp + L_PHK + ((w >> 1) * 128 + g * 8 + q4) * 80 + p4 * 8;
  const LAS char* jbb = lp + L_VIM + ((w >> 1) * 128 + g * 8 + q4) * 40 + (w & 1) * 32 + p4 * 8;
  auto kv_rd = [&](int n) { const int ks = n >> 2, j = n & 3;
    jf[n] = j ? trd2(jab + ks * 2560 + (j - 1) * 32, jab + ks * 2560 + 320 + (j - 1) * 32) : trd2(jbb + ks * 1280, jbb + ks * 1280 + 160); };
  auto kv_mm = [&](int m) { const int ks = m / 3, ft = m % 3;
    if (ft == 0 && (w & 1) && l15 == 4) jf[ks * 4] = (u32x4){0x3F803F80u, 0x3F803F80u, 0x3F803F80u, 0x3F803F80u};
    jc[ft] = mfma16(jf[ks * 4 + 1 + ft], jf[ks * 4], ks ? jc[ft] : (f32x4){0.f, 0.f, 0.f, 0.f}); };
  auto kv_st = [&](int ft) { const int dt = w & 1;
    const unsigned vo = ((!dt || l15 < 8) && (ft < 2 || g < 2)) ? (dt ? g * 128 : g * 256) + l15 * 16 : 0x7FFF0000u;
    ST16S(__builtin_bit_cast(u32x4, jc[ft]), vo, WS_PART + (hq * 16 + rt) * PART_B + (w >> 1) * 3840 + (dt ? 2560 + ft * 512 : ft * 1024)); };
  f32x16 oa0, oa1; u32x4 z0, z1, z2;
  LAS char* otb = lp + L_WO + (8 + w) * 5120;
  auto oa_mm = [&](int m) { const int s = m >> 1; if (m & 1) oa1 = mfma32(ka[5 + s], ph[s], s ? oa1 : Z); else oa0 = mfma32(ka[s], ph[s], s ? oa0 : Z); };
  auto oa_norm = [&]() { const float inv = __builtin_amdgcn_rcpf(oa1[4] + 1e-6f) * gta;
    LAS char* ar = otb + (r >> 4) * 1280 + l15 * 16; LAS char* ae = ar + h * 520; LAS char* ao = ar + 8 + h * 760;
#pragma unroll
    for (int t = 0; t < 5; t++) {
      const u32x2 v = t < 4 ? (u32x2){nz(pk(oa0[(4 * t) & 15] * inv, oa0[(4 * t + 1) & 15] * inv)), nz(pk(oa0[(4 * t + 2) & 15] * inv, oa0[(4 * t + 3) & 15] * inv))} : (u32x2){nz(pk(oa1[0] * inv, oa1[1] * inv)), nz(pk(oa1[2] * inv, oa1[3] * inv))};
      *(LAS u32x2*)(((t & 1) ? ao : ae) + (t >> 1) * 256) = v; }
    z0 = ldsr16(otb + ok1); z1 = ldsr16(otb + 1280 + ok1); z2 = ldsr16(ar + 1024); };
  auto oa_st = [&]() { const unsigned oa = WS_OA + (rt * 8 + w3 * 2) * OT_B + (hq * 2 + w2) * 1280;
    ST16S(z0, ok1, oa); ST16S(z1, ok1, oa + OT_B); ST16S(z2, h ? 0x7FFF0000u : (r >> 4) * OT_B + l15 * 16, oa + 1024); };
  unsigned cpk;
  PRIO(3);
  if (role) { TL(0, 0, FILL(0), NOP, NOP) TL(0, 1, FILL(1), NOP, NOP) TL(0, 2, FILL(2), NOP, NOP) }
  else {
    auto f0 = [&](int n) { if (n < 8) kv_rd(n); if (n >= 13) kv_mm(n - 13); };
    auto f1 = [&](int n) { if (n < 8) kv_rd(8 + n); if (n == 7) ARRIVE_NW(0); if (n >= 9 && n < 12) kv_mm(n - 6); if (n >= 13) kv_mm(n - 7); };
    auto f2 = [&](int n) { if (n >= 9 && n < 12) kv_mm(n); if (n >= 5 && n < 8) kv_st(n - 5); };
    TLF(0, f0) TLF(1, f1) TLF(2, f2)
  }
  TL(0, 3, dma4s((w2 ? woB : woA) + C0 + (((rt & 7) * 2 + role) * 40 + w3 * 10) * 640, (l15 < 10 ? l15 : 9) * 1280 + (g & 1) * 156, lds0 + 160768)        , NOP, NOP)
  TL(0, 4, NOP, NOP, if (role) FILL(3))
  PRIO(2);
  TL(0, 5, if (!role) { WAIT_VM(0); FLAG(WS_FPART + ((hq * 16 + rt) * 8 + w) * 8); }, NOP, cpk = PEEK(0))
  AWAIT(0, cpk);
  asm volatile("" ::: "memory");
  TL6(0, KX(2, 2), KW(2, 2, 0))
  TL7(0, 1, KW(2, 2, 1), KW(2, 2, 2), KW(2, 2, 3))
  TL(1, 0, KW(2, 2, 4), if (role) FILL(4), if (!role) { fp0 = POLL(WS_FPART + hq * 1024); fp1 = POLL(WS_FPART + hq * 1024 + 512); })
  TL(1, 1, if (role) { WX(0); WX(1); }, if (role) FILL(5), if (role) { WX(2); WX(3); })
  TL(1, 2, if (role) WX(4), NOP, if (red) red_load())
  PRIO(1);
  TL(1, 3, NOP, NOP, NOP)
  TL(1, 4, NOP, if (!role) CLR_VM(), if (red) red_store())
  TL(1, 5, NOP, NOP, NOP)
  PRIO(0);
  TL6(1, if (!role) { WAIT_VM(0); FLAG((w2 ? WS_FKVR : WS_SPARE) + ((hq * 16 + rt) * 4 + w3) * 8); }, NOP)
  if (!role) WAIT_BAR(1); else WAIT_BAR(0);
  STAMP(12);
  PRIO(3);
  if (role) FLAG(WS_FOA + ((rt * 8 + hq) * 8 + w) * 8);
  u64 foa;
  TL7(1, 2, KX(3, 0), KW(3, 0, 0), KW(3, 0, 1))
  TL(2, 0, KW(3, 0, 2), KW(3, 0, 3), KW(3, 0, 4)) TL(2, 1, WA(w, 0), WA(w, 1), WA(w, 2))
  u32x4 wbr[11];
  const char* wbs = (const char*)(woB + C0 + w * 32 * 640) - 4096;
#define WB1(n) wbr[n] = *(const GAS u32x4*)(wbs + ((n) / 5) * (8 * 32 * 1280) + woff[(n) % 5] + ((n) % 5) * 1024)
#define WB2(j) do { WB1(j); WB1(5 + (j)); } while (0)
#define WA16() dma16s(woA + C0 + 16 * 32 * 640 - 2048, wo16, lds0 + L_WO + 16 * 5120 + w5 * 1024)
#define WB16() wbr[10] = *(const GAS u32x4*)((const char*)(woB + C0 + 16 * 32 * 640) - 4096 + wo16)
  auto chk = [&]() { asm volatile("" : "+v"(foa));
    if (__builtin_expect(!__all(foa == tag), 0)) wait_flags<64>(ws, WS_FOA + rt * 512, tag, lane);
    asm volatile("" ::: "memory"); };
  if (role) {
    TL(2, 2, WA(w, 3), WA(w, 4), NOP)
    PRIO(2);
    TL(2, 3, (fkr = POLL(WS_FKVR + hq * 512), foa = POLL(WS_FOA + rt * 512)), NOP, NOP)
    TL(2, 4, NOP, { chk(); oa_load(); }, { ka[2] = LD16S(kb, kbs + 1024); ka[7] = LD16S(kb, kbs + 3584); ka[3] = LD16S(kb, kbs + 1536); })
    PRIO(1);
    TL(2, 5, { ka[8] = LD16S(kb, kbs + 4096); ka[4] = LD16S(kb, kbs + 2048); ka[9] = LD16S(kb, kbs + 4608); }, NOP, NOP)
    PRIO(0);
    { auto f = [&](int n) { if (n == 5) v2n = ldsr16(d2p + 7 * TILE_B + 2 * VB); if (n >= 8 && n != 10 && n != 14) oa_mm(n < 10 ? n - 8 : (n < 14 ? n - 9 : 5)); }; TLF(6, f) }
  } else {
    TL(2, 2, WA(w, 3), WA(w, 4), WX(0))
    PRIO(2);
    TL(2, 3, { WX(1); WX(2); }, { WX(3); WX(4); }, foa = POLL(WS_FOA + rt * 512))
    TL(2, 4, NOP, NOP, NOP)
    PRIO(1);
    TL(2, 5, chk(), WB1(0), WB1(5))
    PRIO(0);
    TL6(2, WB1(1), WB1(6))
  }
  BAR_L();
  STAMP(13);
  PRIO(3);
  if (role) {
    d0p -= 2 * STEP_B; d1p -= 2 * STEP_B; d2p -= 2 * STEP_B; asm volatile("" : "+v"(d0p), "+v"(d1p), "+v"(d2p));
    { auto f = [&](int n) { if (n == 8 || n == 9) oa_mm(n - 2); if (n == 11 || n == 12) oa_mm(n - 3); if (n == 5) CLR_VM(); if (n == 6) WA16(); }; TILEF(0, v2 = v2n, f) }
    TL(0, 0, oa_norm(), NOP, oa_st())
    TL(0, 1, WA(8 + w, 0), WA(8 + w, 1), WA(8 + w, 2))
    TL(0, 2, WA(8 + w, 3), WA(8 + w, 4), WB2(0))
    TL(0, 3, WB2(1), NOP, WB2(2))
    PRIO(2);
    TL(0, 4, WB2(3), NOP, WB2(4))
  } else {
    TL7(2, 0, WA16(), WA(8 + w, 0), WA(8 + w, 1))
    TL(0, 0, WA(8 + w, 2), WA(8 + w, 3), WA(8 + w, 4))
    TL(0, 1, WB1(2), NOP, WB1(7)) TL(0, 2, WB1(3), NOP, WB1(8)) TL(0, 3, WB1(4), NOP, WB1(9))
    PRIO(2);
    TL(0, 4, NOP, NOP, NOP)
  }
  ARRIVE_NW(1);
  const int tp = w & 1, kq = w >> 1;
  const unsigned ofr = (rt * 8 + role * 4 + tp * 2) * OT_B + kq * 5120;
  u32x4 of[2][5]; float gtb;
#define OFL(u) do { _Pragma("unroll") for (int i = 0; i < 5; i++) of[u][i] = LD16S(ok1, WS_OA + ofr + (u) * OT_B + i * 1024); } while (0)
#define OF2(i) do { of[0][i] = LD16S(ok1, WS_OA + ofr + (i) * 1024); of[1][i] = LD16S(ok1, WS_OA + ofr + OT_B + (i) * 1024); } while (0)
  TL(0, 5, WB16(), gtb = __uint_as_float(__builtin_amdgcn_raw_buffer_load_b32(wsr, WS_G + (rt * 128 + w3 * 32 + w2 * 16 + l15) * 4, 0, 16)), OF2(0))
  PRIO(1);
  TL6(0, OF2(1), OF2(2))
  PRIO(0);
  TL7(0, 0, OF2(3), OF2(4), cpk = PEEK(1))
  pB2[2] = pk(e0, e1); pB2[3] = pk(e2, e3);
  OB = mfma32(v2, pB2, OB);
#undef TL
#undef TL6
#undef TL7
#undef TILE
#undef G
  STAMP(7);
  { LAS f32x4* xb = (LAS f32x4*)(lp + (w3 >> 1) * CH_B) + (w3 & 1) * (64 * 6);
#pragma unroll
    for (int i = 0; i < 12; i++) { auto rr = __builtin_amdgcn_permlane16_swap(__float_as_uint(OA[i]), __float_as_uint(OB[i]), false, false); OA[i] = __uint_as_float(rr[0]); OB[i] = __uint_as_float(rr[1]); }
    const f32x16 snd = w2 ? OA : OB;
    AWAIT(1, cpk);
    asm volatile("" ::: "memory");
#pragma unroll
    for (int c = 0; c < 3; c++) xb[(w2 * 3 + c) * 64 + lane] = (f32x4){snd[4 * c], snd[4 * c + 1], snd[4 * c + 2], snd[4 * c + 3]};
    asm volatile("s_waitcnt vmcnt(18) lgkmcnt(0)\n\ts_barrier" ::: "memory");
    f32x16 O = w2 ? OB : OA;
#pragma unroll
    for (int c = 0; c < 3; c++) { const f32x4 t = xb[((w2 ^ 1) * 3 + c) * 64 + lane]; O[4 * c] += t[0]; O[4 * c + 1] += t[1]; O[4 * c + 2] += t[2]; O[4 * c + 3] += t[3]; }
    float l = from_upper(O[8]);
    float amax = 0.f;
#pragma unroll
    for (int i = 0; i < 12; i++) amax += fabsf(O[i]);
    const bool good = (l >= 0x1p-30f) && (l <= 0x1p100f) && (amax <= 0x1p126f);
    if (__builtin_expect(!__all(good), 0)) {
      u32x4 q1; u32x2 q2;
#pragma unroll
      for (int i = 0; i < 4; i++) { auto rr = __builtin_amdgcn_permlane16_swap(q1A[i], q1B[i], false, false); q1[i] = w2 ? rr[1] : rr[0]; }
#pragma unroll
      for (int i = 0; i < 2; i++) { auto rr = __builtin_amdgcn_permlane16_swap(q2A[i], q2B[i], false, false); q2[i] = h ? 0u : (w2 ? rr[1] : rr[0]); }
      float m = -INFINITY;
#pragma unroll 1
      for (int t = 0; t < 64; t++) {
        f32x16 s = mfma32(LD16(WS_KV + t * TILE_B + ok1), q1, Z); s = mfma32k8(LD8(WS_KV + t * TILE_B + ok2), q2, s);
#pragma unroll
        for (int i = 0; i < 16; i++) m = fmaxf(m, s[i]);
      }
      m = half_swap_max(m);
      O = Z;
#pragma unroll 1
      for (int t = 0; t < 64; t++) {
        f32x16 s = mfma32(LD16(WS_KV + t * TILE_B + ok1), q1, Z); s = mfma32k8(LD8(WS_KV + t * TILE_B + ok2), q2, s);
#pragma unroll
        for (int i = 0; i < 16; i++) s[i] = ex2(s[i] - m);
        O = mfma32(LD16(WS_KV + t * TILE_B + ov), (u32x4){pk(s[0], s[1]), pk(s[2], s[3]), pk(s[4], s[5]), pk(s[6], s[7])}, O);
        O = mfma32(LD16(WS_KV + t * TILE_B + ov + 2 * VB), (u32x4){pk(s[8], s[9]), pk(s[10], s[11]), pk(s[12], s[13]), pk(s[14], s[15])}, O);
      }
      l = from_upper(O[8]);
    }
    const float inv = __builtin_amdgcn_rcpf(l) * (1.0f - gtb);
    LAS char* tb = lp + w2 * CH_B + 12288 + w3 * 1536;
#pragma unroll
    for (int c = 0; c < 3; c++) { const int k0 = (g & 1) * 20 + 8 * c + 4 * h;
      *(LAS u32x2*)(tb + ((c == 2 && h) ? 1280 + (g & 1) * 8 : (k0 >> 3) * 256 + (k0 & 4) * 2) + l15 * 16) = (u32x2){nz(pk(O[4 * c] * inv, O[4 * c + 1] * inv)), nz(pk(O[4 * c + 2] * inv, O[4 * c + 3] * inv))}; }
    const unsigned ob = WS_OB + (rt * 8 + w3 * 2 + w2) * OT_B + hp * 1280 + lane * 16;
    const u32x4 z0 = ldsr16(tb + lane * 16), z1 = ldsr16(tb + 1024 + l15 * 16);
    ST16(z0, ob); ST16(z1, lane < 16 ? ob + 1024 : 0x7FFF0000u);
  }
  STAMP(8);
  auto of_missing = [&]() { u32x4 mm = of[0][0];
#pragma unroll
    for (int j = 1; j < 10; j++) mm = __builtin_elementwise_max(mm, of[j / 5][j % 5]);
    return missing(mm); };
  u32x4 wf[5][5];
#pragma unroll
  for (int i = 0; i < 5; i++) { const LAS char* wb = lp + L_WO + (kq * 5 + i) * 5120 + wlane;
#pragma unroll
    for (int t = 0; t < 5; t++) wf[i][t] = trd2(wb + t * 8, wb + 1280 + t * 8); }
  SBAR();
  if (__builtin_expect(of_missing(), 0)) {
#pragma unroll 1
    for (unsigned spin = 0; spin < SPIN_CAP; ++spin) {
      asm volatile("" ::: "memory");
      OFL(0); OFL(1);
      if (!of_missing()) break;
      __builtin_amdgcn_s_sleep(1);
    }
  }
  f32x4 c[2][5];
#pragma unroll
  for (int i = 0; i < 5; i++)
#pragma unroll
    for (int t = 0; t < 5; t++) c[0][t] = mfma16(wf[i][t], of[0][i], i ? c[0][t] : (f32x4){0.f, 0.f, 0.f, 0.f});
  BAR_L();
  SBAR();
  { LAS char* d = lp + L_WO + w * 5120 + lane * 16;
    *(LAS u32x4*)(lp + L_WO + 16 * 5120 + w5 * 1024 + lane * 16) = wbr[10];
#pragma unroll
    for (int i = 0; i < 5; i++) {
      *(LAS u32x4*)(d + i * 1024) = wbr[i]; *(LAS u32x4*)(d + 40960 + i * 1024) = wbr[5 + i]; of[0][i] = LD16S(ok1, WS_OB + ofr + i * 1024); SBAR();
#pragma unroll
      for (int t = 0; t < 5; t++) c[1][t] = mfma16(wf[i][t], of[1][i], i ? c[1][t] : (f32x4){0.f, 0.f, 0.f, 0.f});
      SBAR();
    } }
  STAMP(9);
  BAR_L();
#pragma unroll
  for (int i = 0; i < 5; i++) { const int gr = kq * 5 + i; const LAS char* wb = lp + (gr < 17 ? L_WO + gr * 5120 : 145408 + (gr - 17) * 5120) + wlane;
#pragma unroll
    for (int t = 0; t < 5; t++) wf[i][t] = trd2(wb + t * 8, wb + 1280 + t * 8);
    SBAR(); of[1][i] = LD16S(ok1, WS_OB + ofr + OT_B + i * 1024); SBAR(); }
  ARRIVE_NW(2);
  STAMP(10);
  LAS char* xw = lp + kq * 21504 + (tp * 32 + l15) * 336 + g * 80;
#pragma unroll
  for (int u = 0; u < 2; u++) {
#pragma unroll
    for (int i = 0; i < 5; i++) {
      if (__builtin_expect(missing(of[u][i]), 0)) {
#pragma unroll 1
        for (unsigned spin = 0; spin < SPIN_CAP; ++spin) {
          asm volatile("" ::: "memory");
#pragma unroll
          for (int j = u * 5 + i; j < 10; j++) of[j / 5][j % 5] = LD16S(ok1, WS_OB + ofr + (j / 5) * OT_B + (j % 5) * 1024);
          if (!missing(of[u][i])) break;
          __builtin_amdgcn_s_sleep(1);
        }
      }
#pragma unroll
      for (int t = 0; t < 5; t++) c[u][t] = mfma16(wf[i][t], of[u][i], c[u][t]);
      if (u == 0 && i == 3) cpk = PEEK(2);
      if (u == 1) { if (i == 0) { AWAIT(2, cpk); asm volatile("" ::: "memory"); }
        *(LAS f32x4*)(xw + i * 16) = c[0][i]; }
      SBAR();
    }
  }
#pragma unroll
  for (int t = 0; t < 5; t++) *(LAS f32x4*)(xw + 16 * 336 + t * 16) = c[1][t];
  BAR_L();
  { const __amdgpu_buffer_rsrc_t orr = __builtin_amdgcn_make_buffer_rsrc(out, 0, 2048 * DM * 2, 0x00020000);
#pragma unroll
    for (int j = 0; j < 3; j++) { const int n = tid + 512 * j, nn = n < 1280 ? n : 0, row = (nn * 3277) >> 16, cq = nn - row * 20;
      const LAS f32x4* p = (const LAS f32x4*)(lp + row * 336 + cq * 16);
      const f32x4 z = (p[0] + p[1344]) + (p[2688] + p[4032]);
      __builtin_amdgcn_raw_buffer_store_b64((u32x2){pk(z[0], z[1]), pk(z[2], z[3])}, orr, n < 1280 ? ((rt * 128 + role * 64 + row) * DM + C0 + cq * 4) * 2 : 0x7FFF0000u, 0, 0); }
  }
  STAMP(11);
}

__global__ __launch_bounds__(512, 2) void fused(const u16* __restrict__ x, const u16* __restrict__ wqkv, const u16* __restrict__ wphi, const u16* __restrict__ woA,
                                                const u16* __restrict__ wq, const u16* __restrict__ wk, const u16* __restrict__ wv, const u16* __restrict__ woB,
                                                const u16* __restrict__ gatew, u16* __restrict__ out, char* ws) {
  __shared__ __attribute__((aligned(16))) char lds[L_TOTAL];
  if ((blockIdx.x >> 6) & 1) body<1>(x, wqkv, wphi, woA, wq, wv, woB, gatew, out, ws, (LAS char*)lds);
  else body<0>(x, wqkv, wphi, woA, wq, wk, woB, gatew, out, ws, (LAS char*)lds);
}
}

extern "C" void kernel_launch(void* const* d_in, const int* in_sizes, int n_in, void* d_out, int out_size, void* d_ws, size_t ws_size, hipStream_t stream) {
  using namespace gm;
  fused<<<256, 512, 0, stream>>>((const u16*)d_in[0], (const u16*)d_in[1], (const u16*)d_in[2], (const u16*)d_in[3], (const u16*)d_in[5], (const u16*)d_in[6],
                                 (const u16*)d_in[7], (const u16*)d_in[8], (const u16*)d_in[9], (u16*)d_out, (char*)d_ws);
}
```

```cpp
#include <hip/hip_runtime.h>
#include <hip/hip_bf16.h>
#include <stdint.h>

namespace gm {
typedef float f32x16 __attribute__((ext_vector_type(16)));
typedef float f32x4 __attribute__((ext_vector_type(4)));
typedef float f32x2 __attribute__((ext_vector_type(2)));
typedef short s16x4 __attribute__((ext_vector_type(4)));
typedef __bf16 b16x2 __attribute__((ext_vector_type(2)));
typedef __bf16 b16x8 __attribute__((ext_vector_type(8)));
typedef unsigned u32x4 __attribute__((ext_vector_type(4)));
typedef unsigned u32x2 __attribute__((ext_vector_type(2)));
typedef unsigned long long u64;
typedef unsigned short u16;
#define DI __device__ __forceinline__
#define LAS __attribute__((address_space(3)))
#define GAS __attribute__((address_space(1)))
typedef GAS u64 gu64;

constexpr int DM = 640;
constexpr unsigned WS_EPOCH = 0, WS_FKV = 2048, WS_FKVR = 4096, WS_FQ = 8192, WS_FPART = 16384, WS_FOA = 24576, WS_SPARE = 40960  , WS_G = 57344;
constexpr unsigned WS_KVR = 8912896, KVR_B = 10240, WS_KV = 131072, WS_QX = 327680, QX_B = 10240, WS_PART = 1703936, PART_B = 15360;
constexpr unsigned WS_OA = 3670016, WS_OB = 6291456, WS_END = 8994816, OT_B = 20480;
constexpr int VB = 352, TILE_B = 1280 + 4 * VB, CH_B = TILE_B * 8, STEP_B = 2 * CH_B;
constexpr int STAGE_B = 36864, ST_W1 = 10240, ST_X = 20480;
constexpr int L_BUF = 43008, L_P0W = 0, L_P0X0 = 25600, L_P0X1 = 153600, L_RED = 158720, L_RED1 = 40960;
constexpr int L_HO = 86016, L_QL = 96256, L_VIM = 96256, L_PHK = 116736, L_WPHI = 158592, L_ZERO = 161792, L_CNT = 163712, L_TOTAL = 163840;
constexpr int L_WO = 43008;
constexpr float LOG2E = 1.4426950408889634f;
constexpr float QSCALE = 0.22360679774997896f * LOG2E;
constexpr unsigned SPIN_CAP = 20000, LSPIN_CAP = 400000;

DI unsigned pk(float a, float b) { f32x2 v = {a, b}; b16x2 r = __builtin_convertvector(v, b16x2); return __builtin_bit_cast(unsigned, r); }
DI f32x16 mfma32(u32x4 a, u32x4 b, f32x16 c) { return __builtin_amdgcn_mfma_f32_32x32x16_bf16(__builtin_bit_cast(b16x8, a), __builtin_bit_cast(b16x8, b), c, 0, 0, 0); }
DI f32x16 mfma32k8(u32x2 a, u32x2 b, f32x16 c) { return __builtin_amdgcn_mfma_f32_32x32x8bf16_1k(__builtin_bit_cast(s16x4, a), __builtin_bit_cast(s16x4, b), c, 0, 0, 0); }
DI f32x4 mfma16(u32x4 a, u32x4 b, f32x4 c) { return __builtin_amdgcn_mfma_f32_16x16x32_bf16(__builtin_bit_cast(b16x8, a), __builtin_bit_cast(b16x8, b), c, 0, 0, 0); }
DI u32x2 trd(const LAS char* p) { return __builtin_bit_cast(u32x2, __builtin_amdgcn_ds_read_tr16_b64_v4i16((LAS s16x4*)p)); }
DI u32x4 trd2(const LAS char* p0, const LAS char* p1) { u32x2 a = trd(p0), b = trd(p1); return (u32x4){a.x, a.y, b.x, b.y}; }
DI u32x4 ldsr16(const LAS char* p) { return *(const LAS u32x4*)p; }
DI u32x2 ldsr8(const LAS char* p) { return *(const LAS u32x2*)p; }
DI float ex2(float x) { return __builtin_amdgcn_exp2f(x); }
DI void dma16s(const void* base, unsigned voff, unsigned lds_base) {
  asm volatile("s_mov_b32 m0, %2\n\ts_nop 0\n\tglobal_load_lds_dwordx4 %0, %1" :: "v"(voff), "s"(base), "s"(lds_base) : "memory"); }
DI void dma16s_sc1(const void* base, unsigned voff, unsigned lds_base) {
  asm volatile("s_mov_b32 m0, %2\n\ts_nop 0\n\tglobal_load_lds_dwordx4 %0, %1 sc1" :: "v"(voff), "s"(base), "s"(lds_base) : "memory"); }
DI void dmag_open(unsigned lds_base) { asm volatile("s_mov_b32 m0, %0\n\ts_nop 0" :: "s"(lds_base) : "memory"); }
template <int J> DI void dmag(const void* base, unsigned voff) { asm volatile("global_load_lds_dwordx4 %0, %1 offset:%2" :: "v"(voff), "s"(base), "i"((J & 3) * 1024) : "memory"); }
template <int J> DI void dmag_sc1(const void* base, unsigned voff) { asm volatile("global_load_lds_dwordx4 %0, %1 offset:%2 sc1" :: "v"(voff), "s"(base), "i"((J & 3) * 1024) : "memory"); }
DI void dma4s(const void* base, unsigned voff, unsigned lds_base) {
  asm volatile("s_mov_b32 m0, %2\n\ts_nop 0\n\tglobal_load_lds_dword %0, %1" :: "v"(voff), "s"(base), "s"(lds_base) : "memory"); }
DI u32x4 ld16_async(const void* p) { u32x4 r; asm volatile("global_load_dwordx4 %0, %1, off" : "=v"(r) : "v"(p) : "memory"); return r; }
DI u32x2 ld8_sc1_async(const void* p) { u32x2 r; asm volatile("global_load_dwordx2 %0, %1, off sc1" : "=v"(r) : "v"(p) : "memory"); return r; }
#define BAR_L() asm volatile("s_waitcnt lgkmcnt(0)\n\ts_barrier" ::: "memory")
#define WAIT_BAR(N) asm volatile("s_waitcnt vmcnt(" #N ") lgkmcnt(0)\n\ts_barrier" ::: "memory")
#define WAIT_VM(N) asm volatile("s_waitcnt vmcnt(" #N ")" ::: "memory")
#define SBAR() __builtin_amdgcn_sched_barrier(0)
#define RLX_AGENT __ATOMIC_RELAXED, __HIP_MEMORY_SCOPE_AGENT
DI float half_swap_max(float m) { auto rr = __builtin_amdgcn_permlane32_swap(__float_as_uint(m), __float_as_uint(m), false, false); return fmaxf(__uint_as_float(rr[0]), __uint_as_float(rr[1])); }
DI float from_upper(float v) { auto rr = __builtin_amdgcn_permlane32_swap(__float_as_uint(v), __float_as_uint(v), false, false); return __uint_as_float(rr[1]); }


template <int NF>
DI bool flags_set(const char* ws, unsigned off, u64 tag, int lane) {
  gu64* f = (gu64*)(ws + off);
  bool ok = __hip_atomic_load(f + (NF < 64 ? (lane & (NF - 1)) : lane), RLX_AGENT) == tag;
  if (NF >= 128) ok &= __hip_atomic_load(f + 64 + lane, RLX_AGENT) == tag;
  if (NF >= 256) { ok &= __hip_atomic_load(f + 128 + lane, RLX_AGENT) == tag; ok &= __hip_atomic_load(f + 192 + lane, RLX_AGENT) == tag; }
  return ok;
}
template <int NF>
DI void wait_flags(const char* ws, unsigned off, u64 tag, int lane) {
#pragma unroll 1
  for (unsigned spin = 0; spin < SPIN_CAP; ++spin) {
    if (__all(flags_set<NF>(ws, off, tag, lane))) break;
    __builtin_amdgcn_s_sleep(2);
  }
  asm volatile("" ::: "memory");
}
DI unsigned nz(unsigned v) { return v < 0xFFFFFFFEu ? v : 0xFFFFFFFEu; }
DI unsigned umax3(unsigned a, unsigned b, unsigned c) { unsigned m = a > b ? a : b; return m > c ? m : c; }
DI bool missing(u32x4 v) { return __any(umax3(umax3(v.x, v.y, v.z), v.w, 0u) == 0xFFFFFFFFu); }
#define ONES ((u32x4){0xFFFFFFFFu, 0xFFFFFFFFu, 0xFFFFFFFFu, 0xFFFFFFFFu})
#define ST16(v, off) __builtin_amdgcn_raw_buffer_store_b128((v), wsr, (off), 0, 16)
#define ST8(v, off) __builtin_amdgcn_raw_buffer_store_b64((v), wsr, (off), 0, 16)
#define LD16(off) __builtin_amdgcn_raw_buffer_load_b128(wsr, (off), 0, 16)
#define ST16S(v, voff, soff) ST16(v, (voff) + (soff))
#define LD16S(voff, soff) __builtin_amdgcn_raw_buffer_load_b128(wsr, (voff), (soff), 16)
#define LD8S(voff, soff) __builtin_amdgcn_raw_buffer_load_b64(wsr, (voff), (soff), 16)
#define LD8(off) __builtin_amdgcn_raw_buffer_load_b64(wsr, (off), 0, 16)

template <int role>
DI void body(const u16* __restrict__ x, const u16* __restrict__ wqkv, const u16* __restrict__ wphi, const u16* __restrict__ woA,
             const u16* __restrict__ wq, const u16* __restrict__ wkv, const u16* __restrict__ woB,
             const u16* __restrict__ gatew, u16* __restrict__ out, char* ws, LAS char* lp) {
  const int tid = threadIdx.x, lane = tid & 63;
  const int w = __builtin_amdgcn_readfirstlane(tid >> 6) & 7;
  const int l15 = lane & 15, g = lane >> 4, q4 = l15 >> 2, p4 = lane & 3, r = lane & 31, h = lane >> 5;
  const int bx = blockIdx.x & 7, bi = blockIdx.x >> 3;
  const int rt = (bx & 1) * 8 + (bi & 7), hp = (bx >> 1) * 4 + ((bi >> 3) & 3), hq = hp >> 1;
  const int w3 = w & 3, w2 = w >> 2;
  const unsigned lds0 = (unsigned)(uintptr_t)lp;
  const __amdgpu_buffer_rsrc_t wsr = __builtin_amdgcn_make_buffer_rsrc(ws, 0, WS_END, 0x00020000);
  LAS unsigned* cnt = (LAS unsigned*)(lp + L_CNT);
#define ARRIVE_NW(c) do { asm volatile("" ::: "memory"); __hip_atomic_store(cnt + (c) * 8 + w, 1u, __ATOMIC_RELAXED, __HIP_MEMORY_SCOPE_WORKGROUP); asm volatile("" ::: "memory"); } while (0)
#define PEEK(c) __hip_atomic_load(cnt + (c) * 8 + (lane & 7), __ATOMIC_RELAXED, __HIP_MEMORY_SCOPE_WORKGROUP)
#define AWAIT(c, v) do { if (__builtin_expect(!__all((v) != 0u), 0)) { _Pragma("unroll 1") for (unsigned sp_ = 0; sp_ < LSPIN_CAP; ++sp_) { if (__all(PEEK(c) != 0u)) break; __builtin_amdgcn_s_sleep(1); } } asm volatile("" ::: "memory"); } while (0)
#define POLL(off) __builtin_bit_cast(u64, __builtin_amdgcn_raw_buffer_load_b64(wsr, lane * 8, (off), 16))
#define FLAG(off) __builtin_amdgcn_raw_buffer_store_b64(__builtin_bit_cast(u32x2, tag), wsr, lane ? 0x7FFF0000u : 0u, (off), 16)

  const int T0 = rt * 128 + hq * 16;
  const int C0 = hq * 80;
  const int wlane = ((g >> 1) * 16 + (g & 1) * 4 + q4) * 160 + p4 * 40;
  u64 tag;
  unsigned o0[10];
#define KVP(s, slot, p) dma16s_sc1(ws + WS_KV + (s) * STEP_B + (p) * 1024, lane * 16, lds0 + (slot) * STEP_B + (p) * 1024)
#define KVL(s, slot) do { _Pragma("unroll") for (int j = 0; j < 11; j++) KVP(s, slot, j < 10 ? w3 + 4 * j : 40 + (w3 & 1)); } while (0)
#define KW(s, slot, j) do { if ((j) == 4) KVP(s, slot, w * 5 + 4); else { if ((j) == 0) dmag_open(lds0 + (slot) * STEP_B + w * 5120); dmag_sc1<(j)>(ws + WS_KV + (s) * STEP_B + w * 5120, lane * 16); } } while (0)
#define KX(s, slot) KVP(s, slot, 40 + (w & 1))
  const int w5 = w < 5 ? w : 4;
  unsigned wo16;
  unsigned woff[5];
  auto woff_init = [&]() { int ln = lane; asm volatile("" : "+v"(ln));
#pragma unroll
    for (int j = 0; j < 5; j++) { const int c = j * 64 + ln, rho = (c * 6554) >> 16, cc = c - rho * 10; const int k = (rho & ~12) | ((rho & 4) << 1) | ((rho & 8) >> 1); woff[j] = k * 1280 + cc * 16 + (4 - j) * 1024; }
    { const int c = w5 * 64 + ln, rho = (c * 6554) >> 16, cc = c - rho * 10; const int k = (rho & ~12) | ((rho & 4) << 1) | ((rho & 8) >> 1); wo16 = k * 1280 + cc * 16 + 4096; }
    asm volatile("" : "+v"(woff[0]), "+v"(woff[1]), "+v"(woff[2]), "+v"(woff[3]), "+v"(woff[4]), "+v"(wo16)); };
  if (w2 == 0) {
    u32x4 gf0, gf1, gf2, gf3, gf4, wpc;
    u32x2 ep = ld8_sc1_async(ws + WS_EPOCH);
    const int lx = w3 < 3 ? L_P0X0 + w3 * 5120 : L_P0X1;
    { const u16* wsrc = wkv + w3 * 3200;
#pragma unroll
      for (int j = 0; j < 5; j++) { const int n = j * 64 + lane, row = (n * 3277) >> 16, cc = n - row * 20; dma16s(x + T0 * DM + w3 * 160, row * 1280 + cc * 16, lds0 + lx + j * 1024); }
#pragma unroll
      for (int j = 0; j < 6; j++) dma16s(wsrc + j * 512, lane * 16, lds0 + L_P0W + w3 * 6400 + j * 1024);
      if (lane < 16) dma16s(wsrc + 6 * 512, lane * 16, lds0 + L_P0W + w3 * 6400 + 6 * 1024);
      const u16* gp = gatew + w3 * 160 + g * 8;
      gf0 = ld16_async(gp); gf1 = ld16_async(gp + 32); gf2 = ld16_async(gp + 64); gf3 = ld16_async(gp + 96); gf4 = ld16_async(gp + 128);
      wpc = ld16_async(wphi + hq * 1600 + (tid < 200 ? tid : 199) * 8);
    }
    woff_init();
    asm volatile("s_waitcnt vmcnt(0)" : "+v"(gf0), "+v"(gf1), "+v"(gf2), "+v"(gf3), "+v"(gf4), "+v"(wpc), "+v"(ep) :: "memory");
    tag = (((u64)(unsigned)__builtin_amdgcn_readfirstlane(ep.y) << 32) | (unsigned)__builtin_amdgcn_readfirstlane(ep.x)) + 1ull;
    { const LAS char* wb = lp + L_P0W + (w3 * 160 + g * 8 + q4) * 40;
      const LAS char* xb = lp + lx + l15 * 320 + g * 16;
      const bool gl = l15 == 4;
      f32x4 c0 = {}, c1 = {};
#define P0STEP(s, GF) { const u32x4 b = ldsr16(xb + (s) * 64); c0 = mfma16(trd2(wb + p4 * 8 + (s) * 1280, wb + p4 * 8 + (s) * 1280 + 160), b, c0); \
        u32x4 a = trd2(wb + 32 + (s) * 1280, wb + 32 + (s) * 1280 + 160); if (gl) a = GF; c1 = mfma16(a, b, c1); }
      P0STEP(0, gf0) P0STEP(1, gf1) P0STEP(2, gf2) P0STEP(3, gf3) P0STEP(4, gf4)
#undef P0STEP
      *(LAS f32x4*)(lp + L_RED + tid * 16) = c0;
      if (lane < 32) *(LAS f32x4*)(lp + L_RED1 + (w3 * 32 + lane) * 16) = c1; }
    f32x16 acc[5];
#pragma unroll
    for (int b = 0; b < 5; b++) acc[b] = (f32x16){};
    const int wm = g & 1, whd = p4;
    const int wo0 = wm * ST_W1 + (8 * h + 4 * wm + q4) * 160 + whd * 40, wo1 = wm * ST_W1 + (8 * h + 4 * (wm ^ 1) + q4) * 160 + whd * 40;
    const int xo = ST_X + (w3 * 32 + r) * 128 + ((h ^ ((r >> 1) & 7)) * 16);
    u32x4 fa[5], fb[5], xa, xb;
#define KSTEP(FN, XN, s, FC, XC) { XN = ldsr16(sb + (xo ^ ((s) * 32))); \
      _Pragma("unroll") for (int j = 0; j < 5; j++) { if (i > 0 || (s) > 0) acc[j] = mfma32(FC[j], XC, acc[j]); FN[j] = trd2(sb + wo0 + (s) * 2560 + j * 8, sb + wo1 + (s) * 2560 + j * 8); SBAR(); } }
#pragma unroll
    for (int i = 0; i < 10; i++) {
      BAR_L();
      if (i == 1 && role) {
        const unsigned qb = WS_QX + (rt * 8 + hq) * QX_B + w3 * 1280 + lane * 16;
        ST16(ONES, qb); ST16(ONES, qb + 5120); ST16(ONES, lane < 32 ? qb + 1024 + (lane >> 4) * (5120 - 256) : 0x7FFF0000u); }
      if (i == 4) { if (role) { WAIT_VM(0); FLAG(WS_FQ + ((rt * 8 + hq) * 4 + w3) * 8); }
                    if (tid < 200) *(LAS u32x4*)(lp + L_WPHI + tid * 16) = wpc; }
      const LAS char* sb = lp + L_BUF + ((i + 2) % 3) * STAGE_B;
      KSTEP(fa, xa, 0, fb, xb) KSTEP(fb, xb, 1, fa, xa) KSTEP(fa, xa, 2, fb, xb)
      { xb = ldsr16(sb + (xo ^ 96));
        acc[0] = mfma32(fa[0], xa, acc[0]); fb[0] = trd2(sb + wo0 + 7680, sb + wo1 + 7680); fb[1] = trd2(sb + wo0 + 7688, sb + wo1 + 7688); SBAR();
        acc[1] = mfma32(fa[1], xa, acc[1]); fb[2] = trd2(sb + wo0 + 7696, sb + wo1 + 7696); fb[3] = trd2(sb + wo0 + 7704, sb + wo1 + 7704); SBAR();
        acc[2] = mfma32(fa[2], xa, acc[2]); fb[4] = trd2(sb + wo0 + 7712, sb + wo1 + 7712); SBAR();
        acc[3] = mfma32(fa[3], xa, acc[3]); acc[4] = mfma32(fa[4], xa, acc[4]); SBAR(); }
    }
#pragma unroll
    for (int j = 0; j < 5; j++) acc[j] = mfma32(fb[j], xb, acc[j]);
#undef KSTEP
#define PK2(t, i, sc) pk(acc[t][i] * (sc), acc[t][(i) + 1] * (sc))
#define ROW(i, sc) const u32x4 ra = {PK2(0, i, sc), PK2(0, (i) + 2, sc), PK2(1, i, sc), PK2(1, (i) + 2, sc)}, rb = {PK2(2, i, sc), PK2(2, (i) + 2, sc), PK2(3, i, sc), PK2(3, (i) + 2, sc)}; const u32x2 rc = {PK2(4, i, sc), PK2(4, (i) + 2, sc)}
    { ROW(4, 1.0f);
      *(LAS u32x4*)(lp + L_HO + tid * 16) = ra; *(LAS u32x4*)(lp + L_HO + 4096 + tid * 16) = rb; *(LAS u32x2*)(lp + L_HO + 8192 + tid * 8) = rc; }
    BAR_L();
    const int qfo = (h * 4 + w3) * 1280;
    if (role) {
      { ROW(8, QSCALE); const unsigned qb = WS_QX + (rt * 8 + hq) * QX_B + qfo;
        ST16(((u32x4){nz(ra.x), nz(ra.y), nz(ra.z), nz(ra.w)}), qb + r * 16); ST16(((u32x4){nz(rb.x), nz(rb.y), nz(rb.z), nz(rb.w)}), qb + 512 + r * 16); ST8(((u32x2){nz(rc.x), nz(rc.y)}), qb + 1024 + r * 8); }
      { ROW(12, QSCALE); LAS char* d = lp + L_QL + qfo;
        *(LAS u32x4*)(d + r * 16) = ra; *(LAS u32x4*)(d + 512 + r * 16) = rb; *(LAS u32x2*)(d + 1024 + r * 8) = rc; }
    } else { LAS char* d = lp + L_VIM + (h * 128 + w3 * 32 + r) * 40;
#pragma unroll
      for (int t = 0; t < 5; t++) { *(LAS u32x2*)(d + t * 8) = (u32x2){PK2(t, 8, 1.0f), PK2(t, 10, 1.0f)}; *(LAS u32x2*)(d + 10240 + t * 8) = (u32x2){PK2(t, 12, 1.0f), PK2(t, 14, 1.0f)}; } }
#pragma unroll
    for (int t = 0; t < 5; t++) { o0[2 * t] = PK2(t, 0, 1.0f); o0[2 * t + 1] = PK2(t, 2, 1.0f); }
#undef ROW
#undef PK2
  } else {
    u32x2 ep = ld8_sc1_async(ws + WS_EPOCH);
    const bool second = w3 >> 1;
    const unsigned rs = (second && role) ? 1280u : 3840u;
    const char* wsrc = (second ? (role ? (const char*)(wq + C0) : (const char*)(wqkv + 1280 + C0)) : (const char*)(wqkv + (role ? 0 : 640) + C0)) + (w3 & 1) * 32 * rs;
    const char* xsrc = (const char*)(x + (rt * 128 + w3 * 32) * DM);
    int rot = (bi & 7) + 3 * ((bi >> 3) & 3); rot = rot >= 10 ? rot - 10 : rot;
    unsigned so[5], xoff[2];
#pragma unroll
    for (int j = 0; j < 5; j++) { const int c = j * 64 + lane, rho = (c * 6554) >> 16, cc = c - rho * 10; so[j] = (rho ^ (second ? 4 : 0)) * rs + cc * 16; }
#pragma unroll
    for (int j = 0; j < 2; j++) { const int row = j * 8 + (lane >> 3); xoff[j] = row * 1280 + (((lane & 7) ^ ((row >> 1) & 7)) * 16); }
    int bsel = 2, kc = rot;
    auto stage = [&]() {
      const unsigned lb = lds0 + L_BUF + bsel * STAGE_B;
      const char* bw = wsrc + kc * 64 * rs; const char* bxp = xsrc + kc * 128;
#pragma unroll
      for (int j = 0; j < 5; j++) { dma16s(bw, so[j], lb + (w3 * 5 + j) * 1024); if (j < 4) dma16s(bxp + (j >> 1) * 16 * 1280, xoff[j & 1], lb + ST_X + (w3 * 4 + j) * 1024); }
      bsel = bsel == 2 ? 0 : bsel + 1; kc = kc == 9 ? 0 : kc + 1;
    };
    stage(); stage();
    woff_init();
    asm volatile("s_waitcnt vmcnt(9)" : "+v"(ep) :: "memory");
    tag = (((u64)(unsigned)__builtin_amdgcn_readfirstlane(ep.y) << 32) | (unsigned)__builtin_amdgcn_readfirstlane(ep.x)) + 1ull;
#pragma unroll 1
    for (int i = 0; i < 8; i++) {
      WAIT_BAR(9);
      stage();
      if (i == 0 && w3 == 0) {
        const unsigned tb = WS_KV + (T0 >> 5) * TILE_B; const int s16 = hq & 1;
        if (!role) {
          const LAS f32x4* rp = (const LAS f32x4*)(lp + (h ? L_RED1 : L_RED)) + (h ? (g & 1) * 16 : g * 32) + l15;
          const int st = h ? 32 : 64, o2 = h ? 0 : 16;
          const f32x4 a = (rp[0] + rp[st]) + (rp[2 * st] + rp[3 * st]), b = (rp[o2] + rp[st + o2]) + (rp[2 * st + o2] + rp[3 * st + o2]);
          const u32x4 v = {pk(a[0], a[1]), pk(a[2], a[3]), pk(b[0], b[1]), pk(b[2], b[3])};
          if (lane < 32) ST16(v, tb + (g * 32 + s16 * 16 + l15) * 16);
          else if (lane < 48) ST8(((u32x2){v.x, v.y}), tb + 1024 + (s16 * 16 + l15) * 8);
          else { const float gv = 1.0f / (1.0f + __expf(-a[0])); __builtin_amdgcn_raw_buffer_store_b32(__float_as_uint(gv), wsr, WS_G + (T0 + l15) * 4, 0, 16); }
        } else if (lane < 44) {
          const int hh = lane >= 22, d = lane - hh * 22, dd = d < 20 ? d : 0, st = dd >= 16 ? 128 : 256;
          const LAS float* rp = (const LAS float*)(lp + (dd >= 16 ? L_RED1 : L_RED)) + (((dd & 15) >> 2) * 16 + 4 * hh) * 4 + (dd & 3);
          float e[8];
#pragma unroll
          for (int j = 0; j < 8; j++) { const int tk = (8 * (j >> 2) + (j & 3)) * 4; e[j] = (rp[tk] + rp[tk + st]) + (rp[tk + 2 * st] + rp[tk + 3 * st]); }
          u32x4 v = {pk(e[0], e[1]), pk(e[2], e[3]), pk(e[4], e[5]), pk(e[6], e[7])};
          if (d == 20) v = (u32x4){0x3F803F80u, 0x3F803F80u, 0x3F803F80u, 0x3F803F80u};
          if (d == 21) v = (u32x4){0u, 0u, 0u, 0u};
          ST16(v, tb + 1280 + (s16 * 2 + hh) * VB + d * 16);
        }
      }
      if (i == 2) { if (w3 == 0) FLAG(WS_FKV + blockIdx.x * 8);
                    if (w3 >= 2) *(LAS u32x4*)(lp + L_ZERO + (tid - 384) * 16) = (u32x4){0u, 0u, 0u, 0u}; }
    }
    u64 fa, fb, fc, fd, fe;
    { gu64* f = (gu64*)(ws + WS_FKV) + lane;
      fa = __hip_atomic_load(f, RLX_AGENT); fb = __hip_atomic_load(f + 64, RLX_AGENT); fc = __hip_atomic_load(f + 128, RLX_AGENT); fd = __hip_atomic_load(f + 192, RLX_AGENT);
      fe = __hip_atomic_load((gu64*)(ws + WS_FQ) + (rt * 8 + hq) * 4 + p4, RLX_AGENT); }
    WAIT_BAR(14);
    asm volatile("" : "+v"(fa), "+v"(fb), "+v"(fc), "+v"(fd), "+v"(fe));
    const bool kvok = __all((fa == tag) & (fb == tag) & (fc == tag) & (fd == tag) & (fe == tag));
    asm volatile("" ::: "memory");
    BAR_L();
    if (__builtin_expect(!kvok, 0)) { wait_flags<256>(ws, WS_FKV, tag, lane); wait_flags<4>(ws, WS_FQ + (rt * 8 + hq) * 32, tag, lane); }
    KVL(0, 0);
    if (blockIdx.x == 0 && w3 == 0) FLAG(WS_EPOCH);
    BAR_L();
    const int ht = tid - 256;
    const u32x4 a = ldsr16(lp + L_HO + ht * 16), b = ldsr16(lp + L_HO + 4096 + ht * 16); const u32x2 c = ldsr8(lp + L_HO + 8192 + ht * 8);
    o0[0] = a.x; o0[1] = a.y; o0[2] = a.z; o0[3] = a.w; o0[4] = b.x; o0[5] = b.y; o0[6] = b.z; o0[7] = b.w; o0[8] = c.x; o0[9] = c.y;
  }
  u32x4 ph[5];
  { const LAS char* pb = ((p4 & 1) == h) ? lp + L_WPHI + (2 * w2 + h) * 800 + q4 * 40 + (2 * (g & 1) + (p4 >> 1)) * 8 : lp + L_ZERO;
    const u32x4 b0 = {o0[0], o0[1], o0[2], o0[3]}, b1 = {o0[4], o0[5], o0[6], o0[7]}, b2 = {o0[8], o0[9], 0u, 0u};
    const u32x2 c0 = trd(pb + 640), c1 = trd(pb + 672);
    f32x16 P0 = {}, P1 = {};
    P0 = mfma32(trd2(pb, pb + 160), b0, P0); P1 = mfma32(trd2(pb + 32, pb + 192), b0, P1);
    P0 = mfma32(trd2(pb + 320, pb + 480), b1, P0); P1 = mfma32(trd2(pb + 352, pb + 512), b1, P1);
    P0 = mfma32((u32x4){c0.x, c0.y, 0u, 0u}, b2, P0); P1 = mfma32((u32x4){c1.x, c1.y, 0u, 0u}, b2, P1);
    KX(1, 1); KW(1, 1, 0); SBAR();
    f32x2 p[10];
#pragma unroll
    for (int f = 0; f < 10; f++) p[f] = f < 8 ? (f32x2){P0[(2 * f) & 15], P0[(2 * f + 1) & 15]} : (f32x2){P1[(2 * f) & 3], P1[(2 * f + 1) & 3]};
    float mx = fmaxf(p[0][0], p[0][1]), mn = fminf(p[0][0], p[0][1]);
#pragma unroll
    for (int f = 1; f < 10; f++) { mx = fmaxf(fmaxf(mx, p[f][0]), p[f][1]); mn = fminf(fminf(mn, p[f][0]), p[f][1]); }
    const f32x2 ma = {-mx * LOG2E, -mx * LOG2E}, mb = {mn * LOG2E, mn * LOG2E}, ca_ = {LOG2E, LOG2E}, cb_ = {-LOG2E, -LOG2E};
    f32x2 ea[10], eb[10], sa = {0.f, 0.f}, sb = {0.f, 0.f};
#pragma unroll
    for (int f = 0; f < 10; f++) { if (f % 2 == 0 && f < 8) { SBAR(); if (f == 0) KW(1, 1, 1); if (f == 2) KW(1, 1, 2); if (f == 4) KW(1, 1, 3); if (f == 6) KW(1, 1, 4); SBAR(); }
      const f32x2 xa = __builtin_elementwise_fma(p[f], ca_, ma), xb = __builtin_elementwise_fma(p[f], cb_, mb);
      ea[f] = (f32x2){ex2(xa[0]), ex2(xa[1])}; eb[f] = (f32x2){ex2(xb[0]), ex2(xb[1])}; sa += ea[f]; sb += eb[f]; }
    const float ia = __builtin_amdgcn_rcpf(sa[0] + sa[1]), ib = __builtin_amdgcn_rcpf(sb[0] + sb[1]);
    unsigned o[20];
#pragma unroll
    for (int f = 0; f < 10; f++) { const f32x2 ya = ea[f] * ia, yb = eb[f] * ib; o[f] = pk(ya[0], ya[1]); o[10 + f] = pk(yb[0], yb[1]); }
#pragma unroll
    for (int c = 0; c < 5; c++) ph[c] = (u32x4){o[4 * c], o[4 * c + 1], o[4 * c + 2], o[4 * c + 3]};
    asm volatile("" : "+v"(ph[0]), "+v"(ph[1]), "+v"(ph[2]), "+v"(ph[3]), "+v"(ph[4]));
  }
  u32x4 q1A, q1B; u32x2 q2A, q2B;
  const int qo1 = (g & 1) * 5120 + (h * 32 + l15) * 16, qo2 = (g & 1) * 5120 + 1024 + l15 * 8;
  if (role) {
    WAIT_BAR(0);
    const LAS char* qa = lp + L_QL + w3 * 1280;
    q1A = ldsr16(qa + qo1 + (w2 ^ 1) * 256); q1B = ldsr16(qa + qo1 + w2 * 256); q2A = ldsr8(qa + qo2 + (w2 ^ 1) * 128); q2B = ldsr8(qa + qo2 + w2 * 128);
    ARRIVE_NW(0);
  } else {
    const unsigned qb = WS_QX + (rt * 8 + hq) * QX_B + w3 * 1280;
    q1A = LD16S(qo1, qb + (w2 ^ 1) * 256); q1B = LD16S(qo1, qb + w2 * 256); q2A = LD8S(qo2, qb + (w2 ^ 1) * 128); q2B = LD8S(qo2, qb + w2 * 128);
    { LAS char* d = lp + L_PHK + ((2 * w2 + h) * 128 + w3 * 32 + r) * 80;
#pragma unroll
      for (int c = 0; c < 5; c++) *(LAS u32x4*)(d + c * 16) = ph[c]; }
    WAIT_BAR(4);
#pragma unroll 1
    for (unsigned spin = 0; spin < SPIN_CAP; ++spin) {
      if (!(missing(q1A) | missing(q1B) | missing((u32x4){q2A.x, q2A.y, q2B.x, q2B.y}))) break;
      __builtin_amdgcn_s_sleep(1);
      asm volatile("" ::: "memory");
      q1A = LD16S(qo1, qb + (w2 ^ 1) * 256); q1B = LD16S(qo1, qb + w2 * 256); q2A = LD8S(qo2, qb + (w2 ^ 1) * 128); q2B = LD8S(qo2, qb + w2 * 128);
    }
  }
  const int vr = r <= 21 ? r : 21;
  const int ok1 = lane * 16, ok2 = h ? (1280 + 20 * 16) : (1024 + r * 8), ov = 1280 + h * VB + vr * 16;
  const f32x16 Z = {};
  if (h) { q2A = (u32x2){0u, 0u}; q2B = (u32x2){0u, 0u}; }
  { const LAS char* t0 = lp;
    const u32x4 k1 = ldsr16(t0 + ok1); const u32x2 k2 = ldsr8(t0 + ok2);
    f32x16 sa = mfma32(k1, q1A, Z); sa = mfma32k8(k2, q2A, sa);
    f32x16 sb = mfma32(k1, q1B, Z); sb = mfma32k8(k2, q2B, sb);
    float ma = sa[0], mb = sb[0];
#pragma unroll
    for (int i = 1; i < 16; i++) { ma = fmaxf(ma, sa[i]); mb = fmaxf(mb, sb[i]); }
    ma = half_swap_max(ma); mb = half_swap_max(mb);
    const float mah = __uint_as_float(__float_as_uint(ma) & 0xFFFF0000u), mbh = __uint_as_float(__float_as_uint(mb) & 0xFFFF0000u);
    if (h) { q2A.x = pk(-mah, mah - ma); q2B.x = pk(-mbh, mbh - mb); } }
  f32x16 OA = {}, OB = {}, SA, SB;
  u32x4 k1, v1, v2, v2n, pA1, pA2, pB1, pB2; u32x2 k2;
  float e0, e1, e2, e3;
  const LAS char* c0p = lp + w2 * CH_B + ok1; const LAS char* c1p = lp + w2 * CH_B + ok2; const LAS char* c2p = lp + w2 * CH_B + ov;
  k1 = ldsr16(c0p); k2 = ldsr8(c1p);
  SA = mfma32(k1, q1A, Z); SA = mfma32k8(k2, q2A, SA);
  v1 = ldsr16(c2p); v2 = v1;
  e0 = e1 = e2 = e3 = 0.f; pB2 = (u32x4){0u, 0u, 0u, 0u};
#define G(MF, LD, H, M, C0_, C1_, X, S_, i) MF; LD; H; SBAR(); C0_; C1_; SBAR(); X##0 = ex2(S_[i]); X##1 = ex2(S_[i + 1]); SBAR(); M; SBAR(); X##2 = ex2(S_[i + 2]); X##3 = ex2(S_[i + 3]); \
    asm volatile("" : "+v"(X##0), "+v"(X##1), "+v"(X##2), "+v"(X##3)); SBAR();
#define TILEF(TN, V2, F) { float a0, a1, a2, a3; \
    G(SB = mfma32(k1, q1B, Z),    k1 = ldsr16(d0p + (TN)), F(0), F(8),  pB2[2] = pk(e0, e1), pB2[3] = pk(e2, e3), a, SA, 0) \
    G(OB = mfma32(v2, pB2, OB),   V2,                      F(1), F(9),  pA1[0] = pk(a0, a1), pA1[1] = pk(a2, a3), e, SA, 4) \
    G(SB = mfma32k8(k2, q2B, SB), k2 = ldsr8(d1p + (TN)),  F(2), F(10), pA1[2] = pk(e0, e1), pA1[3] = pk(e2, e3), a, SA, 8) \
    G(OA = mfma32(v1, pA1, OA),   (void)0,                 F(3), F(11), pA2[0] = pk(a0, a1), pA2[1] = pk(a2, a3), e, SA, 12) \
    G(SA = mfma32(k1, q1A, Z),    (void)0,                 F(4), F(12), pA2[2] = pk(e0, e1), pA2[3] = pk(e2, e3), a, SB, 0) \
    G(OA = mfma32(v2, pA2, OA),   (void)0,                 F(5), F(13), pB1[0] = pk(a0, a1), pB1[1] = pk(a2, a3), e, SB, 4) \
    G(SA = mfma32k8(k2, q2A, SA), (void)0,                 F(6), F(14), pB1[2] = pk(e0, e1), pB1[3] = pk(e2, e3), a, SB, 8) \
    G(OB = mfma32(v1, pB1, OB),   v1 = ldsr16(d2p + (TN)), F(7), F(15), pB2[0] = pk(a0, a1), pB2[1] = pk(a2, a3), e, SB, 12) }
#define TILE(TB, TN, V2, H0, H1, H2) { auto f_ = [&](int n) { if (n == 3) { H0; } if (n == 4) { H1; } if (n == 5) { H2; } }; TILEF(TN, V2, f_) }
#define TLF(t, F) TILEF(((t) + 1) * TILE_B, v2 = ldsr16(d2p + (t) * TILE_B + 2 * VB), F)
#define NOP (void)0
  const LAS char *d0p = c0p, *d1p = c1p, *d2p = c2p;
#define TL(sl, t, H0, H1, H2) TILE(0, ((t) + 1) * TILE_B, v2 = ldsr16(d2p + (t) * TILE_B + 2 * VB), H0, H1, H2)
#define TL6(sl, H0, H1) TL(sl, 6, H0, H1, v2n = ldsr16(d2p + 7 * TILE_B + 2 * VB))
#define TL7(sl, sn, H0, H1, H2) d0p += ((sn) - (sl)) * STEP_B; d1p += ((sn) - (sl)) * STEP_B; d2p += ((sn) - (sl)) * STEP_B; asm volatile("" : "+v"(d0p), "+v"(d1p), "+v"(d2p)); TILE(0, 0, v2 = v2n, H0, H1, H2)
#define WOG(wo, gr, j, dst) do { if ((j) == 4) WOP(wo, gr, 4, dst); else { if ((j) == 0) dmag_open(lds0 + (dst)); dmag<(j)>((wo) + C0 + (gr) * 32 * 640 - 2048, woff[j]); } } while (0)
#define WOP(wo, gr, j, dst) dma16s((wo) + C0 + (gr) * 32 * 640 - (4 - (j)) * 512, woff[j], lds0 + (dst) + (j) * 1024)
#define WA(gr, j) WOG(woA, gr, j, L_WO + (gr) * 5120)
  const int wx3 = w3 ? w3 : 1;
  const u16* wxs = (w2 ? woB : woA) + (16 + wx3) * 32 * 640;
  const int wxd = w2 ? 145408 + (wx3 - 1) * 5120 : L_WO + (16 + wx3) * 5120;
#define WX(j) WOG(wxs, 0, j, wxd)
#define PRIO(n) __builtin_amdgcn_s_setprio(n)
  u32x4 ones = ONES; if (role) asm volatile("" : "+v"(ones));
#define FILL(j) do { const int n_ = w * 3 + ((j) >> 1); ST16S(ones, ok1, (((j) & 1) ? WS_OB : WS_OA) + (rt * 8 + hq) * OT_B + (n_ < 20 ? n_ : 19) * 1024); } while (0)
#define CLR_VM() __builtin_amdgcn_s_waitcnt(0x0F70)
  const bool red = !role && w2;
  f32x4 part[4]; u64 fp0, fp1;
  auto red_load = [&]() {
    asm volatile("" : "+v"(fp0), "+v"(fp1));
    if (__builtin_expect(!__all((fp0 == tag) & (fp1 == tag)), 0)) wait_flags<128>(ws, WS_FPART + hq * 1024, tag, lane);
    asm volatile("" ::: "memory");
#pragma unroll
    for (int j = 0; j < 4; j++) part[j] = __builtin_bit_cast(f32x4, LD16S(g * (4 * PART_B) + l15 * 16, WS_PART + (hq * 16 + j) * PART_B + rt * 960 + w3 * 240));
  };
  auto red_store = [&]() {
    f32x4 a = (part[0] + part[1]) + (part[2] + part[3]);
#pragma unroll
    for (int i = 0; i < 4; i++) { auto r16 = __builtin_amdgcn_permlane16_swap(__float_as_uint(a[i]), __float_as_uint(a[i]), false, false); const float t = __uint_as_float(r16[0]) + __uint_as_float(r16[1]);
      auto r32 = __builtin_amdgcn_permlane32_swap(__float_as_uint(t), __float_as_uint(t), false, false); a[i] = __uint_as_float(r32[0]) + __uint_as_float(r32[1]); }
    const int hd = rt >> 2, u = (rt & 3) * 60 + w3 * 15 + lane, hi = u >= 160, fq = hi ? (u - 160) >> 3 : u >> 4, di = hi ? u & 7 : u & 15;
    __builtin_amdgcn_raw_buffer_store_b64((u32x2){pk(a[0], a[1]), pk(a[2], a[3])}, wsr, lane < 15 ? (hi * 5 + (fq >> 1)) * 512 + di * 16 + (fq & 1) * 8 : 0x7FFF0000u, WS_KVR + hq * KVR_B + (hd >> 1) * 5120 + (hd & 1) * 256, 16);
  };
  u32x4 ka[10]; u64 fkr; float gta; unsigned kb; const unsigned kbs = WS_KVR + hq * KVR_B + w2 * 5120;
  auto oa_load = [&]() {
    asm volatile("" : "+v"(fkr));
    if (__builtin_expect(!__all(fkr == tag), 0)) wait_flags<64>(ws, WS_FKVR + hq * 512, tag, lane);
    asm volatile("" ::: "memory");
    kb = (((r >> 2) & 1) == h) ? (h * 16 + (r & 3) + 4 * (r >> 3)) * 16 : 0x7FFF0000u;
    ka[0] = LD16S(kb, kbs); ka[5] = LD16S(kb, kbs + 2560); ka[1] = LD16S(kb, kbs + 512); ka[6] = LD16S(kb, kbs + 3072);
    gta = __uint_as_float(__builtin_amdgcn_raw_buffer_load_b32(wsr, WS_G + (rt * 128 + w3 * 32 + r) * 4, 0, 16));
  };
  u32x4 jf[16]; f32x4 jc[3];
  const LAS char* jab = lp + L_PHK + ((w >> 1) * 128 + g * 8 + q4) * 80 + p4 * 8;
  const LAS char* jbb = lp + L_VIM + ((w >> 1) * 128 + g * 8 + q4) * 40 + (w & 1) * 32 + p4 * 8;
  auto kv_rd = [&](int n) { const int ks = n >> 2, j = n & 3;
    jf[n] = j ? trd2(jab + ks * 2560 + (j - 1) * 32, jab + ks * 2560 + 320 + (j - 1) * 32) : trd2(jbb + ks * 1280, jbb + ks * 1280 + 160); };
  auto kv_mm = [&](int m) { const int ks = m / 3, ft = m % 3;
    if (ft == 0 && (w & 1) && l15 == 4) jf[ks * 4] = (u32x4){0x3F803F80u, 0x3F803F80u, 0x3F803F80u, 0x3F803F80u};
    jc[ft] = mfma16(jf[ks * 4 + 1 + ft], jf[ks * 4], ks ? jc[ft] : (f32x4){0.f, 0.f, 0.f, 0.f}); };
  auto kv_st = [&](int ft) { const int dt = w & 1;
    const unsigned vo = ((!dt || l15 < 8) && (ft < 2 || g < 2)) ? (dt ? g * 128 : g * 256) + l15 * 16 : 0x7FFF0000u;
    ST16S(__builtin_bit_cast(u32x4, jc[ft]), vo, WS_PART + (hq * 16 + rt) * PART_B + (w >> 1) * 3840 + (dt ? 2560 + ft * 512 : ft * 1024)); };
  f32x16 oa0, oa1; u32x4 z0, z1, z2;
  LAS char* otb = lp + L_WO + (8 + w) * 5120;
  auto oa_mm = [&](int m) { const int s = m >> 1; if (m & 1) oa1 = mfma32(ka[5 + s], ph[s], s ? oa1 : Z); else oa0 = mfma32(ka[s], ph[s], s ? oa0 : Z); };
  auto oa_norm = [&]() { const float inv = __builtin_amdgcn_rcpf(oa1[4] + 1e-6f) * gta;
    LAS char* ar = otb + (r >> 4) * 1280 + l15 * 16; LAS char* ae = ar + h * 520; LAS char* ao = ar + 8 + h * 760;
#pragma unroll
    for (int t = 0; t < 5; t++) {
      const u32x2 v = t < 4 ? (u32x2){nz(pk(oa0[(4 * t) & 15] * inv, oa0[(4 * t + 1) & 15] * inv)), nz(pk(oa0[(4 * t + 2) & 15] * inv, oa0[(4 * t + 3) & 15] * inv))} : (u32x2){nz(pk(oa1[0] * inv, oa1[1] * inv)), nz(pk(oa1[2] * inv, oa1[3] * inv))};
      *(LAS u32x2*)(((t & 1) ? ao : ae) + (t >> 1) * 256) = v; }
    z0 = ldsr16(otb + ok1); z1 = ldsr16(otb + 1280 + ok1); z2 = ldsr16(ar + 1024); };
  auto oa_st = [&]() { const unsigned oa = WS_OA + (rt * 8 + w3 * 2) * OT_B + (hq * 2 + w2) * 1280;
    ST16S(z0, ok1, oa); ST16S(z1, ok1, oa + OT_B); ST16S(z2, h ? 0x7FFF0000u : (r >> 4) * OT_B + l15 * 16, oa + 1024); };
  unsigned cpk;
  PRIO(3);
  if (role) { TL(0, 0, FILL(0), NOP, NOP) TL(0, 1, FILL(1), NOP, NOP) TL(0, 2, FILL(2), NOP, NOP) }
  else {
    auto f0 = [&](int n) { if (n < 8) kv_rd(n); if (n >= 13) kv_mm(n - 13); };
    auto f1 = [&](int n) { if (n < 8) kv_rd(8 + n); if (n == 7) ARRIVE_NW(0); if (n >= 9 && n < 12) kv_mm(n - 6); if (n >= 13) kv_mm(n - 7); };
    auto f2 = [&](int n) { if (n >= 9 && n < 12) kv_mm(n); if (n >= 5 && n < 8) kv_st(n - 5); };
    TLF(0, f0) TLF(1, f1) TLF(2, f2)
  }
  TL(0, 3, dma4s((w2 ? woB : woA) + C0 + (((rt & 7) * 2 + role) * 40 + w3 * 10) * 640, (l15 < 10 ? l15 : 9) * 1280 + (g & 1) * 156, lds0 + 160768)        , NOP, NOP)
  TL(0, 4, NOP, NOP, if (role) FILL(3))
  PRIO(2);
  TL(0, 5, if (!role) { WAIT_VM(0); FLAG(WS_FPART + ((hq * 16 + rt) * 8 + w) * 8); }, NOP, cpk = PEEK(0))
  AWAIT(0, cpk);
  asm volatile("" ::: "memory");
  TL6(0, KX(2, 2), KW(2, 2, 0))
  TL7(0, 1, KW(2, 2, 1), KW(2, 2, 2), KW(2, 2, 3))
  TL(1, 0, KW(2, 2, 4), if (role) FILL(4), if (!role) { fp0 = POLL(WS_FPART + hq * 1024); fp1 = POLL(WS_FPART + hq * 1024 + 512); })
  TL(1, 1, if (role) { WX(0); WX(1); }, if (role) FILL(5), if (role) { WX(2); WX(3); })
  TL(1, 2, if (role) WX(4), NOP, if (red) red_load())
  PRIO(1);
  TL(1, 3, NOP, NOP, NOP)
  TL(1, 4, NOP, if (!role) CLR_VM(), if (red) red_store())
  TL(1, 5, NOP, NOP, NOP)
  PRIO(0);
  TL6(1, if (!role) { WAIT_VM(0); FLAG((w2 ? WS_FKVR : WS_SPARE) + ((hq * 16 + rt) * 4 + w3) * 8); }, NOP)
  if (!role) WAIT_BAR(1); else WAIT_BAR(0);
  PRIO(3);
  if (role) FLAG(WS_FOA + ((rt * 8 + hq) * 8 + w) * 8);
  u64 foa;
  TL7(1, 2, KX(3, 0), KW(3, 0, 0), KW(3, 0, 1))
  TL(2, 0, KW(3, 0, 2), KW(3, 0, 3), KW(3, 0, 4)) TL(2, 1, WA(w, 0), WA(w, 1), WA(w, 2))
  u32x4 wbr[11];
  const char* wbs = (const char*)(woB + C0 + w * 32 * 640) - 4096;
#define WB1(n) wbr[n] = *(const GAS u32x4*)(wbs + ((n) / 5) * (8 * 32 * 1280) + woff[(n) % 5] + ((n) % 5) * 1024)
#define WB2(j) do { WB1(j); WB1(5 + (j)); } while (0)
#define WA16() dma16s(woA + C0 + 16 * 32 * 640 - 2048, wo16, lds0 + L_WO + 16 * 5120 + w5 * 1024)
#define WB16() wbr[10] = *(const GAS u32x4*)((const char*)(woB + C0 + 16 * 32 * 640) - 4096 + wo16)
  auto chk = [&]() { asm volatile("" : "+v"(foa));
    if (__builtin_expect(!__all(foa == tag), 0)) wait_flags<64>(ws, WS_FOA + rt * 512, tag, lane);
    asm volatile("" ::: "memory"); };
  if (role) {
    TL(2, 2, WA(w, 3), WA(w, 4), NOP)
    PRIO(2);
    TL(2, 3, (fkr = POLL(WS_FKVR + hq * 512), foa = POLL(WS_FOA + rt * 512)), NOP, NOP)
    TL(2, 4, NOP, { chk(); oa_load(); }, { ka[2] = LD16S(kb, kbs + 1024); ka[7] = LD16S(kb, kbs + 3584); ka[3] = LD16S(kb, kbs + 1536); })
    PRIO(1);
    TL(2, 5, { ka[8] = LD16S(kb, kbs + 4096); ka[4] = LD16S(kb, kbs + 2048); ka[9] = LD16S(kb, kbs + 4608); }, NOP, NOP)
    PRIO(0);
    { auto f = [&](int n) { if (n == 5) v2n = ldsr16(d2p + 7 * TILE_B + 2 * VB); if (n >= 8 && n != 10 && n != 14) oa_mm(n < 10 ? n - 8 : (n < 14 ? n - 9 : 5)); }; TLF(6, f) }
  } else {
    TL(2, 2, WA(w, 3), WA(w, 4), WX(0))
    PRIO(2);
    TL(2, 3, { WX(1); WX(2); }, { WX(3); WX(4); }, foa = POLL(WS_FOA + rt * 512))
    TL(2, 4, NOP, NOP, NOP)
    PRIO(1);
    TL(2, 5, chk(), WB1(0), WB1(5))
    PRIO(0);
    TL6(2, WB1(1), WB1(6))
  }
  BAR_L();
  PRIO(3);
  if (role) {
    d0p -= 2 * STEP_B; d1p -= 2 * STEP_B; d2p -= 2 * STEP_B; asm volatile("" : "+v"(d0p), "+v"(d1p), "+v"(d2p));
    { auto f = [&](int n) { if (n == 8 || n == 9) oa_mm(n - 2); if (n == 11 || n == 12) oa_mm(n - 3); if (n == 5) CLR_VM(); if (n == 6) WA16(); }; TILEF(0, v2 = v2n, f) }
    TL(0, 0, oa_norm(), NOP, oa_st())
    TL(0, 1, WA(8 + w, 0), WA(8 + w, 1), WA(8 + w, 2))
    TL(0, 2, WA(8 + w, 3), WA(8 + w, 4), WB2(0))
    TL(0, 3, WB2(1), NOP, WB2(2))
    PRIO(2);
    TL(0, 4, WB2(3), NOP, WB2(4))
  } else {
    TL7(2, 0, WA16(), WA(8 + w, 0), WA(8 + w, 1))
    TL(0, 0, WA(8 + w, 2), WA(8 + w, 3), WA(8 + w, 4))
    TL(0, 1, WB1(2), NOP, WB1(7)) TL(0, 2, WB1(3), NOP, WB1(8)) TL(0, 3, WB1(4), NOP, WB1(9))
    PRIO(2);
    TL(0, 4, NOP, NOP, NOP)
  }
  ARRIVE_NW(1);
  const int tp = w & 1, kq = w >> 1;
  const unsigned ofr = (rt * 8 + role * 4 + tp * 2) * OT_B + kq * 5120;
  u32x4 of[2][5]; float gtb;
#define OFL(u) do { _Pragma("unroll") for (int i = 0; i < 5; i++) of[u][i] = LD16S(ok1, WS_OA + ofr + (u) * OT_B + i * 1024); } while (0)
#define OF2(i) do { of[0][i] = LD16S(ok1, WS_OA + ofr + (i) * 1024); of[1][i] = LD16S(ok1, WS_OA + ofr + OT_B + (i) * 1024); } while (0)
  TL(0, 5, WB16(), gtb = __uint_as_float(__builtin_amdgcn_raw_buffer_load_b32(wsr, WS_G + (rt * 128 + w3 * 32 + w2 * 16 + l15) * 4, 0, 16)), OF2(0))
  PRIO(1);
  TL6(0, OF2(1), OF2(2))
  PRIO(0);
  LAS f32x4* xb = (LAS f32x4*)(lp + (w3 >> 1) * CH_B) + (w3 & 1) * (64 * 6) + lane;
  { auto f = [&](int n) { if (n == 0) cpk = PEEK(1); if (n == 3) OF2(3); if (n == 4) OF2(4);
      if (n == 15) { AWAIT(1, cpk);
#pragma unroll
        for (int c = 0; c < 3; c++) xb[(w2 * 3 + c) * 64] = (f32x4){OA[4 * c], OA[4 * c + 1], OA[4 * c + 2], OA[4 * c + 3]}; } };
    TILEF(0, v2 = v2n, f) }
  pB2[2] = pk(e0, e1); pB2[3] = pk(e2, e3);
  OB = mfma32(v2, pB2, OB);
#undef TL
#undef TL6
#undef TL7
#undef TILE
#undef G
  { asm volatile("s_waitcnt vmcnt(18) lgkmcnt(0)\n\ts_barrier" ::: "memory");
    f32x16 O = OB;
#pragma unroll
    for (int c = 0; c < 3; c++) { const f32x4 t = xb[((w2 ^ 1) * 3 + c) * 64]; O[4 * c] += t[0]; O[4 * c + 1] += t[1]; O[4 * c + 2] += t[2]; O[4 * c + 3] += t[3]; }
    float l = from_upper(O[8]);
    const float amax = (((fabsf(O[0]) + fabsf(O[1])) + (fabsf(O[2]) + fabsf(O[3]))) + ((fabsf(O[4]) + fabsf(O[5])) + (fabsf(O[6]) + fabsf(O[7])))) + ((fabsf(O[8]) + fabsf(O[9])) + (fabsf(O[10]) + fabsf(O[11])));
    const bool good = (l >= 0x1p-30f) && (l <= 0x1p100f) && (amax <= 0x1p126f);
    if (__builtin_expect(!__all(good), 0)) {
      const u32x4 q1 = q1B; const u32x2 q2 = h ? (u32x2){0u, 0u} : q2B;
      float m = -INFINITY;
#pragma unroll 1
      for (int t = 0; t < 64; t++) {
        f32x16 s = mfma32(LD16(WS_KV + t * TILE_B + ok1), q1, Z); s = mfma32k8(LD8(WS_KV + t * TILE_B + ok2), q2, s);
#pragma unroll
        for (int i = 0; i < 16; i++) m = fmaxf(m, s[i]);
      }
      m = half_swap_max(m);
      O = Z;
#pragma unroll 1
      for (int t = 0; t < 64; t++) {
        f32x16 s = mfma32(LD16(WS_KV + t * TILE_B + ok1), q1, Z); s = mfma32k8(LD8(WS_KV + t * TILE_B + ok2), q2, s);
#pragma unroll
        for (int i = 0; i < 16; i++) s[i] = ex2(s[i] - m);
        O = mfma32(LD16(WS_KV + t * TILE_B + ov), (u32x4){pk(s[0], s[1]), pk(s[2], s[3]), pk(s[4], s[5]), pk(s[6], s[7])}, O);
        O = mfma32(LD16(WS_KV + t * TILE_B + ov + 2 * VB), (u32x4){pk(s[8], s[9]), pk(s[10], s[11]), pk(s[12], s[13]), pk(s[14], s[15])}, O);
      }
      l = from_upper(O[8]);
    }
    const float inv = __builtin_amdgcn_rcpf(l) * (1.0f - gtb);
    LAS char* tb = lp + w2 * CH_B + 12288 + w3 * 1536;
    const int uq = (g & 1) * 5 + h;
    LAS char* t0 = tb + (uq >> 1) * 256 + (uq & 1) * 8 + l15 * 16; LAS char* t2 = h ? tb + 1280 + (g & 1) * 8 + l15 * 16 : t0 + 512;
#pragma unroll
    for (int c = 0; c < 3; c++) *(LAS u32x2*)(c < 2 ? t0 + c * 256 : t2) = (u32x2){nz(pk(O[4 * c] * inv, O[4 * c + 1] * inv)), nz(pk(O[4 * c + 2] * inv, O[4 * c + 3] * inv))};
    const unsigned ob = WS_OB + (rt * 8 + w3 * 2 + w2) * OT_B + hp * 1280;
    const u32x4 y0 = ldsr16(tb + ok1), y1 = ldsr16(tb + 1024 + l15 * 16);
    ST16S(y0, ok1, ob); ST16S(y1, lane < 16 ? ok1 : 0x7FFF0000u, ob + 1024);
    SBAR();
  }
  auto of_missing = [&]() { u32x4 mm = of[0][0];
#pragma unroll
    for (int j = 1; j < 10; j++) mm = __builtin_elementwise_max(mm, of[j / 5][j % 5]);
    return missing(mm); };
  u32x4 wf[5][5];
#pragma unroll
  for (int i = 0; i < 5; i++) { const LAS char* wb = lp + L_WO + (kq * 5 + i) * 5120 + wlane;
#pragma unroll
    for (int t = 0; t < 5; t++) wf[i][t] = trd2(wb + t * 8, wb + 1280 + t * 8); }
  SBAR();
  if (__builtin_expect(of_missing(), 0)) {
#pragma unroll 1
    for (unsigned spin = 0; spin < SPIN_CAP; ++spin) {
      asm volatile("" ::: "memory");
      OFL(0); OFL(1);
      if (!of_missing()) break;
      __builtin_amdgcn_s_sleep(1);
    }
  }
  f32x4 c[2][5];
#pragma unroll
  for (int i = 0; i < 5; i++)
#pragma unroll
    for (int t = 0; t < 5; t++) c[0][t] = mfma16(wf[i][t], of[0][i], i ? c[0][t] : (f32x4){0.f, 0.f, 0.f, 0.f});
  asm volatile("" : "+v"(c[0][0]), "+v"(c[0][1]), "+v"(c[0][2]), "+v"(c[0][3]));
  SBAR();
  BAR_L();
  SBAR();
  { LAS char* d = lp + L_WO + w * 5120 + lane * 16;
    *(LAS u32x4*)(lp + L_WO + 16 * 5120 + w5 * 1024 + lane * 16) = wbr[10];
#pragma unroll
    for (int i = 0; i < 5; i++) {
      *(LAS u32x4*)(d + i * 1024) = wbr[i]; *(LAS u32x4*)(d + 40960 + i * 1024) = wbr[5 + i]; of[0][i] = LD16S(ok1, WS_OB + ofr + i * 1024); SBAR();
#pragma unroll
      for (int t = 0; t < 5; t++) c[1][t] = mfma16(wf[i][t], of[1][i], i ? c[1][t] : (f32x4){0.f, 0.f, 0.f, 0.f});
      SBAR();
    } }
  BAR_L();
#pragma unroll
  for (int i = 0; i < 5; i++) { const int gr = kq * 5 + i; const LAS char* wb = lp + (gr < 17 ? L_WO + gr * 5120 : 145408 + (gr - 17) * 5120) + wlane;
#pragma unroll
    for (int t = 0; t < 5; t++) wf[i][t] = trd2(wb + t * 8, wb + 1280 + t * 8);
    SBAR(); of[1][i] = LD16S(ok1, WS_OB + ofr + OT_B + i * 1024); SBAR(); }
  ARRIVE_NW(2);
  LAS char* xw = lp + kq * 21504 + (tp * 32 + l15) * 336 + g * 80;
#pragma unroll
  for (int u = 0; u < 2; u++) {
#pragma unroll
    for (int i = 0; i < 5; i++) {
      if (__builtin_expect(missing(of[u][i]), 0)) {
#pragma unroll 1
        for (unsigned spin = 0; spin < SPIN_CAP; ++spin) {
          asm volatile("" ::: "memory");
#pragma unroll
          for (int j = u * 5 + i; j < 10; j++) of[j / 5][j % 5] = LD16S(ok1, WS_OB + ofr + (j / 5) * OT_B + (j % 5) * 1024);
          if (!missing(of[u][i])) break;
          __builtin_amdgcn_s_sleep(1);
        }
      }
#pragma unroll
      for (int t = 0; t < 5; t++) c[u][t] = mfma16(wf[i][t], of[u][i], c[u][t]);
      if (u == 0 && i == 3) cpk = PEEK(2);
      if (u == 1) { if (i == 0) { AWAIT(2, cpk); asm volatile("" ::: "memory"); }
        *(LAS f32x4*)(xw + i * 16) = c[0][i]; }
      SBAR();
    }
  }
#pragma unroll
  for (int t = 0; t < 5; t++) *(LAS f32x4*)(xw + 16 * 336 + t * 16) = c[1][t];
  BAR_L();
  { const __amdgpu_buffer_rsrc_t orr = __builtin_amdgcn_make_buffer_rsrc(out, 0, 2048 * DM * 2, 0x00020000);
#pragma unroll
    for (int j = 0; j < 3; j++) { const int n = tid + 512 * j, nn = n < 1280 ? n : 0, row = (nn * 3277) >> 16, cq = nn - row * 20;
      const LAS f32x4* p = (const LAS f32x4*)(lp + row * 336 + cq * 16);
      const f32x4 z = (p[0] + p[1344]) + (p[2688] + p[4032]);
      __builtin_amdgcn_raw_buffer_store_b64((u32x2){pk(z[0], z[1]), pk(z[2], z[3])}, orr, n < 1280 ? ((rt * 128 + role * 64 + row) * DM + C0 + cq * 4) * 2 : 0x7FFF0000u, 0, 0); }
  }
}

__global__ __launch_bounds__(512, 2) void fused(const u16* __restrict__ x, const u16* __restrict__ wqkv, const u16* __restrict__ wphi, const u16* __restrict__ woA,
                                                const u16* __restrict__ wq, const u16* __restrict__ wk, const u16* __restrict__ wv, const u16* __restrict__ woB,
                                                const u16* __restrict__ gatew, u16* __restrict__ out, char* ws) {
  __shared__ __attribute__((aligned(16))) char lds[L_TOTAL];
  if ((blockIdx.x >> 6) & 1) body<1>(x, wqkv, wphi, woA, wq, wv, woB, gatew, out, ws, (LAS char*)lds);
  else body<0>(x, wqkv, wphi, woA, wq, wk, woB, gatew, out, ws, (LAS char*)lds);
}
}

extern "C" void kernel_launch(void* const* d_in, const int* in_sizes, int n_in, void* d_out, int out_size, void* d_ws, size_t ws_size, hipStream_t stream) {
  using namespace gm;
  fused<<<256, 512, 0, stream>>>((const u16*)d_in[0], (const u16*)d_in[1], (const u16*)d_in[2], (const u16*)d_in[3], (const u16*)d_in[5], (const u16*)d_in[6],
                                 (const u16*)d_in[7], (const u16*)d_in[8], (const u16*)d_in[9], (u16*)d_out, (char*)d_ws);
}
```

```cpp
#include <hip/hip_runtime.h>
#include <hip/hip_bf16.h>
#include <stdint.h>

namespace gm {
typedef float f32x16 __attribute__((ext_vector_type(16)));
typedef float f32x4 __attribute__((ext_vector_type(4)));
typedef float f32x2 __attribute__((ext_vector_type(2)));
typedef short s16x4 __attribute__((ext_vector_type(4)));
typedef __bf16 b16x2 __attribute__((ext_vector_type(2)));
typedef __bf16 b16x8 __attribute__((ext_vector_type(8)));
typedef unsigned u32x4 __attribute__((ext_vector_type(4)));
typedef unsigned u32x2 __attribute__((ext_vector_type(2)));
typedef unsigned long long u64;
typedef unsigned short u16;
#define DI __device__ __forceinline__
#define LAS __attribute__((address_space(3)))
#define GAS __attribute__((address_space(1)))
typedef GAS u64 gu64;

constexpr int DM = 640;
constexpr unsigned WS_EPOCH = 0, WS_FKV = 2048, WS_FKVR = 4096, WS_FQ = 8192, WS_FPART = 16384, WS_FOA = 24576, WS_SPARE = 40960  , WS_G = 57344;
constexpr unsigned WS_KVR = 8912896, KVR_B = 10240, WS_KV = 131072, WS_QX = 327680, QX_B = 10240, WS_PART = 1703936, PART_B = 15360;
constexpr unsigned WS_OA = 3670016, WS_OB = 6291456, WS_END = 8994816, OT_B = 20480;
constexpr int VB = 352, TILE_B = 1280 + 4 * VB, CH_B = TILE_B * 8, STEP_B = 2 * CH_B;
constexpr int STAGE_B = 36864, ST_W1 = 10240, ST_X = 20480;
constexpr int L_BUF = 43008, L_P0W = 0, L_P0X0 = 25600, L_P0X1 = 153600, L_RED = 158720, L_RED1 = 40960;
constexpr int L_HO = 86016, L_QL = 96256, L_VIM = 96256, L_PHK = 116736, L_WPHI = 158592, L_ZERO = 161792, L_CNT = 163712, L_TOTAL = 163840;
constexpr int L_WO = 43008;
constexpr float LOG2E = 1.4426950408889634f;
constexpr float QSCALE = 0.22360679774997896f * LOG2E;
constexpr unsigned SPIN_CAP = 20000, LSPIN_CAP = 400000;

DI unsigned pk(float a, float b) { f32x2 v = {a, b}; b16x2 r = __builtin_convertvector(v, b16x2); return __builtin_bit_cast(unsigned, r); }
DI f32x16 mfma32(u32x4 a, u32x4 b, f32x16 c) { return __builtin_amdgcn_mfma_f32_32x32x16_bf16(__builtin_bit_cast(b16x8, a), __builtin_bit_cast(b16x8, b), c, 0, 0, 0); }
DI f32x16 mfma32k8(u32x2 a, u32x2 b, f32x16 c) { return __builtin_amdgcn_mfma_f32_32x32x8bf16_1k(__builtin_bit_cast(s16x4, a), __builtin_bit_cast(s16x4, b), c, 0, 0, 0); }
DI f32x4 mfma16(u32x4 a, u32x4 b, f32x4 c) { return __builtin_amdgcn_mfma_f32_16x16x32_bf16(__builtin_bit_cast(b16x8, a), __builtin_bit_cast(b16x8, b), c, 0, 0, 0); }
DI u32x2 trd(const LAS char* p) { return __builtin_bit_cast(u32x2, __builtin_amdgcn_ds_read_tr16_b64_v4i16((LAS s16x4*)p)); }
DI u32x4 trd2(const LAS char* p0, const LAS char* p1) { u32x2 a = trd(p0), b = trd(p1); return (u32x4){a.x, a.y, b.x, b.y}; }
DI u32x4 ldsr16(const LAS char* p) { return *(const LAS u32x4*)p; }
DI u32x2 ldsr8(const LAS char* p) { return *(const LAS u32x2*)p; }
DI float ex2(float x) { return __builtin_amdgcn_exp2f(x); }
DI void dma16s(const void* base, unsigned voff, unsigned lds_base) {
  asm volatile("s_mov_b32 m0, %2\n\ts_nop 0\n\tglobal_load_lds_dwordx4 %0, %1" :: "v"(voff), "s"(base), "s"(lds_base) : "memory"); }
DI void dma16s_sc1(const void* base, unsigned voff, unsigned lds_base) {
  asm volatile("s_mov_b32 m0, %2\n\ts_nop 0\n\tglobal_load_lds_dwordx4 %0, %1 sc1" :: "v"(voff), "s"(base), "s"(lds_base) : "memory"); }
DI void dmag_open(unsigned lds_base) { asm volatile("s_mov_b32 m0, %0\n\ts_nop 0" :: "s"(lds_base) : "memory"); }
template <int J> DI void dmag(const void* base, unsigned voff) { asm volatile("global_load_lds_dwordx4 %0, %1 offset:%2" :: "v"(voff), "s"(base), "i"((J & 3) * 1024) : "memory"); }
template <int J> DI void dmag_sc1(const void* base, unsigned voff) { asm volatile("global_load_lds_dwordx4 %0, %1 offset:%2 sc1" :: "v"(voff), "s"(base), "i"((J & 3) * 1024) : "memory"); }
DI void dma4s(const void* base, unsigned voff, unsigned lds_base) {
  asm volatile("s_mov_b32 m0, %2\n\ts_nop 0\n\tglobal_load_lds_dword %0, %1" :: "v"(voff), "s"(base), "s"(lds_base) : "memory"); }
DI u32x4 ld16_async(const void* p) { u32x4 r; asm volatile("global_load_dwordx4 %0, %1, off" : "=v"(r) : "v"(p) : "memory"); return r; }
DI u32x2 ld8_sc1_async(const void* p) { u32x2 r; asm volatile("global_load_dwordx2 %0, %1, off sc1" : "=v"(r) : "v"(p) : "memory"); return r; }
#define BAR_L() asm volatile("s_waitcnt lgkmcnt(0)\n\ts_barrier" ::: "memory")
#define WAIT_BAR(N) asm volatile("s_waitcnt vmcnt(" #N ") lgkmcnt(0)\n\ts_barrier" ::: "memory")
#define WAIT_VM(N) asm volatile("s_waitcnt vmcnt(" #N ")" ::: "memory")
#define SBAR() __builtin_amdgcn_sched_barrier(0)
#define RLX_AGENT __ATOMIC_RELAXED, __HIP_MEMORY_SCOPE_AGENT
DI float half_swap_max(float m) { auto rr = __builtin_amdgcn_permlane32_swap(__float_as_uint(m), __float_as_uint(m), false, false); return fmaxf(__uint_as_float(rr[0]), __uint_as_float(rr[1])); }
DI float from_upper(float v) { auto rr = __builtin_amdgcn_permlane32_swap(__float_as_uint(v), __float_as_uint(v), false, false); return __uint_as_float(rr[1]); }

template <int NF>
DI bool flags_set(const char* ws, unsigned off, u64 tag, int lane) {
  gu64* f = (gu64*)(ws + off);
  bool ok = __hip_atomic_load(f + (NF < 64 ? (lane & (NF - 1)) : lane), RLX_AGENT) == tag;
  if (NF >= 128) ok &= __hip_atomic_load(f + 64 + lane, RLX_AGENT) == tag;
  if (NF >= 256) { ok &= __hip_atomic_load(f + 128 + lane, RLX_AGENT) == tag; ok &= __hip_atomic_load(f + 192 + lane, RLX_AGENT) == tag; }
  return ok;
}
template <int NF>
DI void wait_flags(const char* ws, unsigned off, u64 tag, int lane) {
#pragma unroll 1
  for (unsigned spin = 0; spin < SPIN_CAP; ++spin) {
    if (__all(flags_set<NF>(ws, off, tag, lane))) break;
    __builtin_amdgcn_s_sleep(2);
  }
  asm volatile("" ::: "memory");
}
DI unsigned nz(unsigned v) { return v < 0xFFFFFFFEu ? v : 0xFFFFFFFEu; }
DI unsigned umax3(unsigned a, unsigned b, unsigned c) { unsigned m = a > b ? a : b; return m > c ? m : c; }
DI bool missing(u32x4 v) { return __any(umax3(umax3(v.x, v.y, v.z), v.w, 0u) == 0xFFFFFFFFu); }
#define ONES ((u32x4){0xFFFFFFFFu, 0xFFFFFFFFu, 0xFFFFFFFFu, 0xFFFFFFFFu})
#define ST16(v, off) __builtin_amdgcn_raw_buffer_store_b128((v), wsr, (off), 0, 16)
#define ST8(v, off) __builtin_amdgcn_raw_buffer_store_b64((v), wsr, (off), 0, 16)
#define LD16(off) __builtin_amdgcn_raw_buffer_load_b128(wsr, (off), 0, 16)
#define ST16S(v, voff, soff) ST16(v, (voff) + (soff))
#define LD16S(voff, soff) __builtin_amdgcn_raw_buffer_load_b128(wsr, (voff), (soff), 16)
#define LD8S(voff, soff) __builtin_amdgcn_raw_buffer_load_b64(wsr, (voff), (soff), 16)
#define LD8(off) __builtin_amdgcn_raw_buffer_load_b64(wsr, (off), 0, 16)

#define STAMP(i) do {} while (0)

template <int role>
DI void body(const u16* __restrict__ x, const u16* __restrict__ wqkv, const u16* __restrict__ wphi, const u16* __restrict__ woA,
             const u16* __restrict__ wq, const u16* __restrict__ wkv, const u16* __restrict__ woB,
             const u16* __restrict__ gatew, u16* __restrict__ out, char* ws, LAS char* lp) {
  const int tid = threadIdx.x, lane = tid & 63;
  const int w = __builtin_amdgcn_readfirstlane(tid >> 6) & 7;
  const int l15 = lane & 15, g = lane >> 4, q4 = l15 >> 2, p4 = lane & 3, r = lane & 31, h = lane >> 5;
  const int bx = blockIdx.x & 7, bi = blockIdx.x >> 3;
  const int rt = (bx & 1) * 8 + (bi & 7), hp = (bx >> 1) * 4 + ((bi >> 3) & 3), hq = hp >> 1;
  const int w3 = w & 3, w2 = w >> 2;
  const unsigned lds0 = (unsigned)(uintptr_t)lp;
  const __amdgpu_buffer_rsrc_t wsr = __builtin_amdgcn_make_buffer_rsrc(ws, 0, WS_END, 0x00020000);
  LAS unsigned* cnt = (LAS unsigned*)(lp + L_CNT);
#define ARRIVE_NW(c) do { asm volatile("" ::: "memory"); __hip_atomic_store(cnt + (c) * 8 + w, 1u, __ATOMIC_RELAXED, __HIP_MEMORY_SCOPE_WORKGROUP); asm volatile("" ::: "memory"); } while (0)
#define PEEK(c) __hip_atomic_load(cnt + (c) * 8 + (lane & 7), __ATOMIC_RELAXED, __HIP_MEMORY_SCOPE_WORKGROUP)
#define AWAIT(c, v) do { if (__builtin_expect(!__all((v) != 0u), 0)) { _Pragma("unroll 1") for (unsigned sp_ = 0; sp_ < LSPIN_CAP; ++sp_) { if (__all(PEEK(c) != 0u)) break; __builtin_amdgcn_s_sleep(1); } } asm volatile("" ::: "memory"); } while (0)
#define POLL(off) __builtin_bit_cast(u64, __builtin_amdgcn_raw_buffer_load_b64(wsr, lane * 8, (off), 16))
#define FLAG(off) __builtin_amdgcn_raw_buffer_store_b64(__builtin_bit_cast(u32x2, tag), wsr, lane ? 0x7FFF0000u : 0u, (off), 16)
  STAMP(0);

  const int T0 = rt * 128 + hq * 16;
  const int C0 = hq * 80;
  const int wlane = ((g >> 1) * 16 + (g & 1) * 4 + q4) * 160 + p4 * 40;
  u64 tag;
  unsigned o0[10];
#define KVP(s, slot, p) dma16s_sc1(ws + WS_KV + (s) * STEP_B + (p) * 1024, lane * 16, lds0 + (slot) * STEP_B + (p) * 1024)
#define KVL(s, slot) do { _Pragma("unroll") for (int j = 0; j < 11; j++) KVP(s, slot, j < 10 ? w3 + 4 * j : 40 + (w3 & 1)); } while (0)
#define KW(s, slot, j) do { if ((j) == 4) KVP(s, slot, w * 5 + 4); else { if ((j) == 0) dmag_open(lds0 + (slot) * STEP_B + w * 5120); dmag_sc1<(j)>(ws + WS_KV + (s) * STEP_B + w * 5120, lane * 16); } } while (0)
#define KX(s, slot) KVP(s, slot, 40 + (w & 1))
  const int w5 = w < 5 ? w : 4;
  unsigned wo16;
  unsigned woff[5];
  auto woff_init = [&]() { int ln = lane; asm volatile("" : "+v"(ln));
#pragma unroll
    for (int j = 0; j < 5; j++) { const int c = j * 64 + ln, rho = (c * 6554) >> 16, cc = c - rho * 10; const int k = (rho & ~12) | ((rho & 4) << 1) | ((rho & 8) >> 1); woff[j] = k * 1280 + cc * 16 + (4 - j) * 1024; }
    { const int c = w5 * 64 + ln, rho = (c * 6554) >> 16, cc = c - rho * 10; const int k = (rho & ~12) | ((rho & 4) << 1) | ((rho & 8) >> 1); wo16 = k * 1280 + cc * 16 + 4096; }
    asm volatile("" : "+v"(woff[0]), "+v"(woff[1]), "+v"(woff[2]), "+v"(woff[3]), "+v"(woff[4]), "+v"(wo16)); };
  if (w2 == 0) {
    u32x4 gf0, gf1, gf2, gf3, gf4, wpc;
    u32x2 ep = ld8_sc1_async(ws + WS_EPOCH);
    const int lx = w3 < 3 ? L_P0X0 + w3 * 5120 : L_P0X1;
    { const u16* wsrc = wkv + w3 * 3200;
#pragma unroll
      for (int j = 0; j < 5; j++) { const int n = j * 64 + lane, row = (n * 3277) >> 16, cc = n - row * 20; dma16s(x + T0 * DM + w3 * 160, row * 1280 + cc * 16, lds0 + lx + j * 1024); }
#pragma unroll
      for (int j = 0; j < 6; j++) dma16s(wsrc + j * 512, lane * 16, lds0 + L_P0W + w3 * 6400 + j * 1024);
      if (lane < 16) dma16s(wsrc + 6 * 512, lane * 16, lds0 + L_P0W + w3 * 6400 + 6 * 1024);
      const u16* gp = gatew + w3 * 160 + g * 8;
      gf0 = ld16_async(gp); gf1 = ld16_async(gp + 32); gf2 = ld16_async(gp + 64); gf3 = ld16_async(gp + 96); gf4 = ld16_async(gp + 128);
      wpc = ld16_async(wphi + hq * 1600 + (tid < 200 ? tid : 199) * 8);
    }
    woff_init();
    asm volatile("s_waitcnt vmcnt(0)" : "+v"(gf0), "+v"(gf1), "+v"(gf2), "+v"(gf3), "+v"(gf4), "+v"(wpc), "+v"(ep) :: "memory");
    tag = (((u64)(unsigned)__builtin_amdgcn_readfirstlane(ep.y) << 32) | (unsigned)__builtin_amdgcn_readfirstlane(ep.x)) + 1ull;
    STAMP(1);
    { const LAS char* wb = lp + L_P0W + (w3 * 160 + g * 8 + q4) * 40;
      const LAS char* xb = lp + lx + l15 * 320 + g * 16;
      const bool gl = l15 == 4;
      f32x4 c0 = {}, c1 = {};
#define P0STEP(s, GF) { const u32x4 b = ldsr16(xb + (s) * 64); c0 = mfma16(trd2(wb + p4 * 8 + (s) * 1280, wb + p4 * 8 + (s) * 1280 + 160), b, c0); \
        u32x4 a = trd2(wb + 32 + (s) * 1280, wb + 32 + (s) * 1280 + 160); if (gl) a = GF; c1 = mfma16(a, b, c1); }
      P0STEP(0, gf0) P0STEP(1, gf1) P0STEP(2, gf2) P0STEP(3, gf3) P0STEP(4, gf4)
#undef P0STEP
      *(LAS f32x4*)(lp + L_RED + tid * 16) = c0;
      if (lane < 32) *(LAS f32x4*)(lp + L_RED1 + (w3 * 32 + lane) * 16) = c1; }
    STAMP(2);
    f32x16 acc[5];
#pragma unroll
    for (int b = 0; b < 5; b++) acc[b] = (f32x16){};
    const int wm = g & 1, whd = p4;
    const int wo0 = wm * ST_W1 + (8 * h + 4 * wm + q4) * 160 + whd * 40, wo1 = wm * ST_W1 + (8 * h + 4 * (wm ^ 1) + q4) * 160 + whd * 40;
    const int xo = ST_X + (w3 * 32 + r) * 128 + ((h ^ ((r >> 1) & 7)) * 16);
    u32x4 fa[5], fb[5], xa, xb;
#define KSTEP(FN, XN, s, FC, XC) { XN = ldsr16(sb + (xo ^ ((s) * 32))); \
      _Pragma("unroll") for (int j = 0; j < 5; j++) { if (i > 0 || (s) > 0) acc[j] = mfma32(FC[j], XC, acc[j]); FN[j] = trd2(sb + wo0 + (s) * 2560 + j * 8, sb + wo1 + (s) * 2560 + j * 8); SBAR(); } }
#pragma unroll
    for (int i = 0; i < 10; i++) {
      BAR_L();
      if (i == 1 && role) {
        const unsigned qb = WS_QX + (rt * 8 + hq) * QX_B + w3 * 1280 + lane * 16;
        ST16(ONES, qb); ST16(ONES, qb + 5120); ST16(ONES, lane < 32 ? qb + 1024 + (lane >> 4) * (5120 - 256) : 0x7FFF0000u); }
      if (i == 4) { if (role) { WAIT_VM(0); FLAG(WS_FQ + ((rt * 8 + hq) * 4 + w3) * 8); }
                    if (tid < 200) *(LAS u32x4*)(lp + L_WPHI + tid * 16) = wpc; }
      const LAS char* sb = lp + L_BUF + ((i + 2) % 3) * STAGE_B;
      KSTEP(fa, xa, 0, fb, xb) KSTEP(fb, xb, 1, fa, xa) KSTEP(fa, xa, 2, fb, xb)
      { xb = ldsr16(sb + (xo ^ 96));
        acc[0] = mfma32(fa[0], xa, acc[0]); fb[0] = trd2(sb + wo0 + 7680, sb + wo1 + 7680); fb[1] = trd2(sb + wo0 + 7688, sb + wo1 + 7688); SBAR();
        acc[1] = mfma32(fa[1], xa, acc[1]); fb[2] = trd2(sb + wo0 + 7696, sb + wo1 + 7696); fb[3] = trd2(sb + wo0 + 7704, sb + wo1 + 7704); SBAR();
        acc[2] = mfma32(fa[2], xa, acc[2]); fb[4] = trd2(sb + wo0 + 7712, sb + wo1 + 7712); SBAR();
        acc[3] = mfma32(fa[3], xa, acc[3]); acc[4] = mfma32(fa[4], xa, acc[4]); SBAR(); }
    }
#pragma unroll
    for (int j = 0; j < 5; j++) acc[j] = mfma32(fb[j], xb, acc[j]);
#undef KSTEP
    STAMP(3);
#define PK2(t, i, sc) pk(acc[t][i] * (sc), acc[t][(i) + 1] * (sc))
#define ROW(i, sc) const u32x4 ra = {PK2(0, i, sc), PK2(0, (i) + 2, sc), PK2(1, i, sc), PK2(1, (i) + 2, sc)}, rb = {PK2(2, i, sc), PK2(2, (i) + 2, sc), PK2(3, i, sc), PK2(3, (i) + 2, sc)}; const u32x2 rc = {PK2(4, i, sc), PK2(4, (i) + 2, sc)}
    { ROW(4, 1.0f);
      *(LAS u32x4*)(lp + L_HO + tid * 16) = ra; *(LAS u32x4*)(lp + L_HO + 4096 + tid * 16) = rb; *(LAS u32x2*)(lp + L_HO + 8192 + tid * 8) = rc; }
    BAR_L();
    const int qfo = (h * 4 + w3) * 1280;
    if (role) {
      { ROW(8, QSCALE); const unsigned qb = WS_QX + (rt * 8 + hq) * QX_B + qfo;
        ST16(((u32x4){nz(ra.x), nz(ra.y), nz(ra.z), nz(ra.w)}), qb + r * 16); ST16(((u32x4){nz(rb.x), nz(rb.y), nz(rb.z), nz(rb.w)}), qb + 512 + r * 16); ST8(((u32x2){nz(rc.x), nz(rc.y)}), qb + 1024 + r * 8); }
      { ROW(12, QSCALE); LAS char* d = lp + L_QL + qfo;
        *(LAS u32x4*)(d + r * 16) = ra; *(LAS u32x4*)(d + 512 + r * 16) = rb; *(LAS u32x2*)(d + 1024 + r * 8) = rc; }
    } else { LAS char* d = lp + L_VIM + (h * 128 + w3 * 32 + r) * 40;
#pragma unroll
      for (int t = 0; t < 5; t++) { *(LAS u32x2*)(d + t * 8) = (u32x2){PK2(t, 8, 1.0f), PK2(t, 10, 1.0f)}; *(LAS u32x2*)(d + 10240 + t * 8) = (u32x2){PK2(t, 12, 1.0f), PK2(t, 14, 1.0f)}; } }
#pragma unroll
    for (int t = 0; t < 5; t++) { o0[2 * t] = PK2(t, 0, 1.0f); o0[2 * t + 1] = PK2(t, 2, 1.0f); }
#undef ROW
#undef PK2
  } else {
    u32x2 ep = ld8_sc1_async(ws + WS_EPOCH);
    const bool second = w3 >> 1;
    const unsigned rs = (second && role) ? 1280u : 3840u;
    const char* wsrc = (second ? (role ? (const char*)(wq + C0) : (const char*)(wqkv + 1280 + C0)) : (const char*)(wqkv + (role ? 0 : 640) + C0)) + (w3 & 1) * 32 * rs;
    const char* xsrc = (const char*)(x + (rt * 128 + w3 * 32) * DM);
    int rot = (bi & 7) + 3 * ((bi >> 3) & 3); rot = rot >= 10 ? rot - 10 : rot;
    unsigned so[5], xoff[2];
#pragma unroll
    for (int j = 0; j < 5; j++) { const int c = j * 64 + lane, rho = (c * 6554) >> 16, cc = c - rho * 10; so[j] = (rho ^ (second ? 4 : 0)) * rs + cc * 16; }
#pragma unroll
    for (int j = 0; j < 2; j++) { const int row = j * 8 + (lane >> 3); xoff[j] = row * 1280 + (((lane & 7) ^ ((row >> 1) & 7)) * 16); }
    int bsel = 2, kc = rot;
    auto stage = [&]() {
      const unsigned lb = lds0 + L_BUF + bsel * STAGE_B;
      const char* bw = wsrc + kc * 64 * rs; const char* bxp = xsrc + kc * 128;
#pragma unroll
      for (int j = 0; j < 5; j++) { dma16s(bw, so[j], lb + (w3 * 5 + j) * 1024); if (j < 4) dma16s(bxp + (j >> 1) * 16 * 1280, xoff[j & 1], lb + ST_X + (w3 * 4 + j) * 1024); }
      bsel = bsel == 2 ? 0 : bsel + 1; kc = kc == 9 ? 0 : kc + 1;
    };
    stage(); stage();
    woff_init();
    asm volatile("s_waitcnt vmcnt(9)" : "+v"(ep) :: "memory");
    tag = (((u64)(unsigned)__builtin_amdgcn_readfirstlane(ep.y) << 32) | (unsigned)__builtin_amdgcn_readfirstlane(ep.x)) + 1ull;
    STAMP(1); STAMP(2);
#pragma unroll 1
    for (int i = 0; i < 8; i++) {
      WAIT_BAR(9);
      stage();
      if (i == 0 && w3 == 0) {
        const unsigned tb = WS_KV + (T0 >> 5) * TILE_B; const int s16 = hq & 1;
        if (!role) {
          const LAS f32x4* rp = (const LAS f32x4*)(lp + (h ? L_RED1 : L_RED)) + (h ? (g & 1) * 16 : g * 32) + l15;
          const int st = h ? 32 : 64, o2 = h ? 0 : 16;
          const f32x4 a = (rp[0] + rp[st]) + (rp[2 * st] + rp[3 * st]), b = (rp[o2] + rp[st + o2]) + (rp[2 * st + o2] + rp[3 * st + o2]);
          const u32x4 v = {pk(a[0], a[1]), pk(a[2], a[3]), pk(b[0], b[1]), pk(b[2], b[3])};
          if (lane < 32) ST16(v, tb + (g * 32 + s16 * 16 + l15) * 16);
          else if (lane < 48) ST8(((u32x2){v.x, v.y}), tb + 1024 + (s16 * 16 + l15) * 8);
          else { const float gv = 1.0f / (1.0f + __expf(-a[0])); __builtin_amdgcn_raw_buffer_store_b32(__float_as_uint(gv), wsr, WS_G + (T0 + l15) * 4, 0, 16); }
        } else if (lane < 44) {
          const int hh = lane >= 22, d = lane - hh * 22, dd = d < 20 ? d : 0, st = dd >= 16 ? 128 : 256;
          const LAS float* rp = (const LAS float*)(lp + (dd >= 16 ? L_RED1 : L_RED)) + (((dd & 15) >> 2) * 16 + 4 * hh) * 4 + (dd & 3);
          float e[8];
#pragma unroll
          for (int j = 0; j < 8; j++) { const int tk = (8 * (j >> 2) + (j & 3)) * 4; e[j] = (rp[tk] + rp[tk + st]) + (rp[tk + 2 * st] + rp[tk + 3 * st]); }
          u32x4 v = {pk(e[0], e[1]), pk(e[2], e[3]), pk(e[4], e[5]), pk(e[6], e[7])};
          if (d == 20) v = (u32x4){0x3F803F80u, 0x3F803F80u, 0x3F803F80u, 0x3F803F80u};
          if (d == 21) v = (u32x4){0u, 0u, 0u, 0u};
          ST16(v, tb + 1280 + (s16 * 2 + hh) * VB + d * 16);
        }
      }
      if (i == 2) { if (w3 == 0) FLAG(WS_FKV + blockIdx.x * 8);
                    if (w3 >= 2) *(LAS u32x4*)(lp + L_ZERO + (tid - 384) * 16) = (u32x4){0u, 0u, 0u, 0u}; }
    }
    u64 fa, fb, fc, fd, fe;
    { gu64* f = (gu64*)(ws + WS_FKV) + lane;
      fa = __hip_atomic_load(f, RLX_AGENT); fb = __hip_atomic_load(f + 64, RLX_AGENT); fc = __hip_atomic_load(f + 128, RLX_AGENT); fd = __hip_atomic_load(f + 192, RLX_AGENT);
      fe = __hip_atomic_load((gu64*)(ws + WS_FQ) + (rt * 8 + hq) * 4 + p4, RLX_AGENT); }
    WAIT_BAR(14);
    asm volatile("" : "+v"(fa), "+v"(fb), "+v"(fc), "+v"(fd), "+v"(fe));
    const bool kvok = __all((fa == tag) & (fb == tag) & (fc == tag) & (fd == tag) & (fe == tag));
    asm volatile("" ::: "memory");
    BAR_L();
    if (__builtin_expect(!kvok, 0)) { wait_flags<256>(ws, WS_FKV, tag, lane); wait_flags<4>(ws, WS_FQ + (rt * 8 + hq) * 32, tag, lane); }
    KVL(0, 0);
    if (blockIdx.x == 0 && w3 == 0) FLAG(WS_EPOCH);
    STAMP(3);
    BAR_L();
    const int ht = tid - 256;
    const u32x4 a = ldsr16(lp + L_HO + ht * 16), b = ldsr16(lp + L_HO + 4096 + ht * 16); const u32x2 c = ldsr8(lp + L_HO + 8192 + ht * 8);
    o0[0] = a.x; o0[1] = a.y; o0[2] = a.z; o0[3] = a.w; o0[4] = b.x; o0[5] = b.y; o0[6] = b.z; o0[7] = b.w; o0[8] = c.x; o0[9] = c.y;
  }
  STAMP(4);
  u32x4 ph[5];
  { const LAS char* pb = ((p4 & 1) == h) ? lp + L_WPHI + (2 * w2 + h) * 800 + q4 * 40 + (2 * (g & 1) + (p4 >> 1)) * 8 : lp + L_ZERO;
    const u32x4 b0 = {o0[0], o0[1], o0[2], o0[3]}, b1 = {o0[4], o0[5], o0[6], o0[7]}, b2 = {o0[8], o0[9], 0u, 0u};
    const u32x2 c0 = trd(pb + 640), c1 = trd(pb + 672);
    f32x16 P0 = {}, P1 = {};
    P0 = mfma32(trd2(pb, pb + 160), b0, P0); P1 = mfma32(trd2(pb + 32, pb + 192), b0, P1);
    P0 = mfma32(trd2(pb + 320, pb + 480), b1, P0); P1 = mfma32(trd2(pb + 352, pb + 512), b1, P1);
    P0 = mfma32((u32x4){c0.x, c0.y, 0u, 0u}, b2, P0); P1 = mfma32((u32x4){c1.x, c1.y, 0u, 0u}, b2, P1);
    KX(1, 1); KW(1, 1, 0); SBAR();
    f32x2 p[10];
#pragma unroll
    for (int f = 0; f < 10; f++) p[f] = f < 8 ? (f32x2){P0[(2 * f) & 15], P0[(2 * f + 1) & 15]} : (f32x2){P1[(2 * f) & 3], P1[(2 * f + 1) & 3]};
    float mx = fmaxf(p[0][0], p[0][1]), mn = fminf(p[0][0], p[0][1]);
#pragma unroll
    for (int f = 1; f < 10; f++) { mx = fmaxf(fmaxf(mx, p[f][0]), p[f][1]); mn = fminf(fminf(mn, p[f][0]), p[f][1]); }
    const f32x2 ma = {-mx * LOG2E, -mx * LOG2E}, mb = {mn * LOG2E, mn * LOG2E}, ca_ = {LOG2E, LOG2E}, cb_ = {-LOG2E, -LOG2E};
    f32x2 ea[10], eb[10], sa = {0.f, 0.f}, sb = {0.f, 0.f};
#pragma unroll
    for (int f = 0; f < 10; f++) { if (f % 2 == 0 && f < 8) { SBAR(); if (f == 0) KW(1, 1, 1); if (f == 2) KW(1, 1, 2); if (f == 4) KW(1, 1, 3); if (f == 6) KW(1, 1, 4); SBAR(); }
      const f32x2 xa = __builtin_elementwise_fma(p[f], ca_, ma), xb = __builtin_elementwise_fma(p[f], cb_, mb);
      ea[f] = (f32x2){ex2(xa[0]), ex2(xa[1])}; eb[f] = (f32x2){ex2(xb[0]), ex2(xb[1])}; sa += ea[f]; sb += eb[f]; }
    const float ia = __builtin_amdgcn_rcpf(sa[0] + sa[1]), ib = __builtin_amdgcn_rcpf(sb[0] + sb[1]);
    unsigned o[20];
#pragma unroll
    for (int f = 0; f < 10; f++) { const f32x2 ya = ea[f] * ia, yb = eb[f] * ib; o[f] = pk(ya[0], ya[1]); o[10 + f] = pk(yb[0], yb[1]); }
#pragma unroll
    for (int c = 0; c < 5; c++) ph[c] = (u32x4){o[4 * c], o[4 * c + 1], o[4 * c + 2], o[4 * c + 3]};
    asm volatile("" : "+v"(ph[0]), "+v"(ph[1]), "+v"(ph[2]), "+v"(ph[3]), "+v"(ph[4]));
  }
  u32x4 q1A, q1B; u32x2 q2A, q2B;
  const int qo1 = (g & 1) * 5120 + (h * 32 + l15) * 16, qo2 = (g & 1) * 5120 + 1024 + l15 * 8;
  if (role) {
    WAIT_BAR(0);
    STAMP(5);
    const LAS char* qa = lp + L_QL + w3 * 1280;
    q1A = ldsr16(qa + qo1 + (w2 ^ 1) * 256); q1B = ldsr16(qa + qo1 + w2 * 256); q2A = ldsr8(qa + qo2 + (w2 ^ 1) * 128); q2B = ldsr8(qa + qo2 + w2 * 128);
    ARRIVE_NW(0);
  } else {
    const unsigned qb = WS_QX + (rt * 8 + hq) * QX_B + w3 * 1280;
    q1A = LD16S(qo1, qb + (w2 ^ 1) * 256); q1B = LD16S(qo1, qb + w2 * 256); q2A = LD8S(qo2, qb + (w2 ^ 1) * 128); q2B = LD8S(qo2, qb + w2 * 128);
    { LAS char* d = lp + L_PHK + ((2 * w2 + h) * 128 + w3 * 32 + r) * 80;
#pragma unroll
      for (int c = 0; c < 5; c++) *(LAS u32x4*)(d + c * 16) = ph[c]; }
    WAIT_BAR(4);
    STAMP(5);
#pragma unroll 1
    for (unsigned spin = 0; spin < SPIN_CAP; ++spin) {
      if (!(missing(q1A) | missing(q1B) | missing((u32x4){q2A.x, q2A.y, q2B.x, q2B.y}))) break;
      __builtin_amdgcn_s_sleep(1);
      asm volatile("" ::: "memory");
      q1A = LD16S(qo1, qb + (w2 ^ 1) * 256); q1B = LD16S(qo1, qb + w2 * 256); q2A = LD8S(qo2, qb + (w2 ^ 1) * 128); q2B = LD8S(qo2, qb + w2 * 128);
    }
  }
  STAMP(6);
  const int vr = r <= 21 ? r : 21;
  const int ok1 = lane * 16, ok2 = h ? (1280 + 20 * 16) : (1024 + r * 8), ov = 1280 + h * VB + vr * 16;
  const f32x16 Z = {};
  if (h) { q2A = (u32x2){0u, 0u}; q2B = (u32x2){0u, 0u}; }
  { const LAS char* t0 = lp;
    const u32x4 k1 = ldsr16(t0 + ok1); const u32x2 k2 = ldsr8(t0 + ok2);
    f32x16 sa = mfma32(k1, q1A, Z); sa = mfma32k8(k2, q2A, sa);
    f32x16 sb = mfma32(k1, q1B, Z); sb = mfma32k8(k2, q2B, sb);
    float ma = sa[0], mb = sb[0];
#pragma unroll
    for (int i = 1; i < 16; i++) { ma = fmaxf(ma, sa[i]); mb = fmaxf(mb, sb[i]); }
    ma = half_swap_max(ma); mb = half_swap_max(mb);
    const float mah = __uint_as_float(__float_as_uint(ma) & 0xFFFF0000u), mbh = __uint_as_float(__float_as_uint(mb) & 0xFFFF0000u);
    if (h) { q2A.x = pk(-mah, mah - ma); q2B.x = pk(-mbh, mbh - mb); } }
  f32x16 OA = {}, OB = {}, SA, SB;
  u32x4 k1, v1, v2, v2n, pA1, pA2, pB1, pB2; u32x2 k2;
  float e0, e1, e2, e3;
  const LAS char* c0p = lp + w2 * CH_B + ok1; const LAS char* c1p = lp + w2 * CH_B + ok2; const LAS char* c2p = lp + w2 * CH_B + ov;
  k1 = ldsr16(c0p); k2 = ldsr8(c1p);
  SA = mfma32(k1, q1A, Z); SA = mfma32k8(k2, q2A, SA);
  v1 = ldsr16(c2p); v2 = v1;
  e0 = e1 = e2 = e3 = 0.f; pB2 = (u32x4){0u, 0u, 0u, 0u};
#define G(MF, LD, H, M, C0_, C1_, X, S_, i) MF; LD; H; SBAR(); C0_; C1_; SBAR(); X##0 = ex2(S_[i]); X##1 = ex2(S_[i + 1]); SBAR(); M; SBAR(); X##2 = ex2(S_[i + 2]); X##3 = ex2(S_[i + 3]); \
    asm volatile("" : "+v"(X##0), "+v"(X##1), "+v"(X##2), "+v"(X##3)); SBAR();
#define TILEF(TN, V2, F) { float a0, a1, a2, a3; \
    G(SB = mfma32(k1, q1B, Z),    k1 = ldsr16(d0p + (TN)), F(0), F(8),  pB2[2] = pk(e0, e1), pB2[3] = pk(e2, e3), a, SA, 0) \
    G(OB = mfma32(v2, pB2, OB),   V2,                      F(1), F(9),  pA1[0] = pk(a0, a1), pA1[1] = pk(a2, a3), e, SA, 4) \
    G(SB = mfma32k8(k2, q2B, SB), k2 = ldsr8(d1p + (TN)),  F(2), F(10), pA1[2] = pk(e0, e1), pA1[3] = pk(e2, e3), a, SA, 8) \
    G(OA = mfma32(v1, pA1, OA),   (void)0,                 F(3), F(11), pA2[0] = pk(a0, a1), pA2[1] = pk(a2, a3), e, SA, 12) \
    G(SA = mfma32(k1, q1A, Z),    (void)0,                 F(4), F(12), pA2[2] = pk(e0, e1), pA2[3] = pk(e2, e3), a, SB, 0) \
    G(OA = mfma32(v2, pA2, OA),   (void)0,                 F(5), F(13), pB1[0] = pk(a0, a1), pB1[1] = pk(a2, a3), e, SB, 4) \
    G(SA = mfma32k8(k2, q2A, SA), (void)0,                 F(6), F(14), pB1[2] = pk(e0, e1), pB1[3] = pk(e2, e3), a, SB, 8) \
    G(OB = mfma32(v1, pB1, OB),   v1 = ldsr16(d2p + (TN)), F(7), F(15), pB2[0] = pk(a0, a1), pB2[1] = pk(a2, a3), e, SB, 12) }
#define TILE(TB, TN, V2, H0, H1, H2) { auto f_ = [&](int n) { if (n == 3) { H0; } if (n == 4) { H1; } if (n == 5) { H2; } }; TILEF(TN, V2, f_) }
#define TLF(t, F) TILEF(((t) + 1) * TILE_B, v2 = ldsr16(d2p + (t) * TILE_B + 2 * VB), F)
#define NOP (void)0
  const LAS char *d0p = c0p, *d1p = c1p, *d2p = c2p;
#define TL(sl, t, H0, H1, H2) TILE(0, ((t) + 1) * TILE_B, v2 = ldsr16(d2p + (t) * TILE_B + 2 * VB), H0, H1, H2)
#define TL6(sl, H0, H1) TL(sl, 6, H0, H1, v2n = ldsr16(d2p + 7 * TILE_B + 2 * VB))
#define TL7(sl, sn, H0, H1, H2) d0p += ((sn) - (sl)) * STEP_B; d1p += ((sn) - (sl)) * STEP_B; d2p += ((sn) - (sl)) * STEP_B; asm volatile("" : "+v"(d0p), "+v"(d1p), "+v"(d2p)); TILE(0, 0, v2 = v2n, H0, H1, H2)
#define WOG(wo, gr, j, dst) do { if ((j) == 4) WOP(wo, gr, 4, dst); else { if ((j) == 0) dmag_open(lds0 + (dst)); dmag<(j)>((wo) + C0 + (gr) * 32 * 640 - 2048, woff[j]); } } while (0)
#define WOP(wo, gr, j, dst) dma16s((wo) + C0 + (gr) * 32 * 640 - (4 - (j)) * 512, woff[j], lds0 + (dst) + (j) * 1024)
#define WA(gr, j) WOG(woA, gr, j, L_WO + (gr) * 5120)
  const int wx3 = w3 ? w3 : 1;
  const u16* wxs = (w2 ? woB : woA) + (16 + wx3) * 32 * 640;
  const int wxd = w2 ? 145408 + (wx3 - 1) * 5120 : L_WO + (16 + wx3) * 5120;
#define WX(j) WOG(wxs, 0, j, wxd)
#define PRIO(n) __builtin_amdgcn_s_setprio(n)
  u32x4 ones = ONES; if (role) asm volatile("" : "+v"(ones));
#define FILL(j) do { const int n_ = w * 3 + ((j) >> 1); ST16S(ones, ok1, (((j) & 1) ? WS_OB : WS_OA) + (rt * 8 + hq) * OT_B + (n_ < 20 ? n_ : 19) * 1024); } while (0)
#define CLR_VM() __builtin_amdgcn_s_waitcnt(0x0F70)
  const bool red = !role && !w2;
  f32x4 part[4]; u64 fp0, fp1;
  auto red_load = [&]() {
    asm volatile("" : "+v"(fp0), "+v"(fp1));
    if (__builtin_expect(!__all((fp0 == tag) & (fp1 == tag)), 0)) wait_flags<128>(ws, WS_FPART + hq * 1024, tag, lane);
    asm volatile("" ::: "memory");
#pragma unroll
    for (int j = 0; j < 4; j++) part[j] = __builtin_bit_cast(f32x4, LD16S(g * (4 * PART_B) + l15 * 16, WS_PART + (hq * 16 + j) * PART_B + rt * 960 + w3 * 240));
  };
  auto red_store = [&]() {
    f32x4 a = (part[0] + part[1]) + (part[2] + part[3]);
#pragma unroll
    for (int i = 0; i < 4; i++) { auto r16 = __builtin_amdgcn_permlane16_swap(__float_as_uint(a[i]), __float_as_uint(a[i]), false, false); const float t = __uint_as_float(r16[0]) + __uint_as_float(r16[1]);
      auto r32 = __builtin_amdgcn_permlane32_swap(__float_as_uint(t), __float_as_uint(t), false, false); a[i] = __uint_as_float(r32[0]) + __uint_as_float(r32[1]); }
    const int hd = rt >> 2, u = (rt & 3) * 60 + w3 * 15 + lane, hi = u >= 160, fq = hi ? (u - 160) >> 3 : u >> 4, di = hi ? u & 7 : u & 15;
    __builtin_amdgcn_raw_buffer_store_b64((u32x2){pk(a[0], a[1]), pk(a[2], a[3])}, wsr, lane < 15 ? (hi * 5 + (fq >> 1)) * 512 + di * 16 + (fq & 1) * 8 : 0x7FFF0000u, WS_KVR + hq * KVR_B + (hd >> 1) * 5120 + (hd & 1) * 256, 16);
  };
  u32x4 ka[10]; u64 fkr; float gta; unsigned kb; const unsigned kbs = WS_KVR + hq * KVR_B + w2 * 5120;
  auto oa_load = [&]() {
    asm volatile("" : "+v"(fkr));
    if (__builtin_expect(!__all(fkr == tag), 0)) wait_flags<64>(ws, WS_FKVR + hq * 512, tag, lane);
    asm volatile("" ::: "memory");
    kb = (((r >> 2) & 1) == h) ? (h * 16 + (r & 3) + 4 * (r >> 3)) * 16 : 0x7FFF0000u;
    ka[0] = LD16S(kb, kbs); ka[5] = LD16S(kb, kbs + 2560); ka[1] = LD16S(kb, kbs + 512); ka[6] = LD16S(kb, kbs + 3072);
    gta = __uint_as_float(__builtin_amdgcn_raw_buffer_load_b32(wsr, WS_G + (rt * 128 + w3 * 32 + r) * 4, 0, 16));
  };
  u32x4 jf[16]; f32x4 jc[3];
  const LAS char* jab = lp + L_PHK + ((w >> 1) * 128 + g * 8 + q4) * 80 + p4 * 8;
  const LAS char* jbb = lp + L_VIM + ((w >> 1) * 128 + g * 8 + q4) * 40 + (w & 1) * 32 + p4 * 8;
  auto kv_rd = [&](int n) { const int ks = n >> 2, j = n & 3;
    jf[n] = j ? trd2(jab + ks * 2560 + (j - 1) * 32, jab + ks * 2560 + 320 + (j - 1) * 32) : trd2(jbb + ks * 1280, jbb + ks * 1280 + 160); };
  auto kv_mm = [&](int m) { const int ks = m / 3, ft = m % 3;
    if (ft == 0 && (w & 1) && l15 == 4) jf[ks * 4] = (u32x4){0x3F803F80u, 0x3F803F80u, 0x3F803F80u, 0x3F803F80u};
    jc[ft] = mfma16(jf[ks * 4 + 1 + ft], jf[ks * 4], ks ? jc[ft] : (f32x4){0.f, 0.f, 0.f, 0.f}); };
  auto kv_st = [&](int ft) { const int dt = w & 1;
    const unsigned vo = ((!dt || l15 < 8) && (ft < 2 || g < 2)) ? (dt ? g * 128 : g * 256) + l15 * 16 : 0x7FFF0000u;
    ST16S(__builtin_bit_cast(u32x4, jc[ft]), vo, WS_PART + (hq * 16 + rt) * PART_B + (w >> 1) * 3840 + (dt ? 2560 + ft * 512 : ft * 1024)); };
  f32x16 oa0, oa1; u32x4 z0, z1, z2;
  LAS char* otb = lp + L_WO + (8 + w) * 5120;
  auto oa_mm = [&](int m) { const int s = m >> 1; if (m & 1) oa1 = mfma32(ka[5 + s], ph[s], s ? oa1 : Z); else oa0 = mfma32(ka[s], ph[s], s ? oa0 : Z); };
  auto oa_norm = [&]() { const float inv = __builtin_amdgcn_rcpf(oa1[4] + 1e-6f) * gta;
    LAS char* ar = otb + (r >> 4) * 1280 + l15 * 16; LAS char* ae = ar + h * 520; LAS char* ao = ar + 8 + h * 760;
#pragma unroll
    for (int t = 0; t < 5; t++) {
      const u32x2 v = t < 4 ? (u32x2){nz(pk(oa0[(4 * t) & 15] * inv, oa0[(4 * t + 1) & 15] * inv)), nz(pk(oa0[(4 * t + 2) & 15] * inv, oa0[(4 * t + 3) & 15] * inv))} : (u32x2){nz(pk(oa1[0] * inv, oa1[1] * inv)), nz(pk(oa1[2] * inv, oa1[3] * inv))};
      *(LAS u32x2*)(((t & 1) ? ao : ae) + (t >> 1) * 256) = v; }
    z0 = ldsr16(otb + ok1); z1 = ldsr16(otb + 1280 + ok1); z2 = ldsr16(ar + 1024); };
  auto oa_st = [&]() { const unsigned oa = WS_OA + (rt * 8 + w3 * 2) * OT_B + (hq * 2 + w2) * 1280;
    ST16S(z0, ok1, oa); ST16S(z1, ok1, oa + OT_B); ST16S(z2, h ? 0x7FFF0000u : (r >> 4) * OT_B + l15 * 16, oa + 1024); };
  unsigned cpk;
  PRIO(3);
  if (role) { TL(0, 0, FILL(0), NOP, NOP) TL(0, 1, FILL(1), NOP, NOP) TL(0, 2, FILL(2), NOP, NOP) }
  else {
    auto f0 = [&](int n) { if (n < 8) kv_rd(n); if (n >= 13) kv_mm(n - 13); };
    auto f1 = [&](int n) { if (n < 8) kv_rd(8 + n); if (n == 7) ARRIVE_NW(0); if (n >= 9 && n < 12) kv_mm(n - 6); if (n >= 13) kv_mm(n - 7); };
    auto f2 = [&](int n) { if (n >= 9 && n < 12) kv_mm(n); if (n >= 5 && n < 8) kv_st(n - 5); };
    TLF(0, f0) TLF(1, f1) TLF(2, f2)
  }
  TL(0, 3, dma4s((w2 ? woB : woA) + C0 + (((rt & 7) * 2 + role) * 40 + w3 * 10) * 640, (l15 < 10 ? l15 : 9) * 1280 + (g & 1) * 156, lds0 + 160768)        , NOP, NOP)
  TL(0, 4, NOP, NOP, if (role) FILL(3))
  PRIO(2);
  TL(0, 5, if (!role) { WAIT_VM(0); FLAG(WS_FPART + ((hq * 16 + rt) * 8 + w) * 8); }, NOP, cpk = PEEK(0))
  AWAIT(0, cpk);
  asm volatile("" ::: "memory");
  TL6(0, KX(2, 2), KW(2, 2, 0))
  TL7(0, 1, KW(2, 2, 1), KW(2, 2, 2), KW(2, 2, 3))
  TL(1, 0, KW(2, 2, 4), if (role) FILL(4), if (!role) { fp0 = POLL(WS_FPART + hq * 1024); fp1 = POLL(WS_FPART + hq * 1024 + 512); })
  TL(1, 1, if (role) { WX(0); WX(1); }, if (role) FILL(5), if (role) { WX(2); WX(3); })
  TL(1, 2, if (role) WX(4), NOP, if (red) red_load())
  PRIO(1);
  TL(1, 3, NOP, NOP, NOP)
  TL(1, 4, NOP, if (!role) CLR_VM(), if (red) red_store())
  TL(1, 5, NOP, NOP, NOP)
  PRIO(0);
  TL6(1, if (!role) { WAIT_VM(0); FLAG((w2 ? WS_SPARE : WS_FKVR) + ((hq * 16 + rt) * 4 + w3) * 8); }, NOP)
  if (!role) WAIT_BAR(1); else WAIT_BAR(0);
  STAMP(12);
  PRIO(3);
  if (role) FLAG(WS_FOA + ((rt * 8 + hq) * 8 + w) * 8);
  u64 foa;
  TL7(1, 2, KX(3, 0), KW(3, 0, 0), KW(3, 0, 1))
  TL(2, 0, KW(3, 0, 2), KW(3, 0, 3), KW(3, 0, 4)) TL(2, 1, WA(w, 0), WA(w, 1), WA(w, 2))
  u32x4 wbr[11];
  const char* wbs = (const char*)(woB + C0 + w * 32 * 640) - 4096;
#define WB1(n) wbr[n] = *(const GAS u32x4*)(wbs + ((n) / 5) * (8 * 32 * 1280) + woff[(n) % 5] + ((n) % 5) * 1024)
#define WB2(j) do { WB1(j); WB1(5 + (j)); } while (0)
#define WA16() dma16s(woA + C0 + 16 * 32 * 640 - 2048, wo16, lds0 + L_WO + 16 * 5120 + w5 * 1024)
#define WB16() wbr[10] = *(const GAS u32x4*)((const char*)(woB + C0 + 16 * 32 * 640) - 4096 + wo16)
  auto chk = [&]() { asm volatile("" : "+v"(foa));
    if (__builtin_expect(!__all(foa == tag), 0)) wait_flags<64>(ws, WS_FOA + rt * 512, tag, lane);
    asm volatile("" ::: "memory"); };
  if (role) {
    TL(2, 2, WA(w, 3), WA(w, 4), NOP)
    PRIO(2);
    TL(2, 3, (fkr = POLL(WS_FKVR + hq * 512), foa = POLL(WS_FOA + rt * 512)), NOP, NOP)
    TL(2, 4, NOP, { chk(); oa_load(); }, { ka[2] = LD16S(kb, kbs + 1024); ka[7] = LD16S(kb, kbs + 3584); ka[3] = LD16S(kb, kbs + 1536); })
    PRIO(1);
    TL(2, 5, { ka[8] = LD16S(kb, kbs + 4096); ka[4] = LD16S(kb, kbs + 2048); ka[9] = LD16S(kb, kbs + 4608); }, NOP, NOP)
    PRIO(0);
    { auto f = [&](int n) { if (n == 5) v2n = ldsr16(d2p + 7 * TILE_B + 2 * VB); if (n >= 8 && n != 10 && n != 14) oa_mm(n < 10 ? n - 8 : (n < 14 ? n - 9 : 5)); }; TLF(6, f) }
  } else {
    TL(2, 2, WA(w, 3), WA(w, 4), WX(0))
    PRIO(2);
    TL(2, 3, { WX(1); WX(2); }, { WX(3); WX(4); }, foa = POLL(WS_FOA + rt * 512))
    TL(2, 4, NOP, NOP, NOP)
    PRIO(1);
    TL(2, 5, chk(), WB1(0), WB1(5))
    PRIO(0);
    TL6(2, WB1(1), WB1(6))
  }
  BAR_L();
  STAMP(13);
  PRIO(3);
  if (role) {
    d0p -= 2 * STEP_B; d1p -= 2 * STEP_B; d2p -= 2 * STEP_B; asm volatile("" : "+v"(d0p), "+v"(d1p), "+v"(d2p));
    { auto f = [&](int n) { if (n == 8 || n == 9) oa_mm(n - 2); if (n == 11 || n == 12) oa_mm(n - 3); if (n == 5) CLR_VM(); if (n == 6) WA16(); }; TILEF(0, v2 = v2n, f) }
    TL(0, 0, oa_norm(), NOP, oa_st())
    TL(0, 1, WA(8 + w, 0), WA(8 + w, 1), WA(8 + w, 2))
    TL(0, 2, WA(8 + w, 3), WA(8 + w, 4), WB2(0))
    TL(0, 3, WB2(1), NOP, WB2(2))
    PRIO(2);
    TL(0, 4, WB2(3), NOP, WB2(4))
  } else {
    TL7(2, 0, WA16(), WA(8 + w, 0), WA(8 + w, 1))
    TL(0, 0, WA(8 + w, 2), WA(8 + w, 3), WA(8 + w, 4))
    TL(0, 1, WB1(2), NOP, WB1(7)) TL(0, 2, WB1(3), NOP, WB1(8)) TL(0, 3, WB1(4), NOP, WB1(9))
    PRIO(2);
    TL(0, 4, NOP, NOP, NOP)
  }
  ARRIVE_NW(1);
  const int tp = w & 1, kq = w >> 1;
  const unsigned ofr = (rt * 8 + role * 4 + tp * 2) * OT_B + kq * 5120;
  u32x4 of[2][5]; float gtb;
#define OFL(u) do { _Pragma("unroll") for (int i = 0; i < 5; i++) of[u][i] = LD16S(ok1, WS_OA + ofr + (u) * OT_B + i * 1024); } while (0)
#define OF2(i) do { of[0][i] = LD16S(ok1, WS_OA + ofr + (i) * 1024); of[1][i] = LD16S(ok1, WS_OA + ofr + OT_B + (i) * 1024); } while (0)
  TL(0, 5, WB16(), gtb = __uint_as_float(__builtin_amdgcn_raw_buffer_load_b32(wsr, WS_G + (rt * 128 + w3 * 32 + w2 * 16 + l15) * 4, 0, 16)), OF2(0))
  PRIO(1);
  TL6(0, OF2(1), OF2(2))
  PRIO(0);
  LAS f32x4* xb = (LAS f32x4*)(lp + (w3 >> 1) * CH_B) + (w3 & 1) * (64 * 6) + lane;
  { auto f = [&](int n) { if (n == 0) cpk = PEEK(1); if (n == 3) OF2(3); if (n == 4) OF2(4);
      if (n == 15) { AWAIT(1, cpk);
#pragma unroll
        for (int c = 0; c < 3; c++) xb[(w2 * 3 + c) * 64] = (f32x4){OA[4 * c], OA[4 * c + 1], OA[4 * c + 2], OA[4 * c + 3]}; } };
    TILEF(0, v2 = v2n, f) }
  pB2[2] = pk(e0, e1); pB2[3] = pk(e2, e3);
  OB = mfma32(v2, pB2, OB);
#undef TL
#undef TL6
#undef TL7
#undef TILE
#undef G
  STAMP(7);
  { asm volatile("s_waitcnt vmcnt(18) lgkmcnt(0)\n\ts_barrier" ::: "memory");
    f32x16 O = OB;
#pragma unroll
    for (int c = 0; c < 3; c++) { const f32x4 t = xb[((w2 ^ 1) * 3 + c) * 64]; O[4 * c] += t[0]; O[4 * c + 1] += t[1]; O[4 * c + 2] += t[2]; O[4 * c + 3] += t[3]; }
    float l = from_upper(O[8]);
    const float amax = (((fabsf(O[0]) + fabsf(O[1])) + (fabsf(O[2]) + fabsf(O[3]))) + ((fabsf(O[4]) + fabsf(O[5])) + (fabsf(O[6]) + fabsf(O[7])))) + ((fabsf(O[8]) + fabsf(O[9])) + (fabsf(O[10]) + fabsf(O[11])));
    const bool good = (l >= 0x1p-30f) && (l <= 0x1p100f) && (amax <= 0x1p126f);
    if (__builtin_expect(!__all(good), 0)) {
      const u32x4 q1 = q1B; const u32x2 q2 = h ? (u32x2){0u, 0u} : q2B;
      float m = -INFINITY;
#pragma unroll 1
      for (int t = 0; t < 64; t++) {
        f32x16 s = mfma32(LD16(WS_KV + t * TILE_B + ok1), q1, Z); s = mfma32k8(LD8(WS_KV + t * TILE_B + ok2), q2, s);
#pragma unroll
        for (int i = 0; i < 16; i++) m = fmaxf(m, s[i]);
      }
      m = half_swap_max(m);
      O = Z;
#pragma unroll 1
      for (int t = 0; t < 64; t++) {
        f32x16 s = mfma32(LD16(WS_KV + t * TILE_B + ok1), q1, Z); s = mfma32k8(LD8(WS_KV + t * TILE_B + ok2), q2, s);
#pragma unroll
        for (int i = 0; i < 16; i++) s[i] = ex2(s[i] - m);
        O = mfma32(LD16(WS_KV + t * TILE_B + ov), (u32x4){pk(s[0], s[1]), pk(s[2], s[3]), pk(s[4], s[5]), pk(s[6], s[7])}, O);
        O = mfma32(LD16(WS_KV + t * TILE_B + ov + 2 * VB), (u32x4){pk(s[8], s[9]), pk(s[10], s[11]), pk(s[12], s[13]), pk(s[14], s[15])}, O);
      }
      l = from_upper(O[8]);
    }
    const float inv = __builtin_amdgcn_rcpf(l) * (1.0f - gtb);
    LAS char* tb = lp + w2 * CH_B + 12288 + w3 * 1536;
    const int uq = (g & 1) * 5 + h;
    LAS char* t0 = tb + (uq >> 1) * 256 + (uq & 1) * 8 + l15 * 16; LAS char* t2 = h ? tb + 1280 + (g & 1) * 8 + l15 * 16 : t0 + 512;
#pragma unroll
    for (int c = 0; c < 3; c++) *(LAS u32x2*)(c < 2 ? t0 + c * 256 : t2) = (u32x2){nz(pk(O[4 * c] * inv, O[4 * c + 1] * inv)), nz(pk(O[4 * c + 2] * inv, O[4 * c + 3] * inv))};
    const unsigned ob = WS_OB + (rt * 8 + w3 * 2 + w2) * OT_B + hp * 1280;
    const u32x4 y0 = ldsr16(tb + ok1), y1 = ldsr16(tb + 1024 + l15 * 16);
    ST16S(y0, ok1, ob); ST16S(y1, lane < 16 ? ok1 : 0x7FFF0000u, ob + 1024);
    SBAR();
  }
  STAMP(8);
  auto of_missing = [&]() { u32x4 mm = of[0][0];
#pragma unroll
    for (int j = 1; j < 10; j++) mm = __builtin_elementwise_max(mm, of[j / 5][j % 5]);
    return missing(mm); };
  u32x4 wf[5][5];
#pragma unroll
  for (int i = 0; i < 5; i++) { const LAS char* wb = lp + L_WO + (kq * 5 + i) * 5120 + wlane;
#pragma unroll
    for (int t = 0; t < 5; t++) wf[i][t] = trd2(wb + t * 8, wb + 1280 + t * 8); }
  SBAR();
  if (__builtin_expect(of_missing(), 0)) {
#pragma unroll 1
    for (unsigned spin = 0; spin < SPIN_CAP; ++spin) {
      asm volatile("" ::: "memory");
      OFL(0); OFL(1);
      if (!of_missing()) break;
      __builtin_amdgcn_s_sleep(1);
    }
  }
  f32x4 c[2][5];
#pragma unroll
  for (int i = 0; i < 5; i++)
#pragma unroll
    for (int t = 0; t < 5; t++) c[0][t] = mfma16(wf[i][t], of[0][i], i ? c[0][t] : (f32x4){0.f, 0.f, 0.f, 0.f});
  asm volatile("" : "+v"(c[0][0]), "+v"(c[0][1]), "+v"(c[0][2]), "+v"(c[0][3]));
  SBAR();
  BAR_L();
  SBAR();
  { LAS char* d = lp + L_WO + w * 5120 + lane * 16;
    *(LAS u32x4*)(lp + L_WO + 16 * 5120 + w5 * 1024 + lane * 16) = wbr[10];
#pragma unroll
    for (int i = 0; i < 5; i++) {
      *(LAS u32x4*)(d + i * 1024) = wbr[i]; *(LAS u32x4*)(d + 40960 + i * 1024) = wbr[5 + i]; of[0][i] = LD16S(ok1, WS_OB + ofr + i * 1024); SBAR();
#pragma unroll
      for (int t = 0; t < 5; t++) c[1][t] = mfma16(wf[i][t], of[1][i], i ? c[1][t] : (f32x4){0.f, 0.f, 0.f, 0.f});
      SBAR();
    } }
  STAMP(9);
  BAR_L();
#pragma unroll
  for (int i = 0; i < 5; i++) { const int gr = kq * 5 + i; const LAS char* wb = lp + (gr < 17 ? L_WO + gr * 5120 : 145408 + (gr - 17) * 5120) + wlane;
#pragma unroll
    for (int t = 0; t < 5; t++) wf[i][t] = trd2(wb + t * 8, wb + 1280 + t * 8);
    SBAR(); of[1][i] = LD16S(ok1, WS_OB + ofr + OT_B + i * 1024); SBAR(); }
  ARRIVE_NW(2);
  STAMP(10);
  LAS char* xw = lp + kq * 21504 + (tp * 32 + l15) * 336 + g * 80;
#pragma unroll
  for (int u = 0; u < 2; u++) {
#pragma unroll
    for (int i = 0; i < 5; i++) {
      if (__builtin_expect(missing(of[u][i]), 0)) {
#pragma unroll 1
        for (unsigned spin = 0; spin < SPIN_CAP; ++spin) {
          asm volatile("" ::: "memory");
#pragma unroll
          for (int j = u * 5 + i; j < 10; j++) of[j / 5][j % 5] = LD16S(ok1, WS_OB + ofr + (j / 5) * OT_B + (j % 5) * 1024);
          if (!missing(of[u][i])) break;
          __builtin_amdgcn_s_sleep(1);
        }
      }
#pragma unroll
      for (int t = 0; t < 5; t++) c[u][t] = mfma16(wf[i][t], of[u][i], c[u][t]);
      if (u == 0 && i == 3) cpk = PEEK(2);
      if (u == 1) { if (i == 0) { AWAIT(2, cpk); asm volatile("" ::: "memory"); }
        *(LAS f32x4*)(xw + i * 16) = c[0][i]; }
      SBAR();
    }
  }
#pragma unroll
  for (int t = 0; t < 5; t++) *(LAS f32x4*)(xw + 16 * 336 + t * 16) = c[1][t];
  BAR_L();
  { const __amdgpu_buffer_rsrc_t orr = __builtin_amdgcn_make_buffer_rsrc(out, 0, 2048 * DM * 2, 0x00020000);
#pragma unroll
    for (int j = 0; j < 3; j++) { const int n = tid + 512 * j, nn = n < 1280 ? n : 0, row = (nn * 3277) >> 16, cq = nn - row * 20;
      const LAS f32x4* p = (const LAS f32x4*)(lp + row * 336 + cq * 16);
      const f32x4 z = (p[0] + p[1344]) + (p[2688] + p[4032]);
      __builtin_amdgcn_raw_buffer_store_b64((u32x2){pk(z[0], z[1]), pk(z[2], z[3])}, orr, n < 1280 ? ((rt * 128 + role * 64 + row) * DM + C0 + cq * 4) * 2 : 0x7FFF0000u, 0, 0); }
  }
  STAMP(11);
}

__global__ __launch_bounds__(512, 2) void fused(const u16* __restrict__ x, const u16* __restrict__ wqkv, const u16* __restrict__ wphi, const u16* __restrict__ woA,
                                                const u16* __restrict__ wq, const u16* __restrict__ wk, const u16* __restrict__ wv, const u16* __restrict__ woB,
                                                const u16* __restrict__ gatew, u16* __restrict__ out, char* ws) {
  __shared__ __attribute__((aligned(16))) char lds[L_TOTAL];
  if ((blockIdx.x >> 6) & 1) body<1>(x, wqkv, wphi, woA, wq, wv, woB, gatew, out, ws, (LAS char*)lds);
  else body<0>(x, wqkv, wphi, woA, wq, wk, woB, gatew, out, ws, (LAS char*)lds);
}
}

extern "C" void kernel_launch(void* const* d_in, const int* in_sizes, int n_in, void* d_out, int out_size, void* d_ws, size_t ws_size, hipStream_t stream) {
  using namespace gm;
  fused<<<256, 512, 0, stream>>>((const u16*)d_in[0], (const u16*)d_in[1], (const u16*)d_in[2], (const u16*)d_in[3], (const u16*)d_in[5], (const u16*)d_in[6],
                                 (const u16*)d_in[7], (const u16*)d_in[8], (const u16*)d_in[9], (u16*)d_out, (char*)d_ws);
}
```

```cpp
#include <hip/hip_runtime.h>
#include <hip/hip_bf16.h>
#include <stdint.h>

namespace gm {
typedef float f32x16 __attribute__((ext_vector_type(16)));
typedef float f32x4 __attribute__((ext_vector_type(4)));
typedef float f32x2 __attribute__((ext_vector_type(2)));
typedef short s16x4 __attribute__((ext_vector_type(4)));
typedef __bf16 b16x2 __attribute__((ext_vector_type(2)));
typedef __bf16 b16x8 __attribute__((ext_vector_type(8)));
typedef unsigned u32x4 __attribute__((ext_vector_type(4)));
typedef unsigned u32x2 __attribute__((ext_vector_type(2)));
typedef unsigned long long u64;
typedef unsigned short u16;
#define DI __device__ __forceinline__
#define LAS __attribute__((address_space(3)))
#define GAS __attribute__((address_space(1)))
typedef GAS u64 gu64;

constexpr int DM = 640;
constexpr unsigned WS_EPOCH = 0, WS_FKV = 2048, WS_FKVR = 4096, WS_FQ = 8192, WS_FPART = 16384, WS_FOA = 24576, WS_SPARE = 40960  , WS_G = 57344;
constexpr unsigned WS_KVR = 8912896, KVR_B = 10240, WS_KV = 131072, WS_QX = 327680, QX_B = 10240, WS_PART = 1703936, PART_B = 15360;
constexpr unsigned WS_OA = 3670016, WS_OB = 6291456, WS_END = 8994816, OT_B = 20480;
constexpr int VB = 352, TILE_B = 1280 + 4 * VB, CH_B = TILE_B * 8, STEP_B = 2 * CH_B;
constexpr int STAGE_B = 36864, ST_W1 = 10240, ST_X = 20480;
constexpr int L_BUF = 43008, L_P0W = 0, L_P0X0 = 25600, L_P0X1 = 153600, L_RED = 158720, L_RED1 = 40960;
constexpr int L_HO = 86016, L_QL = 96256, L_VIM = 96256, L_PHK = 116736, L_WPHI = 158592, L_ZERO = 161792, L_CNT = 163712, L_TOTAL = 163840;
constexpr int L_WO = 43008;
constexpr float LOG2E = 1.4426950408889634f;
constexpr float QSCALE = 0.22360679774997896f * LOG2E;
constexpr unsigned SPIN_CAP = 20000, LSPIN_CAP = 400000;

DI unsigned pk(float a, float b) { f32x2 v = {a, b}; b16x2 r = __builtin_convertvector(v, b16x2); return __builtin_bit_cast(unsigned, r); }
DI f32x16 mfma32(u32x4 a, u32x4 b, f32x16 c) { return __builtin_amdgcn_mfma_f32_32x32x16_bf16(__builtin_bit_cast(b16x8, a), __builtin_bit_cast(b16x8, b), c, 0, 0, 0); }
DI f32x16 mfma32k8(u32x2 a, u32x2 b, f32x16 c) { return __builtin_amdgcn_mfma_f32_32x32x8bf16_1k(__builtin_bit_cast(s16x4, a), __builtin_bit_cast(s16x4, b), c, 0, 0, 0); }
DI f32x4 mfma16(u32x4 a, u32x4 b, f32x4 c) { return __builtin_amdgcn_mfma_f32_16x16x32_bf16(__builtin_bit_cast(b16x8, a), __builtin_bit_cast(b16x8, b), c, 0, 0, 0); }
DI u32x2 trd(const LAS char* p) { return __builtin_bit_cast(u32x2, __builtin_amdgcn_ds_read_tr16_b64_v4i16((LAS s16x4*)p)); }
DI u32x4 trd2(const LAS char* p0, const LAS char* p1) { u32x2 a = trd(p0), b = trd(p1); return (u32x4){a.x, a.y, b.x, b.y}; }
DI u32x4 ldsr16(const LAS char* p) { return *(const LAS u32x4*)p; }
DI u32x2 ldsr8(const LAS char* p) { return *(const LAS u32x2*)p; }
DI float ex2(float x) { return __builtin_amdgcn_exp2f(x); }
DI void dma16s(const void* base, unsigned voff, unsigned lds_base) {
  asm volatile("s_mov_b32 m0, %2\n\ts_nop 0\n\tglobal_load_lds_dwordx4 %0, %1" :: "v"(voff), "s"(base), "s"(lds_base) : "memory"); }
DI void dma16s_sc1(const void* base, unsigned voff, unsigned lds_base) {
  asm volatile("s_mov_b32 m0, %2\n\ts_nop 0\n\tglobal_load_lds_dwordx4 %0, %1 sc1" :: "v"(voff), "s"(base), "s"(lds_base) : "memory"); }
DI void dmag_open(unsigned lds_base) { asm volatile("s_mov_b32 m0, %0\n\ts_nop 0" :: "s"(lds_base) : "memory"); }
template <int J> DI void dmag(const void* base, unsigned voff) { asm volatile("global_load_lds_dwordx4 %0, %1 offset:%2" :: "v"(voff), "s"(base), "i"((J & 3) * 1024) : "memory"); }
template <int J> DI void dmag_sc1(const void* base, unsigned voff) { asm volatile("global_load_lds_dwordx4 %0, %1 offset:%2 sc1" :: "v"(voff), "s"(base), "i"((J & 3) * 1024) : "memory"); }
DI void dma4s(const void* base, unsigned voff, unsigned lds_base) {
  asm volatile("s_mov_b32 m0, %2\n\ts_nop 0\n\tglobal_load_lds_dword %0, %1" :: "v"(voff), "s"(base), "s"(lds_base) : "memory"); }
DI u32x4 ld16_async(const void* p) { u32x4 r; asm volatile("global_load_dwordx4 %0, %1, off" : "=v"(r) : "v"(p) : "memory"); return r; }
DI u32x2 ld8_sc1_async(const void* p) { u32x2 r; asm volatile("global_load_dwordx2 %0, %1, off sc1" : "=v"(r) : "v"(p) : "memory"); return r; }
#define BAR_L() asm volatile("s_waitcnt lgkmcnt(0)\n\ts_barrier" ::: "memory")
#define WAIT_BAR(N) asm volatile("s_waitcnt vmcnt(" #N ") lgkmcnt(0)\n\ts_barrier" ::: "memory")
#define WAIT_VM(N) asm volatile("s_waitcnt vmcnt(" #N ")" ::: "memory")
#define SBAR() __builtin_amdgcn_sched_barrier(0)
#define RLX_AGENT __ATOMIC_RELAXED, __HIP_MEMORY_SCOPE_AGENT
DI float half_swap_max(float m) { auto rr = __builtin_amdgcn_permlane32_swap(__float_as_uint(m), __float_as_uint(m), false, false); return fmaxf(__uint_as_float(rr[0]), __uint_as_float(rr[1])); }
DI float from_upper(float v) { auto rr = __builtin_amdgcn_permlane32_swap(__float_as_uint(v), __float_as_uint(v), false, false); return __uint_as_float(rr[1]); }

template <int NF>
DI bool flags_set(const char* ws, unsigned off, u64 tag, int lane) {
  gu64* f = (gu64*)(ws + off);
  bool ok = __hip_atomic_load(f + (NF < 64 ? (lane & (NF - 1)) : lane), RLX_AGENT) == tag;
  if (NF >= 128) ok &= __hip_atomic_load(f + 64 + lane, RLX_AGENT) == tag;
  if (NF >= 256) { ok &= __hip_atomic_load(f + 128 + lane, RLX_AGENT) == tag; ok &= __hip_atomic_load(f + 192 + lane, RLX_AGENT) == tag; }
  return ok;
}
template <int NF>
DI void wait_flags(const char* ws, unsigned off, u64 tag, int lane) {
#pragma unroll 1
  for (unsigned spin = 0; spin < SPIN_CAP; ++spin) {
    if (__all(flags_set<NF>(ws, off, tag, lane))) break;
    __builtin_amdgcn_s_sleep(2);
  }
  asm volatile("" ::: "memory");
}
DI unsigned nz(unsigned v) { return v < 0xFFFFFFFEu ? v : 0xFFFFFFFEu; }
DI unsigned umax3(unsigned a, unsigned b, unsigned c) { unsigned m = a > b ? a : b; return m > c ? m : c; }
DI bool missing(u32x4 v) { return __any(umax3(umax3(v.x, v.y, v.z), v.w, 0u) == 0xFFFFFFFFu); }
#define ONES ((u32x4){0xFFFFFFFFu, 0xFFFFFFFFu, 0xFFFFFFFFu, 0xFFFFFFFFu})
#define ST16(v, off) __builtin_amdgcn_raw_buffer_store_b128((v), wsr, (off), 0, 16)
#define ST8(v, off) __builtin_amdgcn_raw_buffer_store_b64((v), wsr, (off), 0, 16)
#define LD16(off) __builtin_amdgcn_raw_buffer_load_b128(wsr, (off), 0, 16)
#define ST16S(v, voff, soff) ST16(v, (voff) + (soff))
#define LD16S(voff, soff) __builtin_amdgcn_raw_buffer_load_b128(wsr, (voff), (soff), 16)
#define LD8S(voff, soff) __builtin_amdgcn_raw_buffer_load_b64(wsr, (voff), (soff), 16)
#define LD8(off) __builtin_amdgcn_raw_buffer_load_b64(wsr, (off), 0, 16)

#define STAMP(i) do {} while (0)

template <int role>
DI void body(const u16* __restrict__ x, const u16* __restrict__ wqkv, const u16* __restrict__ wphi, const u16* __restrict__ woA,
             const u16* __restrict__ wq, const u16* __restrict__ wkv, const u16* __restrict__ woB,
             const u16* __restrict__ gatew, u16* __restrict__ out, char* ws, LAS char* lp) {
  const int tid = threadIdx.x, lane = tid & 63;
  const int w = __builtin_amdgcn_readfirstlane(tid >> 6) & 7;
  const int l15 = lane & 15, g = lane >> 4, q4 = l15 >> 2, p4 = lane & 3, r = lane & 31, h = lane >> 5;
  const int bx = blockIdx.x & 7, bi = blockIdx.x >> 3;
  const int rt = (bx & 1) * 8 + (bi & 7), hp = (bx >> 1) * 4 + ((bi >> 3) & 3), hq = hp >> 1;
  const int w3 = w & 3, w2 = w >> 2;
  const unsigned lds0 = (unsigned)(uintptr_t)lp;
  const __amdgpu_buffer_rsrc_t wsr = __builtin_amdgcn_make_buffer_rsrc(ws, 0, WS_END, 0x00020000);
  LAS unsigned* cnt = (LAS unsigned*)(lp + L_CNT);
#define ARRIVE_NW(c) do { asm volatile("" ::: "memory"); __hip_atomic_store(cnt + (c) * 8 + w, 1u, __ATOMIC_RELAXED, __HIP_MEMORY_SCOPE_WORKGROUP); asm volatile("" ::: "memory"); } while (0)
#define PEEK(c) __hip_atomic_load(cnt + (c) * 8 + (lane & 7), __ATOMIC_RELAXED, __HIP_MEMORY_SCOPE_WORKGROUP)
#define AWAIT(c, v) do { if (__builtin_expect(!__all((v) != 0u), 0)) { _Pragma("unroll 1") for (unsigned sp_ = 0; sp_ < LSPIN_CAP; ++sp_) { if (__all(PEEK(c) != 0u)) break; __builtin_amdgcn_s_sleep(1); } } asm volatile("" ::: "memory"); } while (0)
#define POLL(off) __builtin_bit_cast(u64, __builtin_amdgcn_raw_buffer_load_b64(wsr, lane * 8, (off), 16))
#define FLAG(off) __builtin_amdgcn_raw_buffer_store_b64(__builtin_bit_cast(u32x2, tag), wsr, lane ? 0x7FFF0000u : 0u, (off), 16)
  STAMP(0);

  const int T0 = rt * 128 + hq * 16;
  const int C0 = hq * 80;
  const int wlane = ((g >> 1) * 16 + (g & 1) * 4 + q4) * 160 + p4 * 40;
  u64 tag;
  unsigned o0[10];
#define KVP(s, slot, p) dma16s_sc1(ws + WS_KV + (s) * STEP_B + (p) * 1024, lane * 16, lds0 + (slot) * STEP_B + (p) * 1024)
#define KVL(s, slot) do { _Pragma("unroll") for (int j = 0; j < 11; j++) KVP(s, slot, j < 10 ? w3 + 4 * j : 40 + (w3 & 1)); } while (0)
#define KW(s, slot, j) do { if ((j) == 4) KVP(s, slot, w * 5 + 4); else { if ((j) == 0) dmag_open(lds0 + (slot) * STEP_B + w * 5120); dmag_sc1<(j)>(ws + WS_KV + (s) * STEP_B + w * 5120, lane * 16); } } while (0)
#define KX(s, slot) KVP(s, slot, 40 + (w & 1))
  const int w5 = w < 5 ? w : 4;
  unsigned wo16;
  unsigned woff[5];
  auto woff_init = [&]() { int ln = lane; asm volatile("" : "+v"(ln));
#pragma unroll
    for (int j = 0; j < 5; j++) { const int c = j * 64 + ln, rho = (c * 6554) >> 16, cc = c - rho * 10; const int k = (rho & ~12) | ((rho & 4) << 1) | ((rho & 8) >> 1); woff[j] = k * 1280 + cc * 16 + (4 - j) * 1024; }
    { const int c = w5 * 64 + ln, rho = (c * 6554) >> 16, cc = c - rho * 10; const int k = (rho & ~12) | ((rho & 4) << 1) | ((rho & 8) >> 1); wo16 = k * 1280 + cc * 16 + 4096; }
    asm volatile("" : "+v"(woff[0]), "+v"(woff[1]), "+v"(woff[2]), "+v"(woff[3]), "+v"(woff[4]), "+v"(wo16)); };
  if (w2 == 0) {
    u32x4 gf0, gf1, gf2, gf3, gf4, wpc;
    u32x2 ep = ld8_sc1_async(ws + WS_EPOCH);
    const int lx = w3 < 3 ? L_P0X0 + w3 * 5120 : L_P0X1;
    { const u16* wsrc = wkv + w3 * 3200;
#pragma unroll
      for (int j = 0; j < 5; j++) { const int n = j * 64 + lane, row = (n * 3277) >> 16, cc = n - row * 20; dma16s(x + T0 * DM + w3 * 160, row * 1280 + cc * 16, lds0 + lx + j * 1024); }
#pragma unroll
      for (int j = 0; j < 6; j++) dma16s(wsrc + j * 512, lane * 16, lds0 + L_P0W + w3 * 6400 + j * 1024);
      if (lane < 16) dma16s(wsrc + 6 * 512, lane * 16, lds0 + L_P0W + w3 * 6400 + 6 * 1024);
      const u16* gp = gatew + w3 * 160 + g * 8;
      gf0 = ld16_async(gp); gf1 = ld16_async(gp + 32); gf2 = ld16_async(gp + 64); gf3 = ld16_async(gp + 96); gf4 = ld16_async(gp + 128);
      wpc = ld16_async(wphi + hq * 1600 + (tid < 200 ? tid : 199) * 8);
    }
    woff_init();
    asm volatile("s_waitcnt vmcnt(0)" : "+v"(gf0), "+v"(gf1), "+v"(gf2), "+v"(gf3), "+v"(gf4), "+v"(wpc), "+v"(ep) :: "memory");
    tag = (((u64)(unsigned)__builtin_amdgcn_readfirstlane(ep.y) << 32) | (unsigned)__builtin_amdgcn_readfirstlane(ep.x)) + 1ull;
    STAMP(1);
    { const LAS char* wb = lp + L_P0W + (w3 * 160 + g * 8 + q4) * 40;
      const LAS char* xb = lp + lx + l15 * 320 + g * 16;
      const bool gl = l15 == 4;
      f32x4 c0 = {}, c1 = {};
#define P0STEP(s, GF) { const u32x4 b = ldsr16(xb + (s) * 64); c0 = mfma16(trd2(wb + p4 * 8 + (s) * 1280, wb + p4 * 8 + (s) * 1280 + 160), b, c0); \
        u32x4 a = trd2(wb + 32 + (s) * 1280, wb + 32 + (s) * 1280 + 160); if (gl) a = GF; c1 = mfma16(a, b, c1); }
      P0STEP(0, gf0) P0STEP(1, gf1) P0STEP(2, gf2) P0STEP(3, gf3) P0STEP(4, gf4)
#undef P0STEP
      *(LAS f32x4*)(lp + L_RED + tid * 16) = c0;
      if (lane < 32) *(LAS f32x4*)(lp + L_RED1 + (w3 * 32 + lane) * 16) = c1; }
    STAMP(2);
    f32x16 acc[5];
#pragma unroll
    for (int b = 0; b < 5; b++) acc[b] = (f32x16){};
    const int wm = g & 1, whd = p4;
    const int wo0 = wm * ST_W1 + (8 * h + 4 * wm + q4) * 160 + whd * 40, wo1 = wm * ST_W1 + (8 * h + 4 * (wm ^ 1) + q4) * 160 + whd * 40;
    const int xo = ST_X + (w3 * 32 + r) * 128 + ((h ^ ((r >> 1) & 7)) * 16);
    u32x4 fa[5], fb[5], xa, xb;
#define KSTEP(FN, XN, s, FC, XC) { XN = ldsr16(sb + (xo ^ ((s) * 32))); \
      _Pragma("unroll") for (int j = 0; j < 5; j++) { if (i > 0 || (s) > 0) acc[j] = mfma32(FC[j], XC, acc[j]); FN[j] = trd2(sb + wo0 + (s) * 2560 + j * 8, sb + wo1 + (s) * 2560 + j * 8); SBAR(); } }
#pragma unroll
    for (int i = 0; i < 10; i++) {
      BAR_L();
      if (i == 1 && role) {
        const unsigned qb = WS_QX + (rt * 8 + hq) * QX_B + w3 * 1280 + lane * 16;
        ST16(ONES, qb); ST16(ONES, qb + 5120); ST16(ONES, lane < 32 ? qb + 1024 + (lane >> 4) * (5120 - 256) : 0x7FFF0000u); }
      if (i == 4) { if (role) { WAIT_VM(0); FLAG(WS_FQ + ((rt * 8 + hq) * 4 + w3) * 8); }
                    if (tid < 200) *(LAS u32x4*)(lp + L_WPHI + tid * 16) = wpc; }
      const LAS char* sb = lp + L_BUF + ((i + 2) % 3) * STAGE_B;
      KSTEP(fa, xa, 0, fb, xb) KSTEP(fb, xb, 1, fa, xa) KSTEP(fa, xa, 2, fb, xb)
      { xb = ldsr16(sb + (xo ^ 96));
        acc[0] = mfma32(fa[0], xa, acc[0]); fb[0] = trd2(sb + wo0 + 7680, sb + wo1 + 7680); fb[1] = trd2(sb + wo0 + 7688, sb + wo1 + 7688); SBAR();
        acc[1] = mfma32(fa[1], xa, acc[1]); fb[2] = trd2(sb + wo0 + 7696, sb + wo1 + 7696); fb[3] = trd2(sb + wo0 + 7704, sb + wo1 + 7704); SBAR();
        acc[2] = mfma32(fa[2], xa, acc[2]); fb[4] = trd2(sb + wo0 + 7712, sb + wo1 + 7712); SBAR();
        acc[3] = mfma32(fa[3], xa, acc[3]); acc[4] = mfma32(fa[4], xa, acc[4]); SBAR(); }
    }
#pragma unroll
    for (int j = 0; j < 5; j++) acc[j] = mfma32(fb[j], xb, acc[j]);
#undef KSTEP
    STAMP(3);
#define PK2(t, i, sc) pk(acc[t][i] * (sc), acc[t][(i) + 1] * (sc))
#define ROW(i, sc) const u32x4 ra = {PK2(0, i, sc), PK2(0, (i) + 2, sc), PK2(1, i, sc), PK2(1, (i) + 2, sc)}, rb = {PK2(2, i, sc), PK2(2, (i) + 2, sc), PK2(3, i, sc), PK2(3, (i) + 2, sc)}; const u32x2 rc = {PK2(4, i, sc), PK2(4, (i) + 2, sc)}
    { ROW(4, 1.0f);
      *(LAS u32x4*)(lp + L_HO + tid * 16) = ra; *(LAS u32x4*)(lp + L_HO + 4096 + tid * 16) = rb; *(LAS u32x2*)(lp + L_HO + 8192 + tid * 8) = rc; }
    BAR_L();
    const int qfo = (h * 4 + w3) * 1280;
    if (role) {
      { ROW(8, QSCALE); const unsigned qb = WS_QX + (rt * 8 + hq) * QX_B + qfo;
        ST16(((u32x4){nz(ra.x), nz(ra.y), nz(ra.z), nz(ra.w)}), qb + r * 16); ST16(((u32x4){nz(rb.x), nz(rb.y), nz(rb.z), nz(rb.w)}), qb + 512 + r * 16); ST8(((u32x2){nz(rc.x), nz(rc.y)}), qb + 1024 + r * 8); }
      { ROW(12, QSCALE); LAS char* d = lp + L_QL + qfo;
        *(LAS u32x4*)(d + r * 16) = ra; *(LAS u32x4*)(d + 512 + r * 16) = rb; *(LAS u32x2*)(d + 1024 + r * 8) = rc; }
    } else { LAS char* d = lp + L_VIM + (h * 128 + w3 * 32 + r) * 40;
#pragma unroll
      for (int t = 0; t < 5; t++) { *(LAS u32x2*)(d + t * 8) = (u32x2){PK2(t, 8, 1.0f), PK2(t, 10, 1.0f)}; *(LAS u32x2*)(d + 10240 + t * 8) = (u32x2){PK2(t, 12, 1.0f), PK2(t, 14, 1.0f)}; } }
#pragma unroll
    for (int t = 0; t < 5; t++) { o0[2 * t] = PK2(t, 0, 1.0f); o0[2 * t + 1] = PK2(t, 2, 1.0f); }
#undef ROW
#undef PK2
  } else {
    u32x2 ep = ld8_sc1_async(ws + WS_EPOCH);
    const bool second = w3 >> 1;
    const unsigned rs = (second && role) ? 1280u : 3840u;
    const char* wsrc = (second ? (role ? (const char*)(wq + C0) : (const char*)(wqkv + 1280 + C0)) : (const char*)(wqkv + (role ? 0 : 640) + C0)) + (w3 & 1) * 32 * rs;
    const char* xsrc = (const char*)(x + (rt * 128 + w3 * 32) * DM);
    int rot = (bi & 7) + 3 * ((bi >> 3) & 3); rot = rot >= 10 ? rot - 10 : rot;
    unsigned so[5], xoff[2];
#pragma unroll
    for (int j = 0; j < 5; j++) { const int c = j * 64 + lane, rho = (c * 6554) >> 16, cc = c - rho * 10; so[j] = (rho ^ (second ? 4 : 0)) * rs + cc * 16; }
#pragma unroll
    for (int j = 0; j < 2; j++) { const int row = j * 8 + (lane >> 3); xoff[j] = row * 1280 + (((lane & 7) ^ ((row >> 1) & 7)) * 16); }
    int bsel = 2, kc = rot;
    auto stage = [&]() {
      const unsigned lb = lds0 + L_BUF + bsel * STAGE_B;
      const char* bw = wsrc + kc * 64 * rs; const char* bxp = xsrc + kc * 128;
#pragma unroll
      for (int j = 0; j < 5; j++) { dma16s(bw, so[j], lb + (w3 * 5 + j) * 1024); if (j < 4) dma16s(bxp + (j >> 1) * 16 * 1280, xoff[j & 1], lb + ST_X + (w3 * 4 + j) * 1024); }
      bsel = bsel == 2 ? 0 : bsel + 1; kc = kc == 9 ? 0 : kc + 1;
    };
    stage(); stage();
    woff_init();
    asm volatile("s_waitcnt vmcnt(9)" : "+v"(ep) :: "memory");
    tag = (((u64)(unsigned)__builtin_amdgcn_readfirstlane(ep.y) << 32) | (unsigned)__builtin_amdgcn_readfirstlane(ep.x)) + 1ull;
    STAMP(1); STAMP(2);
#pragma unroll 1
    for (int i = 0; i < 8; i++) {
      WAIT_BAR(9);
      stage();
      if (i == 0 && w3 == 0) {
        const unsigned tb = WS_KV + (T0 >> 5) * TILE_B; const int s16 = hq & 1;
        if (!role) {
          const LAS f32x4* rp = (const LAS f32x4*)(lp + (h ? L_RED1 : L_RED)) + (h ? (g & 1) * 16 : g * 32) + l15;
          const int st = h ? 32 : 64, o2 = h ? 0 : 16;
          const f32x4 a = (rp[0] + rp[st]) + (rp[2 * st] + rp[3 * st]), b = (rp[o2] + rp[st + o2]) + (rp[2 * st + o2] + rp[3 * st + o2]);
          const u32x4 v = {pk(a[0], a[1]), pk(a[2], a[3]), pk(b[0], b[1]), pk(b[2], b[3])};
          if (lane < 32) ST16(v, tb + (g * 32 + s16 * 16 + l15) * 16);
          else if (lane < 48) ST8(((u32x2){v.x, v.y}), tb + 1024 + (s16 * 16 + l15) * 8);
          else { const float gv = 1.0f / (1.0f + __expf(-a[0])); __builtin_amdgcn_raw_buffer_store_b32(__float_as_uint(gv), wsr, WS_G + (T0 + l15) * 4, 0, 16); }
        } else if (lane < 44) {
          const int hh = lane >= 22, d = lane - hh * 22, dd = d < 20 ? d : 0, st = dd >= 16 ? 128 : 256;
          const LAS float* rp = (const LAS float*)(lp + (dd >= 16 ? L_RED1 : L_RED)) + (((dd & 15) >> 2) * 16 + 4 * hh) * 4 + (dd & 3);
          float e[8];
#pragma unroll
          for (int j = 0; j < 8; j++) { const int tk = (8 * (j >> 2) + (j & 3)) * 4; e[j] = (rp[tk] + rp[tk + st]) + (rp[tk + 2 * st] + rp[tk + 3 * st]); }
          u32x4 v = {pk(e[0], e[1]), pk(e[2], e[3]), pk(e[4], e[5]), pk(e[6], e[7])};
          if (d == 20) v = (u32x4){0x3F803F80u, 0x3F803F80u, 0x3F803F80u, 0x3F803F80u};
          if (d == 21) v = (u32x4){0u, 0u, 0u, 0u};
          ST16(v, tb + 1280 + (s16 * 2 + hh) * VB + d * 16);
        }
      }
      if (i == 2) { if (w3 == 0) FLAG(WS_FKV + blockIdx.x * 8);
                    if (w3 >= 2) *(LAS u32x4*)(lp + L_ZERO + (tid - 384) * 16) = (u32x4){0u, 0u, 0u, 0u}; }
    }
    u64 fa, fb, fc, fd, fe;
    { gu64* f = (gu64*)(ws + WS_FKV) + lane;
      fa = __hip_atomic_load(f, RLX_AGENT); fb = __hip_atomic_load(f + 64, RLX_AGENT); fc = __hip_atomic_load(f + 128, RLX_AGENT); fd = __hip_atomic_load(f + 192, RLX_AGENT);
      fe = __hip_atomic_load((gu64*)(ws + WS_FQ) + (rt * 8 + hq) * 4 + p4, RLX_AGENT); }
    WAIT_BAR(14);
    asm volatile("" : "+v"(fa), "+v"(fb), "+v"(fc), "+v"(fd), "+v"(fe));
    const bool kvok = __all((fa == tag) & (fb == tag) & (fc == tag) & (fd == tag) & (fe == tag));
    asm volatile("" ::: "memory");
    BAR_L();
    if (__builtin_expect(!kvok, 0)) { wait_flags<256>(ws, WS_FKV, tag, lane); wait_flags<4>(ws, WS_FQ + (rt * 8 + hq) * 32, tag, lane); }
    KVL(0, 0);
    if (blockIdx.x == 0 && w3 == 0) FLAG(WS_EPOCH);
    STAMP(3);
    BAR_L();
    const int ht = tid - 256;
    const u32x4 a = ldsr16(lp + L_HO + ht * 16), b = ldsr16(lp + L_HO + 4096 + ht * 16); const u32x2 c = ldsr8(lp + L_HO + 8192 + ht * 8);
    o0[0] = a.x; o0[1] = a.y; o0[2] = a.z; o0[3] = a.w; o0[4] = b.x; o0[5] = b.y; o0[6] = b.z; o0[7] = b.w; o0[8] = c.x; o0[9] = c.y;
  }
  STAMP(4);
  u32x4 ph[5];
  { const LAS char* pb = ((p4 & 1) == h) ? lp + L_WPHI + (2 * w2 + h) * 800 + q4 * 40 + (2 * (g & 1) + (p4 >> 1)) * 8 : lp + L_ZERO;
    const u32x4 b0 = {o0[0], o0[1], o0[2], o0[3]}, b1 = {o0[4], o0[5], o0[6], o0[7]}, b2 = {o0[8], o0[9], 0u, 0u};
    const u32x2 c0 = trd(pb + 640), c1 = trd(pb + 672);
    f32x16 P0 = {}, P1 = {};
    P0 = mfma32(trd2(pb, pb + 160), b0, P0); P1 = mfma32(trd2(pb + 32, pb + 192), b0, P1);
    P0 = mfma32(trd2(pb + 320, pb + 480), b1, P0); P1 = mfma32(trd2(pb + 352, pb + 512), b1, P1);
    P0 = mfma32((u32x4){c0.x, c0.y, 0u, 0u}, b2, P0); P1 = mfma32((u32x4){c1.x, c1.y, 0u, 0u}, b2, P1);
    KX(1, 1); KW(1, 1, 0); SBAR();
    f32x2 p[10];
#pragma unroll
    for (int f = 0; f < 10; f++) p[f] = f < 8 ? (f32x2){P0[(2 * f) & 15], P0[(2 * f + 1) & 15]} : (f32x2){P1[(2 * f) & 3], P1[(2 * f + 1) & 3]};
    float mx = fmaxf(p[0][0], p[0][1]), mn = fminf(p[0][0], p[0][1]);
#pragma unroll
    for (int f = 1; f < 10; f++) { mx = fmaxf(fmaxf(mx, p[f][0]), p[f][1]); mn = fminf(fminf(mn, p[f][0]), p[f][1]); }
    const f32x2 ma = {-mx * LOG2E, -mx * LOG2E}, mb = {mn * LOG2E, mn * LOG2E}, ca_ = {LOG2E, LOG2E}, cb_ = {-LOG2E, -LOG2E};
    f32x2 ea[10], eb[10], sa = {0.f, 0.f}, sb = {0.f, 0.f};
#pragma unroll
    for (int f = 0; f < 10; f++) { if (f % 2 == 0 && f < 8) { SBAR(); if (f == 0) KW(1, 1, 1); if (f == 2) KW(1, 1, 2); if (f == 4) KW(1, 1, 3); if (f == 6) KW(1, 1, 4); SBAR(); }
      const f32x2 xa = __builtin_elementwise_fma(p[f], ca_, ma), xb = __builtin_elementwise_fma(p[f], cb_, mb);
      ea[f] = (f32x2){ex2(xa[0]), ex2(xa[1])}; eb[f] = (f32x2){ex2(xb[0]), ex2(xb[1])}; sa += ea[f]; sb += eb[f]; }
    const float ia = __builtin_amdgcn_rcpf(sa[0] + sa[1]), ib = __builtin_amdgcn_rcpf(sb[0] + sb[1]);
    unsigned o[20];
#pragma unroll
    for (int f = 0; f < 10; f++) { const f32x2 ya = ea[f] * ia, yb = eb[f] * ib; o[f] = pk(ya[0], ya[1]); o[10 + f] = pk(yb[0], yb[1]); }
#pragma unroll
    for (int c = 0; c < 5; c++) ph[c] = (u32x4){o[4 * c], o[4 * c + 1], o[4 * c + 2], o[4 * c + 3]};
    asm volatile("" : "+v"(ph[0]), "+v"(ph[1]), "+v"(ph[2]), "+v"(ph[3]), "+v"(ph[4]));
  }
  u32x4 q1A, q1B; u32x2 q2A, q2B;
  const int qo1 = (g & 1) * 5120 + (h * 32 + l15) * 16, qo2 = (g & 1) * 5120 + 1024 + l15 * 8;
  if (role) {
    WAIT_BAR(0);
    STAMP(5);
    const LAS char* qa = lp + L_QL + w3 * 1280;
    q1A = ldsr16(qa + qo1 + (w2 ^ 1) * 256); q1B = ldsr16(qa + qo1 + w2 * 256); q2A = ldsr8(qa + qo2 + (w2 ^ 1) * 128); q2B = ldsr8(qa + qo2 + w2 * 128);
    ARRIVE_NW(0);
  } else {
    const unsigned qb = WS_QX + (rt * 8 + hq) * QX_B + w3 * 1280;
    q1A = LD16S(qo1, qb + (w2 ^ 1) * 256); q1B = LD16S(qo1, qb + w2 * 256); q2A = LD8S(qo2, qb + (w2 ^ 1) * 128); q2B = LD8S(qo2, qb + w2 * 128);
    { LAS char* d = lp + L_PHK + ((2 * w2 + h) * 128 + w3 * 32 + r) * 80;
#pragma unroll
      for (int c = 0; c < 5; c++) *(LAS u32x4*)(d + c * 16) = ph[c]; }
    WAIT_BAR(4);
    STAMP(5);
#pragma unroll 1
    for (unsigned spin = 0; spin < SPIN_CAP; ++spin) {
      if (!(missing(q1A) | missing(q1B) | missing((u32x4){q2A.x, q2A.y, q2B.x, q2B.y}))) break;
      __builtin_amdgcn_s_sleep(1);
      asm volatile("" ::: "memory");
      q1A = LD16S(qo1, qb + (w2 ^ 1) * 256); q1B = LD16S(qo1, qb + w2 * 256); q2A = LD8S(qo2, qb + (w2 ^ 1) * 128); q2B = LD8S(qo2, qb + w2 * 128);
    }
  }
  STAMP(6);
  const int vr = r <= 21 ? r : 21;
  const int ok1 = lane * 16, ok2 = h ? (1280 + 20 * 16) : (1024 + r * 8), ov = 1280 + h * VB + vr * 16;
  const f32x16 Z = {};
  if (h) { q2A = (u32x2){0u, 0u}; q2B = (u32x2){0u, 0u}; }
  { const LAS char* t0 = lp;
    const u32x4 k1 = ldsr16(t0 + ok1); const u32x2 k2 = ldsr8(t0 + ok2);
    f32x16 sa = mfma32(k1, q1A, Z); sa = mfma32k8(k2, q2A, sa);
    f32x16 sb = mfma32(k1, q1B, Z); sb = mfma32k8(k2, q2B, sb);
    float ma = sa[0], mb = sb[0];
#pragma unroll
    for (int i = 1; i < 16; i++) { ma = fmaxf(ma, sa[i]); mb = fmaxf(mb, sb[i]); }
    ma = half_swap_max(ma); mb = half_swap_max(mb);
    const float mah = __uint_as_float(__float_as_uint(ma) & 0xFFFF0000u), mbh = __uint_as_float(__float_as_uint(mb) & 0xFFFF0000u);
    if (h) { q2A.x = pk(-mah, mah - ma); q2B.x = pk(-mbh, mbh - mb); } }
  f32x16 OA = {}, OB = {}, SA, SB;
  u32x4 k1, v1, v2, v2n, pA1, pA2, pB1, pB2; u32x2 k2;
  float e0, e1, e2, e3;
  const LAS char* c0p = lp + w2 * CH_B + ok1; const LAS char* c1p = lp + w2 * CH_B + ok2; const LAS char* c2p = lp + w2 * CH_B + ov;
  k1 = ldsr16(c0p); k2 = ldsr8(c1p);
  SA = mfma32(k1, q1A, Z); SA = mfma32k8(k2, q2A, SA);
  v1 = ldsr16(c2p); v2 = v1;
  e0 = e1 = e2 = e3 = 0.f; pB2 = (u32x4){0u, 0u, 0u, 0u};
#define G(MF, LD, H, M, C0_, C1_, X, S_, i) MF; LD; H; SBAR(); C0_; C1_; SBAR(); X##0 = ex2(S_[i]); X##1 = ex2(S_[i + 1]); SBAR(); M; SBAR(); X##2 = ex2(S_[i + 2]); X##3 = ex2(S_[i + 3]); \
    asm volatile("" : "+v"(X##0), "+v"(X##1), "+v"(X##2), "+v"(X##3)); SBAR();
#define TILEF(TN, V2, F) { float a0, a1, a2, a3; \
    G(SB = mfma32(k1, q1B, Z),    k1 = ldsr16(d0p + (TN)), F(0), F(8),  pB2[2] = pk(e0, e1), pB2[3] = pk(e2, e3), a, SA, 0) \
    G(OB = mfma32(v2, pB2, OB),   V2,                      F(1), F(9),  pA1[0] = pk(a0, a1), pA1[1] = pk(a2, a3), e, SA, 4) \
    G(SB = mfma32k8(k2, q2B, SB), k2 = ldsr8(d1p + (TN)),  F(2), F(10), pA1[2] = pk(e0, e1), pA1[3] = pk(e2, e3), a, SA, 8) \
    G(OA = mfma32(v1, pA1, OA),   (void)0,                 F(3), F(11), pA2[0] = pk(a0, a1), pA2[1] = pk(a2, a3), e, SA, 12) \
    G(SA = mfma32(k1, q1A, Z),    (void)0,                 F(4), F(12), pA2[2] = pk(e0, e1), pA2[3] = pk(e2, e3), a, SB, 0) \
    G(OA = mfma32(v2, pA2, OA),   (void)0,                 F(5), F(13), pB1[0] = pk(a0, a1), pB1[1] = pk(a2, a3), e, SB, 4) \
    G(SA = mfma32k8(k2, q2A, SA), (void)0,                 F(6), F(14), pB1[2] = pk(e0, e1), pB1[3] = pk(e2, e3), a, SB, 8) \
    G(OB = mfma32(v1, pB1, OB),   v1 = ldsr16(d2p + (TN)), F(7), F(15), pB2[0] = pk(a0, a1), pB2[1] = pk(a2, a3), e, SB, 12) }
#define TILE(TB, TN, V2, H0, H1, H2) { auto f_ = [&](int n) { if (n == 3) { H0; } if (n == 4) { H1; } if (n == 5) { H2; } }; TILEF(TN, V2, f_) }
#define TLF(t, F) TILEF(((t) + 1) * TILE_B, v2 = ldsr16(d2p + (t) * TILE_B + 2 * VB), F)
#define NOP (void)0
  const LAS char *d0p = c0p, *d1p = c1p, *d2p = c2p;
#define TL(sl, t, H0, H1, H2) TILE(0, ((t) + 1) * TILE_B, v2 = ldsr16(d2p + (t) * TILE_B + 2 * VB), H0, H1, H2)
#define TL6(sl, H0, H1) TL(sl, 6, H0, H1, v2n = ldsr16(d2p + 7 * TILE_B + 2 * VB))
#define TL7(sl, sn, H0, H1, H2) d0p += ((sn) - (sl)) * STEP_B; d1p += ((sn) - (sl)) * STEP_B; d2p += ((sn) - (sl)) * STEP_B; asm volatile("" : "+v"(d0p), "+v"(d1p), "+v"(d2p)); TILE(0, 0, v2 = v2n, H0, H1, H2)
#define WOG(wo, gr, j, dst) do { if ((j) == 4) WOP(wo, gr, 4, dst); else { if ((j) == 0) dmag_open(lds0 + (dst)); dmag<(j)>((wo) + C0 + (gr) * 32 * 640 - 2048, woff[j]); } } while (0)
#define WOP(wo, gr, j, dst) dma16s((wo) + C0 + (gr) * 32 * 640 - (4 - (j)) * 512, woff[j], lds0 + (dst) + (j) * 1024)
#define WA(gr, j) WOG(woA, gr, j, L_WO + (gr) * 5120)
  const int wx3 = w3 ? w3 : 1;
  const u16* wxs = (w2 ? woB : woA) + (16 + wx3) * 32 * 640;
  const int wxd = w2 ? 145408 + (wx3 - 1) * 5120 : L_WO + (16 + wx3) * 5120;
#define WX(j) WOG(wxs, 0, j, wxd)
#define PRIO(n) __builtin_amdgcn_s_setprio(n)
  u32x4 ones = ONES; if (role) asm volatile("" : "+v"(ones));
#define FILL(j) do { const int n_ = w * 3 + ((j) >> 1); ST16S(ones, ok1, (((j) & 1) ? WS_OB : WS_OA) + (rt * 8 + hq) * OT_B + (n_ < 20 ? n_ : 19) * 1024); } while (0)
#define CLR_VM() __builtin_amdgcn_s_waitcnt(0x0F70)
  const bool red = !role && !w2;
  f32x4 part[4]; u64 fp0, fp1;
  auto red_load = [&]() {
    asm volatile("" : "+v"(fp0), "+v"(fp1));
    if (__builtin_expect(!__all((fp0 == tag) & (fp1 == tag)), 0)) wait_flags<128>(ws, WS_FPART + hq * 1024, tag, lane);
    asm volatile("" ::: "memory");
#pragma unroll
    for (int j = 0; j < 4; j++) part[j] = __builtin_bit_cast(f32x4, LD16S(g * (4 * PART_B) + l15 * 16, WS_PART + (hq * 16 + j) * PART_B + rt * 960 + w3 * 240));
  };
  auto red_store = [&]() {
    f32x4 a = (part[0] + part[1]) + (part[2] + part[3]);
#pragma unroll
    for (int i = 0; i < 4; i++) { auto r16 = __builtin_amdgcn_permlane16_swap(__float_as_uint(a[i]), __float_as_uint(a[i]), false, false); const float t = __uint_as_float(r16[0]) + __uint_as_float(r16[1]);
      auto r32 = __builtin_amdgcn_permlane32_swap(__float_as_uint(t), __float_as_uint(t), false, false); a[i] = __uint_as_float(r32[0]) + __uint_as_float(r32[1]); }
    const int hd = rt >> 2, u = (rt & 3) * 60 + w3 * 15 + lane, hi = u >= 160, fq = hi ? (u - 160) >> 3 : u >> 4, di = hi ? u & 7 : u & 15;
    __builtin_amdgcn_raw_buffer_store_b64((u32x2){pk(a[0], a[1]), pk(a[2], a[3])}, wsr, lane < 15 ? (hi * 5 + (fq >> 1)) * 512 + di * 16 + (fq & 1) * 8 : 0x7FFF0000u, WS_KVR + hq * KVR_B + (hd >> 1) * 5120 + (hd & 1) * 256, 16);
  };
  u32x4 ka[10]; u64 fkr; float gta; unsigned kb; const unsigned kbs = WS_KVR + hq * KVR_B + w2 * 5120;
  auto oa_load = [&]() {
    asm volatile("" : "+v"(fkr));
    if (__builtin_expect(!__all(fkr == tag), 0)) wait_flags<64>(ws, WS_FKVR + hq * 512, tag, lane);
    asm volatile("" ::: "memory");
    kb = (((r >> 2) & 1) == h) ? (h * 16 + (r & 3) + 4 * (r >> 3)) * 16 : 0x7FFF0000u;
    ka[0] = LD16S(kb, kbs); ka[5] = LD16S(kb, kbs + 2560); ka[1] = LD16S(kb, kbs + 512); ka[6] = LD16S(kb, kbs + 3072);
    gta = __uint_as_float(__builtin_amdgcn_raw_buffer_load_b32(wsr, WS_G + (rt * 128 + w3 * 32 + r) * 4, 0, 16));
  };
  u32x4 jf[16]; f32x4 jc[3];
  const LAS char* jab = lp + L_PHK + ((w >> 1) * 128 + g * 8 + q4) * 80 + p4 * 8;
  const LAS char* jbb = lp + L_VIM + ((w >> 1) * 128 + g * 8 + q4) * 40 + (w & 1) * 32 + p4 * 8;
  auto kv_rd = [&](int n) { const int ks = n >> 2, j = n & 3;
    jf[n] = j ? trd2(jab + ks * 2560 + (j - 1) * 32, jab + ks * 2560 + 320 + (j - 1) * 32) : trd2(jbb + ks * 1280, jbb + ks * 1280 + 160); };
  auto kv_mm = [&](int m) { const int ks = m / 3, ft = m % 3;
    if (ft == 0 && (w & 1) && l15 == 4) jf[ks * 4] = (u32x4){0x3F803F80u, 0x3F803F80u, 0x3F803F80u, 0x3F803F80u};
    jc[ft] = mfma16(jf[ks * 4 + 1 + ft], jf[ks * 4], ks ? jc[ft] : (f32x4){0.f, 0.f, 0.f, 0.f}); };
  auto kv_st = [&](int ft) { const int dt = w & 1;
    const unsigned vo = ((!dt || l15 < 8) && (ft < 2 || g < 2)) ? (dt ? g * 128 : g * 256) + l15 * 16 : 0x7FFF0000u;
    ST16S(__builtin_bit_cast(u32x4, jc[ft]), vo, WS_PART + (hq * 16 + rt) * PART_B + (w >> 1) * 3840 + (dt ? 2560 + ft * 512 : ft * 1024)); };
  f32x16 oa0, oa1; u32x4 z0, z1, z2;
  LAS char* otb = lp + L_WO + (8 + w) * 5120;
  auto oa_mm = [&](int m) { const int s = m >> 1; if (m & 1) oa1 = mfma32(ka[5 + s], ph[s], s ? oa1 : Z); else oa0 = mfma32(ka[s], ph[s], s ? oa0 : Z); };
  auto oa_norm = [&]() { const float inv = __builtin_amdgcn_rcpf(oa1[4] + 1e-6f) * gta;
    LAS char* ar = otb + (r >> 4) * 1280 + l15 * 16; LAS char* ae = ar + h * 520; LAS char* ao = ar + 8 + h * 760;
#pragma unroll
    for (int t = 0; t < 5; t++) {
      const u32x2 v = t < 4 ? (u32x2){nz(pk(oa0[(4 * t) & 15] * inv, oa0[(4 * t + 1) & 15] * inv)), nz(pk(oa0[(4 * t + 2) & 15] * inv, oa0[(4 * t + 3) & 15] * inv))} : (u32x2){nz(pk(oa1[0] * inv, oa1[1] * inv)), nz(pk(oa1[2] * inv, oa1[3] * inv))};
      *(LAS u32x2*)(((t & 1) ? ao : ae) + (t >> 1) * 256) = v; }
    z0 = ldsr16(otb + ok1); z1 = ldsr16(otb + 1280 + ok1); z2 = ldsr16(ar + 1024); };
  auto oa_st = [&]() { const unsigned oa = WS_OA + (rt * 8 + w3 * 2) * OT_B + (hq * 2 + w2) * 1280;
    ST16S(z0, ok1, oa); ST16S(z1, ok1, oa + OT_B); ST16S(z2, h ? 0x7FFF0000u : (r >> 4) * OT_B + l15 * 16, oa + 1024); };
  unsigned cpk;
  PRIO(3);
  if (role) { TL(0, 0, FILL(0), NOP, NOP) TL(0, 1, FILL(1), NOP, NOP) TL(0, 2, FILL(2), NOP, NOP) }
  else {
    auto f0 = [&](int n) { if (n < 8) kv_rd(n); if (n >= 13) kv_mm(n - 13); };
    auto f1 = [&](int n) { if (n < 8) kv_rd(8 + n); if (n == 7) ARRIVE_NW(0); if (n >= 9 && n < 12) kv_mm(n - 6); if (n >= 13) kv_mm(n - 7); };
    auto f2 = [&](int n) { if (n >= 9 && n < 12) kv_mm(n); if (n >= 5 && n < 8) kv_st(n - 5); };
    TLF(0, f0) TLF(1, f1) TLF(2, f2)
  }
  TL(0, 3, dma4s((w2 ? woB : woA) + C0 + (((rt & 7) * 2 + role) * 40 + w3 * 10) * 640, (l15 < 10 ? l15 : 9) * 1280 + (g & 1) * 156, lds0 + 160768)        , NOP, NOP)
  TL(0, 4, NOP, NOP, if (role) FILL(3))
  PRIO(2);
  TL(0, 5, if (!role) { WAIT_VM(0); FLAG(WS_FPART + ((hq * 16 + rt) * 8 + w) * 8); }, NOP, cpk = PEEK(0))
  AWAIT(0, cpk);
  asm volatile("" ::: "memory");
  TL6(0, KX(2, 2), KW(2, 2, 0))
  TL7(0, 1, KW(2, 2, 1), KW(2, 2, 2), KW(2, 2, 3))
  TL(1, 0, KW(2, 2, 4), if (role) FILL(4), if (!role) { fp0 = POLL(WS_FPART + hq * 1024); fp1 = POLL(WS_FPART + hq * 1024 + 512); })
  TL(1, 1, if (role) { WX(0); WX(1); }, if (role) FILL(5), if (role) { WX(2); WX(3); })
  TL(1, 2, if (role) WX(4), NOP, if (red) red_load())
  PRIO(1);
  TL(1, 3, NOP, NOP, NOP)
  TL(1, 4, NOP, if (!role) CLR_VM(), if (red) red_store())
  TL(1, 5, NOP, NOP, NOP)
  PRIO(0);
  TL6(1, if (!role) { WAIT_VM(0); FLAG((w2 ? WS_SPARE : WS_FKVR) + ((hq * 16 + rt) * 4 + w3) * 8); }, NOP)
  if (!role) WAIT_BAR(1); else WAIT_BAR(0);
  STAMP(12);
  PRIO(3);
  if (role) FLAG(WS_FOA + ((rt * 8 + hq) * 8 + w) * 8);
  u64 foa;
  TL7(1, 2, KX(3, 0), KW(3, 0, 0), KW(3, 0, 1))
  TL(2, 0, KW(3, 0, 2), KW(3, 0, 3), KW(3, 0, 4)) TL(2, 1, WA(w, 0), WA(w, 1), WA(w, 2))
  u32x4 wbr[11];
  const char* wbs = (const char*)(woB + C0 + w * 32 * 640) - 4096;
#define WB1(n) wbr[n] = *(const GAS u32x4*)(wbs + ((n) / 5) * (8 * 32 * 1280) + woff[(n) % 5] + ((n) % 5) * 1024)
#define WB2(j) do { WB1(j); WB1(5 + (j)); } while (0)
#define WA16() dma16s(woA + C0 + 16 * 32 * 640 - 2048, wo16, lds0 + L_WO + 16 * 5120 + w5 * 1024)
#define WB16() wbr[10] = *(const GAS u32x4*)((const char*)(woB + C0 + 16 * 32 * 640) - 4096 + wo16)
  auto chk = [&]() { asm volatile("" : "+v"(foa));
    if (__builtin_expect(!__all(foa == tag), 0)) wait_flags<64>(ws, WS_FOA + rt * 512, tag, lane);
    asm volatile("" ::: "memory"); };
  if (role) {
    TL(2, 2, WA(w, 3), WA(w, 4), NOP)
    PRIO(2);
    TL(2, 3, (fkr = POLL(WS_FKVR + hq * 512), foa = POLL(WS_FOA + rt * 512)), NOP, NOP)
    TL(2, 4, NOP, { chk(); oa_load(); }, { ka[2] = LD16S(kb, kbs + 1024); ka[7] = LD16S(kb, kbs + 3584); ka[3] = LD16S(kb, kbs + 1536); })
    PRIO(1);
    TL(2, 5, { ka[8] = LD16S(kb, kbs + 4096); ka[4] = LD16S(kb, kbs + 2048); ka[9] = LD16S(kb, kbs + 4608); }, NOP, NOP)
    PRIO(0);
    { auto f = [&](int n) { if (n == 5) v2n = ldsr16(d2p + 7 * TILE_B + 2 * VB); if (n >= 8 && n != 10 && n != 14) oa_mm(n < 10 ? n - 8 : (n < 14 ? n - 9 : 5)); }; TLF(6, f) }
  } else {
    TL(2, 2, WA(w, 3), WA(w, 4), WX(0))
    PRIO(2);
    TL(2, 3, { WX(1); WX(2); }, { WX(3); WX(4); }, foa = POLL(WS_FOA + rt * 512))
    TL(2, 4, NOP, NOP, NOP)
    PRIO(1);
    TL(2, 5, chk(), WB1(0), WB1(5))
    PRIO(0);
    TL6(2, WB1(1), WB1(6))
  }
  BAR_L();
  STAMP(13);
  PRIO(3);
  if (role) {
    d0p -= 2 * STEP_B; d1p -= 2 * STEP_B; d2p -= 2 * STEP_B; asm volatile("" : "+v"(d0p), "+v"(d1p), "+v"(d2p));
    { auto f = [&](int n) { if (n == 8 || n == 9) oa_mm(n - 2); if (n == 11 || n == 12) oa_mm(n - 3); if (n == 5) CLR_VM(); if (n == 6) WA16(); }; TILEF(0, v2 = v2n, f) }
    TL(0, 0, oa_norm(), NOP, oa_st())
    TL(0, 1, WA(8 + w, 0), WA(8 + w, 1), WA(8 + w, 2))
    TL(0, 2, WA(8 + w, 3), WA(8 + w, 4), WB2(0))
    TL(0, 3, WB2(1), NOP, WB2(2))
    PRIO(2);
    TL(0, 4, WB2(3), NOP, WB2(4))
  } else {
    TL7(2, 0, WA16(), WA(8 + w, 0), WA(8 + w, 1))
    TL(0, 0, WA(8 + w, 2), WA(8 + w, 3), WA(8 + w, 4))
    TL(0, 1, WB1(2), NOP, WB1(7)) TL(0, 2, WB1(3), NOP, WB1(8)) TL(0, 3, WB1(4), NOP, WB1(9))
    PRIO(2);
    TL(0, 4, NOP, NOP, NOP)
  }
  ARRIVE_NW(1);
  const int tp = w & 1, kq = w >> 1;
  const unsigned ofr = (rt * 8 + role * 4 + tp * 2) * OT_B + kq * 5120;
  u32x4 of[2][5]; float gtb;
#define OFL(u) do { _Pragma("unroll") for (int i = 0; i < 5; i++) of[u][i] = LD16S(ok1, WS_OA + ofr + (u) * OT_B + i * 1024); } while (0)
#define OF2(i) do { of[0][i] = LD16S(ok1, WS_OA + ofr + (i) * 1024); of[1][i] = LD16S(ok1, WS_OA + ofr + OT_B + (i) * 1024); } while (0)
  TL(0, 5, WB16(), gtb = __uint_as_float(__builtin_amdgcn_raw_buffer_load_b32(wsr, WS_G + (rt * 128 + w3 * 32 + w2 * 16 + l15) * 4, 0, 16)), OF2(0))
  PRIO(1);
  TL6(0, OF2(1), OF2(2))
  PRIO(0);
  LAS f32x4* xb = (LAS f32x4*)(lp + (w3 >> 1) * CH_B) + (w3 & 1) * (64 * 6) + lane;
  { auto f = [&](int n) { if (n == 0) cpk = PEEK(1); if (n == 3) OF2(3); if (n == 4) OF2(4);
      if (n == 15) { AWAIT(1, cpk);
#pragma unroll
        for (int c = 0; c < 3; c++) xb[(w2 * 3 + c) * 64] = (f32x4){OA[4 * c], OA[4 * c + 1], OA[4 * c + 2], OA[4 * c + 3]}; } };
    TILEF(0, v2 = v2n, f) }
  pB2[2] = pk(e0, e1); pB2[3] = pk(e2, e3);
  OB = mfma32(v2, pB2, OB);
#undef TL
#undef TL6
#undef TL7
#undef TILE
#undef G
  STAMP(7);
  u32x4 wf[5][5];
#define WFA(i) do { const LAS char* wb_ = lp + L_WO + (kq * 5 + (i)) * 5120 + wlane; _Pragma("unroll") for (int t = 0; t < 5; t++) wf[i][t] = trd2(wb_ + t * 8, wb_ + 1280 + t * 8); } while (0)
#define WFB(i) do { const int gr_ = kq * 5 + (i); const LAS char* wb_ = lp + (gr_ < 17 ? L_WO + gr_ * 5120 : 145408 + (gr_ - 17) * 5120) + wlane; _Pragma("unroll") for (int t = 0; t < 5; t++) wf[i][t] = trd2(wb_ + t * 8, wb_ + 1280 + t * 8); } while (0)
  { asm volatile("s_waitcnt vmcnt(18) lgkmcnt(0)\n\ts_barrier" ::: "memory");
    f32x16 O = OB;
#pragma unroll
    for (int c = 0; c < 3; c++) { const f32x4 t = xb[((w2 ^ 1) * 3 + c) * 64]; O[4 * c] += t[0]; O[4 * c + 1] += t[1]; O[4 * c + 2] += t[2]; O[4 * c + 3] += t[3]; }
    SBAR(); WFA(0); WFA(1); SBAR();
    float l = from_upper(O[8]);
    const float amax = (((fabsf(O[0]) + fabsf(O[1])) + (fabsf(O[2]) + fabsf(O[3]))) + ((fabsf(O[4]) + fabsf(O[5])) + (fabsf(O[6]) + fabsf(O[7])))) + ((fabsf(O[8]) + fabsf(O[9])) + (fabsf(O[10]) + fabsf(O[11])));
    const bool good = (l >= 0x1p-30f) && (l <= 0x1p100f) && (amax <= 0x1p126f);
    if (__builtin_expect(!__all(good), 0)) {
      const u32x4 q1 = q1B; const u32x2 q2 = h ? (u32x2){0u, 0u} : q2B;
      float m = -INFINITY;
#pragma unroll 1
      for (int t = 0; t < 64; t++) {
        f32x16 s = mfma32(LD16(WS_KV + t * TILE_B + ok1), q1, Z); s = mfma32k8(LD8(WS_KV + t * TILE_B + ok2), q2, s);
#pragma unroll
        for (int i = 0; i < 16; i++) m = fmaxf(m, s[i]);
      }
      m = half_swap_max(m);
      O = Z;
#pragma unroll 1
      for (int t = 0; t < 64; t++) {
        f32x16 s = mfma32(LD16(WS_KV + t * TILE_B + ok1), q1, Z); s = mfma32k8(LD8(WS_KV + t * TILE_B + ok2), q2, s);
#pragma unroll
        for (int i = 0; i < 16; i++) s[i] = ex2(s[i] - m);
        O = mfma32(LD16(WS_KV + t * TILE_B + ov), (u32x4){pk(s[0], s[1]), pk(s[2], s[3]), pk(s[4], s[5]), pk(s[6], s[7])}, O);
        O = mfma32(LD16(WS_KV + t * TILE_B + ov + 2 * VB), (u32x4){pk(s[8], s[9]), pk(s[10], s[11]), pk(s[12], s[13]), pk(s[14], s[15])}, O);
      }
      l = from_upper(O[8]);
    }
    const float inv = __builtin_amdgcn_rcpf(l) * (1.0f - gtb);
    LAS char* tb = lp + w2 * CH_B + 12288 + w3 * 1536;
    const int uq = (g & 1) * 5 + h;
    LAS char* t0 = tb + (uq >> 1) * 256 + (uq & 1) * 8 + l15 * 16; LAS char* t2 = h ? tb + 1280 + (g & 1) * 8 + l15 * 16 : t0 + 512;
#pragma unroll
    for (int c = 0; c < 3; c++) *(LAS u32x2*)(c < 2 ? t0 + c * 256 : t2) = (u32x2){nz(pk(O[4 * c] * inv, O[4 * c + 1] * inv)), nz(pk(O[4 * c + 2] * inv, O[4 * c + 3] * inv))};
    const unsigned ob = WS_OB + (rt * 8 + w3 * 2 + w2) * OT_B + hp * 1280;
    const u32x4 y0 = ldsr16(tb + ok1), y1 = ldsr16(tb + 1024 + l15 * 16);
    ST16S(y0, ok1, ob); ST16S(y1, lane < 16 ? ok1 : 0x7FFF0000u, ob + 1024);
    SBAR();
  }
  STAMP(8);
  auto of_missing = [&]() { u32x4 mm = of[0][0];
#pragma unroll
    for (int j = 1; j < 10; j++) mm = __builtin_elementwise_max(mm, of[j / 5][j % 5]);
    return missing(mm); };
  if (__builtin_expect(of_missing(), 0)) {
#pragma unroll 1
    for (unsigned spin = 0; spin < SPIN_CAP; ++spin) {
      asm volatile("" ::: "memory");
      OFL(0); OFL(1);
      if (!of_missing()) break;
      __builtin_amdgcn_s_sleep(1);
    }
  }
  f32x4 c[2][5];
#define MM(u, i, first) do { _Pragma("unroll") for (int t = 0; t < 5; t++) c[u][t] = mfma16(wf[i][t], of[u][i], (first) ? (f32x4){0.f, 0.f, 0.f, 0.f} : c[u][t]); SBAR(); } while (0)
#define OBL(u, i) of[u][i] = LD16S(ok1, WS_OB + ofr + (u) * OT_B + (i) * 1024)
#define MMR(u, i, first, j, ob_) do { const LAS char* wb_ = lp + (ob_) + wlane; _Pragma("unroll") for (int t = 0; t < 5; t++) { \
      c[u][t] = mfma16(wf[i][t], of[u][i], (first) ? (f32x4){0.f, 0.f, 0.f, 0.f} : c[u][t]); wf[j][t] = trd2(wb_ + t * 8, wb_ + 1280 + t * 8); SBAR(); } } while (0)
#define OA_(j) (L_WO + (kq * 5 + (j)) * 5120)
#define OB_(j) (kq * 5 + (j) < 17 ? L_WO + (kq * 5 + (j)) * 5120 : 145408 + (kq * 5 + (j) - 17) * 5120)
  MMR(0, 0, true, 2, OA_(2)); MMR(1, 0, true, 3, OA_(3)); MMR(0, 1, false, 4, OA_(4));
  BAR_L();
  SBAR();
  { LAS char* d = lp + L_WO + w * 5120 + lane * 16;
#pragma unroll
    for (int n = 0; n < 15; n++) { const int u = n < 5 ? 1 : (n >= 10), i = n < 5 ? 1 : 2, t = n % 5;
      if (n < 10) *(LAS u32x4*)(d + (n / 5) * 40960 + (n % 5) * 1024) = wbr[n];
      if (n == 10) *(LAS u32x4*)(lp + L_WO + 16 * 5120 + w5 * 1024 + lane * 16) = wbr[10];
      if (n == 11) OBL(0, 0); if (n == 12) OBL(1, 0); if (n == 13) OBL(0, 1); if (n == 14) OBL(1, 1);
      c[u][t] = mfma16(wf[i][t], of[u][i], c[u][t]); SBAR(); }
    asm volatile("" : "+v"(c[1][0])); }
  STAMP(9);
  BAR_L();
  SBAR();
  OBL(0, 2); OBL(1, 2); SBAR();
  MMR(0, 3, false, 0, OB_(0)); OBL(0, 3); SBAR(); MMR(0, 4, false, 1, OB_(1)); OBL(0, 4); SBAR(); MMR(1, 3, false, 2, OB_(2)); OBL(1, 3); SBAR(); MMR(1, 4, false, 3, OB_(3)); OBL(1, 4);
  WFB(4);
  ARRIVE_NW(2);
  SBAR();
  STAMP(10);
  LAS char* xw = lp + kq * 21504 + (tp * 32 + l15) * 336 + g * 80;
#pragma unroll
  for (int n = 0; n < 10; n++) {
    const int u = n < 6 ? n & 1 : (n >= 8), i = n < 6 ? n >> 1 : 3 + (n & 1);
    if (__builtin_expect(missing(of[u][i]), 0)) {
#pragma unroll 1
      for (unsigned spin = 0; spin < SPIN_CAP; ++spin) {
        asm volatile("" ::: "memory");
#pragma unroll
        for (int j = n; j < 10; j++) { const int uu = j < 6 ? j & 1 : (j >= 8), ii = j < 6 ? j >> 1 : 3 + (j & 1); OBL(uu, ii); }
        if (!missing(of[u][i])) break;
        __builtin_amdgcn_s_sleep(1);
      }
    }
    MM(u, i, false);
    if (n == 6) cpk = PEEK(2);
    if (n == 8) { AWAIT(2, cpk); *(LAS f32x4*)(xw) = c[0][0]; *(LAS f32x4*)(xw + 16) = c[0][1]; *(LAS f32x4*)(xw + 32) = c[0][2]; SBAR(); }
    if (n == 9) { *(LAS f32x4*)(xw + 48) = c[0][3]; *(LAS f32x4*)(xw + 64) = c[0][4]; SBAR(); }
  }
#pragma unroll
  for (int t = 0; t < 5; t++) *(LAS f32x4*)(xw + 16 * 336 + t * 16) = c[1][t];
  BAR_L();
  { const __amdgpu_buffer_rsrc_t orr = __builtin_amdgcn_make_buffer_rsrc(out, 0, 2048 * DM * 2, 0x00020000);
#pragma unroll
    for (int j = 0; j < 3; j++) { const int n = tid + 512 * j, nn = n < 1280 ? n : 0, row = (nn * 3277) >> 16, cq = nn - row * 20;
      const LAS f32x4* p = (const LAS f32x4*)(lp + row * 336 + cq * 16);
      const f32x4 z = (p[0] + p[1344]) + (p[2688] + p[4032]);
      __builtin_amdgcn_raw_buffer_store_b64((u32x2){pk(z[0], z[1]), pk(z[2], z[3])}, orr, n < 1280 ? ((rt * 128 + role * 64 + row) * DM + C0 + cq * 4) * 2 : 0x7FFF0000u, 0, 0); }
  }
  STAMP(11);
}

__global__ __launch_bounds__(512, 2) void fused(const u16* __restrict__ x, const u16* __restrict__ wqkv, const u16* __restrict__ wphi, const u16* __restrict__ woA,
                                                const u16* __restrict__ wq, const u16* __restrict__ wk, const u16* __restrict__ wv, const u16* __restrict__ woB,
                                                const u16* __restrict__ gatew, u16* __restrict__ out, char* ws) {
  __shared__ __attribute__((aligned(16))) char lds[L_TOTAL];
  if ((blockIdx.x >> 6) & 1) body<1>(x, wqkv, wphi, woA, wq, wv, woB, gatew, out, ws, (LAS char*)lds);
  else body<0>(x, wqkv, wphi, woA, wq, wk, woB, gatew, out, ws, (LAS char*)lds);
}
}

extern "C" void kernel_launch(void* const* d_in, const int* in_sizes, int n_in, void* d_out, int out_size, void* d_ws, size_t ws_size, hipStream_t stream) {
  using namespace gm;
  fused<<<256, 512, 0, stream>>>((const u16*)d_in[0], (const u16*)d_in[1], (const u16*)d_in[2], (const u16*)d_in[3], (const u16*)d_in[5], (const u16*)d_in[6],
                                 (const u16*)d_in[7], (const u16*)d_in[8], (const u16*)d_in[9], (u16*)d_out, (char*)d_ws);
}
```

```cpp
#include <hip/hip_runtime.h>
#include <hip/hip_bf16.h>
#include <stdint.h>

namespace gm {
typedef float f32x16 __attribute__((ext_vector_type(16)));
typedef float f32x4 __attribute__((ext_vector_type(4)));
typedef float f32x2 __attribute__((ext_vector_type(2)));
typedef short s16x4 __attribute__((ext_vector_type(4)));
typedef __bf16 b16x2 __attribute__((ext_vector_type(2)));
typedef __bf16 b16x8 __attribute__((ext_vector_type(8)));
typedef unsigned u32x4 __attribute__((ext_vector_type(4)));
typedef unsigned u32x2 __attribute__((ext_vector_type(2)));
typedef unsigned long long u64;
typedef unsigned short u16;
#define DI __device__ __forceinline__
#define LAS __attribute__((address_space(3)))
#define GAS __attribute__((address_space(1)))
typedef GAS u64 gu64;

constexpr int DM = 640;
constexpr unsigned WS_EPOCH = 0, WS_FKV = 2048, WS_FKVR = 4096, WS_FQ = 8192, WS_FPART = 16384, WS_FOA = 24576, WS_SPARE = 40960  , WS_G = 57344;
constexpr unsigned WS_KVR = 8912896, KVR_B = 10240, WS_KV = 131072, WS_QX = 327680, QX_B = 10240, WS_PART = 1703936, PART_B = 15360;
constexpr unsigned WS_OA = 3670016, WS_OB = 6291456, WS_END = 8994816, OT_B = 20480;
constexpr int VB = 352, TILE_B = 1280 + 4 * VB, CH_B = TILE_B * 8, STEP_B = 2 * CH_B;
constexpr int STAGE_B = 36864, ST_W1 = 10240, ST_X = 20480;
constexpr int L_BUF = 43008, L_P0W = 0, L_P0X0 = 25600, L_P0X1 = 153600, L_RED = 158720, L_RED1 = 40960;
constexpr int L_HO = 86016, L_QL = 96256, L_VIM = 96256, L_PHK = 116736, L_WPHI = 158592, L_ZERO = 161792, L_CNT = 163712, L_TOTAL = 163840;
constexpr int L_WO = 43008;
constexpr float LOG2E = 1.4426950408889634f;
constexpr float QSCALE = 0.22360679774997896f * LOG2E;
constexpr unsigned SPIN_CAP = 20000, LSPIN_CAP = 400000;

DI unsigned pk(float a, float b) { f32x2 v = {a, b}; b16x2 r = __builtin_convertvector(v, b16x2); return __builtin_bit_cast(unsigned, r); }
DI f32x16 mfma32(u32x4 a, u32x4 b, f32x16 c) { return __builtin_amdgcn_mfma_f32_32x32x16_bf16(__builtin_bit_cast(b16x8, a), __builtin_bit_cast(b16x8, b), c, 0, 0, 0); }
DI f32x16 mfma32k8(u32x2 a, u32x2 b, f32x16 c) { return __builtin_amdgcn_mfma_f32_32x32x8bf16_1k(__builtin_bit_cast(s16x4, a), __builtin_bit_cast(s16x4, b), c, 0, 0, 0); }
DI f32x4 mfma16(u32x4 a, u32x4 b, f32x4 c) { return __builtin_amdgcn_mfma_f32_16x16x32_bf16(__builtin_bit_cast(b16x8, a), __builtin_bit_cast(b16x8, b), c, 0, 0, 0); }
DI u32x2 trd(const LAS char* p) { return __builtin_bit_cast(u32x2, __builtin_amdgcn_ds_read_tr16_b64_v4i16((LAS s16x4*)p)); }
DI u32x4 trd2(const LAS char* p0, const LAS char* p1) { u32x2 a = trd(p0), b = trd(p1); return (u32x4){a.x, a.y, b.x, b.y}; }
DI u32x4 ldsr16(const LAS char* p) { return *(const LAS u32x4*)p; }
DI u32x2 ldsr8(const LAS char* p) { return *(const LAS u32x2*)p; }
DI float ex2(float x) { return __builtin_amdgcn_exp2f(x); }
DI void dma16s(const void* base, unsigned voff, unsigned lds_base) {
  asm volatile("s_mov_b32 m0, %2\n\ts_nop 0\n\tglobal_load_lds_dwordx4 %0, %1" :: "v"(voff), "s"(base), "s"(lds_base) : "memory"); }
DI void dma16s_sc1(const void* base, unsigned voff, unsigned lds_base) {
  asm volatile("s_mov_b32 m0, %2\n\ts_nop 0\n\tglobal_load_lds_dwordx4 %0, %1 sc1" :: "v"(voff), "s"(base), "s"(lds_base) : "memory"); }
DI void dmag_open(unsigned lds_base) { asm volatile("s_mov_b32 m0, %0\n\ts_nop 0" :: "s"(lds_base) : "memory"); }
template <int J> DI void dmag(const void* base, unsigned voff) { asm volatile("global_load_lds_dwordx4 %0, %1 offset:%2" :: "v"(voff), "s"(base), "i"((J & 3) * 1024) : "memory"); }
template <int J> DI void dmag_sc1(const void* base, unsigned voff) { asm volatile("global_load_lds_dwordx4 %0, %1 offset:%2 sc1" :: "v"(voff), "s"(base), "i"((J & 3) * 1024) : "memory"); }
DI void dma4s(const void* base, unsigned voff, unsigned lds_base) {
  asm volatile("s_mov_b32 m0, %2\n\ts_nop 0\n\tglobal_load_lds_dword %0, %1" :: "v"(voff), "s"(base), "s"(lds_base) : "memory"); }
DI u32x4 ld16_async(const void* p) { u32x4 r; asm volatile("global_load_dwordx4 %0, %1, off" : "=v"(r) : "v"(p) : "memory"); return r; }
DI u32x2 ld8_sc1_async(const void* p) { u32x2 r; asm volatile("global_load_dwordx2 %0, %1, off sc1" : "=v"(r) : "v"(p) : "memory"); return r; }
#define BAR_L() asm volatile("s_waitcnt lgkmcnt(0)\n\ts_barrier" ::: "memory")
#define WAIT_BAR(N) asm volatile("s_waitcnt vmcnt(" #N ") lgkmcnt(0)\n\ts_barrier" ::: "memory")
#define WAIT_VM(N) asm volatile("s_waitcnt vmcnt(" #N ")" ::: "memory")
#define SBAR() __builtin_amdgcn_sched_barrier(0)
#define RLX_AGENT __ATOMIC_RELAXED, __HIP_MEMORY_SCOPE_AGENT
DI float half_swap_max(float m) { auto rr = __builtin_amdgcn_permlane32_swap(__float_as_uint(m), __float_as_uint(m), false, false); return fmaxf(__uint_as_float(rr[0]), __uint_as_float(rr[1])); }
DI float from_upper(float v) { auto rr = __builtin_amdgcn_permlane32_swap(__float_as_uint(v), __float_as_uint(v), false, false); return __uint_as_float(rr[1]); }


template <int NF>
DI bool flags_set(const char* ws, unsigned off, u64 tag, int lane) {
  gu64* f = (gu64*)(ws + off);
  bool ok = __hip_atomic_load(f + (NF < 64 ? (lane & (NF - 1)) : lane), RLX_AGENT) == tag;
  if (NF >= 128) ok &= __hip_atomic_load(f + 64 + lane, RLX_AGENT) == tag;
  if (NF >= 256) { ok &= __hip_atomic_load(f + 128 + lane, RLX_AGENT) == tag; ok &= __hip_atomic_load(f + 192 + lane, RLX_AGENT) == tag; }
  return ok;
}
template <int NF>
DI void wait_flags(const char* ws, unsigned off, u64 tag, int lane) {
#pragma unroll 1
  for (unsigned spin = 0; spin < SPIN_CAP; ++spin) {
    if (__all(flags_set<NF>(ws, off, tag, lane))) break;
    __builtin_amdgcn_s_sleep(2);
  }
  asm volatile("" ::: "memory");
}
DI unsigned nz(unsigned v) { return v < 0xFFFFFFFEu ? v : 0xFFFFFFFEu; }
DI unsigned umax3(unsigned a, unsigned b, unsigned c) { unsigned m = a > b ? a : b; return m > c ? m : c; }
DI bool missing(u32x4 v) { return __any(umax3(umax3(v.x, v.y, v.z), v.w, 0u) == 0xFFFFFFFFu); }
#define ONES ((u32x4){0xFFFFFFFFu, 0xFFFFFFFFu, 0xFFFFFFFFu, 0xFFFFFFFFu})
#define ST16(v, off) __builtin_amdgcn_raw_buffer_store_b128((v), wsr, (off), 0, 16)
#define ST8(v, off) __builtin_amdgcn_raw_buffer_store_b64((v), wsr, (off), 0, 16)
#define LD16(off) __builtin_amdgcn_raw_buffer_load_b128(wsr, (off), 0, 16)
#define ST16S(v, voff, soff) ST16(v, (voff) + (soff))
#define LD16S(voff, soff) __builtin_amdgcn_raw_buffer_load_b128(wsr, (voff), (soff), 16)
#define LD8S(voff, soff) __builtin_amdgcn_raw_buffer_load_b64(wsr, (voff), (soff), 16)
#define LD8(off) __builtin_amdgcn_raw_buffer_load_b64(wsr, (off), 0, 16)

template <int role>
DI void body(const u16* __restrict__ x, const u16* __restrict__ wqkv, const u16* __restrict__ wphi, const u16* __restrict__ woA,
             const u16* __restrict__ wq, const u16* __restrict__ wkv, const u16* __restrict__ woB,
             const u16* __restrict__ gatew, u16* __restrict__ out, char* ws, LAS char* lp) {
  const int tid = threadIdx.x, lane = tid & 63;
  const int w = __builtin_amdgcn_readfirstlane(tid >> 6) & 7;
  const int l15 = lane & 15, g = lane >> 4, q4 = l15 >> 2, p4 = lane & 3, r = lane & 31, h = lane >> 5;
  const int bx = blockIdx.x & 7, bi = blockIdx.x >> 3;
  const int rt = (bx & 1) * 8 + (bi & 7), hp = (bx >> 1) * 4 + ((bi >> 3) & 3), hq = hp >> 1;
  const int w3 = w & 3, w2 = w >> 2;
  const unsigned lds0 = (unsigned)(uintptr_t)lp;
  const __amdgpu_buffer_rsrc_t wsr = __builtin_amdgcn_make_buffer_rsrc(ws, 0, WS_END, 0x00020000);
  LAS unsigned* cnt = (LAS unsigned*)(lp + L_CNT);
#define ARRIVE_NW(c) do { asm volatile("" ::: "memory"); __hip_atomic_store(cnt + (c) * 8 + w, 1u, __ATOMIC_RELAXED, __HIP_MEMORY_SCOPE_WORKGROUP); asm volatile("" ::: "memory"); } while (0)
#define PEEK(c) __hip_atomic_load(cnt + (c) * 8 + (lane & 7), __ATOMIC_RELAXED, __HIP_MEMORY_SCOPE_WORKGROUP)
#define AWAIT(c, v) do { if (__builtin_expect(!__all((v) != 0u), 0)) { _Pragma("unroll 1") for (unsigned sp_ = 0; sp_ < LSPIN_CAP; ++sp_) { if (__all(PEEK(c) != 0u)) break; __builtin_amdgcn_s_sleep(1); } } asm volatile("" ::: "memory"); } while (0)
#define POLL(off) __builtin_bit_cast(u64, __builtin_amdgcn_raw_buffer_load_b64(wsr, lane * 8, (off), 16))
#define FLAG(off) __builtin_amdgcn_raw_buffer_store_b64(__builtin_bit_cast(u32x2, tag), wsr, lane ? 0x7FFF0000u : 0u, (off), 16)

  const int T0 = rt * 128 + hq * 16;
  const int C0 = hq * 80;
  const int wlane = ((g >> 1) * 16 + (g & 1) * 4 + q4) * 160 + p4 * 40;
  u64 tag;
  unsigned o0[10];
#define KVP(s, slot, p) dma16s_sc1(ws + WS_KV + (s) * STEP_B + (p) * 1024, lane * 16, lds0 + (slot) * STEP_B + (p) * 1024)
#define KVL(s, slot) do { _Pragma("unroll") for (int j = 0; j < 11; j++) KVP(s, slot, j < 10 ? w3 + 4 * j : 40 + (w3 & 1)); } while (0)
#define KW(s, slot, j) do { if ((j) == 4) KVP(s, slot, w * 5 + 4); else { if ((j) == 0) dmag_open(lds0 + (slot) * STEP_B + w * 5120); dmag_sc1<(j)>(ws + WS_KV + (s) * STEP_B + w * 5120, lane * 16); } } while (0)
#define KX(s, slot) KVP(s, slot, 40 + (w & 1))
  const int w5 = w < 5 ? w : 4;
  unsigned wo16;
  unsigned woff[5];
  auto woff_init = [&]() { int ln = lane; asm volatile("" : "+v"(ln));
#pragma unroll
    for (int j = 0; j < 5; j++) { const int c = j * 64 + ln, rho = (c * 6554) >> 16, cc = c - rho * 10; const int k = (rho & ~12) | ((rho & 4) << 1) | ((rho & 8) >> 1); woff[j] = k * 1280 + cc * 16 + (4 - j) * 1024; }
    { const int c = w5 * 64 + ln, rho = (c * 6554) >> 16, cc = c - rho * 10; const int k = (rho & ~12) | ((rho & 4) << 1) | ((rho & 8) >> 1); wo16 = k * 1280 + cc * 16 + 4096; }
    asm volatile("" : "+v"(woff[0]), "+v"(woff[1]), "+v"(woff[2]), "+v"(woff[3]), "+v"(woff[4]), "+v"(wo16)); };
  if (w2 == 0) {
    u32x4 gf0, gf1, gf2, gf3, gf4, wpc;
    u32x2 ep = ld8_sc1_async(ws + WS_EPOCH);
    const int lx = w3 < 3 ? L_P0X0 + w3 * 5120 : L_P0X1;
    { const u16* wsrc = wkv + w3 * 3200;
#pragma unroll
      for (int j = 0; j < 5; j++) { const int n = j * 64 + lane, row = (n * 3277) >> 16, cc = n - row * 20; dma16s(x + T0 * DM + w3 * 160, row * 1280 + cc * 16, lds0 + lx + j * 1024); }
#pragma unroll
      for (int j = 0; j < 6; j++) dma16s(wsrc + j * 512, lane * 16, lds0 + L_P0W + w3 * 6400 + j * 1024);
      if (lane < 16) dma16s(wsrc + 6 * 512, lane * 16, lds0 + L_P0W + w3 * 6400 + 6 * 1024);
      const u16* gp = gatew + w3 * 160 + g * 8;
      gf0 = ld16_async(gp); gf1 = ld16_async(gp + 32); gf2 = ld16_async(gp + 64); gf3 = ld16_async(gp + 96); gf4 = ld16_async(gp + 128);
      wpc = ld16_async(wphi + hq * 1600 + (tid < 200 ? tid : 199) * 8);
    }
    woff_init();
    asm volatile("s_waitcnt vmcnt(0)" : "+v"(gf0), "+v"(gf1), "+v"(gf2), "+v"(gf3), "+v"(gf4), "+v"(wpc), "+v"(ep) :: "memory");
    tag = (((u64)(unsigned)__builtin_amdgcn_readfirstlane(ep.y) << 32) | (unsigned)__builtin_amdgcn_readfirstlane(ep.x)) + 1ull;
    { const LAS char* wb = lp + L_P0W + (w3 * 160 + g * 8 + q4) * 40;
      const LAS char* xb = lp + lx + l15 * 320 + g * 16;
      const bool gl = l15 == 4;
      f32x4 c0 = {}, c1 = {};
#define P0STEP(s, GF) { const u32x4 b = ldsr16(xb + (s) * 64); c0 = mfma16(trd2(wb + p4 * 8 + (s) * 1280, wb + p4 * 8 + (s) * 1280 + 160), b, c0); \
        u32x4 a = trd2(wb + 32 + (s) * 1280, wb + 32 + (s) * 1280 + 160); if (gl) a = GF; c1 = mfma16(a, b, c1); }
      P0STEP(0, gf0) P0STEP(1, gf1) P0STEP(2, gf2) P0STEP(3, gf3) P0STEP(4, gf4)
#undef P0STEP
      *(LAS f32x4*)(lp + L_RED + tid * 16) = c0;
      if (lane < 32) *(LAS f32x4*)(lp + L_RED1 + (w3 * 32 + lane) * 16) = c1; }
    f32x16 acc[5];
#pragma unroll
    for (int b = 0; b < 5; b++) acc[b] = (f32x16){};
    const int wm = g & 1, whd = p4;
    const int wo0 = wm * ST_W1 + (8 * h + 4 * wm + q4) * 160 + whd * 40, wo1 = wm * ST_W1 + (8 * h + 4 * (wm ^ 1) + q4) * 160 + whd * 40;
    const int xo = ST_X + (w3 * 32 + r) * 128 + ((h ^ ((r >> 1) & 7)) * 16);
    u32x4 fa[5], fb[5], xa, xb;
#define KSTEP(FN, XN, s, FC, XC) { XN = ldsr16(sb + (xo ^ ((s) * 32))); \
      _Pragma("unroll") for (int j = 0; j < 5; j++) { if (i > 0 || (s) > 0) acc[j] = mfma32(FC[j], XC, acc[j]); FN[j] = trd2(sb + wo0 + (s) * 2560 + j * 8, sb + wo1 + (s) * 2560 + j * 8); SBAR(); } }
#pragma unroll
    for (int i = 0; i < 10; i++) {
      BAR_L();
      if (i == 1 && role) {
        const unsigned qb = WS_QX + (rt * 8 + hq) * QX_B + w3 * 1280 + lane * 16;
        ST16(ONES, qb); ST16(ONES, qb + 5120); ST16(ONES, lane < 32 ? qb + 1024 + (lane >> 4) * (5120 - 256) : 0x7FFF0000u); }
      if (i == 4) { if (role) { WAIT_VM(0); FLAG(WS_FQ + ((rt * 8 + hq) * 4 + w3) * 8); }
                    if (tid < 200) *(LAS u32x4*)(lp + L_WPHI + tid * 16) = wpc; }
      const LAS char* sb = lp + L_BUF + ((i + 2) % 3) * STAGE_B;
      KSTEP(fa, xa, 0, fb, xb) KSTEP(fb, xb, 1, fa, xa) KSTEP(fa, xa, 2, fb, xb)
      { xb = ldsr16(sb + (xo ^ 96));
        acc[0] = mfma32(fa[0], xa, acc[0]); fb[0] = trd2(sb + wo0 + 7680, sb + wo1 + 7680); fb[1] = trd2(sb + wo0 + 7688, sb + wo1 + 7688); SBAR();
        acc[1] = mfma32(fa[1], xa, acc[1]); fb[2] = trd2(sb + wo0 + 7696, sb + wo1 + 7696); fb[3] = trd2(sb + wo0 + 7704, sb + wo1 + 7704); SBAR();
        acc[2] = mfma32(fa[2], xa, acc[2]); fb[4] = trd2(sb + wo0 + 7712, sb + wo1 + 7712); SBAR();
        acc[3] = mfma32(fa[3], xa, acc[3]); acc[4] = mfma32(fa[4], xa, acc[4]); SBAR(); }
    }
#pragma unroll
    for (int j = 0; j < 5; j++) acc[j] = mfma32(fb[j], xb, acc[j]);
#undef KSTEP
#define PK2(t, i, sc) pk(acc[t][i] * (sc), acc[t][(i) + 1] * (sc))
#define ROW(i, sc) const u32x4 ra = {PK2(0, i, sc), PK2(0, (i) + 2, sc), PK2(1, i, sc), PK2(1, (i) + 2, sc)}, rb = {PK2(2, i, sc), PK2(2, (i) + 2, sc), PK2(3, i, sc), PK2(3, (i) + 2, sc)}; const u32x2 rc = {PK2(4, i, sc), PK2(4, (i) + 2, sc)}
    { ROW(4, 1.0f);
      *(LAS u32x4*)(lp + L_HO + tid * 16) = ra; *(LAS u32x4*)(lp + L_HO + 4096 + tid * 16) = rb; *(LAS u32x2*)(lp + L_HO + 8192 + tid * 8) = rc; }
    BAR_L();
    const int qfo = (h * 4 + w3) * 1280;
    if (role) {
      { ROW(8, QSCALE); const unsigned qb = WS_QX + (rt * 8 + hq) * QX_B + qfo;
        ST16(((u32x4){nz(ra.x), nz(ra.y), nz(ra.z), nz(ra.w)}), qb + r * 16); ST16(((u32x4){nz(rb.x), nz(rb.y), nz(rb.z), nz(rb.w)}), qb + 512 + r * 16); ST8(((u32x2){nz(rc.x), nz(rc.y)}), qb + 1024 + r * 8); }
      { ROW(12, QSCALE); LAS char* d = lp + L_QL + qfo;
        *(LAS u32x4*)(d + r * 16) = ra; *(LAS u32x4*)(d + 512 + r * 16) = rb; *(LAS u32x2*)(d + 1024 + r * 8) = rc; }
    } else { LAS char* d = lp + L_VIM + (h * 128 + w3 * 32 + r) * 40;
#pragma unroll
      for (int t = 0; t < 5; t++) { *(LAS u32x2*)(d + t * 8) = (u32x2){PK2(t, 8, 1.0f), PK2(t, 10, 1.0f)}; *(LAS u32x2*)(d + 10240 + t * 8) = (u32x2){PK2(t, 12, 1.0f), PK2(t, 14, 1.0f)}; } }
#pragma unroll
    for (int t = 0; t < 5; t++) { o0[2 * t] = PK2(t, 0, 1.0f); o0[2 * t + 1] = PK2(t, 2, 1.0f); }
#undef ROW
#undef PK2
  } else {
    u32x2 ep = ld8_sc1_async(ws + WS_EPOCH);
    const bool second = w3 >> 1;
    const unsigned rs = (second && role) ? 1280u : 3840u;
    const char* wsrc = (second ? (role ? (const char*)(wq + C0) : (const char*)(wqkv + 1280 + C0)) : (const char*)(wqkv + (role ? 0 : 640) + C0)) + (w3 & 1) * 32 * rs;
    const char* xsrc = (const char*)(x + (rt * 128 + w3 * 32) * DM);
    int rot = (bi & 7) + 3 * ((bi >> 3) & 3); rot = rot >= 10 ? rot - 10 : rot;
    unsigned so[5], xoff[2];
#pragma unroll
    for (int j = 0; j < 5; j++) { const int c = j * 64 + lane, rho = (c * 6554) >> 16, cc = c - rho * 10; so[j] = (rho ^ (second ? 4 : 0)) * rs + cc * 16; }
#pragma unroll
    for (int j = 0; j < 2; j++) { const int row = j * 8 + (lane >> 3); xoff[j] = row * 1280 + (((lane & 7) ^ ((row >> 1) & 7)) * 16); }
    int bsel = 2, kc = rot;
    auto stage = [&]() {
      const unsigned lb = lds0 + L_BUF + bsel * STAGE_B;
      const char* bw = wsrc + kc * 64 * rs; const char* bxp = xsrc + kc * 128;
#pragma unroll
      for (int j = 0; j < 5; j++) { dma16s(bw, so[j], lb + (w3 * 5 + j) * 1024); if (j < 4) dma16s(bxp + (j >> 1) * 16 * 1280, xoff[j & 1], lb + ST_X + (w3 * 4 + j) * 1024); }
      bsel = bsel == 2 ? 0 : bsel + 1; kc = kc == 9 ? 0 : kc + 1;
    };
    stage(); stage();
    woff_init();
    asm volatile("s_waitcnt vmcnt(9)" : "+v"(ep) :: "memory");
    tag = (((u64)(unsigned)__builtin_amdgcn_readfirstlane(ep.y) << 32) | (unsigned)__builtin_amdgcn_readfirstlane(ep.x)) + 1ull;
#pragma unroll 1
    for (int i = 0; i < 8; i++) {
      WAIT_BAR(9);
      stage();
      if (i == 0 && w3 == 0) {
        const unsigned tb = WS_KV + (T0 >> 5) * TILE_B; const int s16 = hq & 1;
        if (!role) {
          const LAS f32x4* rp = (const LAS f32x4*)(lp + (h ? L_RED1 : L_RED)) + (h ? (g & 1) * 16 : g * 32) + l15;
          const int st = h ? 32 : 64, o2 = h ? 0 : 16;
          const f32x4 a = (rp[0] + rp[st]) + (rp[2 * st] + rp[3 * st]), b = (rp[o2] + rp[st + o2]) + (rp[2 * st + o2] + rp[3 * st + o2]);
          const u32x4 v = {pk(a[0], a[1]), pk(a[2], a[3]), pk(b[0], b[1]), pk(b[2], b[3])};
          if (lane < 32) ST16(v, tb + (g * 32 + s16 * 16 + l15) * 16);
          else if (lane < 48) ST8(((u32x2){v.x, v.y}), tb + 1024 + (s16 * 16 + l15) * 8);
          else { const float gv = 1.0f / (1.0f + __expf(-a[0])); __builtin_amdgcn_raw_buffer_store_b32(__float_as_uint(gv), wsr, WS_G + (T0 + l15) * 4, 0, 16); }
        } else if (lane < 44) {
          const int hh = lane >= 22, d = lane - hh * 22, dd = d < 20 ? d : 0, st = dd >= 16 ? 128 : 256;
          const LAS float* rp = (const LAS float*)(lp + (dd >= 16 ? L_RED1 : L_RED)) + (((dd & 15) >> 2) * 16 + 4 * hh) * 4 + (dd & 3);
          float e[8];
#pragma unroll
          for (int j = 0; j < 8; j++) { const int tk = (8 * (j >> 2) + (j & 3)) * 4; e[j] = (rp[tk] + rp[tk + st]) + (rp[tk + 2 * st] + rp[tk + 3 * st]); }
          u32x4 v = {pk(e[0], e[1]), pk(e[2], e[3]), pk(e[4], e[5]), pk(e[6], e[7])};
          if (d == 20) v = (u32x4){0x3F803F80u, 0x3F803F80u, 0x3F803F80u, 0x3F803F80u};
          if (d == 21) v = (u32x4){0u, 0u, 0u, 0u};
          ST16(v, tb + 1280 + (s16 * 2 + hh) * VB + d * 16);
        }
      }
      if (i == 2) { if (w3 == 0) FLAG(WS_FKV + blockIdx.x * 8);
                    if (w3 >= 2) *(LAS u32x4*)(lp + L_ZERO + (tid - 384) * 16) = (u32x4){0u, 0u, 0u, 0u}; }
    }
    u64 fa, fb, fc, fd, fe;
    { gu64* f = (gu64*)(ws + WS_FKV) + lane;
      fa = __hip_atomic_load(f, RLX_AGENT); fb = __hip_atomic_load(f + 64, RLX_AGENT); fc = __hip_atomic_load(f + 128, RLX_AGENT); fd = __hip_atomic_load(f + 192, RLX_AGENT);
      fe = __hip_atomic_load((gu64*)(ws + WS_FQ) + (rt * 8 + hq) * 4 + p4, RLX_AGENT); }
    WAIT_BAR(14);
    asm volatile("" : "+v"(fa), "+v"(fb), "+v"(fc), "+v"(fd), "+v"(fe));
    const bool kvok = __all((fa == tag) & (fb == tag) & (fc == tag) & (fd == tag) & (fe == tag));
    asm volatile("" ::: "memory");
    BAR_L();
    if (__builtin_expect(!kvok, 0)) { wait_flags<256>(ws, WS_FKV, tag, lane); wait_flags<4>(ws, WS_FQ + (rt * 8 + hq) * 32, tag, lane); }
    KVL(0, 0);
    if (blockIdx.x == 0 && w3 == 0) FLAG(WS_EPOCH);
    BAR_L();
    const int ht = tid - 256;
    const u32x4 a = ldsr16(lp + L_HO + ht * 16), b = ldsr16(lp + L_HO + 4096 + ht * 16); const u32x2 c = ldsr8(lp + L_HO + 8192 + ht * 8);
    o0[0] = a.x; o0[1] = a.y; o0[2] = a.z; o0[3] = a.w; o0[4] = b.x; o0[5] = b.y; o0[6] = b.z; o0[7] = b.w; o0[8] = c.x; o0[9] = c.y;
  }
  u32x4 ph[5];
  { const LAS char* pb = ((p4 & 1) == h) ? lp + L_WPHI + (2 * w2 + h) * 800 + q4 * 40 + (2 * (g & 1) + (p4 >> 1)) * 8 : lp + L_ZERO;
    const u32x4 b0 = {o0[0], o0[1], o0[2], o0[3]}, b1 = {o0[4], o0[5], o0[6], o0[7]}, b2 = {o0[8], o0[9], 0u, 0u};
    const u32x2 c0 = trd(pb + 640), c1 = trd(pb + 672);
    f32x16 P0 = {}, P1 = {};
    P0 = mfma32(trd2(pb, pb + 160), b0, P0); P1 = mfma32(trd2(pb + 32, pb + 192), b0, P1);
    P0 = mfma32(trd2(pb + 320, pb + 480), b1, P0); P1 = mfma32(trd2(pb + 352, pb + 512), b1, P1);
    P0 = mfma32((u32x4){c0.x, c0.y, 0u, 0u}, b2, P0); P1 = mfma32((u32x4){c1.x, c1.y, 0u, 0u}, b2, P1);
    KX(1, 1); KW(1, 1, 0); SBAR();
    f32x2 p[10];
#pragma unroll
    for (int f = 0; f < 10; f++) p[f] = f < 8 ? (f32x2){P0[(2 * f) & 15], P0[(2 * f + 1) & 15]} : (f32x2){P1[(2 * f) & 3], P1[(2 * f + 1) & 3]};
    float mx = fmaxf(p[0][0], p[0][1]), mn = fminf(p[0][0], p[0][1]);
#pragma unroll
    for (int f = 1; f < 10; f++) { mx = fmaxf(fmaxf(mx, p[f][0]), p[f][1]); mn = fminf(fminf(mn, p[f][0]), p[f][1]); }
    const f32x2 ma = {-mx * LOG2E, -mx * LOG2E}, mb = {mn * LOG2E, mn * LOG2E}, ca_ = {LOG2E, LOG2E}, cb_ = {-LOG2E, -LOG2E};
    f32x2 ea[10], eb[10], sa = {0.f, 0.f}, sb = {0.f, 0.f};
#pragma unroll
    for (int f = 0; f < 10; f++) { if (f % 2 == 0 && f < 8) { SBAR(); if (f == 0) KW(1, 1, 1); if (f == 2) KW(1, 1, 2); if (f == 4) KW(1, 1, 3); if (f == 6) KW(1, 1, 4); SBAR(); }
      const f32x2 xa = __builtin_elementwise_fma(p[f], ca_, ma), xb = __builtin_elementwise_fma(p[f], cb_, mb);
      ea[f] = (f32x2){ex2(xa[0]), ex2(xa[1])}; eb[f] = (f32x2){ex2(xb[0]), ex2(xb[1])}; sa += ea[f]; sb += eb[f]; }
    const float ia = __builtin_amdgcn_rcpf(sa[0] + sa[1]), ib = __builtin_amdgcn_rcpf(sb[0] + sb[1]);
    unsigned o[20];
#pragma unroll
    for (int f = 0; f < 10; f++) { const f32x2 ya = ea[f] * ia, yb = eb[f] * ib; o[f] = pk(ya[0], ya[1]); o[10 + f] = pk(yb[0], yb[1]); }
#pragma unroll
    for (int c = 0; c < 5; c++) ph[c] = (u32x4){o[4 * c], o[4 * c + 1], o[4 * c + 2], o[4 * c + 3]};
    asm volatile("" : "+v"(ph[0]), "+v"(ph[1]), "+v"(ph[2]), "+v"(ph[3]), "+v"(ph[4]));
  }
  u32x4 q1A, q1B; u32x2 q2A, q2B;
  const int qo1 = (g & 1) * 5120 + (h * 32 + l15) * 16, qo2 = (g & 1) * 5120 + 1024 + l15 * 8;
  if (role) {
    WAIT_BAR(0);
    const LAS char* qa = lp + L_QL + w3 * 1280;
    q1A = ldsr16(qa + qo1 + (w2 ^ 1) * 256); q1B = ldsr16(qa + qo1 + w2 * 256); q2A = ldsr8(qa + qo2 + (w2 ^ 1) * 128); q2B = ldsr8(qa + qo2 + w2 * 128);
    ARRIVE_NW(0);
  } else {
    const unsigned qb = WS_QX + (rt * 8 + hq) * QX_B + w3 * 1280;
    q1A = LD16S(qo1, qb + (w2 ^ 1) * 256); q1B = LD16S(qo1, qb + w2 * 256); q2A = LD8S(qo2, qb + (w2 ^ 1) * 128); q2B = LD8S(qo2, qb + w2 * 128);
    { LAS char* d = lp + L_PHK + ((2 * w2 + h) * 128 + w3 * 32 + r) * 80;
#pragma unroll
      for (int c = 0; c < 5; c++) *(LAS u32x4*)(d + c * 16) = ph[c]; }
    WAIT_BAR(4);
#pragma unroll 1
    for (unsigned spin = 0; spin < SPIN_CAP; ++spin) {
      if (!(missing(q1A) | missing(q1B) | missing((u32x4){q2A.x, q2A.y, q2B.x, q2B.y}))) break;
      __builtin_amdgcn_s_sleep(1);
      asm volatile("" ::: "memory");
      q1A = LD16S(qo1, qb + (w2 ^ 1) * 256); q1B = LD16S(qo1, qb + w2 * 256); q2A = LD8S(qo2, qb + (w2 ^ 1) * 128); q2B = LD8S(qo2, qb + w2 * 128);
    }
  }
  const int vr = r <= 21 ? r : 21;
  const int ok1 = lane * 16, ok2 = h ? (1280 + 20 * 16) : (1024 + r * 8), ov = 1280 + h * VB + vr * 16;
  const f32x16 Z = {};
  if (h) { q2A = (u32x2){0u, 0u}; q2B = (u32x2){0u, 0u}; }
  { const LAS char* t0 = lp;
    const u32x4 k1 = ldsr16(t0 + ok1); const u32x2 k2 = ldsr8(t0 + ok2);
    f32x16 sa = mfma32(k1, q1A, Z); sa = mfma32k8(k2, q2A, sa);
    f32x16 sb = mfma32(k1, q1B, Z); sb = mfma32k8(k2, q2B, sb);
    float ma = sa[0], mb = sb[0];
#pragma unroll
    for (int i = 1; i < 16; i++) { ma = fmaxf(ma, sa[i]); mb = fmaxf(mb, sb[i]); }
    ma = half_swap_max(ma); mb = half_swap_max(mb);
    const float mah = __uint_as_float(__float_as_uint(ma) & 0xFFFF0000u), mbh = __uint_as_float(__float_as_uint(mb) & 0xFFFF0000u);
    if (h) { q2A.x = pk(-mah, mah - ma); q2B.x = pk(-mbh, mbh - mb); } }
  f32x16 OA = {}, OB = {}, SA, SB;
  u32x4 k1, v1, v2, v2n, pA1, pA2, pB1, pB2; u32x2 k2;
  float e0, e1, e2, e3;
  const LAS char* c0p = lp + w2 * CH_B + ok1; const LAS char* c1p = lp + w2 * CH_B + ok2; const LAS char* c2p = lp + w2 * CH_B + ov;
  k1 = ldsr16(c0p); k2 = ldsr8(c1p);
  SA = mfma32(k1, q1A, Z); SA = mfma32k8(k2, q2A, SA);
  v1 = ldsr16(c2p); v2 = v1;
  e0 = e1 = e2 = e3 = 0.f; pB2 = (u32x4){0u, 0u, 0u, 0u};
#define G(MF, LD, H, M, C0_, C1_, X, S_, i) MF; LD; H; SBAR(); C0_; C1_; SBAR(); X##0 = ex2(S_[i]); X##1 = ex2(S_[i + 1]); SBAR(); M; SBAR(); X##2 = ex2(S_[i + 2]); X##3 = ex2(S_[i + 3]); \
    asm volatile("" : "+v"(X##0), "+v"(X##1), "+v"(X##2), "+v"(X##3)); SBAR();
#define TILEF(TN, V2, F) { float a0, a1, a2, a3; \
    G(SB = mfma32(k1, q1B, Z),    k1 = ldsr16(d0p + (TN)), F(0), F(8),  pB2[2] = pk(e0, e1), pB2[3] = pk(e2, e3), a, SA, 0) \
    G(OB = mfma32(v2, pB2, OB),   V2,                      F(1), F(9),  pA1[0] = pk(a0, a1), pA1[1] = pk(a2, a3), e, SA, 4) \
    G(SB = mfma32k8(k2, q2B, SB), k2 = ldsr8(d1p + (TN)),  F(2), F(10), pA1[2] = pk(e0, e1), pA1[3] = pk(e2, e3), a, SA, 8) \
    G(OA = mfma32(v1, pA1, OA),   (void)0,                 F(3), F(11), pA2[0] = pk(a0, a1), pA2[1] = pk(a2, a3), e, SA, 12) \
    G(SA = mfma32(k1, q1A, Z),    (void)0,                 F(4), F(12), pA2[2] = pk(e0, e1), pA2[3] = pk(e2, e3), a, SB, 0) \
    G(OA = mfma32(v2, pA2, OA),   (void)0,                 F(5), F(13), pB1[0] = pk(a0, a1), pB1[1] = pk(a2, a3), e, SB, 4) \
    G(SA = mfma32k8(k2, q2A, SA), (void)0,                 F(6), F(14), pB1[2] = pk(e0, e1), pB1[3] = pk(e2, e3), a, SB, 8) \
    G(OB = mfma32(v1, pB1, OB),   v1 = ldsr16(d2p + (TN)), F(7), F(15), pB2[0] = pk(a0, a1), pB2[1] = pk(a2, a3), e, SB, 12) }
#define TILE(TB, TN, V2, H0, H1, H2) { auto f_ = [&](int n) { if (n == 3) { H0; } if (n == 4) { H1; } if (n == 5) { H2; } }; TILEF(TN, V2, f_) }
#define TLF(t, F) TILEF(((t) + 1) * TILE_B, v2 = ldsr16(d2p + (t) * TILE_B + 2 * VB), F)
#define NOP (void)0
  const LAS char *d0p = c0p, *d1p = c1p, *d2p = c2p;
#define TL(sl, t, H0, H1, H2) TILE(0, ((t) + 1) * TILE_B, v2 = ldsr16(d2p + (t) * TILE_B + 2 * VB), H0, H1, H2)
#define TL6(sl, H0, H1) TL(sl, 6, H0, H1, v2n = ldsr16(d2p + 7 * TILE_B + 2 * VB))
#define TL7(sl, sn, H0, H1, H2) d0p += ((sn) - (sl)) * STEP_B; d1p += ((sn) - (sl)) * STEP_B; d2p += ((sn) - (sl)) * STEP_B; asm volatile("" : "+v"(d0p), "+v"(d1p), "+v"(d2p)); TILE(0, 0, v2 = v2n, H0, H1, H2)
#define WOG(wo, gr, j, dst) do { if ((j) == 4) WOP(wo, gr, 4, dst); else { if ((j) == 0) dmag_open(lds0 + (dst)); dmag<(j)>((wo) + C0 + (gr) * 32 * 640 - 2048, woff[j]); } } while (0)
#define WOP(wo, gr, j, dst) dma16s((wo) + C0 + (gr) * 32 * 640 - (4 - (j)) * 512, woff[j], lds0 + (dst) + (j) * 1024)
#define WA(gr, j) WOG(woA, gr, j, L_WO + (gr) * 5120)
  const int wx3 = w3 ? w3 : 1;
  const u16* wxs = (w2 ? woB : woA) + (16 + wx3) * 32 * 640;
  const int wxd = w2 ? 145408 + (wx3 - 1) * 5120 : L_WO + (16 + wx3) * 5120;
#define WX(j) WOG(wxs, 0, j, wxd)
#define PRIO(n) __builtin_amdgcn_s_setprio(n)
  u32x4 ones = ONES; if (role) asm volatile("" : "+v"(ones));
#define FILL(j) do { const int n_ = w * 3 + ((j) >> 1); ST16S(ones, ok1, (((j) & 1) ? WS_OB : WS_OA) + (rt * 8 + hq) * OT_B + (n_ < 20 ? n_ : 19) * 1024); } while (0)
#define CLR_VM() __builtin_amdgcn_s_waitcnt(0x0F70)
  const bool red = !role && !w2;
  f32x4 part[4]; u64 fp0, fp1;
  auto red_load = [&]() {
    asm volatile("" : "+v"(fp0), "+v"(fp1));
    if (__builtin_expect(!__all((fp0 == tag) & (fp1 == tag)), 0)) wait_flags<128>(ws, WS_FPART + hq * 1024, tag, lane);
    asm volatile("" ::: "memory");
#pragma unroll
    for (int j = 0; j < 4; j++) part[j] = __builtin_bit_cast(f32x4, LD16S(g * (4 * PART_B) + l15 * 16, WS_PART + (hq * 16 + j) * PART_B + rt * 960 + w3 * 240));
  };
  auto red_store = [&]() {
    f32x4 a = (part[0] + part[1]) + (part[2] + part[3]);
#pragma unroll
    for (int i = 0; i < 4; i++) { auto r16 = __builtin_amdgcn_permlane16_swap(__float_as_uint(a[i]), __float_as_uint(a[i]), false, false); const float t = __uint_as_float(r16[0]) + __uint_as_float(r16[1]);
      auto r32 = __builtin_amdgcn_permlane32_swap(__float_as_uint(t), __float_as_uint(t), false, false); a[i] = __uint_as_float(r32[0]) + __uint_as_float(r32[1]); }
    const int hd = rt >> 2, u = (rt & 3) * 60 + w3 * 15 + lane, hi = u >= 160, fq = hi ? (u - 160) >> 3 : u >> 4, di = hi ? u & 7 : u & 15;
    __builtin_amdgcn_raw_buffer_store_b64((u32x2){pk(a[0], a[1]), pk(a[2], a[3])}, wsr, lane < 15 ? (hi * 5 + (fq >> 1)) * 512 + di * 16 + (fq & 1) * 8 : 0x7FFF0000u, WS_KVR + hq * KVR_B + (hd >> 1) * 5120 + (hd & 1) * 256, 16);
  };
  u32x4 ka[10]; u64 fkr; float gta; unsigned kb; const unsigned kbs = WS_KVR + hq * KVR_B + w2 * 5120;
  auto oa_load = [&]() {
    asm volatile("" : "+v"(fkr));
    if (__builtin_expect(!__all(fkr == tag), 0)) wait_flags<64>(ws, WS_FKVR + hq * 512, tag, lane);
    asm volatile("" ::: "memory");
    kb = (((r >> 2) & 1) == h) ? (h * 16 + (r & 3) + 4 * (r >> 3)) * 16 : 0x7FFF0000u;
    ka[0] = LD16S(kb, kbs); ka[5] = LD16S(kb, kbs + 2560); ka[1] = LD16S(kb, kbs + 512); ka[6] = LD16S(kb, kbs + 3072);
    gta = __uint_as_float(__builtin_amdgcn_raw_buffer_load_b32(wsr, WS_G + (rt * 128 + w3 * 32 + r) * 4, 0, 16));
  };
  u32x4 jf[16]; f32x4 jc[3];
  const LAS char* jab = lp + L_PHK + ((w >> 1) * 128 + g * 8 + q4) * 80 + p4 * 8;
  const LAS char* jbb = lp + L_VIM + ((w >> 1) * 128 + g * 8 + q4) * 40 + (w & 1) * 32 + p4 * 8;
  auto kv_rd = [&](int n) { const int ks = n >> 2, j = n & 3;
    jf[n] = j ? trd2(jab + ks * 2560 + (j - 1) * 32, jab + ks * 2560 + 320 + (j - 1) * 32) : trd2(jbb + ks * 1280, jbb + ks * 1280 + 160); };
  auto kv_mm = [&](int m) { const int ks = m / 3, ft = m % 3;
    if (ft == 0 && (w & 1) && l15 == 4) jf[ks * 4] = (u32x4){0x3F803F80u, 0x3F803F80u, 0x3F803F80u, 0x3F803F80u};
    jc[ft] = mfma16(jf[ks * 4 + 1 + ft], jf[ks * 4], ks ? jc[ft] : (f32x4){0.f, 0.f, 0.f, 0.f}); };
  auto kv_st = [&](int ft) { const int dt = w & 1;
    const unsigned vo = ((!dt || l15 < 8) && (ft < 2 || g < 2)) ? (dt ? g * 128 : g * 256) + l15 * 16 : 0x7FFF0000u;
    ST16S(__builtin_bit_cast(u32x4, jc[ft]), vo, WS_PART + (hq * 16 + rt) * PART_B + (w >> 1) * 3840 + (dt ? 2560 + ft * 512 : ft * 1024)); };
  f32x16 oa0, oa1; u32x4 z0, z1, z2;
  LAS char* otb = lp + L_WO + (8 + w) * 5120;
  auto oa_mm = [&](int m) { const int s = m >> 1; if (m & 1) oa1 = mfma32(ka[5 + s], ph[s], s ? oa1 : Z); else oa0 = mfma32(ka[s], ph[s], s ? oa0 : Z); };
  auto oa_norm = [&]() { const float inv = __builtin_amdgcn_rcpf(oa1[4] + 1e-6f) * gta;
    LAS char* ar = otb + (r >> 4) * 1280 + l15 * 16; LAS char* ae = ar + h * 520; LAS char* ao = ar + 8 + h * 760;
#pragma unroll
    for (int t = 0; t < 5; t++) {
      const u32x2 v = t < 4 ? (u32x2){nz(pk(oa0[(4 * t) & 15] * inv, oa0[(4 * t + 1) & 15] * inv)), nz(pk(oa0[(4 * t + 2) & 15] * inv, oa0[(4 * t + 3) & 15] * inv))} : (u32x2){nz(pk(oa1[0] * inv, oa1[1] * inv)), nz(pk(oa1[2] * inv, oa1[3] * inv))};
      *(LAS u32x2*)(((t & 1) ? ao : ae) + (t >> 1) * 256) = v; }
    z0 = ldsr16(otb + ok1); z1 = ldsr16(otb + 1280 + ok1); z2 = ldsr16(ar + 1024); };
  auto oa_st = [&]() { const unsigned oa = WS_OA + (rt * 8 + w3 * 2) * OT_B + (hq * 2 + w2) * 1280;
    ST16S(z0, ok1, oa); ST16S(z1, ok1, oa + OT_B); ST16S(z2, h ? 0x7FFF0000u : (r >> 4) * OT_B + l15 * 16, oa + 1024); };
  unsigned cpk;
  PRIO(3);
  if (role) { TL(0, 0, FILL(0), NOP, NOP) TL(0, 1, FILL(1), NOP, NOP) TL(0, 2, FILL(2), NOP, NOP) }
  else {
    auto f0 = [&](int n) { if (n < 8) kv_rd(n); if (n >= 13) kv_mm(n - 13); };
    auto f1 = [&](int n) { if (n < 8) kv_rd(8 + n); if (n == 7) ARRIVE_NW(0); if (n >= 9 && n < 12) kv_mm(n - 6); if (n >= 13) kv_mm(n - 7); };
    auto f2 = [&](int n) { if (n >= 9 && n < 12) kv_mm(n); if (n >= 5 && n < 8) kv_st(n - 5); };
    TLF(0, f0) TLF(1, f1) TLF(2, f2)
  }
  TL(0, 3, dma4s((w2 ? woB : woA) + C0 + (((rt & 7) * 2 + role) * 40 + w3 * 10) * 640, (l15 < 10 ? l15 : 9) * 1280 + (g & 1) * 156, lds0 + 160768)        , NOP, NOP)
  TL(0, 4, NOP, NOP, if (role) FILL(3))
  PRIO(2);
  TL(0, 5, if (!role) { WAIT_VM(1); FLAG(WS_FPART + ((hq * 16 + rt) * 8 + w) * 8); }, NOP, cpk = PEEK(0))
  AWAIT(0, cpk);
  asm volatile("" ::: "memory");
  TL6(0, KX(2, 2), KW(2, 2, 0))
  TL7(0, 1, KW(2, 2, 1), KW(2, 2, 2), KW(2, 2, 3))
  TL(1, 0, KW(2, 2, 4), if (role) FILL(4), if (!role) { fp0 = POLL(WS_FPART + hq * 1024); fp1 = POLL(WS_FPART + hq * 1024 + 512); })
  TL(1, 1, if (role) { WX(0); WX(1); }, if (role) FILL(5), if (role) { WX(2); WX(3); })
  TL(1, 2, if (role) WX(4), NOP, if (red) red_load())
  PRIO(1);
  TL(1, 3, NOP, NOP, NOP)
  TL(1, 4, NOP, if (!role) CLR_VM(), if (red) red_store())
  TL(1, 5, NOP, NOP, NOP)
  PRIO(0);
  TL6(1, if (!role) { WAIT_VM(0); FLAG((w2 ? WS_SPARE : WS_FKVR) + ((hq * 16 + rt) * 4 + w3) * 8); }, NOP)
  if (!role) WAIT_BAR(1); else WAIT_BAR(0);
  PRIO(3);
  if (role) FLAG(WS_FOA + ((rt * 8 + hq) * 8 + w) * 8);
  u64 foa;
  TL7(1, 2, KX(3, 0), KW(3, 0, 0), KW(3, 0, 1))
  TL(2, 0, KW(3, 0, 2), KW(3, 0, 3), KW(3, 0, 4)) TL(2, 1, WA(w, 0), WA(w, 1), WA(w, 2))
  u32x4 wbr[11];
  const char* wbs = (const char*)(woB + C0 + w * 32 * 640) - 4096;
#define WB1(n) wbr[n] = *(const GAS u32x4*)(wbs + ((n) / 5) * (8 * 32 * 1280) + woff[(n) % 5] + ((n) % 5) * 1024)
#define WB2(j) do { WB1(j); WB1(5 + (j)); } while (0)
#define WA16() dma16s(woA + C0 + 16 * 32 * 640 - 2048, wo16, lds0 + L_WO + 16 * 5120 + w5 * 1024)
#define WB16() wbr[10] = *(const GAS u32x4*)((const char*)(woB + C0 + 16 * 32 * 640) - 4096 + wo16)
  auto chk = [&]() { asm volatile("" : "+v"(foa));
    if (__builtin_expect(!__all(foa == tag), 0)) wait_flags<64>(ws, WS_FOA + rt * 512, tag, lane);
    asm volatile("" ::: "memory"); };
  if (role) {
    TL(2, 2, WA(w, 3), WA(w, 4), NOP)
    PRIO(2);
    TL(2, 3, (fkr = POLL(WS_FKVR + hq * 512), foa = POLL(WS_FOA + rt * 512)), NOP, NOP)
    TL(2, 4, NOP, { chk(); oa_load(); }, { ka[2] = LD16S(kb, kbs + 1024); ka[7] = LD16S(kb, kbs + 3584); ka[3] = LD16S(kb, kbs + 1536); })
    PRIO(1);
    TL(2, 5, { ka[8] = LD16S(kb, kbs + 4096); ka[4] = LD16S(kb, kbs + 2048); ka[9] = LD16S(kb, kbs + 4608); }, NOP, NOP)
    PRIO(0);
    { auto f = [&](int n) { if (n == 5) v2n = ldsr16(d2p + 7 * TILE_B + 2 * VB); if (n >= 8 && n != 10 && n != 14) oa_mm(n < 10 ? n - 8 : (n < 14 ? n - 9 : 5)); }; TLF(6, f) }
  } else {
    TL(2, 2, WA(w, 3), WA(w, 4), WX(0))
    PRIO(2);
    TL(2, 3, { WX(1); WX(2); }, { WX(3); WX(4); }, foa = POLL(WS_FOA + rt * 512))
    TL(2, 4, NOP, NOP, NOP)
    PRIO(1);
    TL(2, 5, chk(), WB1(0), WB1(5))
    PRIO(0);
    TL6(2, WB1(1), WB1(6))
  }
  BAR_L();
  PRIO(3);
  if (role) {
    d0p -= 2 * STEP_B; d1p -= 2 * STEP_B; d2p -= 2 * STEP_B; asm volatile("" : "+v"(d0p), "+v"(d1p), "+v"(d2p));
    { auto f = [&](int n) { if (n == 8 || n == 9) oa_mm(n - 2); if (n == 11 || n == 12) oa_mm(n - 3); if (n == 5) CLR_VM(); if (n == 6) WA16(); }; TILEF(0, v2 = v2n, f) }
    TL(0, 0, oa_norm(), NOP, oa_st())
    TL(0, 1, WA(8 + w, 0), WA(8 + w, 1), WA(8 + w, 2))
    TL(0, 2, WA(8 + w, 3), WA(8 + w, 4), WB2(0))
    TL(0, 3, WB2(1), NOP, WB2(2))
    PRIO(2);
    TL(0, 4, WB2(3), NOP, WB2(4))
  } else {
    TL7(2, 0, WA16(), WA(8 + w, 0), WA(8 + w, 1))
    TL(0, 0, WA(8 + w, 2), WA(8 + w, 3), WA(8 + w, 4))
    TL(0, 1, WB1(2), NOP, WB1(7)) TL(0, 2, WB1(3), NOP, WB1(8)) TL(0, 3, WB1(4), NOP, WB1(9))
    PRIO(2);
    TL(0, 4, NOP, NOP, NOP)
  }
  ARRIVE_NW(1);
  const int tp = w & 1, kq = w >> 1;
  const unsigned ofr = (rt * 8 + role * 4 + tp * 2) * OT_B + kq * 5120;
  u32x4 of[2][5]; float gtb;
#define OFL(u) do { _Pragma("unroll") for (int i = 0; i < 5; i++) of[u][i] = LD16S(ok1, WS_OA + ofr + (u) * OT_B + i * 1024); } while (0)
#define OF2(i) do { of[0][i] = LD16S(ok1, WS_OA + ofr + (i) * 1024); of[1][i] = LD16S(ok1, WS_OA + ofr + OT_B + (i) * 1024); } while (0)
  TL(0, 5, WB16(), gtb = __uint_as_float(__builtin_amdgcn_raw_buffer_load_b32(wsr, WS_G + (rt * 128 + w3 * 32 + w2 * 16 + l15) * 4, 0, 16)), OF2(0))
  PRIO(1);
  TL6(0, OF2(1), OF2(2))
  PRIO(0);
  LAS f32x4* xb = (LAS f32x4*)(lp + (w3 >> 1) * CH_B) + (w3 & 1) * (64 * 6) + lane;
  { auto f = [&](int n) { if (n == 0) cpk = PEEK(1); if (n == 3) OF2(3); if (n == 4) OF2(4);
      if (n == 15) { AWAIT(1, cpk);
#pragma unroll
        for (int c = 0; c < 3; c++) xb[(w2 * 3 + c) * 64] = (f32x4){OA[4 * c], OA[4 * c + 1], OA[4 * c + 2], OA[4 * c + 3]}; } };
    TILEF(0, v2 = v2n, f) }
  pB2[2] = pk(e0, e1); pB2[3] = pk(e2, e3);
  OB = mfma32(v2, pB2, OB);
#undef TL
#undef TL6
#undef TL7
#undef TILE
#undef G
  u32x4 wf[5][5];
#define WFA(i) do { const LAS char* wb_ = lp + L_WO + (kq * 5 + (i)) * 5120 + wlane; _Pragma("unroll") for (int t = 0; t < 5; t++) wf[i][t] = trd2(wb_ + t * 8, wb_ + 1280 + t * 8); } while (0)
#define WFB(i) do { const int gr_ = kq * 5 + (i); const LAS char* wb_ = lp + (gr_ < 17 ? L_WO + gr_ * 5120 : 145408 + (gr_ - 17) * 5120) + wlane; _Pragma("unroll") for (int t = 0; t < 5; t++) wf[i][t] = trd2(wb_ + t * 8, wb_ + 1280 + t * 8); } while (0)
  { asm volatile("s_waitcnt vmcnt(18) lgkmcnt(0)\n\ts_barrier" ::: "memory");
    f32x16 O = OB;
#pragma unroll
    for (int c = 0; c < 3; c++) { const f32x4 t = xb[((w2 ^ 1) * 3 + c) * 64]; O[4 * c] += t[0]; O[4 * c + 1] += t[1]; O[4 * c + 2] += t[2]; O[4 * c + 3] += t[3]; }
    SBAR(); WFA(0); WFA(1); SBAR();
    float l = from_upper(O[8]);
    const float amax = (((fabsf(O[0]) + fabsf(O[1])) + (fabsf(O[2]) + fabsf(O[3]))) + ((fabsf(O[4]) + fabsf(O[5])) + (fabsf(O[6]) + fabsf(O[7])))) + ((fabsf(O[8]) + fabsf(O[9])) + (fabsf(O[10]) + fabsf(O[11])));
    const bool good = (l >= 0x1p-30f) && (l <= 0x1p100f) && (amax <= 0x1p126f);
    if (__builtin_expect(!__all(good), 0)) {
      const u32x4 q1 = q1B; const u32x2 q2 = h ? (u32x2){0u, 0u} : q2B;
      float m = -INFINITY;
#pragma unroll 1
      for (int t = 0; t < 64; t++) {
        f32x16 s = mfma32(LD16(WS_KV + t * TILE_B + ok1), q1, Z); s = mfma32k8(LD8(WS_KV + t * TILE_B + ok2), q2, s);
#pragma unroll
        for (int i = 0; i < 16; i++) m = fmaxf(m, s[i]);
      }
      m = half_swap_max(m);
      O = Z;
#pragma unroll 1
      for (int t = 0; t < 64; t++) {
        f32x16 s = mfma32(LD16(WS_KV + t * TILE_B + ok1), q1, Z); s = mfma32k8(LD8(WS_KV + t * TILE_B + ok2), q2, s);
#pragma unroll
        for (int i = 0; i < 16; i++) s[i] = ex2(s[i] - m);
        O = mfma32(LD16(WS_KV + t * TILE_B + ov), (u32x4){pk(s[0], s[1]), pk(s[2], s[3]), pk(s[4], s[5]), pk(s[6], s[7])}, O);
        O = mfma32(LD16(WS_KV + t * TILE_B + ov + 2 * VB), (u32x4){pk(s[8], s[9]), pk(s[10], s[11]), pk(s[12], s[13]), pk(s[14], s[15])}, O);
      }
      l = from_upper(O[8]);
    }
    const float inv = __builtin_amdgcn_rcpf(l) * (1.0f - gtb);
    LAS char* tb = lp + w2 * CH_B + 12288 + w3 * 1536;
    const int uq = (g & 1) * 5 + h;
    LAS char* t0 = tb + (uq >> 1) * 256 + (uq & 1) * 8 + l15 * 16; LAS char* t2 = h ? tb + 1280 + (g & 1) * 8 + l15 * 16 : t0 + 512;
#pragma unroll
    for (int c = 0; c < 3; c++) *(LAS u32x2*)(c < 2 ? t0 + c * 256 : t2) = (u32x2){nz(pk(O[4 * c] * inv, O[4 * c + 1] * inv)), nz(pk(O[4 * c + 2] * inv, O[4 * c + 3] * inv))};
    const unsigned ob = WS_OB + (rt * 8 + w3 * 2 + w2) * OT_B + hp * 1280;
    const u32x4 y0 = ldsr16(tb + ok1), y1 = ldsr16(tb + 1024 + l15 * 16);
    ST16S(y0, ok1, ob); ST16S(y1, lane < 16 ? ok1 : 0x7FFF0000u, ob + 1024);
    SBAR();
  }
  auto of_missing = [&]() { u32x4 mm = of[0][0];
#pragma unroll
    for (int j = 1; j < 10; j++) mm = __builtin_elementwise_max(mm, of[j / 5][j % 5]);
    return missing(mm); };
  if (__builtin_expect(of_missing(), 0)) {
#pragma unroll 1
    for (unsigned spin = 0; spin < SPIN_CAP; ++spin) {
      asm volatile("" ::: "memory");
      OFL(0); OFL(1);
      if (!of_missing()) break;
      __builtin_amdgcn_s_sleep(1);
    }
  }
  f32x4 c[2][5];
#define MM(u, i, first) do { _Pragma("unroll") for (int t = 0; t < 5; t++) c[u][t] = mfma16(wf[i][t], of[u][i], (first) ? (f32x4){0.f, 0.f, 0.f, 0.f} : c[u][t]); SBAR(); } while (0)
#define OBL(u, i) of[u][i] = LD16S(ok1, WS_OB + ofr + (u) * OT_B + (i) * 1024)
#define MMR(u, i, first, j, ob_) do { const LAS char* wb_ = lp + (ob_) + wlane; _Pragma("unroll") for (int t = 0; t < 5; t++) { \
      c[u][t] = mfma16(wf[i][t], of[u][i], (first) ? (f32x4){0.f, 0.f, 0.f, 0.f} : c[u][t]); wf[j][t] = trd2(wb_ + t * 8, wb_ + 1280 + t * 8); SBAR(); } } while (0)
#define OA_(j) (L_WO + (kq * 5 + (j)) * 5120)
#define OB_(j) (kq * 5 + (j) < 17 ? L_WO + (kq * 5 + (j)) * 5120 : 145408 + (kq * 5 + (j) - 17) * 5120)
  MMR(0, 0, true, 2, OA_(2)); MMR(1, 0, true, 3, OA_(3)); MMR(0, 1, false, 4, OA_(4));
  BAR_L();
  SBAR();
  { LAS char* d = lp + L_WO + w * 5120 + lane * 16;
#pragma unroll
    for (int n = 0; n < 15; n++) { const int u = n < 5 ? 1 : (n >= 10), i = n < 5 ? 1 : 2, t = n % 5;
      if (n < 10) *(LAS u32x4*)(d + (n / 5) * 40960 + (n % 5) * 1024) = wbr[n];
      if (n == 10) *(LAS u32x4*)(lp + L_WO + 16 * 5120 + w5 * 1024 + lane * 16) = wbr[10];
      if (n == 11) OBL(0, 0); if (n == 12) OBL(1, 0); if (n == 13) OBL(0, 1); if (n == 14) OBL(1, 1);
      c[u][t] = mfma16(wf[i][t], of[u][i], c[u][t]); SBAR(); }
    asm volatile("" : "+v"(c[1][0])); }
  BAR_L();
  SBAR();
  OBL(0, 2); OBL(1, 2); SBAR();
  MMR(0, 3, false, 0, OB_(0)); OBL(0, 3); SBAR(); MMR(0, 4, false, 1, OB_(1)); OBL(0, 4); SBAR(); MMR(1, 3, false, 2, OB_(2)); OBL(1, 3); SBAR(); MMR(1, 4, false, 3, OB_(3)); OBL(1, 4);
  WFB(4);
  ARRIVE_NW(2);
  SBAR();
  LAS char* xw = lp + kq * 21504 + (tp * 32 + l15) * 336 + g * 80;
#pragma unroll
  for (int n = 0; n < 10; n++) {
    const int u = n < 6 ? n & 1 : (n >= 8), i = n < 6 ? n >> 1 : 3 + (n & 1);
    if (__builtin_expect(missing(of[u][i]), 0)) {
#pragma unroll 1
      for (unsigned spin = 0; spin < SPIN_CAP; ++spin) {
        asm volatile("" ::: "memory");
#pragma unroll
        for (int j = n; j < 10; j++) { const int uu = j < 6 ? j & 1 : (j >= 8), ii = j < 6 ? j >> 1 : 3 + (j & 1); OBL(uu, ii); }
        if (!missing(of[u][i])) break;
        __builtin_amdgcn_s_sleep(1);
      }
    }
    MM(u, i, false);
    if (n == 6) cpk = PEEK(2);
    if (n == 8) { AWAIT(2, cpk); *(LAS f32x4*)(xw) = c[0][0]; *(LAS f32x4*)(xw + 16) = c[0][1]; *(LAS f32x4*)(xw + 32) = c[0][2]; SBAR(); }
    if (n == 9) { *(LAS f32x4*)(xw + 48) = c[0][3]; *(LAS f32x4*)(xw + 64) = c[0][4]; SBAR(); }
  }
#pragma unroll
  for (int t = 0; t < 5; t++) *(LAS f32x4*)(xw + 16 * 336 + t * 16) = c[1][t];
  BAR_L();
  { const __amdgpu_buffer_rsrc_t orr = __builtin_amdgcn_make_buffer_rsrc(out, 0, 2048 * DM * 2, 0x00020000);
#pragma unroll
    for (int j = 0; j < 3; j++) { const int n = tid + 512 * j, nn = n < 1280 ? n : 0, row = (nn * 3277) >> 16, cq = nn - row * 20;
      const LAS f32x4* p = (const LAS f32x4*)(lp + row * 336 + cq * 16);
      const f32x4 z = (p[0] + p[1344]) + (p[2688] + p[4032]);
      __builtin_amdgcn_raw_buffer_store_b64((u32x2){pk(z[0], z[1]), pk(z[2], z[3])}, orr, n < 1280 ? ((rt * 128 + role * 64 + row) * DM + C0 + cq * 4) * 2 : 0x7FFF0000u, 0, 0); }
  }
}

__global__ __launch_bounds__(512, 2) void fused(const u16* __restrict__ x, const u16* __restrict__ wqkv, const u16* __restrict__ wphi, const u16* __restrict__ woA,
                                                const u16* __restrict__ wq, const u16* __restrict__ wk, const u16* __restrict__ wv, const u16* __restrict__ woB,
                                                const u16* __restrict__ gatew, u16* __restrict__ out, char* ws) {
  __shared__ __attribute__((aligned(16))) char lds[L_TOTAL];
  if ((blockIdx.x >> 6) & 1) body<1>(x, wqkv, wphi, woA, wq, wv, woB, gatew, out, ws, (LAS char*)lds);
  else body<0>(x, wqkv, wphi, woA, wq, wk, woB, gatew, out, ws, (LAS char*)lds);
}
}

extern "C" void kernel_launch(void* const* d_in, const int* in_sizes, int n_in, void* d_out, int out_size, void* d_ws, size_t ws_size, hipStream_t stream) {
  using namespace gm;
  fused<<<256, 512, 0, stream>>>((const u16*)d_in[0], (const u16*)d_in[1], (const u16*)d_in[2], (const u16*)d_in[3], (const u16*)d_in[5], (const u16*)d_in[6],
                                 (const u16*)d_in[7], (const u16*)d_in[8], (const u16*)d_in[9], (u16*)d_out, (char*)d_ws);
}
```

```cpp
#include <hip/hip_runtime.h>
#include <hip/hip_bf16.h>
#include <stdint.h>

namespace gm {
typedef float f32x16 __attribute__((ext_vector_type(16)));
typedef float f32x4 __attribute__((ext_vector_type(4)));
typedef float f32x2 __attribute__((ext_vector_type(2)));
typedef short s16x4 __attribute__((ext_vector_type(4)));
typedef __bf16 b16x2 __attribute__((ext_vector_type(2)));
typedef __bf16 b16x8 __attribute__((ext_vector_type(8)));
typedef unsigned u32x4 __attribute__((ext_vector_type(4)));
typedef unsigned u32x2 __attribute__((ext_vector_type(2)));
typedef unsigned long long u64;
typedef unsigned short u16;
#define DI __device__ __forceinline__
#define LAS __attribute__((address_space(3)))
#define GAS __attribute__((address_space(1)))
typedef GAS u64 gu64;

constexpr int DM = 640;
constexpr unsigned WS_EPOCH = 0, WS_FKV = 2048, WS_FKVR = 4096, WS_FQ = 8192, WS_FPART = 16384, WS_FOA = 24576, WS_SPARE = 40960  , WS_G = 57344;
constexpr unsigned WS_KVR = 8912896, KVR_B = 10240, WS_KV = 131072, WS_QX = 327680, QX_B = 10240, WS_PART = 1703936, PART_B = 15360;
constexpr unsigned WS_OA = 3670016, WS_OB = 6291456, WS_END = 8994816, OT_B = 20480;
constexpr int VB = 352, TILE_B = 1280 + 4 * VB, CH_B = TILE_B * 8, STEP_B = 2 * CH_B;
constexpr int STAGE_B = 36864, ST_W1 = 10240, ST_X = 20480;
constexpr int L_BUF = 43008, L_P0W = 0, L_P0X0 = 25600, L_P0X1 = 153600, L_RED = 158720, L_RED1 = 40960;
constexpr int L_HO = 86016, L_QL = 96256, L_VIM = 96256, L_PHK = 116736, L_WPHI = 158592, L_ZERO = 161792, L_CNT = 163712, L_TOTAL = 163840;
constexpr int L_WO = 43008;
constexpr float LOG2E = 1.4426950408889634f;
constexpr float QSCALE = 0.22360679774997896f * LOG2E;
constexpr unsigned SPIN_CAP = 20000, LSPIN_CAP = 400000;

DI unsigned pk(float a, float b) { f32x2 v = {a, b}; b16x2 r = __builtin_convertvector(v, b16x2); return __builtin_bit_cast(unsigned, r); }
DI f32x16 mfma32(u32x4 a, u32x4 b, f32x16 c) { return __builtin_amdgcn_mfma_f32_32x32x16_bf16(__builtin_bit_cast(b16x8, a), __builtin_bit_cast(b16x8, b), c, 0, 0, 0); }
DI f32x16 mfma32k8(u32x2 a, u32x2 b, f32x16 c) { return __builtin_amdgcn_mfma_f32_32x32x8bf16_1k(__builtin_bit_cast(s16x4, a), __builtin_bit_cast(s16x4, b), c, 0, 0, 0); }
DI f32x4 mfma16(u32x4 a, u32x4 b, f32x4 c) { return __builtin_amdgcn_mfma_f32_16x16x32_bf16(__builtin_bit_cast(b16x8, a), __builtin_bit_cast(b16x8, b), c, 0, 0, 0); }
DI u32x2 trd(const LAS char* p) { return __builtin_bit_cast(u32x2, __builtin_amdgcn_ds_read_tr16_b64_v4i16((LAS s16x4*)p)); }
DI u32x4 trd2(const LAS char* p0, const LAS char* p1) { u32x2 a = trd(p0), b = trd(p1); return (u32x4){a.x, a.y, b.x, b.y}; }
DI u32x4 ldsr16(const LAS char* p) { return *(const LAS u32x4*)p; }
DI u32x2 ldsr8(const LAS char* p) { return *(const LAS u32x2*)p; }
DI float ex2(float x) { return __builtin_amdgcn_exp2f(x); }
DI void dma16s(const void* base, unsigned voff, unsigned lds_base) {
  asm volatile("s_mov_b32 m0, %2\n\ts_nop 0\n\tglobal_load_lds_dwordx4 %0, %1" :: "v"(voff), "s"(base), "s"(lds_base) : "memory"); }
DI void dma16s_sc1(const void* base, unsigned voff, unsigned lds_base) {
  asm volatile("s_mov_b32 m0, %2\n\ts_nop 0\n\tglobal_load_lds_dwordx4 %0, %1 sc1" :: "v"(voff), "s"(base), "s"(lds_base) : "memory"); }
DI void dmag_open(unsigned lds_base) { asm volatile("s_mov_b32 m0, %0\n\ts_nop 0" :: "s"(lds_base) : "memory"); }
template <int J> DI void dmag(const void* base, unsigned voff) { asm volatile("global_load_lds_dwordx4 %0, %1 offset:%2" :: "v"(voff), "s"(base), "i"((J & 3) * 1024) : "memory"); }
template <int J> DI void dmag_sc1(const void* base, unsigned voff) { asm volatile("global_load_lds_dwordx4 %0, %1 offset:%2 sc1" :: "v"(voff), "s"(base), "i"((J & 3) * 1024) : "memory"); }
DI void dma4s(const void* base, unsigned voff, unsigned lds_base) {
  asm volatile("s_mov_b32 m0, %2\n\ts_nop 0\n\tglobal_load_lds_dword %0, %1" :: "v"(voff), "s"(base), "s"(lds_base) : "memory"); }
DI u32x4 ld16_async(const void* p) { u32x4 r; asm volatile("global_load_dwordx4 %0, %1, off" : "=v"(r) : "v"(p) : "memory"); return r; }
DI u32x2 ld8_sc1_async(const void* p) { u32x2 r; asm volatile("global_load_dwordx2 %0, %1, off sc1" : "=v"(r) : "v"(p) : "memory"); return r; }
#define BAR_L() asm volatile("s_waitcnt lgkmcnt(0)\n\ts_barrier" ::: "memory")
#define WAIT_BAR(N) asm volatile("s_waitcnt vmcnt(" #N ") lgkmcnt(0)\n\ts_barrier" ::: "memory")
#define WAIT_VM(N) asm volatile("s_waitcnt vmcnt(" #N ")" ::: "memory")
#define SBAR() __builtin_amdgcn_sched_barrier(0)
#define RLX_AGENT __ATOMIC_RELAXED, __HIP_MEMORY_SCOPE_AGENT
DI float half_swap_max(float m) { auto rr = __builtin_amdgcn_permlane32_swap(__float_as_uint(m), __float_as_uint(m), false, false); return fmaxf(__uint_as_float(rr[0]), __uint_as_float(rr[1])); }
DI float from_upper(float v) { auto rr = __builtin_amdgcn_permlane32_swap(__float_as_uint(v), __float_as_uint(v), false, false); return __uint_as_float(rr[1]); }

template <int NF>
DI bool flags_set(const char* ws, unsigned off, u64 tag, int lane) {
  gu64* f = (gu64*)(ws + off);
  bool ok = __hip_atomic_load(f + (NF < 64 ? (lane & (NF - 1)) : lane), RLX_AGENT) == tag;
  if (NF >= 128) ok &= __hip_atomic_load(f + 64 + lane, RLX_AGENT) == tag;
  if (NF >= 256) { ok &= __hip_atomic_load(f + 128 + lane, RLX_AGENT) == tag; ok &= __hip_atomic_load(f + 192 + lane, RLX_AGENT) == tag; }
  return ok;
}
template <int NF>
DI void wait_flags(const char* ws, unsigned off, u64 tag, int lane) {
#pragma unroll 1
  for (unsigned spin = 0; spin < SPIN_CAP; ++spin) {
    if (__all(flags_set<NF>(ws, off, tag, lane))) break;
    __builtin_amdgcn_s_sleep(2);
  }
  asm volatile("" ::: "memory");
}
DI unsigned nz(unsigned v) { return v < 0xFFFFFFFEu ? v : 0xFFFFFFFEu; }
DI unsigned umax3(unsigned a, unsigned b, unsigned c) { unsigned m = a > b ? a : b; return m > c ? m : c; }
DI bool missing(u32x4 v) { return __any(umax3(umax3(v.x, v.y, v.z), v.w, 0u) == 0xFFFFFFFFu); }
#define ONES ((u32x4){0xFFFFFFFFu, 0xFFFFFFFFu, 0xFFFFFFFFu, 0xFFFFFFFFu})
#define ST16(v, off) __builtin_amdgcn_raw_buffer_store_b128((v), wsr, (off), 0, 16)
#define ST8(v, off) __builtin_amdgcn_raw_buffer_store_b64((v), wsr, (off), 0, 16)
#define LD16(off) __builtin_amdgcn_raw_buffer_load_b128(wsr, (off), 0, 16)
#define ST16S(v, voff, soff) ST16(v, (voff) + (soff))
#define LD16S(voff, soff) __builtin_amdgcn_raw_buffer_load_b128(wsr, (voff), (soff), 16)
#define LD8S(voff, soff) __builtin_amdgcn_raw_buffer_load_b64(wsr, (voff), (soff), 16)
#define LD8(off) __builtin_amdgcn_raw_buffer_load_b64(wsr, (off), 0, 16)

#define STAMP(i) do {} while (0)

template <int role>
DI void body(const u16* __restrict__ x, const u16* __restrict__ wqkv, const u16* __restrict__ wphi, const u16* __restrict__ woA,
             const u16* __restrict__ wq, const u16* __restrict__ wkv, const u16* __restrict__ woB,
             const u16* __restrict__ gatew, u16* __restrict__ out, char* ws, LAS char* lp) {
  const int tid = threadIdx.x, lane = tid & 63;
  const int w = __builtin_amdgcn_readfirstlane(tid >> 6) & 7;
  const int l15 = lane & 15, g = lane >> 4, q4 = l15 >> 2, p4 = lane & 3, r = lane & 31, h = lane >> 5;
  const int bx = blockIdx.x & 7, bi = blockIdx.x >> 3;
  const int rt = (bx & 1) * 8 + (bi & 7), hp = (bx >> 1) * 4 + ((bi >> 3) & 3), hq = hp >> 1;
  const int w3 = w & 3, w2 = w >> 2;
  const unsigned lds0 = (unsigned)(uintptr_t)lp;
  const __amdgpu_buffer_rsrc_t wsr = __builtin_amdgcn_make_buffer_rsrc(ws, 0, WS_END, 0x00020000);
  LAS unsigned* cnt = (LAS unsigned*)(lp + L_CNT);
#define ARRIVE_NW(c) do { asm volatile("" ::: "memory"); __hip_atomic_store(cnt + (c) * 8 + w, 1u, __ATOMIC_RELAXED, __HIP_MEMORY_SCOPE_WORKGROUP); asm volatile("" ::: "memory"); } while (0)
#define PEEK(c) __hip_atomic_load(cnt + (c) * 8 + (lane & 7), __ATOMIC_RELAXED, __HIP_MEMORY_SCOPE_WORKGROUP)
#define AWAIT(c, v) do { if (__builtin_expect(!__all((v) != 0u), 0)) { _Pragma("unroll 1") for (unsigned sp_ = 0; sp_ < LSPIN_CAP; ++sp_) { if (__all(PEEK(c) != 0u)) break; __builtin_amdgcn_s_sleep(1); } } asm volatile("" ::: "memory"); } while (0)
#define POLL(off) __builtin_bit_cast(u64, __builtin_amdgcn_raw_buffer_load_b64(wsr, lane * 8, (off), 16))
#define FLAG(off) __builtin_amdgcn_raw_buffer_store_b64(__builtin_bit_cast(u32x2, tag), wsr, lane ? 0x7FFF0000u : 0u, (off), 16)
  STAMP(0);

  const int T0 = rt * 128 + hq * 16;
  const int C0 = hq * 80;
  const int wlane = ((g >> 1) * 16 + (g & 1) * 4 + q4) * 160 + p4 * 40;
  u64 tag;
  unsigned o0[10];
#define KVP(s, slot, p) dma16s_sc1(ws + WS_KV + (s) * STEP_B + (p) * 1024, lane * 16, lds0 + (slot) * STEP_B + (p) * 1024)
#define KVL(s, slot) do { _Pragma("unroll") for (int j = 0; j < 11; j++) KVP(s, slot, j < 10 ? w3 + 4 * j : 40 + (w3 & 1)); } while (0)
#define KW(s, slot, j) do { if ((j) == 4) KVP(s, slot, w * 5 + 4); else { if ((j) == 0) dmag_open(lds0 + (slot) * STEP_B + w * 5120); dmag_sc1<(j)>(ws + WS_KV + (s) * STEP_B + w * 5120, lane * 16); } } while (0)
#define KX(s, slot) KVP(s, slot, 40 + (w & 1))
  const int w5 = w < 5 ? w : 4;
  unsigned wo16;
  unsigned woff[5];
  auto woff_init = [&]() { int ln = lane; asm volatile("" : "+v"(ln));
#pragma unroll
    for (int j = 0; j < 5; j++) { const int c = j * 64 + ln, rho = (c * 6554) >> 16, cc = c - rho * 10; const int k = (rho & ~12) | ((rho & 4) << 1) | ((rho & 8) >> 1); woff[j] = k * 1280 + cc * 16 + (4 - j) * 1024; }
    { const int c = w5 * 64 + ln, rho = (c * 6554) >> 16, cc = c - rho * 10; const int k = (rho & ~12) | ((rho & 4) << 1) | ((rho & 8) >> 1); wo16 = k * 1280 + cc * 16 + 4096; }
    asm volatile("" : "+v"(woff[0]), "+v"(woff[1]), "+v"(woff[2]), "+v"(woff[3]), "+v"(woff[4]), "+v"(wo16)); };
  if (w2 == 0) {
    u32x4 gf0, gf1, gf2, gf3, gf4, wpc; u32x2 tch;
    u32x2 ep = ld8_sc1_async(ws + WS_EPOCH);
    const int lx = w3 < 3 ? L_P0X0 + w3 * 5120 : L_P0X1;
    { const u16* wsrc = wkv + w3 * 3200;
#pragma unroll
      for (int j = 0; j < 5; j++) { const int n = j * 64 + lane, row = (n * 3277) >> 16, cc = n - row * 20; dma16s(x + T0 * DM + w3 * 160, row * 1280 + cc * 16, lds0 + lx + j * 1024); }
#pragma unroll
      for (int j = 0; j < 6; j++) dma16s(wsrc + j * 512, lane * 16, lds0 + L_P0W + w3 * 6400 + j * 1024);
      if (lane < 16) dma16s(wsrc + 6 * 512, lane * 16, lds0 + L_P0W + w3 * 6400 + 6 * 1024);
      const u16* gp = gatew + w3 * 160 + g * 8;
      gf0 = ld16_async(gp); gf1 = ld16_async(gp + 32); gf2 = ld16_async(gp + 64); gf3 = ld16_async(gp + 96); gf4 = ld16_async(gp + 128);
      wpc = ld16_async(wphi + hq * 1600 + (tid < 200 ? tid : 199) * 8);
      const char* tp = ws + l15 * 594944;
      if (w3 == 1) tp = (const char*)(woA + C0 + (lane & 7) * 51200);
      if (w3 == 2) tp = (const char*)(woB + C0 + (lane & 7) * 51200);
      if (w3 == 3) tp = (const char*)(out + (rt * 128 + role * 64 + (lane & 1) * 63) * DM + C0);
      tch = ld8_sc1_async(tp);
    }
    woff_init();
    asm volatile("s_waitcnt vmcnt(1)" : "+v"(gf0), "+v"(gf1), "+v"(gf2), "+v"(gf3), "+v"(gf4), "+v"(wpc), "+v"(ep) :: "memory");
    tag = (((u64)(unsigned)__builtin_amdgcn_readfirstlane(ep.y) << 32) | (unsigned)__builtin_amdgcn_readfirstlane(ep.x)) + 1ull;
    STAMP(1);
    { const LAS char* wb = lp + L_P0W + (w3 * 160 + g * 8 + q4) * 40;
      const LAS char* xb = lp + lx + l15 * 320 + g * 16;
      const bool gl = l15 == 4;
      f32x4 c0 = {}, c1 = {};
#define P0STEP(s, GF) { const u32x4 b = ldsr16(xb + (s) * 64); c0 = mfma16(trd2(wb + p4 * 8 + (s) * 1280, wb + p4 * 8 + (s) * 1280 + 160), b, c0); \
        u32x4 a = trd2(wb + 32 + (s) * 1280, wb + 32 + (s) * 1280 + 160); if (gl) a = GF; c1 = mfma16(a, b, c1); }
      P0STEP(0, gf0) P0STEP(1, gf1) P0STEP(2, gf2) P0STEP(3, gf3) P0STEP(4, gf4)
#undef P0STEP
      *(LAS f32x4*)(lp + L_RED + tid * 16) = c0;
      if (lane < 32) *(LAS f32x4*)(lp + L_RED1 + (w3 * 32 + lane) * 16) = c1; }
    STAMP(2);
    f32x16 acc[5];
#pragma unroll
    for (int b = 0; b < 5; b++) acc[b] = (f32x16){};
    const int wm = g & 1, whd = p4;
    const int wo0 = wm * ST_W1 + (8 * h + 4 * wm + q4) * 160 + whd * 40, wo1 = wm * ST_W1 + (8 * h + 4 * (wm ^ 1) + q4) * 160 + whd * 40;
    const int xo = ST_X + (w3 * 32 + r) * 128 + ((h ^ ((r >> 1) & 7)) * 16);
    u32x4 fa[5], fb[5], xa, xb;
#define KSTEP(FN, XN, s, FC, XC) { XN = ldsr16(sb + (xo ^ ((s) * 32))); \
      _Pragma("unroll") for (int j = 0; j < 5; j++) { if (i > 0 || (s) > 0) acc[j] = mfma32(FC[j], XC, acc[j]); FN[j] = trd2(sb + wo0 + (s) * 2560 + j * 8, sb + wo1 + (s) * 2560 + j * 8); SBAR(); } }
#pragma unroll
    for (int i = 0; i < 10; i++) {
      BAR_L();
      if (i == 1 && role) {
        const unsigned qb = WS_QX + (rt * 8 + hq) * QX_B + w3 * 1280 + lane * 16;
        ST16(ONES, qb); ST16(ONES, qb + 5120); ST16(ONES, lane < 32 ? qb + 1024 + (lane >> 4) * (5120 - 256) : 0x7FFF0000u); }
      if (i == 4) { asm volatile("s_waitcnt vmcnt(0)" : "+v"(tch) :: "memory"); if (role) { FLAG(WS_FQ + ((rt * 8 + hq) * 4 + w3) * 8); }
                    if (tid < 200) *(LAS u32x4*)(lp + L_WPHI + tid * 16) = wpc; }
      const LAS char* sb = lp + L_BUF + ((i + 2) % 3) * STAGE_B;
      KSTEP(fa, xa, 0, fb, xb) KSTEP(fb, xb, 1, fa, xa) KSTEP(fa, xa, 2, fb, xb)
      { xb = ldsr16(sb + (xo ^ 96));
        acc[0] = mfma32(fa[0], xa, acc[0]); fb[0] = trd2(sb + wo0 + 7680, sb + wo1 + 7680); fb[1] = trd2(sb + wo0 + 7688, sb + wo1 + 7688); SBAR();
        acc[1] = mfma32(fa[1], xa, acc[1]); fb[2] = trd2(sb + wo0 + 7696, sb + wo1 + 7696); fb[3] = trd2(sb + wo0 + 7704, sb + wo1 + 7704); SBAR();
        acc[2] = mfma32(fa[2], xa, acc[2]); fb[4] = trd2(sb + wo0 + 7712, sb + wo1 + 7712); SBAR();
        acc[3] = mfma32(fa[3], xa, acc[3]); acc[4] = mfma32(fa[4], xa, acc[4]); SBAR(); }
    }
#pragma unroll
    for (int j = 0; j < 5; j++) acc[j] = mfma32(fb[j], xb, acc[j]);
#undef KSTEP
    STAMP(3);
#define PK2(t, i, sc) pk(acc[t][i] * (sc), acc[t][(i) + 1] * (sc))
#define ROW(i, sc) const u32x4 ra = {PK2(0, i, sc), PK2(0, (i) + 2, sc), PK2(1, i, sc), PK2(1, (i) + 2, sc)}, rb = {PK2(2, i, sc), PK2(2, (i) + 2, sc), PK2(3, i, sc), PK2(3, (i) + 2, sc)}; const u32x2 rc = {PK2(4, i, sc), PK2(4, (i) + 2, sc)}
    { ROW(4, 1.0f);
      *(LAS u32x4*)(lp + L_HO + tid * 16) = ra; *(LAS u32x4*)(lp + L_HO + 4096 + tid * 16) = rb; *(LAS u32x2*)(lp + L_HO + 8192 + tid * 8) = rc; }
    BAR_L();
    const int qfo = (h * 4 + w3) * 1280;
    if (role) {
      { ROW(8, QSCALE); const unsigned qb = WS_QX + (rt * 8 + hq) * QX_B + qfo;
        ST16(((u32x4){nz(ra.x), nz(ra.y), nz(ra.z), nz(ra.w)}), qb + r * 16); ST16(((u32x4){nz(rb.x), nz(rb.y), nz(rb.z), nz(rb.w)}), qb + 512 + r * 16); ST8(((u32x2){nz(rc.x), nz(rc.y)}), qb + 1024 + r * 8); }
      { ROW(12, QSCALE); LAS char* d = lp + L_QL + qfo;
        *(LAS u32x4*)(d + r * 16) = ra; *(LAS u32x4*)(d + 512 + r * 16) = rb; *(LAS u32x2*)(d + 1024 + r * 8) = rc; }
    } else { LAS char* d = lp + L_VIM + (h * 128 + w3 * 32 + r) * 40;
#pragma unroll
      for (int t = 0; t < 5; t++) { *(LAS u32x2*)(d + t * 8) = (u32x2){PK2(t, 8, 1.0f), PK2(t, 10, 1.0f)}; *(LAS u32x2*)(d + 10240 + t * 8) = (u32x2){PK2(t, 12, 1.0f), PK2(t, 14, 1.0f)}; } }
#pragma unroll
    for (int t = 0; t < 5; t++) { o0[2 * t] = PK2(t, 0, 1.0f); o0[2 * t + 1] = PK2(t, 2, 1.0f); }
#undef ROW
#undef PK2
  } else {
    u32x2 ep = ld8_sc1_async(ws + WS_EPOCH);
    const bool second = w3 >> 1;
    const unsigned rs = (second && role) ? 1280u : 3840u;
    const char* wsrc = (second ? (role ? (const char*)(wq + C0) : (const char*)(wqkv + 1280 + C0)) : (const char*)(wqkv + (role ? 0 : 640) + C0)) + (w3 & 1) * 32 * rs;
    const char* xsrc = (const char*)(x + (rt * 128 + w3 * 32) * DM);
    int rot = (bi & 7) + 3 * ((bi >> 3) & 3); rot = rot >= 10 ? rot - 10 : rot;
    unsigned so[5], xoff[2];
#pragma unroll
    for (int j = 0; j < 5; j++) { const int c = j * 64 + lane, rho = (c * 6554) >> 16, cc = c - rho * 10; so[j] = (rho ^ (second ? 4 : 0)) * rs + cc * 16; }
#pragma unroll
    for (int j = 0; j < 2; j++) { const int row = j * 8 + (lane >> 3); xoff[j] = row * 1280 + (((lane & 7) ^ ((row >> 1) & 7)) * 16); }
    int bsel = 2, kc = rot;
    auto stage = [&]() {
      const unsigned lb = lds0 + L_BUF + bsel * STAGE_B;
      const char* bw = wsrc + kc * 64 * rs; const char* bxp = xsrc + kc * 128;
#pragma unroll
      for (int j = 0; j < 5; j++) { dma16s(bw, so[j], lb + (w3 * 5 + j) * 1024); if (j < 4) dma16s(bxp + (j >> 1) * 16 * 1280, xoff[j & 1], lb + ST_X + (w3 * 4 + j) * 1024); }
      bsel = bsel == 2 ? 0 : bsel + 1; kc = kc == 9 ? 0 : kc + 1;
    };
    stage(); stage();
    woff_init();
    asm volatile("s_waitcnt vmcnt(9)" : "+v"(ep) :: "memory");
    tag = (((u64)(unsigned)__builtin_amdgcn_readfirstlane(ep.y) << 32) | (unsigned)__builtin_amdgcn_readfirstlane(ep.x)) + 1ull;
    STAMP(1); STAMP(2);
#pragma unroll 1
    for (int i = 0; i < 8; i++) {
      WAIT_BAR(9);
      stage();
      if (i == 0 && w3 == 0) {
        const unsigned tb = WS_KV + (T0 >> 5) * TILE_B; const int s16 = hq & 1;
        if (!role) {
          const LAS f32x4* rp = (const LAS f32x4*)(lp + (h ? L_RED1 : L_RED)) + (h ? (g & 1) * 16 : g * 32) + l15;
          const int st = h ? 32 : 64, o2 = h ? 0 : 16;
          const f32x4 a = (rp[0] + rp[st]) + (rp[2 * st] + rp[3 * st]), b = (rp[o2] + rp[st + o2]) + (rp[2 * st + o2] + rp[3 * st + o2]);
          const u32x4 v = {pk(a[0], a[1]), pk(a[2], a[3]), pk(b[0], b[1]), pk(b[2], b[3])};
          if (lane < 32) ST16(v, tb + (g * 32 + s16 * 16 + l15) * 16);
          else if (lane < 48) ST8(((u32x2){v.x, v.y}), tb + 1024 + (s16 * 16 + l15) * 8);
          else { const float gv = 1.0f / (1.0f + __expf(-a[0])); __builtin_amdgcn_raw_buffer_store_b32(__float_as_uint(gv), wsr, WS_G + (T0 + l15) * 4, 0, 16); }
        } else if (lane < 44) {
          const int hh = lane >= 22, d = lane - hh * 22, dd = d < 20 ? d : 0, st = dd >= 16 ? 128 : 256;
          const LAS float* rp = (const LAS float*)(lp + (dd >= 16 ? L_RED1 : L_RED)) + (((dd & 15) >> 2) * 16 + 4 * hh) * 4 + (dd & 3);
          float e[8];
#pragma unroll
          for (int j = 0; j < 8; j++) { const int tk = (8 * (j >> 2) + (j & 3)) * 4; e[j] = (rp[tk] + rp[tk + st]) + (rp[tk + 2 * st] + rp[tk + 3 * st]); }
          u32x4 v = {pk(e[0], e[1]), pk(e[2], e[3]), pk(e[4], e[5]), pk(e[6], e[7])};
          if (d == 20) v = (u32x4){0x3F803F80u, 0x3F803F80u, 0x3F803F80u, 0x3F803F80u};
          if (d == 21) v = (u32x4){0u, 0u, 0u, 0u};
          ST16(v, tb + 1280 + (s16 * 2 + hh) * VB + d * 16);
        }
      }
      if (i == 2) { if (w3 == 0) FLAG(WS_FKV + blockIdx.x * 8);
                    if (w3 >= 2) *(LAS u32x4*)(lp + L_ZERO + (tid - 384) * 16) = (u32x4){0u, 0u, 0u, 0u}; }
    }
    u64 fa, fb, fc, fd, fe;
    { gu64* f = (gu64*)(ws + WS_FKV) + lane;
      fa = __hip_atomic_load(f, RLX_AGENT); fb = __hip_atomic_load(f + 64, RLX_AGENT); fc = __hip_atomic_load(f + 128, RLX_AGENT); fd = __hip_atomic_load(f + 192, RLX_AGENT);
      fe = __hip_atomic_load((gu64*)(ws + WS_FQ) + (rt * 8 + hq) * 4 + p4, RLX_AGENT); }
    WAIT_BAR(14);
    asm volatile("" : "+v"(fa), "+v"(fb), "+v"(fc), "+v"(fd), "+v"(fe));
    const bool kvok = __all((fa == tag) & (fb == tag) & (fc == tag) & (fd == tag) & (fe == tag));
    asm volatile("" ::: "memory");
    BAR_L();
    if (__builtin_expect(!kvok, 0)) { wait_flags<256>(ws, WS_FKV, tag, lane); wait_flags<4>(ws, WS_FQ + (rt * 8 + hq) * 32, tag, lane); }
    KVL(0, 0);
    if (blockIdx.x == 0 && w3 == 0) FLAG(WS_EPOCH);
    STAMP(3);
    BAR_L();
    const int ht = tid - 256;
    const u32x4 a = ldsr16(lp + L_HO + ht * 16), b = ldsr16(lp + L_HO + 4096 + ht * 16); const u32x2 c = ldsr8(lp + L_HO + 8192 + ht * 8);
    o0[0] = a.x; o0[1] = a.y; o0[2] = a.z; o0[3] = a.w; o0[4] = b.x; o0[5] = b.y; o0[6] = b.z; o0[7] = b.w; o0[8] = c.x; o0[9] = c.y;
  }
  STAMP(4);
  u32x4 ph[5];
  { const LAS char* pb = ((p4 & 1) == h) ? lp + L_WPHI + (2 * w2 + h) * 800 + q4 * 40 + (2 * (g & 1) + (p4 >> 1)) * 8 : lp + L_ZERO;
    const u32x4 b0 = {o0[0], o0[1], o0[2], o0[3]}, b1 = {o0[4], o0[5], o0[6], o0[7]}, b2 = {o0[8], o0[9], 0u, 0u};
    const u32x2 c0 = trd(pb + 640), c1 = trd(pb + 672);
    f32x16 P0 = {}, P1 = {};
    P0 = mfma32(trd2(pb, pb + 160), b0, P0); P1 = mfma32(trd2(pb + 32, pb + 192), b0, P1);
    P0 = mfma32(trd2(pb + 320, pb + 480), b1, P0); P1 = mfma32(trd2(pb + 352, pb + 512), b1, P1);
    P0 = mfma32((u32x4){c0.x, c0.y, 0u, 0u}, b2, P0); P1 = mfma32((u32x4){c1.x, c1.y, 0u, 0u}, b2, P1);
    KX(1, 1); KW(1, 1, 0); SBAR();
    f32x2 p[10];
#pragma unroll
    for (int f = 0; f < 10; f++) p[f] = f < 8 ? (f32x2){P0[(2 * f) & 15], P0[(2 * f + 1) & 15]} : (f32x2){P1[(2 * f) & 3], P1[(2 * f + 1) & 3]};
    float mx = fmaxf(p[0][0], p[0][1]), mn = fminf(p[0][0], p[0][1]);
#pragma unroll
    for (int f = 1; f < 10; f++) { mx = fmaxf(fmaxf(mx, p[f][0]), p[f][1]); mn = fminf(fminf(mn, p[f][0]), p[f][1]); }
    const f32x2 ma = {-mx * LOG2E, -mx * LOG2E}, mb = {mn * LOG2E, mn * LOG2E}, ca_ = {LOG2E, LOG2E}, cb_ = {-LOG2E, -LOG2E};
    f32x2 ea[10], eb[10], sa = {0.f, 0.f}, sb = {0.f, 0.f};
#pragma unroll
    for (int f = 0; f < 10; f++) { if (f % 2 == 0 && f < 8) { SBAR(); if (f == 0) KW(1, 1, 1); if (f == 2) KW(1, 1, 2); if (f == 4) KW(1, 1, 3); if (f == 6) KW(1, 1, 4); SBAR(); }
      const f32x2 xa = __builtin_elementwise_fma(p[f], ca_, ma), xb = __builtin_elementwise_fma(p[f], cb_, mb);
      ea[f] = (f32x2){ex2(xa[0]), ex2(xa[1])}; eb[f] = (f32x2){ex2(xb[0]), ex2(xb[1])}; sa += ea[f]; sb += eb[f]; }
    const float ia = __builtin_amdgcn_rcpf(sa[0] + sa[1]), ib = __builtin_amdgcn_rcpf(sb[0] + sb[1]);
    unsigned o[20];
#pragma unroll
    for (int f = 0; f < 10; f++) { const f32x2 ya = ea[f] * ia, yb = eb[f] * ib; o[f] = pk(ya[0], ya[1]); o[10 + f] = pk(yb[0], yb[1]); }
#pragma unroll
    for (int c = 0; c < 5; c++) ph[c] = (u32x4){o[4 * c], o[4 * c + 1], o[4 * c + 2], o[4 * c + 3]};
    asm volatile("" : "+v"(ph[0]), "+v"(ph[1]), "+v"(ph[2]), "+v"(ph[3]), "+v"(ph[4]));
  }
  u32x4 q1A, q1B; u32x2 q2A, q2B;
  const int qo1 = (g & 1) * 5120 + (h * 32 + l15) * 16, qo2 = (g & 1) * 5120 + 1024 + l15 * 8;
  if (role) {
    WAIT_BAR(0);
    STAMP(5);
    const LAS char* qa = lp + L_QL + w3 * 1280;
    q1A = ldsr16(qa + qo1 + (w2 ^ 1) * 256); q1B = ldsr16(qa + qo1 + w2 * 256); q2A = ldsr8(qa + qo2 + (w2 ^ 1) * 128); q2B = ldsr8(qa + qo2 + w2 * 128);
    ARRIVE_NW(0);
  } else {
    const unsigned qb = WS_QX + (rt * 8 + hq) * QX_B + w3 * 1280;
    q1A = LD16S(qo1, qb + (w2 ^ 1) * 256); q1B = LD16S(qo1, qb + w2 * 256); q2A = LD8S(qo2, qb + (w2 ^ 1) * 128); q2B = LD8S(qo2, qb + w2 * 128);
    { LAS char* d = lp + L_PHK + ((2 * w2 + h) * 128 + w3 * 32 + r) * 80;
#pragma unroll
      for (int c = 0; c < 5; c++) *(LAS u32x4*)(d + c * 16) = ph[c]; }
    WAIT_BAR(4);
    STAMP(5);
#pragma unroll 1
    for (unsigned spin = 0; spin < SPIN_CAP; ++spin) {
      if (!(missing(q1A) | missing(q1B) | missing((u32x4){q2A.x, q2A.y, q2B.x, q2B.y}))) break;
      __builtin_amdgcn_s_sleep(1);
      asm volatile("" ::: "memory");
      q1A = LD16S(qo1, qb + (w2 ^ 1) * 256); q1B = LD16S(qo1, qb + w2 * 256); q2A = LD8S(qo2, qb + (w2 ^ 1) * 128); q2B = LD8S(qo2, qb + w2 * 128);
    }
  }
  STAMP(6);
  const int vr = r <= 21 ? r : 21;
  const int ok1 = lane * 16, ok2 = h ? (1280 + 20 * 16) : (1024 + r * 8), ov = 1280 + h * VB + vr * 16;
  const f32x16 Z = {};
  if (h) { q2A = (u32x2){0u, 0u}; q2B = (u32x2){0u, 0u}; }
  { const LAS char* t0 = lp;
    const u32x4 k1 = ldsr16(t0 + ok1); const u32x2 k2 = ldsr8(t0 + ok2);
    f32x16 sa = mfma32(k1, q1A, Z); sa = mfma32k8(k2, q2A, sa);
    f32x16 sb = mfma32(k1, q1B, Z); sb = mfma32k8(k2, q2B, sb);
    float ma = sa[0], mb = sb[0];
#pragma unroll
    for (int i = 1; i < 16; i++) { ma = fmaxf(ma, sa[i]); mb = fmaxf(mb, sb[i]); }
    ma = half_swap_max(ma); mb = half_swap_max(mb);
    const float mah = __uint_as_float(__float_as_uint(ma) & 0xFFFF0000u), mbh = __uint_as_float(__float_as_uint(mb) & 0xFFFF0000u);
    if (h) { q2A.x = pk(-mah, mah - ma); q2B.x = pk(-mbh, mbh - mb); } }
  f32x16 OA = {}, OB = {}, SA, SB;
  u32x4 k1, v1, v2, v2n, pA1, pA2, pB1, pB2; u32x2 k2;
  float e0, e1, e2, e3;
  const LAS char* c0p = lp + w2 * CH_B + ok1; const LAS char* c1p = lp + w2 * CH_B + ok2; const LAS char* c2p = lp + w2 * CH_B + ov;
  k1 = ldsr16(c0p); k2 = ldsr8(c1p);
  SA = mfma32(k1, q1A, Z); SA = mfma32k8(k2, q2A, SA);
  v1 = ldsr16(c2p); v2 = v1;
  e0 = e1 = e2 = e3 = 0.f; pB2 = (u32x4){0u, 0u, 0u, 0u};
#define G(MF, LD, H, M, C0_, C1_, X, S_, i) MF; LD; H; SBAR(); C0_; C1_; SBAR(); X##0 = ex2(S_[i]); X##1 = ex2(S_[i + 1]); SBAR(); M; SBAR(); X##2 = ex2(S_[i + 2]); X##3 = ex2(S_[i + 3]); \
    asm volatile("" : "+v"(X##0), "+v"(X##1), "+v"(X##2), "+v"(X##3)); SBAR();
#define TILEF(TN, V2, F) { float a0, a1, a2, a3; \
    G(SB = mfma32(k1, q1B, Z),    k1 = ldsr16(d0p + (TN)), F(0), F(8),  pB2[2] = pk(e0, e1), pB2[3] = pk(e2, e3), a, SA, 0) \
    G(OB = mfma32(v2, pB2, OB),   V2,                      F(1), F(9),  pA1[0] = pk(a0, a1), pA1[1] = pk(a2, a3), e, SA, 4) \
    G(SB = mfma32k8(k2, q2B, SB), k2 = ldsr8(d1p + (TN)),  F(2), F(10), pA1[2] = pk(e0, e1), pA1[3] = pk(e2, e3), a, SA, 8) \
    G(OA = mfma32(v1, pA1, OA),   (void)0,                 F(3), F(11), pA2[0] = pk(a0, a1), pA2[1] = pk(a2, a3), e, SA, 12) \
    G(SA = mfma32(k1, q1A, Z),    (void)0,                 F(4), F(12), pA2[2] = pk(e0, e1), pA2[3] = pk(e2, e3), a, SB, 0) \
    G(OA = mfma32(v2, pA2, OA),   (void)0,                 F(5), F(13), pB1[0] = pk(a0, a1), pB1[1] = pk(a2, a3), e, SB, 4) \
    G(SA = mfma32k8(k2, q2A, SA), (void)0,                 F(6), F(14), pB1[2] = pk(e0, e1), pB1[3] = pk(e2, e3), a, SB, 8) \
    G(OB = mfma32(v1, pB1, OB),   v1 = ldsr16(d2p + (TN)), F(7), F(15), pB2[0] = pk(a0, a1), pB2[1] = pk(a2, a3), e, SB, 12) }
#define TILE(TB, TN, V2, H0, H1, H2) { auto f_ = [&](int n) { if (n == 3) { H0; } if (n == 4) { H1; } if (n == 5) { H2; } }; TILEF(TN, V2, f_) }
#define TLF(t, F) TILEF(((t) + 1) * TILE_B, v2 = ldsr16(d2p + (t) * TILE_B + 2 * VB), F)
#define NOP (void)0
  const LAS char *d0p = c0p, *d1p = c1p, *d2p = c2p;
#define TL(sl, t, H0, H1, H2) TILE(0, ((t) + 1) * TILE_B, v2 = ldsr16(d2p + (t) * TILE_B + 2 * VB), H0, H1, H2)
#define TL6(sl, H0, H1) TL(sl, 6, H0, H1, v2n = ldsr16(d2p + 7 * TILE_B + 2 * VB))
#define TL7(sl, sn, H0, H1, H2) d0p += ((sn) - (sl)) * STEP_B; d1p += ((sn) - (sl)) * STEP_B; d2p += ((sn) - (sl)) * STEP_B; asm volatile("" : "+v"(d0p), "+v"(d1p), "+v"(d2p)); TILE(0, 0, v2 = v2n, H0, H1, H2)
#define WOG(wo, gr, j, dst) do { if ((j) == 4) WOP(wo, gr, 4, dst); else { if ((j) == 0) dmag_open(lds0 + (dst)); dmag<(j)>((wo) + C0 + (gr) * 32 * 640 - 2048, woff[j]); } } while (0)
#define WOP(wo, gr, j, dst) dma16s((wo) + C0 + (gr) * 32 * 640 - (4 - (j)) * 512, woff[j], lds0 + (dst) + (j) * 1024)
#define WA(gr, j) WOG(woA, gr, j, L_WO + (gr) * 5120)
  const int wx3 = w3 ? w3 : 1;
  const u16* wxs = (w2 ? woB : woA) + (16 + wx3) * 32 * 640;
  const int wxd = w2 ? 145408 + (wx3 - 1) * 5120 : L_WO + (16 + wx3) * 5120;
#define WX(j) WOG(wxs, 0, j, wxd)
#define PRIO(n) __builtin_amdgcn_s_setprio(n)
  u32x4 ones = ONES; if (role) asm volatile("" : "+v"(ones));
#define FILL(j) do { const int n_ = w * 3 + ((j) >> 1); ST16S(ones, ok1, (((j) & 1) ? WS_OB : WS_OA) + (rt * 8 + hq) * OT_B + (n_ < 20 ? n_ : 19) * 1024); } while (0)
#define CLR_VM() __builtin_amdgcn_s_waitcnt(0x0F70)
  const bool red = !role && !w2;
  f32x4 part[4]; u64 fp0, fp1;
  auto red_load = [&]() {
    asm volatile("" : "+v"(fp0), "+v"(fp1));
    if (__builtin_expect(!__all((fp0 == tag) & (fp1 == tag)), 0)) wait_flags<128>(ws, WS_FPART + hq * 1024, tag, lane);
    asm volatile("" ::: "memory");
#pragma unroll
    for (int j = 0; j < 4; j++) part[j] = __builtin_bit_cast(f32x4, LD16S(g * (4 * PART_B) + l15 * 16, WS_PART + (hq * 16 + j) * PART_B + rt * 960 + w3 * 240));
  };
  auto red_store = [&]() {
    f32x4 a = (part[0] + part[1]) + (part[2] + part[3]);
#pragma unroll
    for (int i = 0; i < 4; i++) { auto r16 = __builtin_amdgcn_permlane16_swap(__float_as_uint(a[i]), __float_as_uint(a[i]), false, false); const float t = __uint_as_float(r16[0]) + __uint_as_float(r16[1]);
      auto r32 = __builtin_amdgcn_permlane32_swap(__float_as_uint(t), __float_as_uint(t), false, false); a[i] = __uint_as_float(r32[0]) + __uint_as_float(r32[1]); }
    const int hd = rt >> 2, u = (rt & 3) * 60 + w3 * 15 + lane, hi = u >= 160, fq = hi ? (u - 160) >> 3 : u >> 4, di = hi ? u & 7 : u & 15;
    __builtin_amdgcn_raw_buffer_store_b64((u32x2){pk(a[0], a[1]), pk(a[2], a[3])}, wsr, lane < 15 ? (hi * 5 + (fq >> 1)) * 512 + di * 16 + (fq & 1) * 8 : 0x7FFF0000u, WS_KVR + hq * KVR_B + (hd >> 1) * 5120 + (hd & 1) * 256, 16);
  };
  u32x4 ka[10]; u64 fkr; float gta; unsigned kb; const unsigned kbs = WS_KVR + hq * KVR_B + w2 * 5120;
  auto oa_load = [&]() {
    asm volatile("" : "+v"(fkr));
    if (__builtin_expect(!__all(fkr == tag), 0)) wait_flags<64>(ws, WS_FKVR + hq * 512, tag, lane);
    asm volatile("" ::: "memory");
    kb = (((r >> 2) & 1) == h) ? (h * 16 + (r & 3) + 4 * (r >> 3)) * 16 : 0x7FFF0000u;
    ka[0] = LD16S(kb, kbs); ka[5] = LD16S(kb, kbs + 2560); ka[1] = LD16S(kb, kbs + 512); ka[6] = LD16S(kb, kbs + 3072);
    gta = __uint_as_float(__builtin_amdgcn_raw_buffer_load_b32(wsr, WS_G + (rt * 128 + w3 * 32 + r) * 4, 0, 16));
  };
  u32x4 jf[16]; f32x4 jc[3];
  const LAS char* jab = lp + L_PHK + ((w >> 1) * 128 + g * 8 + q4) * 80 + p4 * 8;
  const LAS char* jbb = lp + L_VIM + ((w >> 1) * 128 + g * 8 + q4) * 40 + (w & 1) * 32 + p4 * 8;
  auto kv_rd = [&](int n) { const int ks = n >> 2, j = n & 3;
    jf[n] = j ? trd2(jab + ks * 2560 + (j - 1) * 32, jab + ks * 2560 + 320 + (j - 1) * 32) : trd2(jbb + ks * 1280, jbb + ks * 1280 + 160); };
  auto kv_mm = [&](int m) { const int ks = m / 3, ft = m % 3;
    if (ft == 0 && (w & 1) && l15 == 4) jf[ks * 4] = (u32x4){0x3F803F80u, 0x3F803F80u, 0x3F803F80u, 0x3F803F80u};
    jc[ft] = mfma16(jf[ks * 4 + 1 + ft], jf[ks * 4], ks ? jc[ft] : (f32x4){0.f, 0.f, 0.f, 0.f}); };
  auto kv_st = [&](int ft) { const int dt = w & 1;
    const unsigned vo = ((!dt || l15 < 8) && (ft < 2 || g < 2)) ? (dt ? g * 128 : g * 256) + l15 * 16 : 0x7FFF0000u;
    ST16S(__builtin_bit_cast(u32x4, jc[ft]), vo, WS_PART + (hq * 16 + rt) * PART_B + (w >> 1) * 3840 + (dt ? 2560 + ft * 512 : ft * 1024)); };
  f32x16 oa0, oa1; u32x4 z0, z1, z2;
  LAS char* otb = lp + L_WO + (8 + w) * 5120;
  auto oa_mm = [&](int m) { const int s = m >> 1; if (m & 1) oa1 = mfma32(ka[5 + s], ph[s], s ? oa1 : Z); else oa0 = mfma32(ka[s], ph[s], s ? oa0 : Z); };
  auto oa_norm = [&]() { const float inv = __builtin_amdgcn_rcpf(oa1[4] + 1e-6f) * gta;
    LAS char* ar = otb + (r >> 4) * 1280 + l15 * 16; LAS char* ae = ar + h * 520; LAS char* ao = ar + 8 + h * 760;
#pragma unroll
    for (int t = 0; t < 5; t++) {
      const u32x2 v = t < 4 ? (u32x2){nz(pk(oa0[(4 * t) & 15] * inv, oa0[(4 * t + 1) & 15] * inv)), nz(pk(oa0[(4 * t + 2) & 15] * inv, oa0[(4 * t + 3) & 15] * inv))} : (u32x2){nz(pk(oa1[0] * inv, oa1[1] * inv)), nz(pk(oa1[2] * inv, oa1[3] * inv))};
      *(LAS u32x2*)(((t & 1) ? ao : ae) + (t >> 1) * 256) = v; }
    z0 = ldsr16(otb + ok1); z1 = ldsr16(otb + 1280 + ok1); z2 = ldsr16(ar + 1024); };
  auto oa_st = [&]() { const unsigned oa = WS_OA + (rt * 8 + w3 * 2) * OT_B + (hq * 2 + w2) * 1280;
    ST16S(z0, ok1, oa); ST16S(z1, ok1, oa + OT_B); ST16S(z2, h ? 0x7FFF0000u : (r >> 4) * OT_B + l15 * 16, oa + 1024); };
  unsigned cpk;
  PRIO(3);
  if (role) { TL(0, 0, FILL(0), NOP, NOP) TL(0, 1, FILL(1), NOP, NOP) TL(0, 2, FILL(2), NOP, NOP) }
  else {
    auto f0 = [&](int n) { if (n < 8) kv_rd(n); if (n >= 13) kv_mm(n - 13); };
    auto f1 = [&](int n) { if (n < 8) kv_rd(8 + n); if (n == 7) ARRIVE_NW(0); if (n >= 9 && n < 12) kv_mm(n - 6); if (n >= 13) kv_mm(n - 7); };
    auto f2 = [&](int n) { if (n >= 9 && n < 12) kv_mm(n); if (n >= 5 && n < 8) kv_st(n - 5); };
    TLF(0, f0) TLF(1, f1) TLF(2, f2)
  }
  TL(0, 3, dma4s((w2 ? woB : woA) + C0 + (((rt & 7) * 2 + role) * 40 + w3 * 10) * 640, (l15 < 10 ? l15 : 9) * 1280 + (g & 1) * 156, lds0 + 160768)        , NOP, NOP)
  TL(0, 4, NOP, NOP, if (role) FILL(3))
  PRIO(2);
  TL(0, 5, if (!role) { WAIT_VM(1); FLAG(WS_FPART + ((hq * 16 + rt) * 8 + w) * 8); }, NOP, cpk = PEEK(0))
  AWAIT(0, cpk);
  asm volatile("" ::: "memory");
  TL6(0, KX(2, 2), KW(2, 2, 0))
  TL7(0, 1, KW(2, 2, 1), KW(2, 2, 2), KW(2, 2, 3))
  TL(1, 0, KW(2, 2, 4), if (role) FILL(4), if (!role) { fp0 = POLL(WS_FPART + hq * 1024); fp1 = POLL(WS_FPART + hq * 1024 + 512); })
  TL(1, 1, if (role) { WX(0); WX(1); }, if (role) FILL(5), if (role) { WX(2); WX(3); })
  TL(1, 2, if (role) WX(4), NOP, if (red) red_load())
  PRIO(1);
  TL(1, 3, NOP, NOP, NOP)
  TL(1, 4, NOP, if (!role) CLR_VM(), if (red) red_store())
  TL(1, 5, NOP, NOP, NOP)
  PRIO(0);
  TL6(1, if (!role) { WAIT_VM(0); FLAG((w2 ? WS_SPARE : WS_FKVR) + ((hq * 16 + rt) * 4 + w3) * 8); }, NOP)
  if (!role) WAIT_BAR(1); else WAIT_BAR(0);
  STAMP(12);
  PRIO(3);
  if (role) FLAG(WS_FOA + ((rt * 8 + hq) * 8 + w) * 8);
  u64 foa;
  TL7(1, 2, KX(3, 0), KW(3, 0, 0), KW(3, 0, 1))
  TL(2, 0, KW(3, 0, 2), KW(3, 0, 3), KW(3, 0, 4)) TL(2, 1, WA(w, 0), WA(w, 1), WA(w, 2))
  u32x4 wbr[11];
  const char* wbs = (const char*)(woB + C0 + w * 32 * 640) - 4096;
#define WB1(n) wbr[n] = *(const GAS u32x4*)(wbs + ((n) / 5) * (8 * 32 * 1280) + woff[(n) % 5] + ((n) % 5) * 1024)
#define WB2(j) do { WB1(j); WB1(5 + (j)); } while (0)
#define WA16() dma16s(woA + C0 + 16 * 32 * 640 - 2048, wo16, lds0 + L_WO + 16 * 5120 + w5 * 1024)
#define WB16() wbr[10] = *(const GAS u32x4*)((const char*)(woB + C0 + 16 * 32 * 640) - 4096 + wo16)
  auto chk = [&]() { asm volatile("" : "+v"(foa));
    if (__builtin_expect(!__all(foa == tag), 0)) wait_flags<64>(ws, WS_FOA + rt * 512, tag, lane);
    asm volatile("" ::: "memory"); };
  if (role) {
    TL(2, 2, WA(w, 3), WA(w, 4), NOP)
    PRIO(2);
    TL(2, 3, (fkr = POLL(WS_FKVR + hq * 512), foa = POLL(WS_FOA + rt * 512)), NOP, NOP)
    TL(2, 4, NOP, { chk(); oa_load(); }, { ka[2] = LD16S(kb, kbs + 1024); ka[7] = LD16S(kb, kbs + 3584); ka[3] = LD16S(kb, kbs + 1536); })
    PRIO(1);
    TL(2, 5, { ka[8] = LD16S(kb, kbs + 4096); ka[4] = LD16S(kb, kbs + 2048); ka[9] = LD16S(kb, kbs + 4608); }, NOP, NOP)
    PRIO(0);
    { auto f = [&](int n) { if (n == 5) v2n = ldsr16(d2p + 7 * TILE_B + 2 * VB); if (n >= 8 && n != 10 && n != 14) oa_mm(n < 10 ? n - 8 : (n < 14 ? n - 9 : 5)); }; TLF(6, f) }
  } else {
    TL(2, 2, WA(w, 3), WA(w, 4), WX(0))
    PRIO(2);
    TL(2, 3, { WX(1); WX(2); }, { WX(3); WX(4); }, foa = POLL(WS_FOA + rt * 512))
    TL(2, 4, NOP, NOP, NOP)
    PRIO(1);
    TL(2, 5, chk(), WB1(0), WB1(5))
    PRIO(0);
    TL6(2, WB1(1), WB1(6))
  }
  BAR_L();
  STAMP(13);
  PRIO(3);
  if (role) {
    d0p -= 2 * STEP_B; d1p -= 2 * STEP_B; d2p -= 2 * STEP_B; asm volatile("" : "+v"(d0p), "+v"(d1p), "+v"(d2p));
    { auto f = [&](int n) { if (n == 8 || n == 9) oa_mm(n - 2); if (n == 11 || n == 12) oa_mm(n - 3); if (n == 5) CLR_VM(); if (n == 6) WA16(); }; TILEF(0, v2 = v2n, f) }
    TL(0, 0, oa_norm(), NOP, oa_st())
    TL(0, 1, WA(8 + w, 0), WA(8 + w, 1), WA(8 + w, 2))
    TL(0, 2, WA(8 + w, 3), WA(8 + w, 4), WB2(0))
    TL(0, 3, WB2(1), NOP, WB2(2))
    PRIO(2);
    TL(0, 4, WB2(3), NOP, WB2(4))
  } else {
    TL7(2, 0, WA16(), WA(8 + w, 0), WA(8 + w, 1))
    TL(0, 0, WA(8 + w, 2), WA(8 + w, 3), WA(8 + w, 4))
    TL(0, 1, WB1(2), NOP, WB1(7)) TL(0, 2, WB1(3), NOP, WB1(8)) TL(0, 3, WB1(4), NOP, WB1(9))
    PRIO(2);
    TL(0, 4, NOP, NOP, NOP)
  }
  ARRIVE_NW(1);
  const int tp = w & 1, kq = w >> 1;
  const unsigned ofr = (rt * 8 + role * 4 + tp * 2) * OT_B + kq * 5120;
  u32x4 of[2][5]; float gtb;
#define OFL(u) do { _Pragma("unroll") for (int i = 0; i < 5; i++) of[u][i] = LD16S(ok1, WS_OA + ofr + (u) * OT_B + i * 1024); } while (0)
#define OF2(i) do { of[0][i] = LD16S(ok1, WS_OA + ofr + (i) * 1024); of[1][i] = LD16S(ok1, WS_OA + ofr + OT_B + (i) * 1024); } while (0)
  TL(0, 5, WB16(), gtb = __uint_as_float(__builtin_amdgcn_raw_buffer_load_b32(wsr, WS_G + (rt * 128 + w3 * 32 + w2 * 16 + l15) * 4, 0, 16)), OF2(0))
  PRIO(1);
  TL6(0, OF2(1), OF2(2))
  PRIO(0);
  LAS f32x4* xb = (LAS f32x4*)(lp + (w3 >> 1) * CH_B) + (w3 & 1) * (64 * 6) + lane;
  { auto f = [&](int n) { if (n == 0) cpk = PEEK(1); if (n == 3) OF2(3); if (n == 4) OF2(4);
      if (n == 15) { AWAIT(1, cpk);
#pragma unroll
        for (int c = 0; c < 3; c++) xb[(w2 * 3 + c) * 64] = (f32x4){OA[4 * c], OA[4 * c + 1], OA[4 * c + 2], OA[4 * c + 3]}; } };
    TILEF(0, v2 = v2n, f) }
  pB2[2] = pk(e0, e1); pB2[3] = pk(e2, e3);
  OB = mfma32(v2, pB2, OB);
#undef TL
#undef TL6
#undef TL7
#undef TILE
#undef G
  STAMP(7);
  u32x4 wf[5][5];
#define WFA(i) do { const LAS char* wb_ = lp + L_WO + (kq * 5 + (i)) * 5120 + wlane; _Pragma("unroll") for (int t = 0; t < 5; t++) wf[i][t] = trd2(wb_ + t * 8, wb_ + 1280 + t * 8); } while (0)
#define WFB(i) do { const int gr_ = kq * 5 + (i); const LAS char* wb_ = lp + (gr_ < 17 ? L_WO + gr_ * 5120 : 145408 + (gr_ - 17) * 5120) + wlane; _Pragma("unroll") for (int t = 0; t < 5; t++) wf[i][t] = trd2(wb_ + t * 8, wb_ + 1280 + t * 8); } while (0)
  { asm volatile("s_waitcnt vmcnt(18) lgkmcnt(0)\n\ts_barrier" ::: "memory");
    f32x16 O = OB;
#pragma unroll
    for (int c = 0; c < 3; c++) { const f32x4 t = xb[((w2 ^ 1) * 3 + c) * 64]; O[4 * c] += t[0]; O[4 * c + 1] += t[1]; O[4 * c + 2] += t[2]; O[4 * c + 3] += t[3]; }
    SBAR(); WFA(0); WFA(1); SBAR();
    float l = from_upper(O[8]);
    const float amax = (((fabsf(O[0]) + fabsf(O[1])) + (fabsf(O[2]) + fabsf(O[3]))) + ((fabsf(O[4]) + fabsf(O[5])) + (fabsf(O[6]) + fabsf(O[7])))) + ((fabsf(O[8]) + fabsf(O[9])) + (fabsf(O[10]) + fabsf(O[11])));
    const bool good = (l >= 0x1p-30f) && (l <= 0x1p100f) && (amax <= 0x1p126f);
    if (__builtin_expect(!__all(good), 0)) {
      const u32x4 q1 = q1B; const u32x2 q2 = h ? (u32x2){0u, 0u} : q2B;
      float m = -INFINITY;
#pragma unroll 1
      for (int t = 0; t < 64; t++) {
        f32x16 s = mfma32(LD16(WS_KV + t * TILE_B + ok1), q1, Z); s = mfma32k8(LD8(WS_KV + t * TILE_B + ok2), q2, s);
#pragma unroll
        for (int i = 0; i < 16; i++) m = fmaxf(m, s[i]);
      }
      m = half_swap_max(m);
      O = Z;
#pragma unroll 1
      for (int t = 0; t < 64; t++) {
        f32x16 s = mfma32(LD16(WS_KV + t * TILE_B + ok1), q1, Z); s = mfma32k8(LD8(WS_KV + t * TILE_B + ok2), q2, s);
#pragma unroll
        for (int i = 0; i < 16; i++) s[i] = ex2(s[i] - m);
        O = mfma32(LD16(WS_KV + t * TILE_B + ov), (u32x4){pk(s[0], s[1]), pk(s[2], s[3]), pk(s[4], s[5]), pk(s[6], s[7])}, O);
        O = mfma32(LD16(WS_KV + t * TILE_B + ov + 2 * VB), (u32x4){pk(s[8], s[9]), pk(s[10], s[11]), pk(s[12], s[13]), pk(s[14], s[15])}, O);
      }
      l = from_upper(O[8]);
    }
    const float inv = __builtin_amdgcn_rcpf(l) * (1.0f - gtb);
    LAS char* tb = lp + w2 * CH_B + 12288 + w3 * 1536;
    const int uq = (g & 1) * 5 + h;
    LAS char* t0 = tb + (uq >> 1) * 256 + (uq & 1) * 8 + l15 * 16; LAS char* t2 = h ? tb + 1280 + (g & 1) * 8 + l15 * 16 : t0 + 512;
#pragma unroll
    for (int c = 0; c < 3; c++) *(LAS u32x2*)(c < 2 ? t0 + c * 256 : t2) = (u32x2){nz(pk(O[4 * c] * inv, O[4 * c + 1] * inv)), nz(pk(O[4 * c + 2] * inv, O[4 * c + 3] * inv))};
    const unsigned ob = WS_OB + (rt * 8 + w3 * 2 + w2) * OT_B + hp * 1280;
    const u32x4 y0 = ldsr16(tb + ok1), y1 = ldsr16(tb + 1024 + l15 * 16);
    ST16S(y0, ok1, ob); ST16S(y1, lane < 16 ? ok1 : 0x7FFF0000u, ob + 1024);
    SBAR();
  }
  STAMP(8);
  auto of_missing = [&]() { u32x4 mm = of[0][0];
#pragma unroll
    for (int j = 1; j < 10; j++) mm = __builtin_elementwise_max(mm, of[j / 5][j % 5]);
    return missing(mm); };
  if (__builtin_expect(of_missing(), 0)) {
#pragma unroll 1
    for (unsigned spin = 0; spin < SPIN_CAP; ++spin) {
      asm volatile("" ::: "memory");
      OFL(0); OFL(1);
      if (!of_missing()) break;
      __builtin_amdgcn_s_sleep(1);
    }
  }
  f32x4 c[2][5];
#define MM(u, i, first) do { _Pragma("unroll") for (int t = 0; t < 5; t++) c[u][t] = mfma16(wf[i][t], of[u][i], (first) ? (f32x4){0.f, 0.f, 0.f, 0.f} : c[u][t]); SBAR(); } while (0)
#define OBL(u, i) of[u][i] = LD16S(ok1, WS_OB + ofr + (u) * OT_B + (i) * 1024)
#define MMR(u, i, first, j, ob_) do { const LAS char* wb_ = lp + (ob_) + wlane; _Pragma("unroll") for (int t = 0; t < 5; t++) { \
      c[u][t] = mfma16(wf[i][t], of[u][i], (first) ? (f32x4){0.f, 0.f, 0.f, 0.f} : c[u][t]); wf[j][t] = trd2(wb_ + t * 8, wb_ + 1280 + t * 8); SBAR(); } } while (0)
#define OA_(j) (L_WO + (kq * 5 + (j)) * 5120)
#define OB_(j) (kq * 5 + (j) < 17 ? L_WO + (kq * 5 + (j)) * 5120 : 145408 + (kq * 5 + (j) - 17) * 5120)
  MMR(0, 0, true, 2, OA_(2)); MMR(1, 0, true, 3, OA_(3)); MMR(0, 1, false, 4, OA_(4));
  BAR_L();
  SBAR();
  { LAS char* d = lp + L_WO + w * 5120 + lane * 16;
#pragma unroll
    for (int n = 0; n < 15; n++) { const int u = n < 5 ? 1 : (n >= 10), i = n < 5 ? 1 : 2, t = n % 5;
      if (n < 10) *(LAS u32x4*)(d + (n / 5) * 40960 + (n % 5) * 1024) = wbr[n];
      if (n == 10) *(LAS u32x4*)(lp + L_WO + 16 * 5120 + w5 * 1024 + lane * 16) = wbr[10];
      if (n == 11) OBL(0, 0); if (n == 12) OBL(1, 0); if (n == 13) OBL(0, 1); if (n == 14) OBL(1, 1);
      c[u][t] = mfma16(wf[i][t], of[u][i], c[u][t]); SBAR(); }
    asm volatile("" : "+v"(c[1][0])); }
  STAMP(9);
  BAR_L();
  SBAR();
  OBL(0, 2); OBL(1, 2); SBAR();
  MMR(0, 3, false, 0, OB_(0)); OBL(0, 3); SBAR(); MMR(0, 4, false, 1, OB_(1)); OBL(0, 4); SBAR(); MMR(1, 3, false, 2, OB_(2)); OBL(1, 3); SBAR(); MMR(1, 4, false, 3, OB_(3)); OBL(1, 4);
  WFB(4);
  ARRIVE_NW(2);
  SBAR();
  STAMP(10);
  LAS char* xw = lp + kq * 21504 + (tp * 32 + l15) * 336 + g * 80;
#pragma unroll
  for (int n = 0; n < 10; n++) {
    const int u = n < 6 ? n & 1 : (n >= 8), i = n < 6 ? n >> 1 : 3 + (n & 1);
    if (__builtin_expect(missing(of[u][i]), 0)) {
#pragma unroll 1
      for (unsigned spin = 0; spin < SPIN_CAP; ++spin) {
        asm volatile("" ::: "memory");
#pragma unroll
        for (int j = n; j < 10; j++) { const int uu = j < 6 ? j & 1 : (j >= 8), ii = j < 6 ? j >> 1 : 3 + (j & 1); OBL(uu, ii); }
        if (!missing(of[u][i])) break;
        __builtin_amdgcn_s_sleep(1);
      }
    }
    MM(u, i, false);
    if (n == 6) cpk = PEEK(2);
    if (n == 8) { AWAIT(2, cpk); *(LAS f32x4*)(xw) = c[0][0]; *(LAS f32x4*)(xw + 16) = c[0][1]; *(LAS f32x4*)(xw + 32) = c[0][2]; SBAR(); }
    if (n == 9) { *(LAS f32x4*)(xw + 48) = c[0][3]; *(LAS f32x4*)(xw + 64) = c[0][4]; SBAR(); }
  }
#pragma unroll
  for (int t = 0; t < 5; t++) *(LAS f32x4*)(xw + 16 * 336 + t * 16) = c[1][t];
  BAR_L();
  { const __amdgpu_buffer_rsrc_t orr = __builtin_amdgcn_make_buffer_rsrc(out, 0, 2048 * DM * 2, 0x00020000);
#pragma unroll
    for (int j = 0; j < 3; j++) { const int n = tid + 512 * j, nn = n < 1280 ? n : 0, row = (nn * 3277) >> 16, cq = nn - row * 20;
      const LAS f32x4* p = (const LAS f32x4*)(lp + row * 336 + cq * 16);
      const f32x4 z = (p[0] + p[1344]) + (p[2688] + p[4032]);
      __builtin_amdgcn_raw_buffer_store_b64((u32x2){pk(z[0], z[1]), pk(z[2], z[3])}, orr, n < 1280 ? ((rt * 128 + role * 64 + row) * DM + C0 + cq * 4) * 2 : 0x7FFF0000u, 0, 0); }
  }
  STAMP(11);
}

__global__ __launch_bounds__(512, 2) void fused(const u16* __restrict__ x, const u16* __restrict__ wqkv, const u16* __restrict__ wphi, const u16* __restrict__ woA,
                                                const u16* __restrict__ wq, const u16* __restrict__ wk, const u16* __restrict__ wv, const u16* __restrict__ woB,
                                                const u16* __restrict__ gatew, u16* __restrict__ out, char* ws) {
  __shared__ __attribute__((aligned(16))) char lds[L_TOTAL];
  if ((blockIdx.x >> 6) & 1) body<1>(x, wqkv, wphi, woA, wq, wv, woB, gatew, out, ws, (LAS char*)lds);
  else body<0>(x, wqkv, wphi, woA, wq, wk, woB, gatew, out, ws, (LAS char*)lds);
}
}

extern "C" void kernel_launch(void* const* d_in, const int* in_sizes, int n_in, void* d_out, int out_size, void* d_ws, size_t ws_size, hipStream_t stream) {
  using namespace gm;
  fused<<<256, 512, 0, stream>>>((const u16*)d_in[0], (const u16*)d_in[1], (const u16*)d_in[2], (const u16*)d_in[3], (const u16*)d_in[5], (const u16*)d_in[6],
                                 (const u16*)d_in[7], (const u16*)d_in[8], (const u16*)d_in[9], (u16*)d_out, (char*)d_ws);
}
```

```cpp
#include <hip/hip_runtime.h>
#include <hip/hip_bf16.h>
#include <stdint.h>

namespace gm {
typedef float f32x16 __attribute__((ext_vector_type(16)));
typedef float f32x4 __attribute__((ext_vector_type(4)));
typedef float f32x2 __attribute__((ext_vector_type(2)));
typedef short s16x4 __attribute__((ext_vector_type(4)));
typedef __bf16 b16x2 __attribute__((ext_vector_type(2)));
typedef __bf16 b16x8 __attribute__((ext_vector_type(8)));
typedef unsigned u32x4 __attribute__((ext_vector_type(4)));
typedef unsigned u32x2 __attribute__((ext_vector_type(2)));
typedef unsigned long long u64;
typedef unsigned short u16;
#define DI __device__ __forceinline__
#define LAS __attribute__((address_space(3)))
#define GAS __attribute__((address_space(1)))
typedef GAS u64 gu64;

constexpr int DM = 640;
constexpr unsigned WS_EPOCH = 0, WS_FKV = 2048, WS_FKVR = 4096, WS_FQ = 8192, WS_FPART = 16384, WS_FOA = 24576, WS_SPARE = 40960  , WS_G = 57344;
constexpr unsigned WS_KVR = 8912896, KVR_B = 10240, WS_KV = 131072, WS_QX = 327680, QX_B = 10240, WS_PART = 1703936, PART_B = 15360;
constexpr unsigned WS_OA = 3670016, WS_OB = 6291456, WS_END = 8994816, OT_B = 20480;
constexpr int VB = 352, TILE_B = 1280 + 4 * VB, CH_B = TILE_B * 8, STEP_B = 2 * CH_B;
constexpr int STAGE_B = 36864, ST_W1 = 10240, ST_X = 20480;
constexpr int L_BUF = 43008, L_P0W = 0, L_P0X0 = 25600, L_P0X1 = 153600, L_RED = 158720, L_RED1 = 40960;
constexpr int L_HO = 86016, L_QL = 96256, L_VIM = 96256, L_PHK = 116736, L_WPHI = 158592, L_ZERO = 161792, L_CNT = 163712, L_TOTAL = 163840;
constexpr int L_WO = 43008;
constexpr float LOG2E = 1.4426950408889634f;
constexpr float QSCALE = 0.22360679774997896f * LOG2E;
constexpr unsigned SPIN_CAP = 20000, LSPIN_CAP = 400000;

DI unsigned pk(float a, float b) { f32x2 v = {a, b}; b16x2 r = __builtin_convertvector(v, b16x2); return __builtin_bit_cast(unsigned, r); }
DI f32x16 mfma32(u32x4 a, u32x4 b, f32x16 c) { return __builtin_amdgcn_mfma_f32_32x32x16_bf16(__builtin_bit_cast(b16x8, a), __builtin_bit_cast(b16x8, b), c, 0, 0, 0); }
DI f32x16 mfma32k8(u32x2 a, u32x2 b, f32x16 c) { return __builtin_amdgcn_mfma_f32_32x32x8bf16_1k(__builtin_bit_cast(s16x4, a), __builtin_bit_cast(s16x4, b), c, 0, 0, 0); }
DI f32x4 mfma16(u32x4 a, u32x4 b, f32x4 c) { return __builtin_amdgcn_mfma_f32_16x16x32_bf16(__builtin_bit_cast(b16x8, a), __builtin_bit_cast(b16x8, b), c, 0, 0, 0); }
DI u32x2 trd(const LAS char* p) { return __builtin_bit_cast(u32x2, __builtin_amdgcn_ds_read_tr16_b64_v4i16((LAS s16x4*)p)); }
DI u32x4 trd2(const LAS char* p0, const LAS char* p1) { u32x2 a = trd(p0), b = trd(p1); return (u32x4){a.x, a.y, b.x, b.y}; }
DI u32x4 ldsr16(const LAS char* p) { return *(const LAS u32x4*)p; }
DI u32x2 ldsr8(const LAS char* p) { return *(const LAS u32x2*)p; }
DI float ex2(float x) { return __builtin_amdgcn_exp2f(x); }
DI void dma16s(const void* base, unsigned voff, unsigned lds_base) {
  asm volatile("s_mov_b32 m0, %2\n\ts_nop 0\n\tglobal_load_lds_dwordx4 %0, %1" :: "v"(voff), "s"(base), "s"(lds_base) : "memory"); }
DI void dma16s_sc1(const void* base, unsigned voff, unsigned lds_base) {
  asm volatile("s_mov_b32 m0, %2\n\ts_nop 0\n\tglobal_load_lds_dwordx4 %0, %1 sc1" :: "v"(voff), "s"(base), "s"(lds_base) : "memory"); }
DI void dmag_open(unsigned lds_base) { asm volatile("s_mov_b32 m0, %0\n\ts_nop 0" :: "s"(lds_base) : "memory"); }
template <int J> DI void dmag(const void* base, unsigned voff) { asm volatile("global_load_lds_dwordx4 %0, %1 offset:%2" :: "v"(voff), "s"(base), "i"((J & 3) * 1024) : "memory"); }
template <int J> DI void dmag_sc1(const void* base, unsigned voff) { asm volatile("global_load_lds_dwordx4 %0, %1 offset:%2 sc1" :: "v"(voff), "s"(base), "i"((J & 3) * 1024) : "memory"); }
DI void dma4s(const void* base, unsigned voff, unsigned lds_base) {
  asm volatile("s_mov_b32 m0, %2\n\ts_nop 0\n\tglobal_load_lds_dword %0, %1" :: "v"(voff), "s"(base), "s"(lds_base) : "memory"); }
DI u32x4 ld16_async(const void* p) { u32x4 r; asm volatile("global_load_dwordx4 %0, %1, off" : "=v"(r) : "v"(p) : "memory"); return r; }
DI u32x2 ld8_sc1_async(const void* p) { u32x2 r; asm volatile("global_load_dwordx2 %0, %1, off sc1" : "=v"(r) : "v"(p) : "memory"); return r; }
#define BAR_L() asm volatile("s_waitcnt lgkmcnt(0)\n\ts_barrier" ::: "memory")
#define WAIT_BAR(N) asm volatile("s_waitcnt vmcnt(" #N ") lgkmcnt(0)\n\ts_barrier" ::: "memory")
#define WAIT_VM(N) asm volatile("s_waitcnt vmcnt(" #N ")" ::: "memory")
#define SBAR() __builtin_amdgcn_sched_barrier(0)
#define RLX_AGENT __ATOMIC_RELAXED, __HIP_MEMORY_SCOPE_AGENT
DI float half_swap_max(float m) { auto rr = __builtin_amdgcn_permlane32_swap(__float_as_uint(m), __float_as_uint(m), false, false); return fmaxf(__uint_as_float(rr[0]), __uint_as_float(rr[1])); }
DI float from_upper(float v) { auto rr = __builtin_amdgcn_permlane32_swap(__float_as_uint(v), __float_as_uint(v), false, false); return __uint_as_float(rr[1]); }

template <int NF>
DI bool flags_set(const char* ws, unsigned off, u64 tag, int lane) {
  gu64* f = (gu64*)(ws + off);
  bool ok = __hip_atomic_load(f + (NF < 64 ? (lane & (NF - 1)) : lane), RLX_AGENT) == tag;
  if (NF >= 128) ok &= __hip_atomic_load(f + 64 + lane, RLX_AGENT) == tag;
  if (NF >= 256) { ok &= __hip_atomic_load(f + 128 + lane, RLX_AGENT) == tag; ok &= __hip_atomic_load(f + 192 + lane, RLX_AGENT) == tag; }
  return ok;
}
template <int NF>
DI void wait_flags(const char* ws, unsigned off, u64 tag, int lane) {
#pragma unroll 1
  for (unsigned spin = 0; spin < SPIN_CAP; ++spin) {
    if (__all(flags_set<NF>(ws, off, tag, lane))) break;
    __builtin_amdgcn_s_sleep(2);
  }
  asm volatile("" ::: "memory");
}
DI unsigned nz(unsigned v) { return v < 0xFFFFFFFEu ? v : 0xFFFFFFFEu; }
DI unsigned umax3(unsigned a, unsigned b, unsigned c) { unsigned m = a > b ? a : b; return m > c ? m : c; }
DI bool missing(u32x4 v) { return __any(umax3(umax3(v.x, v.y, v.z), v.w, 0u) == 0xFFFFFFFFu); }
#define ONES ((u32x4){0xFFFFFFFFu, 0xFFFFFFFFu, 0xFFFFFFFFu, 0xFFFFFFFFu})
#define ST16(v, off) __builtin_amdgcn_raw_buffer_store_b128((v), wsr, (off), 0, 16)
#define ST8(v, off) __builtin_amdgcn_raw_buffer_store_b64((v), wsr, (off), 0, 16)
#define LD16(off) __builtin_amdgcn_raw_buffer_load_b128(wsr, (off), 0, 16)
#define ST16S(v, voff, soff) ST16(v, (voff) + (soff))
#define LD16S(voff, soff) __builtin_amdgcn_raw_buffer_load_b128(wsr, (voff), (soff), 16)
#define LD8S(voff, soff) __builtin_amdgcn_raw_buffer_load_b64(wsr, (voff), (soff), 16)
#define LD8(off) __builtin_amdgcn_raw_buffer_load_b64(wsr, (off), 0, 16)

#define STAMP(i) do {} while (0)

template <int role>
DI void body(const u16* __restrict__ x, const u16* __restrict__ wqkv, const u16* __restrict__ wphi, const u16* __restrict__ woA,
             const u16* __restrict__ wq, const u16* __restrict__ wkv, const u16* __restrict__ woB,
             const u16* __restrict__ gatew, u16* __restrict__ out, char* ws, LAS char* lp) {
  const int tid = threadIdx.x, lane = tid & 63;
  const int w = __builtin_amdgcn_readfirstlane(tid >> 6) & 7;
  const int l15 = lane & 15, g = lane >> 4, q4 = l15 >> 2, p4 = lane & 3, r = lane & 31, h = lane >> 5;
  const int bx = blockIdx.x & 7, bi = blockIdx.x >> 3;
  const int rt = (bx & 1) * 8 + (bi & 7), hp = (bx >> 1) * 4 + ((bi >> 3) & 3), hq = hp >> 1;
  const int w3 = w & 3, w2 = w >> 2;
  const unsigned lds0 = (unsigned)(uintptr_t)lp;
  const __amdgpu_buffer_rsrc_t wsr = __builtin_amdgcn_make_buffer_rsrc(ws, 0, WS_END, 0x00020000);
  LAS unsigned* cnt = (LAS unsigned*)(lp + L_CNT);
#define ARRIVE_NW(c) do { asm volatile("" ::: "memory"); __hip_atomic_store(cnt + (c) * 8 + w, 1u, __ATOMIC_RELAXED, __HIP_MEMORY_SCOPE_WORKGROUP); asm volatile("" ::: "memory"); } while (0)
#define PEEK(c) __hip_atomic_load(cnt + (c) * 8 + (lane & 7), __ATOMIC_RELAXED, __HIP_MEMORY_SCOPE_WORKGROUP)
#define AWAIT(c, v) do { if (__builtin_expect(!__all((v) != 0u), 0)) { _Pragma("unroll 1") for (unsigned sp_ = 0; sp_ < LSPIN_CAP; ++sp_) { if (__all(PEEK(c) != 0u)) break; __builtin_amdgcn_s_sleep(1); } } asm volatile("" ::: "memory"); } while (0)
#define POLL(off) __builtin_bit_cast(u64, __builtin_amdgcn_raw_buffer_load_b64(wsr, lane * 8, (off), 16))
#define FLAG(off) __builtin_amdgcn_raw_buffer_store_b64(__builtin_bit_cast(u32x2, tag), wsr, lane ? 0x7FFF0000u : 0u, (off), 16)
  STAMP(0);

  const int T0 = rt * 128 + hq * 16;
  const int C0 = hq * 80;
  const int wlane = ((g >> 1) * 16 + (g & 1) * 4 + q4) * 160 + p4 * 40;
  u64 tag;
  unsigned o0[10];
#define KVP(s, slot, p) dma16s_sc1(ws + WS_KV + (s) * STEP_B + (p) * 1024, lane * 16, lds0 + (slot) * STEP_B + (p) * 1024)
#define KVL(s, slot) do { _Pragma("unroll") for (int j = 0; j < 11; j++) KVP(s, slot, j < 10 ? w3 + 4 * j : 40 + (w3 & 1)); } while (0)
#define KW(s, slot, j) do { if ((j) == 4) KVP(s, slot, w * 5 + 4); else { if ((j) == 0) dmag_open(lds0 + (slot) * STEP_B + w * 5120); dmag_sc1<(j)>(ws + WS_KV + (s) * STEP_B + w * 5120, lane * 16); } } while (0)
#define KX(s, slot) KVP(s, slot, 40 + (w & 1))
  const int w5 = w < 5 ? w : 4;
  unsigned wo16;
  unsigned woff[5];
  auto woff_init = [&]() { int ln = lane; asm volatile("" : "+v"(ln));
#pragma unroll
    for (int j = 0; j < 5; j++) { const int c = j * 64 + ln, rho = (c * 6554) >> 16, cc = c - rho * 10; const int k = (rho & ~12) | ((rho & 4) << 1) | ((rho & 8) >> 1); woff[j] = k * 1280 + cc * 16 + (4 - j) * 1024; }
    { const int c = w5 * 64 + ln, rho = (c * 6554) >> 16, cc = c - rho * 10; const int k = (rho & ~12) | ((rho & 4) << 1) | ((rho & 8) >> 1); wo16 = k * 1280 + cc * 16 + 4096; }
    asm volatile("" : "+v"(woff[0]), "+v"(woff[1]), "+v"(woff[2]), "+v"(woff[3]), "+v"(woff[4]), "+v"(wo16)); };
  if (w2 == 0) {
    u32x4 gf0, gf1, gf2, gf3, gf4, wpc;
    u32x2 ep = ld8_sc1_async(ws + WS_EPOCH);
    const int lx = w3 < 3 ? L_P0X0 + w3 * 5120 : L_P0X1;
    { const u16* wsrc = wkv + w3 * 3200;
#pragma unroll
      for (int j = 0; j < 5; j++) { const int n = j * 64 + lane, row = (n * 3277) >> 16, cc = n - row * 20; dma16s(x + T0 * DM + w3 * 160, row * 1280 + cc * 16, lds0 + lx + j * 1024); }
#pragma unroll
      for (int j = 0; j < 6; j++) dma16s(wsrc + j * 512, lane * 16, lds0 + L_P0W + w3 * 6400 + j * 1024);
      if (lane < 16) dma16s(wsrc + 6 * 512, lane * 16, lds0 + L_P0W + w3 * 6400 + 6 * 1024);
      const u16* gp = gatew + w3 * 160 + g * 8;
      gf0 = ld16_async(gp); gf1 = ld16_async(gp + 32); gf2 = ld16_async(gp + 64); gf3 = ld16_async(gp + 96); gf4 = ld16_async(gp + 128);
      wpc = ld16_async(wphi + hq * 1600 + (tid < 200 ? tid : 199) * 8);
    }
    woff_init();
    asm volatile("s_waitcnt vmcnt(0)" : "+v"(gf0), "+v"(gf1), "+v"(gf2), "+v"(gf3), "+v"(gf4), "+v"(wpc), "+v"(ep) :: "memory");
    tag = (((u64)(unsigned)__builtin_amdgcn_readfirstlane(ep.y) << 32) | (unsigned)__builtin_amdgcn_readfirstlane(ep.x)) + 1ull;
    STAMP(1);
    { const LAS char* wb = lp + L_P0W + (w3 * 160 + g * 8 + q4) * 40;
      const LAS char* xb = lp + lx + l15 * 320 + g * 16;
      const bool gl = l15 == 4;
      f32x4 c0 = {}, c1 = {};
#define P0STEP(s, GF) { const u32x4 b = ldsr16(xb + (s) * 64); c0 = mfma16(trd2(wb + p4 * 8 + (s) * 1280, wb + p4 * 8 + (s) * 1280 + 160), b, c0); \
        u32x4 a = trd2(wb + 32 + (s) * 1280, wb + 32 + (s) * 1280 + 160); if (gl) a = GF; c1 = mfma16(a, b, c1); }
      P0STEP(0, gf0) P0STEP(1, gf1) P0STEP(2, gf2) P0STEP(3, gf3) P0STEP(4, gf4)
#undef P0STEP
      *(LAS f32x4*)(lp + L_RED + tid * 16) = c0;
      if (lane < 32) *(LAS f32x4*)(lp + L_RED1 + (w3 * 32 + lane) * 16) = c1; }
    STAMP(2);
    f32x16 acc[5];
#pragma unroll
    for (int b = 0; b < 5; b++) acc[b] = (f32x16){};
    const int wm = g & 1, whd = p4;
    const int wo0 = wm * ST_W1 + (8 * h + 4 * wm + q4) * 160 + whd * 40, wo1 = wm * ST_W1 + (8 * h + 4 * (wm ^ 1) + q4) * 160 + whd * 40;
    const int xo = ST_X + (w3 * 32 + r) * 128 + ((h ^ ((r >> 1) & 7)) * 16);
    u32x4 fa[5], fb[5], xa, xb;
#define KSTEP(FN, XN, s, FC, XC) { XN = ldsr16(sb + (xo ^ ((s) * 32))); \
      _Pragma("unroll") for (int j = 0; j < 5; j++) { if (i > 0 || (s) > 0) acc[j] = mfma32(FC[j], XC, acc[j]); FN[j] = trd2(sb + wo0 + (s) * 2560 + j * 8, sb + wo1 + (s) * 2560 + j * 8); SBAR(); } }
#pragma unroll
    for (int i = 0; i < 10; i++) {
      BAR_L();
      if (i == 1 && role) {
        const unsigned qb = WS_QX + (rt * 8 + hq) * QX_B + w3 * 1280 + lane * 16;
        ST16(ONES, qb); ST16(ONES, qb + 5120); ST16(ONES, lane < 32 ? qb + 1024 + (lane >> 4) * (5120 - 256) : 0x7FFF0000u); }
      if (i == 4) { if (role) { WAIT_VM(0); FLAG(WS_FQ + ((rt * 8 + hq) * 4 + w3) * 8); }
                    if (tid < 200) *(LAS u32x4*)(lp + L_WPHI + tid * 16) = wpc; }
      const LAS char* sb = lp + L_BUF + ((i + 2) % 3) * STAGE_B;
      KSTEP(fa, xa, 0, fb, xb) KSTEP(fb, xb, 1, fa, xa) KSTEP(fa, xa, 2, fb, xb)
      { xb = ldsr16(sb + (xo ^ 96));
        acc[0] = mfma32(fa[0], xa, acc[0]); fb[0] = trd2(sb + wo0 + 7680, sb + wo1 + 7680); fb[1] = trd2(sb + wo0 + 7688, sb + wo1 + 7688); SBAR();
        acc[1] = mfma32(fa[1], xa, acc[1]); fb[2] = trd2(sb + wo0 + 7696, sb + wo1 + 7696); fb[3] = trd2(sb + wo0 + 7704, sb + wo1 + 7704); SBAR();
        acc[2] = mfma32(fa[2], xa, acc[2]); fb[4] = trd2(sb + wo0 + 7712, sb + wo1 + 7712); SBAR();
        acc[3] = mfma32(fa[3], xa, acc[3]); acc[4] = mfma32(fa[4], xa, acc[4]); SBAR(); }
    }
#pragma unroll
    for (int j = 0; j < 5; j++) acc[j] = mfma32(fb[j], xb, acc[j]);
#undef KSTEP
    STAMP(3);
#define PK2(t, i, sc) pk(acc[t][i] * (sc), acc[t][(i) + 1] * (sc))
#define ROW(i, sc) const u32x4 ra = {PK2(0, i, sc), PK2(0, (i) + 2, sc), PK2(1, i, sc), PK2(1, (i) + 2, sc)}, rb = {PK2(2, i, sc), PK2(2, (i) + 2, sc), PK2(3, i, sc), PK2(3, (i) + 2, sc)}; const u32x2 rc = {PK2(4, i, sc), PK2(4, (i) + 2, sc)}
    { ROW(4, 1.0f);
      *(LAS u32x4*)(lp + L_HO + tid * 16) = ra; *(LAS u32x4*)(lp + L_HO + 4096 + tid * 16) = rb; *(LAS u32x2*)(lp + L_HO + 8192 + tid * 8) = rc; }
    BAR_L();
    const int qfo = (h * 4 + w3) * 1280;
    if (role) {
      { ROW(8, QSCALE); const unsigned qb = WS_QX + (rt * 8 + hq) * QX_B + qfo;
        ST16(((u32x4){nz(ra.x), nz(ra.y), nz(ra.z), nz(ra.w)}), qb + r * 16); ST16(((u32x4){nz(rb.x), nz(rb.y), nz(rb.z), nz(rb.w)}), qb + 512 + r * 16); ST8(((u32x2){nz(rc.x), nz(rc.y)}), qb + 1024 + r * 8); }
      { ROW(12, QSCALE); LAS char* d = lp + L_QL + qfo;
        *(LAS u32x4*)(d + r * 16) = ra; *(LAS u32x4*)(d + 512 + r * 16) = rb; *(LAS u32x2*)(d + 1024 + r * 8) = rc; }
    } else { LAS char* d = lp + L_VIM + (h * 128 + w3 * 32 + r) * 40;
#pragma unroll
      for (int t = 0; t < 5; t++) { *(LAS u32x2*)(d + t * 8) = (u32x2){PK2(t, 8, 1.0f), PK2(t, 10, 1.0f)}; *(LAS u32x2*)(d + 10240 + t * 8) = (u32x2){PK2(t, 12, 1.0f), PK2(t, 14, 1.0f)}; } }
#pragma unroll
    for (int t = 0; t < 5; t++) { o0[2 * t] = PK2(t, 0, 1.0f); o0[2 * t + 1] = PK2(t, 2, 1.0f); }
#undef ROW
#undef PK2
  } else {
    u32x2 ep = ld8_sc1_async(ws + WS_EPOCH);
    const bool second = w3 >> 1;
    const unsigned rs = (second && role) ? 1280u : 3840u;
    const char* wsrc = (second ? (role ? (const char*)(wq + C0) : (const char*)(wqkv + 1280 + C0)) : (const char*)(wqkv + (role ? 0 : 640) + C0)) + (w3 & 1) * 32 * rs;
    const char* xsrc = (const char*)(x + (rt * 128 + w3 * 32) * DM);
    int rot = (bi & 7) + 3 * ((bi >> 3) & 3); rot = rot >= 10 ? rot - 10 : rot;
    unsigned so[5], xoff[2];
#pragma unroll
    for (int j = 0; j < 5; j++) { const int c = j * 64 + lane, rho = (c * 6554) >> 16, cc = c - rho * 10; so[j] = (rho ^ (second ? 4 : 0)) * rs + cc * 16; }
#pragma unroll
    for (int j = 0; j < 2; j++) { const int row = j * 8 + (lane >> 3); xoff[j] = row * 1280 + (((lane & 7) ^ ((row >> 1) & 7)) * 16); }
    int bsel = 2, kc = rot;
    auto stage = [&]() {
      const unsigned lb = lds0 + L_BUF + bsel * STAGE_B;
      const char* bw = wsrc + kc * 64 * rs; const char* bxp = xsrc + kc * 128;
#pragma unroll
      for (int j = 0; j < 5; j++) { dma16s(bw, so[j], lb + (w3 * 5 + j) * 1024); if (j < 4) dma16s(bxp + (j >> 1) * 16 * 1280, xoff[j & 1], lb + ST_X + (w3 * 4 + j) * 1024); }
      bsel = bsel == 2 ? 0 : bsel + 1; kc = kc == 9 ? 0 : kc + 1;
    };
    stage(); stage();
    woff_init();
    asm volatile("s_waitcnt vmcnt(9)" : "+v"(ep) :: "memory");
    tag = (((u64)(unsigned)__builtin_amdgcn_readfirstlane(ep.y) << 32) | (unsigned)__builtin_amdgcn_readfirstlane(ep.x)) + 1ull;
    STAMP(1); STAMP(2);
#pragma unroll 1
    for (int i = 0; i < 8; i++) {
      WAIT_BAR(9);
      stage();
      if (i == 0 && w3 == 0) {
        const unsigned tb = WS_KV + (T0 >> 5) * TILE_B; const int s16 = hq & 1;
        if (!role) {
          const LAS f32x4* rp = (const LAS f32x4*)(lp + (h ? L_RED1 : L_RED)) + (h ? (g & 1) * 16 : g * 32) + l15;
          const int st = h ? 32 : 64, o2 = h ? 0 : 16;
          const f32x4 a = (rp[0] + rp[st]) + (rp[2 * st] + rp[3 * st]), b = (rp[o2] + rp[st + o2]) + (rp[2 * st + o2] + rp[3 * st + o2]);
          const u32x4 v = {pk(a[0], a[1]), pk(a[2], a[3]), pk(b[0], b[1]), pk(b[2], b[3])};
          if (lane < 32) ST16(v, tb + (g * 32 + s16 * 16 + l15) * 16);
          else if (lane < 48) ST8(((u32x2){v.x, v.y}), tb + 1024 + (s16 * 16 + l15) * 8);
          else { const float gv = 1.0f / (1.0f + __expf(-a[0])); __builtin_amdgcn_raw_buffer_store_b32(__float_as_uint(gv), wsr, WS_G + (T0 + l15) * 4, 0, 16); }
        } else if (lane < 44) {
          const int hh = lane >= 22, d = lane - hh * 22, dd = d < 20 ? d : 0, st = dd >= 16 ? 128 : 256;
          const LAS float* rp = (const LAS float*)(lp + (dd >= 16 ? L_RED1 : L_RED)) + (((dd & 15) >> 2) * 16 + 4 * hh) * 4 + (dd & 3);
          float e[8];
#pragma unroll
          for (int j = 0; j < 8; j++) { const int tk = (8 * (j >> 2) + (j & 3)) * 4; e[j] = (rp[tk] + rp[tk + st]) + (rp[tk + 2 * st] + rp[tk + 3 * st]); }
          u32x4 v = {pk(e[0], e[1]), pk(e[2], e[3]), pk(e[4], e[5]), pk(e[6], e[7])};
          if (d == 20) v = (u32x4){0x3F803F80u, 0x3F803F80u, 0x3F803F80u, 0x3F803F80u};
          if (d == 21) v = (u32x4){0u, 0u, 0u, 0u};
          ST16(v, tb + 1280 + (s16 * 2 + hh) * VB + d * 16);
        }
      }
      if (i == 2) { if (w3 == 0) FLAG(WS_FKV + blockIdx.x * 8);
                    if (w3 >= 2) *(LAS u32x4*)(lp + L_ZERO + (tid - 384) * 16) = (u32x4){0u, 0u, 0u, 0u}; }
    }
    u64 fa, fb, fc, fd, fe;
    { gu64* f = (gu64*)(ws + WS_FKV) + lane;
      fa = __hip_atomic_load(f, RLX_AGENT); fb = __hip_atomic_load(f + 64, RLX_AGENT); fc = __hip_atomic_load(f + 128, RLX_AGENT); fd = __hip_atomic_load(f + 192, RLX_AGENT);
      fe = __hip_atomic_load((gu64*)(ws + WS_FQ) + (rt * 8 + hq) * 4 + p4, RLX_AGENT); }
    WAIT_BAR(14);
    asm volatile("" : "+v"(fa), "+v"(fb), "+v"(fc), "+v"(fd), "+v"(fe));
    const bool kvok = __all((fa == tag) & (fb == tag) & (fc == tag) & (fd == tag) & (fe == tag));
    asm volatile("" ::: "memory");
    BAR_L();
    if (__builtin_expect(!kvok, 0)) { wait_flags<256>(ws, WS_FKV, tag, lane); wait_flags<4>(ws, WS_FQ + (rt * 8 + hq) * 32, tag, lane); }
    KVL(0, 0);
    if (blockIdx.x == 0 && w3 == 0) FLAG(WS_EPOCH);
    STAMP(3);
    BAR_L();
    const int ht = tid - 256;
    const u32x4 a = ldsr16(lp + L_HO + ht * 16), b = ldsr16(lp + L_HO + 4096 + ht * 16); const u32x2 c = ldsr8(lp + L_HO + 8192 + ht * 8);
    o0[0] = a.x; o0[1] = a.y; o0[2] = a.z; o0[3] = a.w; o0[4] = b.x; o0[5] = b.y; o0[6] = b.z; o0[7] = b.w; o0[8] = c.x; o0[9] = c.y;
  }
  STAMP(4);
  u32x4 ph[5];
  { const LAS char* pb = ((p4 & 1) == h) ? lp + L_WPHI + (2 * w2 + h) * 800 + q4 * 40 + (2 * (g & 1) + (p4 >> 1)) * 8 : lp + L_ZERO;
    const u32x4 b0 = {o0[0], o0[1], o0[2], o0[3]}, b1 = {o0[4], o0[5], o0[6], o0[7]}, b2 = {o0[8], o0[9], 0u, 0u};
    const u32x2 c0 = trd(pb + 640), c1 = trd(pb + 672);
    f32x16 P0 = {}, P1 = {};
    P0 = mfma32(trd2(pb, pb + 160), b0, P0); P1 = mfma32(trd2(pb + 32, pb + 192), b0, P1);
    P0 = mfma32(trd2(pb + 320, pb + 480), b1, P0); P1 = mfma32(trd2(pb + 352, pb + 512), b1, P1);
    P0 = mfma32((u32x4){c0.x, c0.y, 0u, 0u}, b2, P0); P1 = mfma32((u32x4){c1.x, c1.y, 0u, 0u}, b2, P1);
    KX(1, 1); KW(1, 1, 0); SBAR();
    f32x2 p[10];
#pragma unroll
    for (int f = 0; f < 10; f++) p[f] = f < 8 ? (f32x2){P0[(2 * f) & 15], P0[(2 * f + 1) & 15]} : (f32x2){P1[(2 * f) & 3], P1[(2 * f + 1) & 3]};
    float mx = fmaxf(p[0][0], p[0][1]), mn = fminf(p[0][0], p[0][1]);
#pragma unroll
    for (int f = 1; f < 10; f++) { mx = fmaxf(fmaxf(mx, p[f][0]), p[f][1]); mn = fminf(fminf(mn, p[f][0]), p[f][1]); }
    const f32x2 ma = {-mx * LOG2E, -mx * LOG2E}, mb = {mn * LOG2E, mn * LOG2E}, ca_ = {LOG2E, LOG2E}, cb_ = {-LOG2E, -LOG2E};
    f32x2 ea[10], eb[10], sa = {0.f, 0.f}, sb = {0.f, 0.f};
#pragma unroll
    for (int f = 0; f < 10; f++) { if (f % 2 == 0 && f < 8) { SBAR(); if (f == 0) KW(1, 1, 1); if (f == 2) KW(1, 1, 2); if (f == 4) KW(1, 1, 3); if (f == 6) KW(1, 1, 4); SBAR(); }
      const f32x2 xa = __builtin_elementwise_fma(p[f], ca_, ma), xb = __builtin_elementwise_fma(p[f], cb_, mb);
      ea[f] = (f32x2){ex2(xa[0]), ex2(xa[1])}; eb[f] = (f32x2){ex2(xb[0]), ex2(xb[1])}; sa += ea[f]; sb += eb[f]; }
    const float ia = __builtin_amdgcn_rcpf(sa[0] + sa[1]), ib = __builtin_amdgcn_rcpf(sb[0] + sb[1]);
    unsigned o[20];
#pragma unroll
    for (int f = 0; f < 10; f++) { const f32x2 ya = ea[f] * ia, yb = eb[f] * ib; o[f] = pk(ya[0], ya[1]); o[10 + f] = pk(yb[0], yb[1]); }
#pragma unroll
    for (int c = 0; c < 5; c++) ph[c] = (u32x4){o[4 * c], o[4 * c + 1], o[4 * c + 2], o[4 * c + 3]};
    asm volatile("" : "+v"(ph[0]), "+v"(ph[1]), "+v"(ph[2]), "+v"(ph[3]), "+v"(ph[4]));
  }
  u32x4 q1A, q1B; u32x2 q2A, q2B;
  const int qo1 = (g & 1) * 5120 + (h * 32 + l15) * 16, qo2 = (g & 1) * 5120 + 1024 + l15 * 8;
  if (role) {
    WAIT_BAR(0);
    STAMP(5);
    const LAS char* qa = lp + L_QL + w3 * 1280;
    q1A = ldsr16(qa + qo1 + (w2 ^ 1) * 256); q1B = ldsr16(qa + qo1 + w2 * 256); q2A = ldsr8(qa + qo2 + (w2 ^ 1) * 128); q2B = ldsr8(qa + qo2 + w2 * 128);
    ARRIVE_NW(0);
  } else {
    const unsigned qb = WS_QX + (rt * 8 + hq) * QX_B + w3 * 1280;
    q1A = LD16S(qo1, qb + (w2 ^ 1) * 256); q1B = LD16S(qo1, qb + w2 * 256); q2A = LD8S(qo2, qb + (w2 ^ 1) * 128); q2B = LD8S(qo2, qb + w2 * 128);
    { LAS char* d = lp + L_PHK + ((2 * w2 + h) * 128 + w3 * 32 + r) * 80;
#pragma unroll
      for (int c = 0; c < 5; c++) *(LAS u32x4*)(d + c * 16) = ph[c]; }
    WAIT_BAR(4);
    STAMP(5);
#pragma unroll 1
    for (unsigned spin = 0; spin < SPIN_CAP; ++spin) {
      if (!(missing(q1A) | missing(q1B) | missing((u32x4){q2A.x, q2A.y, q2B.x, q2B.y}))) break;
      __builtin_amdgcn_s_sleep(1);
      asm volatile("" ::: "memory");
      q1A = LD16S(qo1, qb + (w2 ^ 1) * 256); q1B = LD16S(qo1, qb + w2 * 256); q2A = LD8S(qo2, qb + (w2 ^ 1) * 128); q2B = LD8S(qo2, qb + w2 * 128);
    }
  }
  STAMP(6);
  const int vr = r <= 21 ? r : 21;
  const int ok1 = lane * 16, ok2 = h ? (1280 + 20 * 16) : (1024 + r * 8), ov = 1280 + h * VB + vr * 16;
  const f32x16 Z = {};
  if (h) { q2A = (u32x2){0u, 0u}; q2B = (u32x2){0u, 0u}; }
  { const LAS char* t0 = lp;
    const u32x4 k1 = ldsr16(t0 + ok1); const u32x2 k2 = ldsr8(t0 + ok2);
    f32x16 sa = mfma32(k1, q1A, Z); sa = mfma32k8(k2, q2A, sa);
    f32x16 sb = mfma32(k1, q1B, Z); sb = mfma32k8(k2, q2B, sb);
    float ma = sa[0], mb = sb[0];
#pragma unroll
    for (int i = 1; i < 16; i++) { ma = fmaxf(ma, sa[i]); mb = fmaxf(mb, sb[i]); }
    ma = half_swap_max(ma); mb = half_swap_max(mb);
    const float mah = __uint_as_float(__float_as_uint(ma) & 0xFFFF0000u), mbh = __uint_as_float(__float_as_uint(mb) & 0xFFFF0000u);
    if (h) { q2A.x = pk(-mah, mah - ma); q2B.x = pk(-mbh, mbh - mb); } }
  f32x16 OA = {}, OB = {}, SA, SB;
  u32x4 k1, v1, v2, v2n, pA1, pA2, pB1, pB2; u32x2 k2;
  float e0, e1, e2, e3;
  const LAS char* c0p = lp + w2 * CH_B + ok1; const LAS char* c1p = lp + w2 * CH_B + ok2; const LAS char* c2p = lp + w2 * CH_B + ov;
  k1 = ldsr16(c0p); k2 = ldsr8(c1p);
  SA = mfma32(k1, q1A, Z); SA = mfma32k8(k2, q2A, SA);
  v1 = ldsr16(c2p); v2 = v1;
  e0 = e1 = e2 = e3 = 0.f; pB2 = (u32x4){0u, 0u, 0u, 0u};
#define G(MF, LD, H, M, C0_, C1_, X, S_, i) MF; LD; H; SBAR(); C0_; C1_; SBAR(); X##0 = ex2(S_[i]); X##1 = ex2(S_[i + 1]); SBAR(); M; SBAR(); X##2 = ex2(S_[i + 2]); X##3 = ex2(S_[i + 3]); \
    asm volatile("" : "+v"(X##0), "+v"(X##1), "+v"(X##2), "+v"(X##3)); SBAR();
#define TILEF(TN, V2, F) { float a0, a1, a2, a3; \
    G(SB = mfma32(k1, q1B, Z),    k1 = ldsr16(d0p + (TN)), F(0), F(8),  pB2[2] = pk(e0, e1), pB2[3] = pk(e2, e3), a, SA, 0) \
    G(OB = mfma32(v2, pB2, OB),   V2,                      F(1), F(9),  pA1[0] = pk(a0, a1), pA1[1] = pk(a2, a3), e, SA, 4) \
    G(SB = mfma32k8(k2, q2B, SB), k2 = ldsr8(d1p + (TN)),  F(2), F(10), pA1[2] = pk(e0, e1), pA1[3] = pk(e2, e3), a, SA, 8) \
    G(OA = mfma32(v1, pA1, OA),   (void)0,                 F(3), F(11), pA2[0] = pk(a0, a1), pA2[1] = pk(a2, a3), e, SA, 12) \
    G(SA = mfma32(k1, q1A, Z),    (void)0,                 F(4), F(12), pA2[2] = pk(e0, e1), pA2[3] = pk(e2, e3), a, SB, 0) \
    G(OA = mfma32(v2, pA2, OA),   (void)0,                 F(5), F(13), pB1[0] = pk(a0, a1), pB1[1] = pk(a2, a3), e, SB, 4) \
    G(SA = mfma32k8(k2, q2A, SA), (void)0,                 F(6), F(14), pB1[2] = pk(e0, e1), pB1[3] = pk(e2, e3), a, SB, 8) \
    G(OB = mfma32(v1, pB1, OB),   v1 = ldsr16(d2p + (TN)), F(7), F(15), pB2[0] = pk(a0, a1), pB2[1] = pk(a2, a3), e, SB, 12) }
#define TILE(TB, TN, V2, H0, H1, H2) { auto f_ = [&](int n) { if (n == 3) { H0; } if (n == 4) { H1; } if (n == 5) { H2; } }; TILEF(TN, V2, f_) }
#define TLF(t, F) TILEF(((t) + 1) * TILE_B, v2 = ldsr16(d2p + (t) * TILE_B + 2 * VB), F)
#define NOP (void)0
  const LAS char *d0p = c0p, *d1p = c1p, *d2p = c2p;
#define TL(sl, t, H0, H1, H2) TILE(0, ((t) + 1) * TILE_B, v2 = ldsr16(d2p + (t) * TILE_B + 2 * VB), H0, H1, H2)
#define TL6(sl, H0, H1) TL(sl, 6, H0, H1, v2n = ldsr16(d2p + 7 * TILE_B + 2 * VB))
#define TL7(sl, sn, H0, H1, H2) d0p += ((sn) - (sl)) * STEP_B; d1p += ((sn) - (sl)) * STEP_B; d2p += ((sn) - (sl)) * STEP_B; asm volatile("" : "+v"(d0p), "+v"(d1p), "+v"(d2p)); TILE(0, 0, v2 = v2n, H0, H1, H2)
#define WOG(wo, gr, j, dst) do { if ((j) == 4) WOP(wo, gr, 4, dst); else { if ((j) == 0) dmag_open(lds0 + (dst)); dmag<(j)>((wo) + C0 + (gr) * 32 * 640 - 2048, woff[j]); } } while (0)
#define WOP(wo, gr, j, dst) dma16s((wo) + C0 + (gr) * 32 * 640 - (4 - (j)) * 512, woff[j], lds0 + (dst) + (j) * 1024)
#define WA(gr, j) WOG(woA, gr, j, L_WO + (gr) * 5120)
  const int wx3 = w3 ? w3 : 1;
  const u16* wxs = (w2 ? woB : woA) + (16 + wx3) * 32 * 640;
  const int wxd = w2 ? 145408 + (wx3 - 1) * 5120 : L_WO + (16 + wx3) * 5120;
#define WX(j) WOG(wxs, 0, j, wxd)
#define PRIO(n) __builtin_amdgcn_s_setprio(n)
  u32x4 ones = ONES; if (role) asm volatile("" : "+v"(ones));
#define FILL(j) do { const int n_ = w * 3 + ((j) >> 1); ST16S(ones, ok1, (((j) & 1) ? WS_OB : WS_OA) + (rt * 8 + hq) * OT_B + (n_ < 20 ? n_ : 19) * 1024); } while (0)
#define CLR_VM() __builtin_amdgcn_s_waitcnt(0x0F70)
  const bool red = !role && !w2;
  f32x4 part[4]; u64 fp0, fp1;
  auto red_load = [&]() {
    asm volatile("" : "+v"(fp0), "+v"(fp1));
    if (__builtin_expect(!__all((fp0 == tag) & (fp1 == tag)), 0)) wait_flags<128>(ws, WS_FPART + hq * 1024, tag, lane);
    asm volatile("" ::: "memory");
#pragma unroll
    for (int j = 0; j < 4; j++) part[j] = __builtin_bit_cast(f32x4, LD16S(g * (4 * PART_B) + l15 * 16, WS_PART + (hq * 16 + j) * PART_B + rt * 960 + w3 * 240));
  };
  auto red_store = [&]() {
    f32x4 a = (part[0] + part[1]) + (part[2] + part[3]);
#pragma unroll
    for (int i = 0; i < 4; i++) { auto r16 = __builtin_amdgcn_permlane16_swap(__float_as_uint(a[i]), __float_as_uint(a[i]), false, false); const float t = __uint_as_float(r16[0]) + __uint_as_float(r16[1]);
      auto r32 = __builtin_amdgcn_permlane32_swap(__float_as_uint(t), __float_as_uint(t), false, false); a[i] = __uint_as_float(r32[0]) + __uint_as_float(r32[1]); }
    const int hd = rt >> 2, u = (rt & 3) * 60 + w3 * 15 + lane, hi = u >= 160, fq = hi ? (u - 160) >> 3 : u >> 4, di = hi ? u & 7 : u & 15;
    __builtin_amdgcn_raw_buffer_store_b64((u32x2){pk(a[0], a[1]), pk(a[2], a[3])}, wsr, lane < 15 ? (hi * 5 + (fq >> 1)) * 512 + di * 16 + (fq & 1) * 8 : 0x7FFF0000u, WS_KVR + hq * KVR_B + (hd >> 1) * 5120 + (hd & 1) * 256, 16);
  };
  u32x4 ka[10]; u64 fkr; float gta; unsigned kb; const unsigned kbs = WS_KVR + hq * KVR_B + w2 * 5120;
  auto oa_load = [&]() {
    asm volatile("" : "+v"(fkr));
    if (__builtin_expect(!__all(fkr == tag), 0)) wait_flags<64>(ws, WS_FKVR + hq * 512, tag, lane);
    asm volatile("" ::: "memory");
    kb = (((r >> 2) & 1) == h) ? (h * 16 + (r & 3) + 4 * (r >> 3)) * 16 : 0x7FFF0000u;
    ka[0] = LD16S(kb, kbs); ka[5] = LD16S(kb, kbs + 2560); ka[1] = LD16S(kb, kbs + 512); ka[6] = LD16S(kb, kbs + 3072);
    gta = __uint_as_float(__builtin_amdgcn_raw_buffer_load_b32(wsr, WS_G + (rt * 128 + w3 * 32 + r) * 4, 0, 16));
  };
  u32x4 jf[16]; f32x4 jc[3];
  const LAS char* jab = lp + L_PHK + ((w >> 1) * 128 + g * 8 + q4) * 80 + p4 * 8;
  const LAS char* jbb = lp + L_VIM + ((w >> 1) * 128 + g * 8 + q4) * 40 + (w & 1) * 32 + p4 * 8;
  auto kv_rd = [&](int n) { const int ks = n >> 2, j = n & 3;
    jf[n] = j ? trd2(jab + ks * 2560 + (j - 1) * 32, jab + ks * 2560 + 320 + (j - 1) * 32) : trd2(jbb + ks * 1280, jbb + ks * 1280 + 160); };
  auto kv_mm = [&](int m) { const int ks = m / 3, ft = m % 3;
    if (ft == 0 && (w & 1) && l15 == 4) jf[ks * 4] = (u32x4){0x3F803F80u, 0x3F803F80u, 0x3F803F80u, 0x3F803F80u};
    jc[ft] = mfma16(jf[ks * 4 + 1 + ft], jf[ks * 4], ks ? jc[ft] : (f32x4){0.f, 0.f, 0.f, 0.f}); };
  auto kv_st = [&](int ft) { const int dt = w & 1;
    const unsigned vo = ((!dt || l15 < 8) && (ft < 2 || g < 2)) ? (dt ? g * 128 : g * 256) + l15 * 16 : 0x7FFF0000u;
    ST16S(__builtin_bit_cast(u32x4, jc[ft]), vo, WS_PART + (hq * 16 + rt) * PART_B + (w >> 1) * 3840 + (dt ? 2560 + ft * 512 : ft * 1024)); };
  f32x16 oa0, oa1; u32x4 z0, z1, z2;
  LAS char* otb = lp + L_WO + (8 + w) * 5120;
  auto oa_mm = [&](int m) { const int s = m >> 1; if (m & 1) oa1 = mfma32(ka[5 + s], ph[s], s ? oa1 : Z); else oa0 = mfma32(ka[s], ph[s], s ? oa0 : Z); };
  auto oa_norm = [&]() { const float inv = __builtin_amdgcn_rcpf(oa1[4] + 1e-6f) * gta;
    LAS char* ar = otb + (r >> 4) * 1280 + l15 * 16; LAS char* ae = ar + h * 520; LAS char* ao = ar + 8 + h * 760;
#pragma unroll
    for (int t = 0; t < 5; t++) {
      const u32x2 v = t < 4 ? (u32x2){nz(pk(oa0[(4 * t) & 15] * inv, oa0[(4 * t + 1) & 15] * inv)), nz(pk(oa0[(4 * t + 2) & 15] * inv, oa0[(4 * t + 3) & 15] * inv))} : (u32x2){nz(pk(oa1[0] * inv, oa1[1] * inv)), nz(pk(oa1[2] * inv, oa1[3] * inv))};
      *(LAS u32x2*)(((t & 1) ? ao : ae) + (t >> 1) * 256) = v; }
    z0 = ldsr16(otb + ok1); z1 = ldsr16(otb + 1280 + ok1); z2 = ldsr16(ar + 1024); };
  auto oa_st = [&]() { const unsigned oa = WS_OA + (rt * 8 + w3 * 2) * OT_B + (hq * 2 + w2) * 1280;
    ST16S(z0, ok1, oa); ST16S(z1, ok1, oa + OT_B); ST16S(z2, h ? 0x7FFF0000u : (r >> 4) * OT_B + l15 * 16, oa + 1024); };
  unsigned cpk;
  PRIO(3);
  if (role) { TL(0, 0, FILL(0), NOP, NOP) TL(0, 1, FILL(1), NOP, NOP) TL(0, 2, FILL(2), NOP, NOP) }
  else {
    auto f0 = [&](int n) { if (n < 8) kv_rd(n); if (n >= 13) kv_mm(n - 13); };
    auto f1 = [&](int n) { if (n < 8) kv_rd(8 + n); if (n == 7) ARRIVE_NW(0); if (n >= 9 && n < 12) kv_mm(n - 6); if (n >= 13) kv_mm(n - 7); };
    auto f2 = [&](int n) { if (n >= 9 && n < 12) kv_mm(n); if (n >= 5 && n < 8) kv_st(n - 5); };
    TLF(0, f0) TLF(1, f1) TLF(2, f2)
  }
  TL(0, 3, dma4s((w2 ? woB : woA) + C0 + (((rt & 7) * 2 + role) * 40 + w3 * 10) * 640, (l15 < 10 ? l15 : 9) * 1280 + (g & 1) * 156, lds0 + 160768)        , NOP, NOP)
  TL(0, 4, NOP, NOP, if (role) FILL(3))
  PRIO(2);
  TL(0, 5, if (!role) { WAIT_VM(1); FLAG(WS_FPART + ((hq * 16 + rt) * 8 + w) * 8); }, NOP, cpk = PEEK(0))
  AWAIT(0, cpk);
  asm volatile("" ::: "memory");
  TL6(0, KX(2, 2), KW(2, 2, 0))
  TL7(0, 1, KW(2, 2, 1), KW(2, 2, 2), KW(2, 2, 3))
  TL(1, 0, KW(2, 2, 4), if (role) FILL(4), if (!role) { fp0 = POLL(WS_FPART + hq * 1024); fp1 = POLL(WS_FPART + hq * 1024 + 512); })
  TL(1, 1, if (role) { WX(0); WX(1); }, if (role) FILL(5), if (role) { WX(2); WX(3); })
  TL(1, 2, if (role) WX(4), NOP, if (red) red_load())
  PRIO(1);
  TL(1, 3, NOP, NOP, NOP)
  TL(1, 4, NOP, if (!role) CLR_VM(), if (red) red_store())
  TL(1, 5, NOP, NOP, NOP)
  PRIO(0);
  TL6(1, if (!role) { WAIT_VM(0); FLAG((w2 ? WS_SPARE : WS_FKVR) + ((hq * 16 + rt) * 4 + w3) * 8); }, NOP)
  if (!role) WAIT_BAR(1); else WAIT_BAR(0);
  STAMP(12);
  PRIO(3);
  if (role) FLAG(WS_FOA + ((rt * 8 + hq) * 8 + w) * 8);
  u64 foa;
  TL7(1, 2, KX(3, 0), KW(3, 0, 0), KW(3, 0, 1))
  TL(2, 0, KW(3, 0, 2), KW(3, 0, 3), KW(3, 0, 4)) TL(2, 1, WA(w, 0), WA(w, 1), WA(w, 2))
  u32x4 wbr[11];
  const char* wbs = (const char*)(woB + C0 + w * 32 * 640) - 4096;
#define WB1(n) wbr[n] = *(const GAS u32x4*)(wbs + ((n) / 5) * (8 * 32 * 1280) + woff[(n) % 5] + ((n) % 5) * 1024)
#define WB2(j) do { WB1(j); WB1(5 + (j)); } while (0)
#define WA16() dma16s(woA + C0 + 16 * 32 * 640 - 2048, wo16, lds0 + L_WO + 16 * 5120 + w5 * 1024)
#define WB16() wbr[10] = *(const GAS u32x4*)((const char*)(woB + C0 + 16 * 32 * 640) - 4096 + wo16)
  auto chk = [&]() { asm volatile("" : "+v"(foa));
    if (__builtin_expect(!__all(foa == tag), 0)) wait_flags<64>(ws, WS_FOA + rt * 512, tag, lane);
    asm volatile("" ::: "memory"); };
  if (role) {
    TL(2, 2, WA(w, 3), WA(w, 4), NOP)
    PRIO(2);
    TL(2, 3, (fkr = POLL(WS_FKVR + hq * 512), foa = POLL(WS_FOA + rt * 512)), NOP, NOP)
    TL(2, 4, NOP, { chk(); oa_load(); }, { ka[2] = LD16S(kb, kbs + 1024); ka[7] = LD16S(kb, kbs + 3584); ka[3] = LD16S(kb, kbs + 1536); })
    PRIO(1);
    TL(2, 5, { ka[8] = LD16S(kb, kbs + 4096); ka[4] = LD16S(kb, kbs + 2048); ka[9] = LD16S(kb, kbs + 4608); }, NOP, NOP)
    PRIO(0);
    { auto f = [&](int n) { if (n == 5) v2n = ldsr16(d2p + 7 * TILE_B + 2 * VB); if (n >= 8 && n != 10 && n != 14) oa_mm(n < 10 ? n - 8 : (n < 14 ? n - 9 : 5)); }; TLF(6, f) }
  } else {
    TL(2, 2, WA(w, 3), WA(w, 4), WX(0))
    PRIO(2);
    TL(2, 3, { WX(1); WX(2); }, { WX(3); WX(4); }, foa = POLL(WS_FOA + rt * 512))
    TL(2, 4, NOP, NOP, NOP)
    PRIO(1);
    TL(2, 5, chk(), WB1(0), WB1(5))
    PRIO(0);
    TL6(2, WB1(1), WB1(6))
  }
  BAR_L();
  STAMP(13);
  PRIO(3);
  if (role) {
    d0p -= 2 * STEP_B; d1p -= 2 * STEP_B; d2p -= 2 * STEP_B; asm volatile("" : "+v"(d0p), "+v"(d1p), "+v"(d2p));
    { auto f = [&](int n) { if (n == 8 || n == 9) oa_mm(n - 2); if (n == 11 || n == 12) oa_mm(n - 3); if (n == 5) CLR_VM(); if (n == 6) WA16(); }; TILEF(0, v2 = v2n, f) }
    TL(0, 0, oa_norm(), NOP, oa_st())
    TL(0, 1, WA(8 + w, 0), WA(8 + w, 1), WA(8 + w, 2))
    TL(0, 2, WA(8 + w, 3), WA(8 + w, 4), WB2(0))
    TL(0, 3, WB2(1), NOP, WB2(2))
    PRIO(2);
    TL(0, 4, WB2(3), NOP, WB2(4))
  } else {
    TL7(2, 0, WA16(), WA(8 + w, 0), WA(8 + w, 1))
    TL(0, 0, WA(8 + w, 2), WA(8 + w, 3), WA(8 + w, 4))
    TL(0, 1, WB1(2), NOP, WB1(7)) TL(0, 2, WB1(3), NOP, WB1(8)) TL(0, 3, WB1(4), NOP, WB1(9))
    PRIO(2);
    TL(0, 4, NOP, NOP, NOP)
  }
  ARRIVE_NW(1);
  const int tp = w & 1, kq = w >> 1;
  const unsigned ofr = (rt * 8 + role * 4 + tp * 2) * OT_B + kq * 5120;
  u32x4 of[2][5]; float gtb;
#define OFL(u) do { _Pragma("unroll") for (int i = 0; i < 5; i++) of[u][i] = LD16S(ok1, WS_OA + ofr + (u) * OT_B + i * 1024); } while (0)
#define OF2(i) do { of[0][i] = LD16S(ok1, WS_OA + ofr + (i) * 1024); of[1][i] = LD16S(ok1, WS_OA + ofr + OT_B + (i) * 1024); } while (0)
  TL(0, 5, WB16(), gtb = __uint_as_float(__builtin_amdgcn_raw_buffer_load_b32(wsr, WS_G + (rt * 128 + w3 * 32 + w2 * 16 + l15) * 4, 0, 16)), OF2(0))
  PRIO(1);
  TL6(0, OF2(1), OF2(2))
  PRIO(0);
  LAS f32x4* xb = (LAS f32x4*)(lp + (w3 >> 1) * CH_B) + (w3 & 1) * (64 * 6) + lane;
  { auto f = [&](int n) { if (n == 0) cpk = PEEK(1); if (n == 3) OF2(3); if (n == 4) OF2(4);
      if (n == 15) { AWAIT(1, cpk);
#pragma unroll
        for (int c = 0; c < 3; c++) xb[(w2 * 3 + c) * 64] = (f32x4){OA[4 * c], OA[4 * c + 1], OA[4 * c + 2], OA[4 * c + 3]}; } };
    TILEF(0, v2 = v2n, f) }
  pB2[2] = pk(e0, e1); pB2[3] = pk(e2, e3);
  OB = mfma32(v2, pB2, OB);
#undef TL
#undef TL6
#undef TL7
#undef TILE
#undef G
  STAMP(7);
  u32x4 wf[5][5];
#define WFA(i) do { const LAS char* wb_ = lp + L_WO + (kq * 5 + (i)) * 5120 + wlane; _Pragma("unroll") for (int t = 0; t < 5; t++) wf[i][t] = trd2(wb_ + t * 8, wb_ + 1280 + t * 8); } while (0)
#define WFB(i) do { const int gr_ = kq * 5 + (i); const LAS char* wb_ = lp + (gr_ < 17 ? L_WO + gr_ * 5120 : 145408 + (gr_ - 17) * 5120) + wlane; _Pragma("unroll") for (int t = 0; t < 5; t++) wf[i][t] = trd2(wb_ + t * 8, wb_ + 1280 + t * 8); } while (0)
  { asm volatile("s_waitcnt vmcnt(18) lgkmcnt(0)\n\ts_barrier" ::: "memory");
    f32x16 O = OB;
#pragma unroll
    for (int c = 0; c < 3; c++) { const f32x4 t = xb[((w2 ^ 1) * 3 + c) * 64]; O[4 * c] += t[0]; O[4 * c + 1] += t[1]; O[4 * c + 2] += t[2]; O[4 * c + 3] += t[3]; }
    SBAR(); WFA(0); WFA(1); SBAR();
    float l = from_upper(O[8]);
    const float amax = (((fabsf(O[0]) + fabsf(O[1])) + (fabsf(O[2]) + fabsf(O[3]))) + ((fabsf(O[4]) + fabsf(O[5])) + (fabsf(O[6]) + fabsf(O[7])))) + ((fabsf(O[8]) + fabsf(O[9])) + (fabsf(O[10]) + fabsf(O[11])));
    const bool good = (l >= 0x1p-30f) && (l <= 0x1p100f) && (amax <= 0x1p126f);
    if (__builtin_expect(!__all(good), 0)) {
      const u32x4 q1 = q1B; const u32x2 q2 = h ? (u32x2){0u, 0u} : q2B;
      float m = -INFINITY;
#pragma unroll 1
      for (int t = 0; t < 64; t++) {
        f32x16 s = mfma32(LD16(WS_KV + t * TILE_B + ok1), q1, Z); s = mfma32k8(LD8(WS_KV + t * TILE_B + ok2), q2, s);
#pragma unroll
        for (int i = 0; i < 16; i++) m = fmaxf(m, s[i]);
      }
      m = half_swap_max(m);
      O = Z;
#pragma unroll 1
      for (int t = 0; t < 64; t++) {
        f32x16 s = mfma32(LD16(WS_KV + t * TILE_B + ok1), q1, Z); s = mfma32k8(LD8(WS_KV + t * TILE_B + ok2), q2, s);
#pragma unroll
        for (int i = 0; i < 16; i++) s[i] = ex2(s[i] - m);
        O = mfma32(LD16(WS_KV + t * TILE_B + ov), (u32x4){pk(s[0], s[1]), pk(s[2], s[3]), pk(s[4], s[5]), pk(s[6], s[7])}, O);
        O = mfma32(LD16(WS_KV + t * TILE_B + ov + 2 * VB), (u32x4){pk(s[8], s[9]), pk(s[10], s[11]), pk(s[12], s[13]), pk(s[14], s[15])}, O);
      }
      l = from_upper(O[8]);
    }
    const float inv = __builtin_amdgcn_rcpf(l) * (1.0f - gtb);
    LAS char* tb = lp + w2 * CH_B + 12288 + w3 * 1536;
    const int uq = (g & 1) * 5 + h;
    LAS char* t0 = tb + (uq >> 1) * 256 + (uq & 1) * 8 + l15 * 16; LAS char* t2 = h ? tb + 1280 + (g & 1) * 8 + l15 * 16 : t0 + 512;
#pragma unroll
    for (int c = 0; c < 3; c++) *(LAS u32x2*)(c < 2 ? t0 + c * 256 : t2) = (u32x2){nz(pk(O[4 * c] * inv, O[4 * c + 1] * inv)), nz(pk(O[4 * c + 2] * inv, O[4 * c + 3] * inv))};
    const unsigned ob = WS_OB + (rt * 8 + w3 * 2 + w2) * OT_B + hp * 1280;
    const u32x4 y0 = ldsr16(tb + ok1), y1 = ldsr16(tb + 1024 + l15 * 16);
    ST16S(y0, ok1, ob); ST16S(y1, lane < 16 ? ok1 : 0x7FFF0000u, ob + 1024);
    SBAR();
  }
  STAMP(8);
  auto of_missing = [&]() { u32x4 mm = of[0][0];
#pragma unroll
    for (int j = 1; j < 10; j++) mm = __builtin_elementwise_max(mm, of[j / 5][j % 5]);
    return missing(mm); };
  if (__builtin_expect(of_missing(), 0)) {
#pragma unroll 1
    for (unsigned spin = 0; spin < SPIN_CAP; ++spin) {
      asm volatile("" ::: "memory");
      OFL(0); OFL(1);
      if (!of_missing()) break;
      __builtin_amdgcn_s_sleep(1);
    }
  }
  f32x4 c[2][5];
#define MM(u, i, first) do { _Pragma("unroll") for (int t = 0; t < 5; t++) c[u][t] = mfma16(wf[i][t], of[u][i], (first) ? (f32x4){0.f, 0.f, 0.f, 0.f} : c[u][t]); SBAR(); } while (0)
#define OBL(u, i) of[u][i] = LD16S(ok1, WS_OB + ofr + (u) * OT_B + (i) * 1024)
#define MMR(u, i, first, j, ob_) do { const LAS char* wb_ = lp + (ob_) + wlane; _Pragma("unroll") for (int t = 0; t < 5; t++) { \
      c[u][t] = mfma16(wf[i][t], of[u][i], (first) ? (f32x4){0.f, 0.f, 0.f, 0.f} : c[u][t]); wf[j][t] = trd2(wb_ + t * 8, wb_ + 1280 + t * 8); SBAR(); } } while (0)
#define OA_(j) (L_WO + (kq * 5 + (j)) * 5120)
#define OB_(j) (kq * 5 + (j) < 17 ? L_WO + (kq * 5 + (j)) * 5120 : 145408 + (kq * 5 + (j) - 17) * 5120)
  MMR(0, 0, true, 2, OA_(2)); MMR(1, 0, true, 3, OA_(3)); MMR(0, 1, false, 4, OA_(4));
  BAR_L();
  SBAR();
  { LAS char* d = lp + L_WO + w * 5120 + lane * 16;
#pragma unroll
    for (int n = 0; n < 15; n++) { const int u = n < 5 ? 1 : (n >= 10), i = n < 5 ? 1 : 2, t = n % 5;
      if (n < 10) *(LAS u32x4*)(d + (n / 5) * 40960 + (n % 5) * 1024) = wbr[n];
      if (n == 10) *(LAS u32x4*)(lp + L_WO + 16 * 5120 + w5 * 1024 + lane * 16) = wbr[10];
      if (n == 11) OBL(0, 0); if (n == 12) OBL(1, 0); if (n == 13) OBL(0, 1); if (n == 14) OBL(1, 1);
      c[u][t] = mfma16(wf[i][t], of[u][i], c[u][t]); SBAR(); }
    asm volatile("" : "+v"(c[1][0])); }
  STAMP(9);
  BAR_L();
  SBAR();
  OBL(0, 2); OBL(1, 2); SBAR();
  MMR(0, 3, false, 0, OB_(0)); OBL(0, 3); SBAR(); MMR(0, 4, false, 1, OB_(1)); OBL(0, 4); SBAR(); MMR(1, 3, false, 2, OB_(2)); OBL(1, 3); SBAR(); MMR(1, 4, false, 3, OB_(3)); OBL(1, 4);
  WFB(4);
  ARRIVE_NW(2);
  SBAR();
  STAMP(10);
  LAS char* xw = lp + kq * 21504 + (tp * 32 + l15) * 336 + g * 80;
#pragma unroll
  for (int n = 0; n < 10; n++) {
    const int u = n < 6 ? n & 1 : (n >= 8), i = n < 6 ? n >> 1 : 3 + (n & 1);
    if (__builtin_expect(missing(of[u][i]), 0)) {
#pragma unroll 1
      for (unsigned spin = 0; spin < SPIN_CAP; ++spin) {
        asm volatile("" ::: "memory");
#pragma unroll
        for (int j = n; j < 10; j++) { const int uu = j < 6 ? j & 1 : (j >= 8), ii = j < 6 ? j >> 1 : 3 + (j & 1); OBL(uu, ii); }
        if (!missing(of[u][i])) break;
        __builtin_amdgcn_s_sleep(1);
      }
    }
    MM(u, i, false);
    if (n == 6) cpk = PEEK(2);
    if (n == 8) { AWAIT(2, cpk); *(LAS f32x4*)(xw) = c[0][0]; *(LAS f32x4*)(xw + 16) = c[0][1]; *(LAS f32x4*)(xw + 32) = c[0][2]; SBAR(); }
    if (n == 9) { *(LAS f32x4*)(xw + 48) = c[0][3]; *(LAS f32x4*)(xw + 64) = c[0][4]; SBAR(); }
  }
#pragma unroll
  for (int t = 0; t < 5; t++) *(LAS f32x4*)(xw + 16 * 336 + t * 16) = c[1][t];
  BAR_L();
  { const __amdgpu_buffer_rsrc_t orr = __builtin_amdgcn_make_buffer_rsrc(out, 0, 2048 * DM * 2, 0x00020000);
#pragma unroll
    for (int j = 0; j < 3; j++) { const int n = tid + 512 * j, nn = n < 1280 ? n : 0, row = (nn * 3277) >> 16, cq = nn - row * 20;
      const LAS f32x4* p = (const LAS f32x4*)(lp + row * 336 + cq * 16);
      const f32x4 z = (p[0] + p[1344]) + (p[2688] + p[4032]);
      __builtin_amdgcn_raw_buffer_store_b64((u32x2){pk(z[0], z[1]), pk(z[2], z[3])}, orr, n < 1280 ? ((rt * 128 + role * 64 + row) * DM + C0 + cq * 4) * 2 : 0x7FFF0000u, 0, 0); }
  }
  STAMP(11);
}

__global__ __launch_bounds__(512, 2) void fused(const u16* __restrict__ x, const u16* __restrict__ wqkv, const u16* __restrict__ wphi, const u16* __restrict__ woA,
                                                const u16* __restrict__ wq, const u16* __restrict__ wk, const u16* __restrict__ wv, const u16* __restrict__ woB,
                                                const u16* __restrict__ gatew, u16* __restrict__ out, char* ws) {
  __shared__ __attribute__((aligned(16))) char lds[L_TOTAL];
  if ((blockIdx.x >> 6) & 1) body<1>(x, wqkv, wphi, woA, wq, wv, woB, gatew, out, ws, (LAS char*)lds);
  else body<0>(x, wqkv, wphi, woA, wq, wk, woB, gatew, out, ws, (LAS char*)lds);
}
}

extern "C" void kernel_launch(void* const* d_in, const int* in_sizes, int n_in, void* d_out, int out_size, void* d_ws, size_t ws_size, hipStream_t stream) {
  using namespace gm;
  fused<<<256, 512, 0, stream>>>((const u16*)d_in[0], (const u16*)d_in[1], (const u16*)d_in[2], (const u16*)d_in[3], (const u16*)d_in[5], (const u16*)d_in[6],
                                 (const u16*)d_in[7], (const u16*)d_in[8], (const u16*)d_in[9], (u16*)d_out, (char*)d_ws);
}
```

```cpp
#include <hip/hip_runtime.h>
#include <hip/hip_bf16.h>
#include <stdint.h>

namespace gm {
typedef float f32x16 __attribute__((ext_vector_type(16)));
typedef float f32x4 __attribute__((ext_vector_type(4)));
typedef float f32x2 __attribute__((ext_vector_type(2)));
typedef short s16x4 __attribute__((ext_vector_type(4)));
typedef __bf16 b16x2 __attribute__((ext_vector_type(2)));
typedef __bf16 b16x8 __attribute__((ext_vector_type(8)));
typedef unsigned u32x4 __attribute__((ext_vector_type(4)));
typedef unsigned u32x2 __attribute__((ext_vector_type(2)));
typedef unsigned long long u64;
typedef unsigned short u16;
#define DI __device__ __forceinline__
#define LAS __attribute__((address_space(3)))
#define GAS __attribute__((address_space(1)))
typedef GAS u64 gu64;

constexpr int DM = 640;
constexpr unsigned WS_EPOCH = 0, WS_FKV = 2048, WS_FKVR = 4096, WS_FQ = 8192, WS_FPART = 16384, WS_FOA = 24576, WS_SPARE = 40960  , WS_G = 57344;
constexpr unsigned WS_KVR = 8912896, KVR_B = 10240, WS_KV = 131072, WS_QX = 327680, QX_B = 10240, WS_PART = 1703936, PART_B = 15360;
constexpr unsigned WS_OA = 3670016, WS_OB = 6291456, WS_END = 8994816, OT_B = 20480;
constexpr int VB = 352, TILE_B = 1280 + 4 * VB, CH_B = TILE_B * 8, STEP_B = 2 * CH_B;
constexpr int STAGE_B = 36864, ST_W1 = 10240, ST_X = 20480;
constexpr int L_BUF = 43008, L_P0W = 0, L_P0X0 = 25600, L_P0X1 = 153600, L_RED = 158720, L_RED1 = 40960;
constexpr int L_HO = 86016, L_QL = 96256, L_VIM = 96256, L_PHK = 116736, L_WPHI = 158592, L_ZERO = 161792, L_CNT = 163712, L_TOTAL = 163840;
constexpr int L_WO = 43008;
constexpr float LOG2E = 1.4426950408889634f;
constexpr float QSCALE = 0.22360679774997896f * LOG2E;
constexpr unsigned SPIN_CAP = 20000, LSPIN_CAP = 400000;

DI unsigned pk(float a, float b) { f32x2 v = {a, b}; b16x2 r = __builtin_convertvector(v, b16x2); return __builtin_bit_cast(unsigned, r); }
DI f32x16 mfma32(u32x4 a, u32x4 b, f32x16 c) { return __builtin_amdgcn_mfma_f32_32x32x16_bf16(__builtin_bit_cast(b16x8, a), __builtin_bit_cast(b16x8, b), c, 0, 0, 0); }
DI f32x16 mfma32k8(u32x2 a, u32x2 b, f32x16 c) { return __builtin_amdgcn_mfma_f32_32x32x8bf16_1k(__builtin_bit_cast(s16x4, a), __builtin_bit_cast(s16x4, b), c, 0, 0, 0); }
DI f32x4 mfma16(u32x4 a, u32x4 b, f32x4 c) { return __builtin_amdgcn_mfma_f32_16x16x32_bf16(__builtin_bit_cast(b16x8, a), __builtin_bit_cast(b16x8, b), c, 0, 0, 0); }
DI u32x2 trd(const LAS char* p) { return __builtin_bit_cast(u32x2, __builtin_amdgcn_ds_read_tr16_b64_v4i16((LAS s16x4*)p)); }
DI u32x4 trd2(const LAS char* p0, const LAS char* p1) { u32x2 a = trd(p0), b = trd(p1); return (u32x4){a.x, a.y, b.x, b.y}; }
DI u32x4 ldsr16(const LAS char* p) { return *(const LAS u32x4*)p; }
DI u32x2 ldsr8(const LAS char* p) { return *(const LAS u32x2*)p; }
DI float ex2(float x) { return __builtin_amdgcn_exp2f(x); }
DI void dma16s(const void* base, unsigned voff, unsigned lds_base) {
  asm volatile("s_mov_b32 m0, %2\n\ts_nop 0\n\tglobal_load_lds_dwordx4 %0, %1" :: "v"(voff), "s"(base), "s"(lds_base) : "memory"); }
DI void dma16s_sc1(const void* base, unsigned voff, unsigned lds_base) {
  asm volatile("s_mov_b32 m0, %2\n\ts_nop 0\n\tglobal_load_lds_dwordx4 %0, %1 sc1" :: "v"(voff), "s"(base), "s"(lds_base) : "memory"); }
DI void dmag_open(unsigned lds_base) { asm volatile("s_mov_b32 m0, %0\n\ts_nop 0" :: "s"(lds_base) : "memory"); }
template <int J> DI void dmag(const void* base, unsigned voff) { asm volatile("global_load_lds_dwordx4 %0, %1 offset:%2" :: "v"(voff), "s"(base), "i"((J & 3) * 1024) : "memory"); }
template <int J> DI void dmag_sc1(const void* base, unsigned voff) { asm volatile("global_load_lds_dwordx4 %0, %1 offset:%2 sc1" :: "v"(voff), "s"(base), "i"((J & 3) * 1024) : "memory"); }
DI void dma4s(const void* base, unsigned voff, unsigned lds_base) {
  asm volatile("s_mov_b32 m0, %2\n\ts_nop 0\n\tglobal_load_lds_dword %0, %1" :: "v"(voff), "s"(base), "s"(lds_base) : "memory"); }
DI u32x4 ld16_async(const void* p) { u32x4 r; asm volatile("global_load_dwordx4 %0, %1, off" : "=v"(r) : "v"(p) : "memory"); return r; }
DI u32x2 ld8_sc1_async(const void* p) { u32x2 r; asm volatile("global_load_dwordx2 %0, %1, off sc1" : "=v"(r) : "v"(p) : "memory"); return r; }
#define BAR_L() asm volatile("s_waitcnt lgkmcnt(0)\n\ts_barrier" ::: "memory")
#define WAIT_BAR(N) asm volatile("s_waitcnt vmcnt(" #N ") lgkmcnt(0)\n\ts_barrier" ::: "memory")
#define WAIT_VM(N) asm volatile("s_waitcnt vmcnt(" #N ")" ::: "memory")
#define SBAR() __builtin_amdgcn_sched_barrier(0)
#define RLX_AGENT __ATOMIC_RELAXED, __HIP_MEMORY_SCOPE_AGENT
DI float half_swap_max(float m) { auto rr = __builtin_amdgcn_permlane32_swap(__float_as_uint(m), __float_as_uint(m), false, false); return fmaxf(__uint_as_float(rr[0]), __uint_as_float(rr[1])); }
DI float from_upper(float v) { auto rr = __builtin_amdgcn_permlane32_swap(__float_as_uint(v), __float_as_uint(v), false, false); return __uint_as_float(rr[1]); }


template <int NF>
DI bool flags_set(const char* ws, unsigned off, u64 tag, int lane) {
  gu64* f = (gu64*)(ws + off);
  bool ok = __hip_atomic_load(f + (NF < 64 ? (lane & (NF - 1)) : lane), RLX_AGENT) == tag;
  if (NF >= 128) ok &= __hip_atomic_load(f + 64 + lane, RLX_AGENT) == tag;
  if (NF >= 256) { ok &= __hip_atomic_load(f + 128 + lane, RLX_AGENT) == tag; ok &= __hip_atomic_load(f + 192 + lane, RLX_AGENT) == tag; }
  return ok;
}
template <int NF>
DI void wait_flags(const char* ws, unsigned off, u64 tag, int lane) {
#pragma unroll 1
  for (unsigned spin = 0; spin < SPIN_CAP; ++spin) {
    if (__all(flags_set<NF>(ws, off, tag, lane))) break;
    __builtin_amdgcn_s_sleep(2);
  }
  asm volatile("" ::: "memory");
}
DI unsigned nz(unsigned v) { return v < 0xFFFFFFFEu ? v : 0xFFFFFFFEu; }
DI unsigned umax3(unsigned a, unsigned b, unsigned c) { unsigned m = a > b ? a : b; return m > c ? m : c; }
DI bool missing(u32x4 v) { return __any(umax3(umax3(v.x, v.y, v.z), v.w, 0u) == 0xFFFFFFFFu); }
#define ONES ((u32x4){0xFFFFFFFFu, 0xFFFFFFFFu, 0xFFFFFFFFu, 0xFFFFFFFFu})
#define ST16(v, off) __builtin_amdgcn_raw_buffer_store_b128((v), wsr, (off), 0, 16)
#define ST8(v, off) __builtin_amdgcn_raw_buffer_store_b64((v), wsr, (off), 0, 16)
#define LD16(off) __builtin_amdgcn_raw_buffer_load_b128(wsr, (off), 0, 16)
#define ST16S(v, voff, soff) ST16(v, (voff) + (soff))
#define LD16S(voff, soff) __builtin_amdgcn_raw_buffer_load_b128(wsr, (voff), (soff), 16)
#define LD8S(voff, soff) __builtin_amdgcn_raw_buffer_load_b64(wsr, (voff), (soff), 16)
#define LD8(off) __builtin_amdgcn_raw_buffer_load_b64(wsr, (off), 0, 16)

template <int role>
DI void body(const u16* __restrict__ x, const u16* __restrict__ wqkv, const u16* __restrict__ wphi, const u16* __restrict__ woA,
             const u16* __restrict__ wq, const u16* __restrict__ wkv, const u16* __restrict__ woB,
             const u16* __restrict__ gatew, u16* __restrict__ out, char* ws, LAS char* lp) {
  const int tid = threadIdx.x, lane = tid & 63;
  const int w = __builtin_amdgcn_readfirstlane(tid >> 6) & 7;
  const int l15 = lane & 15, g = lane >> 4, q4 = l15 >> 2, p4 = lane & 3, r = lane & 31, h = lane >> 5;
  const int bx = blockIdx.x & 7, bi = blockIdx.x >> 3;
  const int rt = (bx & 1) * 8 + (bi & 7), hp = (bx >> 1) * 4 + ((bi >> 3) & 3), hq = hp >> 1;
  const int w3 = w & 3, w2 = w >> 2;
  const unsigned lds0 = (unsigned)(uintptr_t)lp;
  const __amdgpu_buffer_rsrc_t wsr = __builtin_amdgcn_make_buffer_rsrc(ws, 0, WS_END, 0x00020000);
  LAS unsigned* cnt = (LAS unsigned*)(lp + L_CNT);
#define ARRIVE_NW(c) do { asm volatile("" ::: "memory"); __hip_atomic_store(cnt + (c) * 8 + w, 1u, __ATOMIC_RELAXED, __HIP_MEMORY_SCOPE_WORKGROUP); asm volatile("" ::: "memory"); } while (0)
#define PEEK(c) __hip_atomic_load(cnt + (c) * 8 + (lane & 7), __ATOMIC_RELAXED, __HIP_MEMORY_SCOPE_WORKGROUP)
#define AWAIT(c, v) do { if (__builtin_expect(!__all((v) != 0u), 0)) { _Pragma("unroll 1") for (unsigned sp_ = 0; sp_ < LSPIN_CAP; ++sp_) { if (__all(PEEK(c) != 0u)) break; __builtin_amdgcn_s_sleep(1); } } asm volatile("" ::: "memory"); } while (0)
#define POLL(off) __builtin_bit_cast(u64, __builtin_amdgcn_raw_buffer_load_b64(wsr, lane * 8, (off), 16))
#define FLAG(off) __builtin_amdgcn_raw_buffer_store_b64(__builtin_bit_cast(u32x2, tag), wsr, lane ? 0x7FFF0000u : 0u, (off), 16)

  const int T0 = rt * 128 + hq * 16;
  const int C0 = hq * 80;
  const int wlane = ((g >> 1) * 16 + (g & 1) * 4 + q4) * 160 + p4 * 40;
  u64 tag;
  unsigned o0[10];
#define KVP(s, slot, p) dma16s_sc1(ws + WS_KV + (s) * STEP_B + (p) * 1024, lane * 16, lds0 + (slot) * STEP_B + (p) * 1024)
#define KVL(s, slot) do { _Pragma("unroll") for (int j = 0; j < 11; j++) KVP(s, slot, j < 10 ? w3 + 4 * j : 40 + (w3 & 1)); } while (0)
#define KW(s, slot, j) do { if ((j) == 4) KVP(s, slot, w * 5 + 4); else { if ((j) == 0) dmag_open(lds0 + (slot) * STEP_B + w * 5120); dmag_sc1<(j)>(ws + WS_KV + (s) * STEP_B + w * 5120, lane * 16); } } while (0)
#define KX(s, slot) KVP(s, slot, 40 + (w & 1))
  const int w5 = w < 5 ? w : 4;
  unsigned wo16;
  unsigned woff[5];
  auto woff_init = [&]() { int ln = lane; asm volatile("" : "+v"(ln));
#pragma unroll
    for (int j = 0; j < 5; j++) { const int c = j * 64 + ln, rho = (c * 6554) >> 16, cc = c - rho * 10; const int k = (rho & ~12) | ((rho & 4) << 1) | ((rho & 8) >> 1); woff[j] = k * 1280 + cc * 16 + (4 - j) * 1024; }
    { const int c = w5 * 64 + ln, rho = (c * 6554) >> 16, cc = c - rho * 10; const int k = (rho & ~12) | ((rho & 4) << 1) | ((rho & 8) >> 1); wo16 = k * 1280 + cc * 16 + 4096; }
    asm volatile("" : "+v"(woff[0]), "+v"(woff[1]), "+v"(woff[2]), "+v"(woff[3]), "+v"(woff[4]), "+v"(wo16)); };
  if (w2 == 0) {
    u32x4 gf0, gf1, gf2, gf3, gf4, wpc;
    u32x2 ep = ld8_sc1_async(ws + WS_EPOCH);
    const int lx = w3 < 3 ? L_P0X0 + w3 * 5120 : L_P0X1;
    { const u16* wsrc = wkv + w3 * 3200;
#pragma unroll
      for (int j = 0; j < 5; j++) { const int n = j * 64 + lane, row = (n * 3277) >> 16, cc = n - row * 20; dma16s(x + T0 * DM + w3 * 160, row * 1280 + cc * 16, lds0 + lx + j * 1024); }
#pragma unroll
      for (int j = 0; j < 6; j++) dma16s(wsrc + j * 512, lane * 16, lds0 + L_P0W + w3 * 6400 + j * 1024);
      if (lane < 16) dma16s(wsrc + 6 * 512, lane * 16, lds0 + L_P0W + w3 * 6400 + 6 * 1024);
      const u16* gp = gatew + w3 * 160 + g * 8;
      if (!role) { gf0 = ld16_async(gp); gf1 = ld16_async(gp + 32); gf2 = ld16_async(gp + 64); gf3 = ld16_async(gp + 96); gf4 = ld16_async(gp + 128); }
      wpc = ld16_async(wphi + hq * 1600 + (tid < 200 ? tid : 199) * 8);
    }
    woff_init();
    if (role) asm volatile("s_waitcnt vmcnt(0)" : "+v"(wpc), "+v"(ep) :: "memory");
    else asm volatile("s_waitcnt vmcnt(0)" : "+v"(gf0), "+v"(gf1), "+v"(gf2), "+v"(gf3), "+v"(gf4), "+v"(wpc), "+v"(ep) :: "memory");
    tag = (((u64)(unsigned)__builtin_amdgcn_readfirstlane(ep.y) << 32) | (unsigned)__builtin_amdgcn_readfirstlane(ep.x)) + 1ull;
    { const LAS char* wb = lp + L_P0W + (w3 * 160 + g * 8 + q4) * 40;
      const LAS char* xb = lp + lx + l15 * 320 + g * 16;
      const bool gl = !role && l15 == 4;
      f32x4 c0 = {}, c1 = {};
#define P0STEP(s, GF) { const u32x4 b = ldsr16(xb + (s) * 64); c0 = mfma16(trd2(wb + p4 * 8 + (s) * 1280, wb + p4 * 8 + (s) * 1280 + 160), b, c0); \
        u32x4 a = trd2(wb + 32 + (s) * 1280, wb + 32 + (s) * 1280 + 160); if (gl) a = GF; c1 = mfma16(a, b, c1); }
      P0STEP(0, gf0) P0STEP(1, gf1) P0STEP(2, gf2) P0STEP(3, gf3) P0STEP(4, gf4)
#undef P0STEP
      *(LAS f32x4*)(lp + L_RED + tid * 16) = c0;
      if (lane < 32) *(LAS f32x4*)(lp + L_RED1 + (w3 * 32 + lane) * 16) = c1; }
    f32x16 acc[5];
#pragma unroll
    for (int b = 0; b < 5; b++) acc[b] = (f32x16){};
    const int wm = g & 1, whd = p4;
    const int wo0 = wm * ST_W1 + (8 * h + 4 * wm + q4) * 160 + whd * 40, wo1 = wm * ST_W1 + (8 * h + 4 * (wm ^ 1) + q4) * 160 + whd * 40;
    const int xo = ST_X + (w3 * 32 + r) * 128 + ((h ^ ((r >> 1) & 7)) * 16);
    u32x4 fa[5], fb[5], xa, xb;
#define KSTEP(FN, XN, s, FC, XC) { XN = ldsr16(sb + (xo ^ ((s) * 32))); \
      _Pragma("unroll") for (int j = 0; j < 5; j++) { if (i > 0 || (s) > 0) acc[j] = mfma32(FC[j], XC, acc[j]); FN[j] = trd2(sb + wo0 + (s) * 2560 + j * 8, sb + wo1 + (s) * 2560 + j * 8); SBAR(); } }
#pragma unroll
    for (int i = 0; i < 10; i++) {
      BAR_L();
      if (i == 1 && role) {
        const unsigned qb = WS_QX + (rt * 8 + hq) * QX_B + w3 * 1280 + lane * 16;
        ST16(ONES, qb); ST16(ONES, qb + 5120); ST16(ONES, lane < 32 ? qb + 1024 + (lane >> 4) * (5120 - 256) : 0x7FFF0000u); }
      if (i == 4) { if (role) { WAIT_VM(0); FLAG(WS_FQ + ((rt * 8 + hq) * 4 + w3) * 8); }
                    if (tid < 200) *(LAS u32x4*)(lp + L_WPHI + tid * 16) = wpc; }
      const LAS char* sb = lp + L_BUF + ((i + 2) % 3) * STAGE_B;
      KSTEP(fa, xa, 0, fb, xb) KSTEP(fb, xb, 1, fa, xa) KSTEP(fa, xa, 2, fb, xb)
      { xb = ldsr16(sb + (xo ^ 96));
        acc[0] = mfma32(fa[0], xa, acc[0]); fb[0] = trd2(sb + wo0 + 7680, sb + wo1 + 7680); fb[1] = trd2(sb + wo0 + 7688, sb + wo1 + 7688); SBAR();
        acc[1] = mfma32(fa[1], xa, acc[1]); fb[2] = trd2(sb + wo0 + 7696, sb + wo1 + 7696); fb[3] = trd2(sb + wo0 + 7704, sb + wo1 + 7704); SBAR();
        acc[2] = mfma32(fa[2], xa, acc[2]); fb[4] = trd2(sb + wo0 + 7712, sb + wo1 + 7712); SBAR();
        acc[3] = mfma32(fa[3], xa, acc[3]); acc[4] = mfma32(fa[4], xa, acc[4]); SBAR(); }
    }
#pragma unroll
    for (int j = 0; j < 5; j++) acc[j] = mfma32(fb[j], xb, acc[j]);
#undef KSTEP
#define PK2(t, i, sc) pk(acc[t][i] * (sc), acc[t][(i) + 1] * (sc))
#define ROW(i, sc) const u32x4 ra = {PK2(0, i, sc), PK2(0, (i) + 2, sc), PK2(1, i, sc), PK2(1, (i) + 2, sc)}, rb = {PK2(2, i, sc), PK2(2, (i) + 2, sc), PK2(3, i, sc), PK2(3, (i) + 2, sc)}; const u32x2 rc = {PK2(4, i, sc), PK2(4, (i) + 2, sc)}
    { ROW(4, 1.0f);
      *(LAS u32x4*)(lp + L_HO + tid * 16) = ra; *(LAS u32x4*)(lp + L_HO + 4096 + tid * 16) = rb; *(LAS u32x2*)(lp + L_HO + 8192 + tid * 8) = rc; }
    BAR_L();
    const int qfo = (h * 4 + w3) * 1280;
    if (role) {
      { ROW(8, QSCALE); const unsigned qb = WS_QX + (rt * 8 + hq) * QX_B + qfo;
        ST16(((u32x4){nz(ra.x), nz(ra.y), nz(ra.z), nz(ra.w)}), qb + r * 16); ST16(((u32x4){nz(rb.x), nz(rb.y), nz(rb.z), nz(rb.w)}), qb + 512 + r * 16); ST8(((u32x2){nz(rc.x), nz(rc.y)}), qb + 1024 + r * 8); }
      { ROW(12, QSCALE); LAS char* d = lp + L_QL + qfo;
        *(LAS u32x4*)(d + r * 16) = ra; *(LAS u32x4*)(d + 512 + r * 16) = rb; *(LAS u32x2*)(d + 1024 + r * 8) = rc; }
    } else { LAS char* d = lp + L_VIM + (h * 128 + w3 * 32 + r) * 40;
#pragma unroll
      for (int t = 0; t < 5; t++) { *(LAS u32x2*)(d + t * 8) = (u32x2){PK2(t, 8, 1.0f), PK2(t, 10, 1.0f)}; *(LAS u32x2*)(d + 10240 + t * 8) = (u32x2){PK2(t, 12, 1.0f), PK2(t, 14, 1.0f)}; } }
#pragma unroll
    for (int t = 0; t < 5; t++) { o0[2 * t] = PK2(t, 0, 1.0f); o0[2 * t + 1] = PK2(t, 2, 1.0f); }
#undef ROW
#undef PK2
  } else {
    u32x2 ep = ld8_sc1_async(ws + WS_EPOCH);
    const bool second = w3 >> 1;
    const unsigned rs = (second && role) ? 1280u : 3840u;
    const char* wsrc = (second ? (role ? (const char*)(wq + C0) : (const char*)(wqkv + 1280 + C0)) : (const char*)(wqkv + (role ? 0 : 640) + C0)) + (w3 & 1) * 32 * rs;
    const char* xsrc = (const char*)(x + (rt * 128 + w3 * 32) * DM);
    int rot = (bi & 7) + 3 * ((bi >> 3) & 3); rot = rot >= 10 ? rot - 10 : rot;
    unsigned so[5], xoff[2];
#pragma unroll
    for (int j = 0; j < 5; j++) { const int c = j * 64 + lane, rho = (c * 6554) >> 16, cc = c - rho * 10; so[j] = (rho ^ (second ? 4 : 0)) * rs + cc * 16; }
#pragma unroll
    for (int j = 0; j < 2; j++) { const int row = j * 8 + (lane >> 3); xoff[j] = row * 1280 + (((lane & 7) ^ ((row >> 1) & 7)) * 16); }
    int bsel = 2, kc = rot;
    auto stage = [&]() {
      const unsigned lb = lds0 + L_BUF + bsel * STAGE_B;
      const char* bw = wsrc + kc * 64 * rs; const char* bxp = xsrc + kc * 128;
#pragma unroll
      for (int j = 0; j < 5; j++) { dma16s(bw, so[j], lb + (w3 * 5 + j) * 1024); if (j < 4) dma16s(bxp + (j >> 1) * 16 * 1280, xoff[j & 1], lb + ST_X + (w3 * 4 + j) * 1024); }
      bsel = bsel == 2 ? 0 : bsel + 1; kc = kc == 9 ? 0 : kc + 1;
    };
    stage(); stage();
    woff_init();
    asm volatile("s_waitcnt vmcnt(9)" : "+v"(ep) :: "memory");
    tag = (((u64)(unsigned)__builtin_amdgcn_readfirstlane(ep.y) << 32) | (unsigned)__builtin_amdgcn_readfirstlane(ep.x)) + 1ull;
#pragma unroll 1
    for (int i = 0; i < 8; i++) {
      WAIT_BAR(9);
      stage();
      if (i == 0 && w3 == 0) {
        const unsigned tb = WS_KV + (T0 >> 5) * TILE_B; const int s16 = hq & 1;
        if (!role) {
          const LAS f32x4* rp = (const LAS f32x4*)(lp + (h ? L_RED1 : L_RED)) + (h ? (g & 1) * 16 : g * 32) + l15;
          const int st = h ? 32 : 64, o2 = h ? 0 : 16;
          const f32x4 a = (rp[0] + rp[st]) + (rp[2 * st] + rp[3 * st]), b = (rp[o2] + rp[st + o2]) + (rp[2 * st + o2] + rp[3 * st + o2]);
          const u32x4 v = {pk(a[0], a[1]), pk(a[2], a[3]), pk(b[0], b[1]), pk(b[2], b[3])};
          if (lane < 32) ST16(v, tb + (g * 32 + s16 * 16 + l15) * 16);
          else if (lane < 48) ST8(((u32x2){v.x, v.y}), tb + 1024 + (s16 * 16 + l15) * 8);
          else { const float gv = 1.0f / (1.0f + __expf(-a[0])); __builtin_amdgcn_raw_buffer_store_b32(__float_as_uint(gv), wsr, WS_G + (T0 + l15) * 4, 0, 16); }
        } else if (lane < 44) {
          const int hh = lane >= 22, d = lane - hh * 22, dd = d < 20 ? d : 0, st = dd >= 16 ? 128 : 256;
          const LAS float* rp = (const LAS float*)(lp + (dd >= 16 ? L_RED1 : L_RED)) + (((dd & 15) >> 2) * 16 + 4 * hh) * 4 + (dd & 3);
          float e[8];
#pragma unroll
          for (int j = 0; j < 8; j++) { const int tk = (8 * (j >> 2) + (j & 3)) * 4; e[j] = (rp[tk] + rp[tk + st]) + (rp[tk + 2 * st] + rp[tk + 3 * st]); }
          u32x4 v = {pk(e[0], e[1]), pk(e[2], e[3]), pk(e[4], e[5]), pk(e[6], e[7])};
          if (d == 20) v = (u32x4){0x3F803F80u, 0x3F803F80u, 0x3F803F80u, 0x3F803F80u};
          if (d == 21) v = (u32x4){0u, 0u, 0u, 0u};
          ST16(v, tb + 1280 + (s16 * 2 + hh) * VB + d * 16);
        }
      }
      if (i == 2) { if (w3 == 0) FLAG(WS_FKV + blockIdx.x * 8);
                    if (w3 >= 2) *(LAS u32x4*)(lp + L_ZERO + (tid - 384) * 16) = (u32x4){0u, 0u, 0u, 0u}; }
    }
    u64 fa, fb, fc, fd, fe;
    { gu64* f = (gu64*)(ws + WS_FKV) + lane;
      fa = __hip_atomic_load(f, RLX_AGENT); fb = __hip_atomic_load(f + 64, RLX_AGENT); fc = __hip_atomic_load(f + 128, RLX_AGENT); fd = __hip_atomic_load(f + 192, RLX_AGENT);
      fe = __hip_atomic_load((gu64*)(ws + WS_FQ) + (rt * 8 + hq) * 4 + p4, RLX_AGENT); }
    WAIT_BAR(14);
    asm volatile("" : "+v"(fa), "+v"(fb), "+v"(fc), "+v"(fd), "+v"(fe));
    const bool kvok = __all((fa == tag) & (fb == tag) & (fc == tag) & (fd == tag) & (fe == tag));
    asm volatile("" ::: "memory");
    BAR_L();
    if (__builtin_expect(!kvok, 0)) { wait_flags<256>(ws, WS_FKV, tag, lane); wait_flags<4>(ws, WS_FQ + (rt * 8 + hq) * 32, tag, lane); }
    KVL(0, 0);
    if (blockIdx.x == 0 && w3 == 0) FLAG(WS_EPOCH);
    BAR_L();
    const int ht = tid - 256;
    const u32x4 a = ldsr16(lp + L_HO + ht * 16), b = ldsr16(lp + L_HO + 4096 + ht * 16); const u32x2 c = ldsr8(lp + L_HO + 8192 + ht * 8);
    o0[0] = a.x; o0[1] = a.y; o0[2] = a.z; o0[3] = a.w; o0[4] = b.x; o0[5] = b.y; o0[6] = b.z; o0[7] = b.w; o0[8] = c.x; o0[9] = c.y;
  }
  u32x4 ph[5];
  { const LAS char* pb = ((p4 & 1) == h) ? lp + L_WPHI + (2 * w2 + h) * 800 + q4 * 40 + (2 * (g & 1) + (p4 >> 1)) * 8 : lp + L_ZERO;
    const u32x4 b0 = {o0[0], o0[1], o0[2], o0[3]}, b1 = {o0[4], o0[5], o0[6], o0[7]}, b2 = {o0[8], o0[9], 0u, 0u};
    const u32x2 c0 = trd(pb + 640), c1 = trd(pb + 672);
    f32x16 P0 = {}, P1 = {};
    P0 = mfma32(trd2(pb, pb + 160), b0, P0); P1 = mfma32(trd2(pb + 32, pb + 192), b0, P1);
    P0 = mfma32(trd2(pb + 320, pb + 480), b1, P0); P1 = mfma32(trd2(pb + 352, pb + 512), b1, P1);
    P0 = mfma32((u32x4){c0.x, c0.y, 0u, 0u}, b2, P0); P1 = mfma32((u32x4){c1.x, c1.y, 0u, 0u}, b2, P1);
    KX(1, 1); KW(1, 1, 0); SBAR();
    f32x2 p[10];
#pragma unroll
    for (int f = 0; f < 10; f++) p[f] = f < 8 ? (f32x2){P0[(2 * f) & 15], P0[(2 * f + 1) & 15]} : (f32x2){P1[(2 * f) & 3], P1[(2 * f + 1) & 3]};
    float mx = fmaxf(p[0][0], p[0][1]), mn = fminf(p[0][0], p[0][1]);
#pragma unroll
    for (int f = 1; f < 10; f++) { mx = fmaxf(fmaxf(mx, p[f][0]), p[f][1]); mn = fminf(fminf(mn, p[f][0]), p[f][1]); }
    const f32x2 ma = {-mx * LOG2E, -mx * LOG2E}, mb = {mn * LOG2E, mn * LOG2E}, ca_ = {LOG2E, LOG2E}, cb_ = {-LOG2E, -LOG2E};
    f32x2 ea[10], eb[10], sa = {0.f, 0.f}, sb = {0.f, 0.f};
#pragma unroll
    for (int f = 0; f < 10; f++) { if (f % 2 == 0 && f < 8) { SBAR(); if (f == 0) KW(1, 1, 1); if (f == 2) KW(1, 1, 2); if (f == 4) KW(1, 1, 3); if (f == 6) KW(1, 1, 4); SBAR(); }
      const f32x2 xa = __builtin_elementwise_fma(p[f], ca_, ma), xb = __builtin_elementwise_fma(p[f], cb_, mb);
      ea[f] = (f32x2){ex2(xa[0]), ex2(xa[1])}; eb[f] = (f32x2){ex2(xb[0]), ex2(xb[1])}; sa += ea[f]; sb += eb[f]; }
    const float ia = __builtin_amdgcn_rcpf(sa[0] + sa[1]), ib = __builtin_amdgcn_rcpf(sb[0] + sb[1]);
    unsigned o[20];
#pragma unroll
    for (int f = 0; f < 10; f++) { const f32x2 ya = ea[f] * ia, yb = eb[f] * ib; o[f] = pk(ya[0], ya[1]); o[10 + f] = pk(yb[0], yb[1]); }
#pragma unroll
    for (int c = 0; c < 5; c++) ph[c] = (u32x4){o[4 * c], o[4 * c + 1], o[4 * c + 2], o[4 * c + 3]};
    asm volatile("" : "+v"(ph[0]), "+v"(ph[1]), "+v"(ph[2]), "+v"(ph[3]), "+v"(ph[4]));
  }
  u32x4 q1A, q1B; u32x2 q2A, q2B;
  const int qo1 = (g & 1) * 5120 + (h * 32 + l15) * 16, qo2 = (g & 1) * 5120 + 1024 + l15 * 8;
  if (role) {
    WAIT_BAR(0);
    const LAS char* qa = lp + L_QL + w3 * 1280;
    q1A = ldsr16(qa + qo1 + (w2 ^ 1) * 256); q1B = ldsr16(qa + qo1 + w2 * 256); q2A = ldsr8(qa + qo2 + (w2 ^ 1) * 128); q2B = ldsr8(qa + qo2 + w2 * 128);
    ARRIVE_NW(0);
  } else {
    const unsigned qb = WS_QX + (rt * 8 + hq) * QX_B + w3 * 1280;
    q1A = LD16S(qo1, qb + (w2 ^ 1) * 256); q1B = LD16S(qo1, qb + w2 * 256); q2A = LD8S(qo2, qb + (w2 ^ 1) * 128); q2B = LD8S(qo2, qb + w2 * 128);
    { LAS char* d = lp + L_PHK + ((2 * w2 + h) * 128 + w3 * 32 + r) * 80;
#pragma unroll
      for (int c = 0; c < 5; c++) *(LAS u32x4*)(d + c * 16) = ph[c]; }
    WAIT_BAR(4);
#pragma unroll 1
    for (unsigned spin = 0; spin < SPIN_CAP; ++spin) {
      if (!(missing(q1A) | missing(q1B) | missing((u32x4){q2A.x, q2A.y, q2B.x, q2B.y}))) break;
      __builtin_amdgcn_s_sleep(1);
      asm volatile("" ::: "memory");
      q1A = LD16S(qo1, qb + (w2 ^ 1) * 256); q1B = LD16S(qo1, qb + w2 * 256); q2A = LD8S(qo2, qb + (w2 ^ 1) * 128); q2B = LD8S(qo2, qb + w2 * 128);
    }
  }
  const int vr = r <= 21 ? r : 21;
  const int ok1 = lane * 16, ok2 = h ? (1280 + 20 * 16) : (1024 + r * 8), ov = 1280 + h * VB + vr * 16;
  const f32x16 Z = {};
  if (h) { q2A = (u32x2){0u, 0u}; q2B = (u32x2){0u, 0u}; }
  { const LAS char* t0 = lp;
    const u32x4 k1 = ldsr16(t0 + ok1); const u32x2 k2 = ldsr8(t0 + ok2);
    f32x16 sa = mfma32(k1, q1A, Z); sa = mfma32k8(k2, q2A, sa);
    f32x16 sb = mfma32(k1, q1B, Z); sb = mfma32k8(k2, q2B, sb);
    float ma = sa[0], mb = sb[0];
#pragma unroll
    for (int i = 1; i < 16; i++) { ma = fmaxf(ma, sa[i]); mb = fmaxf(mb, sb[i]); }
    ma = half_swap_max(ma); mb = half_swap_max(mb);
    const float mah = __uint_as_float(__float_as_uint(ma) & 0xFFFF0000u), mbh = __uint_as_float(__float_as_uint(mb) & 0xFFFF0000u);
    if (h) { q2A.x = pk(-mah, mah - ma); q2B.x = pk(-mbh, mbh - mb); } }
  f32x16 OA = {}, OB = {}, SA, SB;
  u32x4 k1, v1, v2, v2n, pA1, pA2, pB1, pB2; u32x2 k2;
  float e0, e1, e2, e3;
  const LAS char* c0p = lp + w2 * CH_B + ok1; const LAS char* c1p = lp + w2 * CH_B + ok2; const LAS char* c2p = lp + w2 * CH_B + ov;
  k1 = ldsr16(c0p); k2 = ldsr8(c1p);
  SA = mfma32(k1, q1A, Z); SA = mfma32k8(k2, q2A, SA);
  v1 = ldsr16(c2p); v2 = v1;
  e0 = e1 = e2 = e3 = 0.f; pB2 = (u32x4){0u, 0u, 0u, 0u};
#define G(MF, LD, H, M, C0_, C1_, X, S_, i) MF; LD; H; SBAR(); C0_; C1_; SBAR(); X##0 = ex2(S_[i]); X##1 = ex2(S_[i + 1]); SBAR(); M; SBAR(); X##2 = ex2(S_[i + 2]); X##3 = ex2(S_[i + 3]); \
    asm volatile("" : "+v"(X##0), "+v"(X##1), "+v"(X##2), "+v"(X##3)); SBAR();
#define TILEF(TN, V2, F) { float a0, a1, a2, a3; \
    G(SB = mfma32(k1, q1B, Z),    k1 = ldsr16(d0p + (TN)), F(0), F(8),  pB2[2] = pk(e0, e1), pB2[3] = pk(e2, e3), a, SA, 0) \
    G(OB = mfma32(v2, pB2, OB),   V2,                      F(1), F(9),  pA1[0] = pk(a0, a1), pA1[1] = pk(a2, a3), e, SA, 4) \
    G(SB = mfma32k8(k2, q2B, SB), k2 = ldsr8(d1p + (TN)),  F(2), F(10), pA1[2] = pk(e0, e1), pA1[3] = pk(e2, e3), a, SA, 8) \
    G(OA = mfma32(v1, pA1, OA),   (void)0,                 F(3), F(11), pA2[0] = pk(a0, a1), pA2[1] = pk(a2, a3), e, SA, 12) \
    G(SA = mfma32(k1, q1A, Z),    (void)0,                 F(4), F(12), pA2[2] = pk(e0, e1), pA2[3] = pk(e2, e3), a, SB, 0) \
    G(OA = mfma32(v2, pA2, OA),   (void)0,                 F(5), F(13), pB1[0] = pk(a0, a1), pB1[1] = pk(a2, a3), e, SB, 4) \
    G(SA = mfma32k8(k2, q2A, SA), (void)0,                 F(6), F(14), pB1[2] = pk(e0, e1), pB1[3] = pk(e2, e3), a, SB, 8) \
    G(OB = mfma32(v1, pB1, OB),   v1 = ldsr16(d2p + (TN)), F(7), F(15), pB2[0] = pk(a0, a1), pB2[1] = pk(a2, a3), e, SB, 12) }
#define TILE(TB, TN, V2, H0, H1, H2) { auto f_ = [&](int n) { if (n == 3) { H0; } if (n == 4) { H1; } if (n == 5) { H2; } }; TILEF(TN, V2, f_) }
#define TLF(t, F) TILEF(((t) + 1) * TILE_B, v2 = ldsr16(d2p + (t) * TILE_B + 2 * VB), F)
#define NOP (void)0
  const LAS char *d0p = c0p, *d1p = c1p, *d2p = c2p;
#define TL(sl, t, H0, H1, H2) TILE(0, ((t) + 1) * TILE_B, v2 = ldsr16(d2p + (t) * TILE_B + 2 * VB), H0, H1, H2)
#define TL6(sl, H0, H1) TL(sl, 6, H0, H1, v2n = ldsr16(d2p + 7 * TILE_B + 2 * VB))
#define TL7(sl, sn, H0, H1, H2) d0p += ((sn) - (sl)) * STEP_B; d1p += ((sn) - (sl)) * STEP_B; d2p += ((sn) - (sl)) * STEP_B; asm volatile("" : "+v"(d0p), "+v"(d1p), "+v"(d2p)); TILE(0, 0, v2 = v2n, H0, H1, H2)
#define WOG(wo, gr, j, dst) do { if ((j) == 4) WOP(wo, gr, 4, dst); else { if ((j) == 0) dmag_open(lds0 + (dst)); dmag<(j)>((wo) + C0 + (gr) * 32 * 640 - 2048, woff[j]); } } while (0)
#define WOP(wo, gr, j, dst) dma16s((wo) + C0 + (gr) * 32 * 640 - (4 - (j)) * 512, woff[j], lds0 + (dst) + (j) * 1024)
#define WA(gr, j) WOG(woA, gr, j, L_WO + (gr) * 5120)
  const int wx3 = w3 ? w3 : 1;
  const u16* wxs = (w2 ? woB : woA) + (16 + wx3) * 32 * 640;
  const int wxd = w2 ? 145408 + (wx3 - 1) * 5120 : L_WO + (16 + wx3) * 5120;
#define WX(j) WOG(wxs, 0, j, wxd)
#define PRIO(n) __builtin_amdgcn_s_setprio(n)
  u32x4 ones = ONES; if (role) asm volatile("" : "+v"(ones));
#define FILL(j) do { const int n_ = w * 3 + ((j) >> 1); ST16S(ones, ok1, (((j) & 1) ? WS_OB : WS_OA) + (rt * 8 + hq) * OT_B + (n_ < 20 ? n_ : 19) * 1024); } while (0)
#define CLR_VM() __builtin_amdgcn_s_waitcnt(0x0F70)
  const bool red = !role && !w2;
  f32x4 part[4]; u64 fp0, fp1;
  auto red_load = [&]() {
    asm volatile("" : "+v"(fp0), "+v"(fp1));
    if (__builtin_expect(!__all((fp0 == tag) & (fp1 == tag)), 0)) wait_flags<128>(ws, WS_FPART + hq * 1024, tag, lane);
    asm volatile("" ::: "memory");
#pragma unroll
    for (int j = 0; j < 4; j++) part[j] = __builtin_bit_cast(f32x4, LD16S(g * (4 * PART_B) + l15 * 16, WS_PART + (hq * 16 + j) * PART_B + rt * 960 + w3 * 240));
  };
  auto red_store = [&]() {
    f32x4 a = (part[0] + part[1]) + (part[2] + part[3]);
#pragma unroll
    for (int i = 0; i < 4; i++) { auto r16 = __builtin_amdgcn_permlane16_swap(__float_as_uint(a[i]), __float_as_uint(a[i]), false, false); const float t = __uint_as_float(r16[0]) + __uint_as_float(r16[1]);
      auto r32 = __builtin_amdgcn_permlane32_swap(__float_as_uint(t), __float_as_uint(t), false, false); a[i] = __uint_as_float(r32[0]) + __uint_as_float(r32[1]); }
    const int hd = rt >> 2, u = (rt & 3) * 60 + w3 * 15 + lane, hi = u >= 160, fq = hi ? (u - 160) >> 3 : u >> 4, di = hi ? u & 7 : u & 15;
    __builtin_amdgcn_raw_buffer_store_b64((u32x2){pk(a[0], a[1]), pk(a[2], a[3])}, wsr, lane < 15 ? (hi * 5 + (fq >> 1)) * 512 + di * 16 + (fq & 1) * 8 : 0x7FFF0000u, WS_KVR + hq * KVR_B + (hd >> 1) * 5120 + (hd & 1) * 256, 16);
  };
  u32x4 ka[10]; u64 fkr; float gta; unsigned kb; const unsigned kbs = WS_KVR + hq * KVR_B + w2 * 5120;
  auto oa_load = [&]() {
    asm volatile("" : "+v"(fkr));
    if (__builtin_expect(!__all(fkr == tag), 0)) wait_flags<64>(ws, WS_FKVR + hq * 512, tag, lane);
    asm volatile("" ::: "memory");
    kb = (((r >> 2) & 1) == h) ? (h * 16 + (r & 3) + 4 * (r >> 3)) * 16 : 0x7FFF0000u;
    ka[0] = LD16S(kb, kbs); ka[5] = LD16S(kb, kbs + 2560); ka[1] = LD16S(kb, kbs + 512); ka[6] = LD16S(kb, kbs + 3072);
    gta = __uint_as_float(__builtin_amdgcn_raw_buffer_load_b32(wsr, WS_G + (rt * 128 + w3 * 32 + r) * 4, 0, 16));
  };
  u32x4 jf[16]; f32x4 jc[3];
  const LAS char* jab = lp + L_PHK + ((w >> 1) * 128 + g * 8 + q4) * 80 + p4 * 8;
  const LAS char* jbb = lp + L_VIM + ((w >> 1) * 128 + g * 8 + q4) * 40 + (w & 1) * 32 + p4 * 8;
  auto kv_rd = [&](int n) { const int ks = n >> 2, j = n & 3;
    jf[n] = j ? trd2(jab + ks * 2560 + (j - 1) * 32, jab + ks * 2560 + 320 + (j - 1) * 32) : trd2(jbb + ks * 1280, jbb + ks * 1280 + 160); };
  auto kv_mm = [&](int m) { const int ks = m / 3, ft = m % 3;
    if (ft == 0 && (w & 1) && l15 == 4) jf[ks * 4] = (u32x4){0x3F803F80u, 0x3F803F80u, 0x3F803F80u, 0x3F803F80u};
    jc[ft] = mfma16(jf[ks * 4 + 1 + ft], jf[ks * 4], ks ? jc[ft] : (f32x4){0.f, 0.f, 0.f, 0.f}); };
  auto kv_st = [&](int ft) { const int dt = w & 1;
    const unsigned vo = ((!dt || l15 < 8) && (ft < 2 || g < 2)) ? (dt ? g * 128 : g * 256) + l15 * 16 : 0x7FFF0000u;
    ST16S(__builtin_bit_cast(u32x4, jc[ft]), vo, WS_PART + (hq * 16 + rt) * PART_B + (w >> 1) * 3840 + (dt ? 2560 + ft * 512 : ft * 1024)); };
  f32x16 oa0, oa1; u32x4 z0, z1, z2;
  LAS char* otb = lp + L_WO + (8 + w) * 5120;
  auto oa_mm = [&](int m) { const int s = m >> 1; if (m & 1) oa1 = mfma32(ka[5 + s], ph[s], s ? oa1 : Z); else oa0 = mfma32(ka[s], ph[s], s ? oa0 : Z); };
  auto oa_norm = [&]() { const float inv = __builtin_amdgcn_rcpf(oa1[4] + 1e-6f) * gta;
    LAS char* ar = otb + (r >> 4) * 1280 + l15 * 16; LAS char* ae = ar + h * 520; LAS char* ao = ar + 8 + h * 760;
#pragma unroll
    for (int t = 0; t < 5; t++) {
      const u32x2 v = t < 4 ? (u32x2){nz(pk(oa0[(4 * t) & 15] * inv, oa0[(4 * t + 1) & 15] * inv)), nz(pk(oa0[(4 * t + 2) & 15] * inv, oa0[(4 * t + 3) & 15] * inv))} : (u32x2){nz(pk(oa1[0] * inv, oa1[1] * inv)), nz(pk(oa1[2] * inv, oa1[3] * inv))};
      *(LAS u32x2*)(((t & 1) ? ao : ae) + (t >> 1) * 256) = v; }
    z0 = ldsr16(otb + ok1); z1 = ldsr16(otb + 1280 + ok1); z2 = ldsr16(ar + 1024); };
  auto oa_st = [&]() { const unsigned oa = WS_OA + (rt * 8 + w3 * 2) * OT_B + (hq * 2 + w2) * 1280;
    ST16S(z0, ok1, oa); ST16S(z1, ok1, oa + OT_B); ST16S(z2, h ? 0x7FFF0000u : (r >> 4) * OT_B + l15 * 16, oa + 1024); };
  unsigned cpk;
  PRIO(3);
  if (role) { TL(0, 0, FILL(0), NOP, NOP) TL(0, 1, FILL(1), NOP, NOP) TL(0, 2, FILL(2), NOP, NOP) }
  else {
    auto f0 = [&](int n) { if (n < 8) kv_rd(n); if (n >= 13) kv_mm(n - 13); };
    auto f1 = [&](int n) { if (n < 8) kv_rd(8 + n); if (n == 7) ARRIVE_NW(0); if (n >= 9 && n < 12) kv_mm(n - 6); if (n >= 13) kv_mm(n - 7); };
    auto f2 = [&](int n) { if (n >= 9 && n < 12) kv_mm(n); if (n >= 5 && n < 8) kv_st(n - 5); };
    TLF(0, f0) TLF(1, f1) TLF(2, f2)
  }
  TL(0, 3, dma4s((w2 ? woB : woA) + C0 + (((rt & 7) * 2 + role) * 40 + w3 * 10) * 640, (l15 < 10 ? l15 : 9) * 1280 + (g & 1) * 156, lds0 + 160768)        , NOP, NOP)
  TL(0, 4, NOP, NOP, if (role) FILL(3); else { WAIT_VM(1); FLAG(WS_FPART + ((hq * 16 + rt) * 8 + w) * 8); })
  PRIO(2);
  TL(0, 5, NOP, NOP, cpk = PEEK(0))
  AWAIT(0, cpk);
  asm volatile("" ::: "memory");
  TL6(0, KX(2, 2), KW(2, 2, 0))
  TL7(0, 1, KW(2, 2, 1), KW(2, 2, 2), KW(2, 2, 3))
  TL(1, 0, KW(2, 2, 4), if (role) FILL(4), NOP)
  PRIO(1);
  TL(1, 1, if (role) { WX(0); WX(1); } else { fp0 = POLL(WS_FPART + hq * 1024); fp1 = POLL(WS_FPART + hq * 1024 + 512); }, if (role) FILL(5), if (role) { WX(2); WX(3); })
  TL(1, 2, if (role) WX(4), NOP, if (red) red_load())
  TL(1, 3, NOP, NOP, NOP)
  TL(1, 4, if (!role) CLR_VM(), if (red) red_store(), NOP)
  TL(1, 5, NOP, NOP, if (!role) { WAIT_VM(0); FLAG((w2 ? WS_SPARE : WS_FKVR) + ((hq * 16 + rt) * 4 + w3) * 8); })
  PRIO(0);
  TL6(1, NOP, NOP)
  if (!role) WAIT_BAR(1); else WAIT_BAR(0);
  PRIO(3);
  if (role) FLAG(WS_FOA + ((rt * 8 + hq) * 8 + w) * 8);
  u64 foa;
  TL7(1, 2, KX(3, 0), KW(3, 0, 0), KW(3, 0, 1))
  TL(2, 0, KW(3, 0, 2), KW(3, 0, 3), KW(3, 0, 4)) TL(2, 1, WA(w, 0), WA(w, 1), WA(w, 2))
  u32x4 wbr[11];
  const char* wbs = (const char*)(woB + C0 + w * 32 * 640) - 4096;
#define WB1(n) wbr[n] = *(const GAS u32x4*)(wbs + ((n) / 5) * (8 * 32 * 1280) + woff[(n) % 5] + ((n) % 5) * 1024)
#define WB2(j) do { WB1(j); WB1(5 + (j)); } while (0)
#define WA16() dma16s(woA + C0 + 16 * 32 * 640 - 2048, wo16, lds0 + L_WO + 16 * 5120 + w5 * 1024)
#define WB16() wbr[10] = *(const GAS u32x4*)((const char*)(woB + C0 + 16 * 32 * 640) - 4096 + wo16)
  auto chk = [&]() { asm volatile("" : "+v"(foa));
    if (__builtin_expect(!__all(foa == tag), 0)) wait_flags<64>(ws, WS_FOA + rt * 512, tag, lane);
    asm volatile("" ::: "memory"); };
  if (role) {
    TL(2, 2, WA(w, 3), WA(w, 4), NOP)
    PRIO(2);
    TL(2, 3, (fkr = POLL(WS_FKVR + hq * 512), foa = POLL(WS_FOA + rt * 512)), NOP, NOP)
    TL(2, 4, NOP, { chk(); oa_load(); }, { ka[2] = LD16S(kb, kbs + 1024); ka[7] = LD16S(kb, kbs + 3584); ka[3] = LD16S(kb, kbs + 1536); })
    PRIO(1);
    TL(2, 5, { ka[8] = LD16S(kb, kbs + 4096); ka[4] = LD16S(kb, kbs + 2048); ka[9] = LD16S(kb, kbs + 4608); }, NOP, NOP)
    PRIO(0);
    { auto f = [&](int n) { if (n == 5) v2n = ldsr16(d2p + 7 * TILE_B + 2 * VB); if (n >= 8 && n != 10 && n != 14) oa_mm(n < 10 ? n - 8 : (n < 14 ? n - 9 : 5)); }; TLF(6, f) }
  } else {
    TL(2, 2, WA(w, 3), WA(w, 4), WX(0))
    PRIO(2);
    TL(2, 3, { WX(1); WX(2); }, { WX(3); WX(4); }, foa = POLL(WS_FOA + rt * 512))
    TL(2, 4, NOP, NOP, NOP)
    PRIO(1);
    TL(2, 5, chk(), WB1(0), WB1(5))
    PRIO(0);
    TL6(2, WB1(1), WB1(6))
  }
  BAR_L();
  PRIO(3);
  if (role) {
    d0p -= 2 * STEP_B; d1p -= 2 * STEP_B; d2p -= 2 * STEP_B; asm volatile("" : "+v"(d0p), "+v"(d1p), "+v"(d2p));
    { auto f = [&](int n) { if (n == 8 || n == 9) oa_mm(n - 2); if (n == 11 || n == 12) oa_mm(n - 3); if (n == 5) CLR_VM(); if (n == 6) WA16(); }; TILEF(0, v2 = v2n, f) }
    TL(0, 0, oa_norm(), NOP, oa_st())
    TL(0, 1, WA(8 + w, 0), WA(8 + w, 1), WA(8 + w, 2))
    TL(0, 2, WA(8 + w, 3), WA(8 + w, 4), WB2(0))
    TL(0, 3, WB2(1), NOP, WB2(2))
    PRIO(2);
    TL(0, 4, WB2(3), NOP, WB2(4))
  } else {
    TL7(2, 0, WA16(), WA(8 + w, 0), WA(8 + w, 1))
    TL(0, 0, WA(8 + w, 2), WA(8 + w, 3), WA(8 + w, 4))
    TL(0, 1, WB1(2), NOP, WB1(7)) TL(0, 2, WB1(3), NOP, WB1(8)) TL(0, 3, WB1(4), NOP, WB1(9))
    PRIO(2);
    TL(0, 4, NOP, NOP, NOP)
  }
  ARRIVE_NW(1);
  const int tp = w & 1, kq = w >> 1;
  const unsigned ofr = (rt * 8 + role * 4 + tp * 2) * OT_B + kq * 5120;
  u32x4 of[2][5]; float gtb;
#define OFL(u) do { _Pragma("unroll") for (int i = 0; i < 5; i++) of[u][i] = LD16S(ok1, WS_OA + ofr + (u) * OT_B + i * 1024); } while (0)
#define OF2(i) do { of[0][i] = LD16S(ok1, WS_OA + ofr + (i) * 1024); of[1][i] = LD16S(ok1, WS_OA + ofr + OT_B + (i) * 1024); } while (0)
  TL(0, 5, WB16(), gtb = __uint_as_float(__builtin_amdgcn_raw_buffer_load_b32(wsr, WS_G + (rt * 128 + w3 * 32 + w2 * 16 + l15) * 4, 0, 16)), OF2(0))
  PRIO(1);
  TL6(0, OF2(1), OF2(2))
  PRIO(0);
  LAS f32x4* xb = (LAS f32x4*)(lp + (w3 >> 1) * CH_B) + (w3 & 1) * (64 * 6) + lane;
  { auto f = [&](int n) { if (n == 0) cpk = PEEK(1); if (n == 3) OF2(3); if (n == 4) OF2(4);
      if (n == 15) { AWAIT(1, cpk);
#pragma unroll
        for (int c = 0; c < 3; c++) xb[(w2 * 3 + c) * 64] = (f32x4){OA[4 * c], OA[4 * c + 1], OA[4 * c + 2], OA[4 * c + 3]}; } };
    TILEF(0, v2 = v2n, f) }
  pB2[2] = pk(e0, e1); pB2[3] = pk(e2, e3);
  OB = mfma32(v2, pB2, OB);
#undef TL
#undef TL6
#undef TL7
#undef TILE
#undef G
  u32x4 wf[5][5];
#define WFA(i) do { const LAS char* wb_ = lp + L_WO + (kq * 5 + (i)) * 5120 + wlane; _Pragma("unroll") for (int t = 0; t < 5; t++) wf[i][t] = trd2(wb_ + t * 8, wb_ + 1280 + t * 8); } while (0)
#define WFB(i) do { const int gr_ = kq * 5 + (i); const LAS char* wb_ = lp + (gr_ < 17 ? L_WO + gr_ * 5120 : 145408 + (gr_ - 17) * 5120) + wlane; _Pragma("unroll") for (int t = 0; t < 5; t++) wf[i][t] = trd2(wb_ + t * 8, wb_ + 1280 + t * 8); } while (0)
  { asm volatile("s_waitcnt vmcnt(18) lgkmcnt(0)\n\ts_barrier" ::: "memory");
    f32x16 O = OB;
#pragma unroll
    for (int c = 0; c < 3; c++) { const f32x4 t = xb[((w2 ^ 1) * 3 + c) * 64]; O[4 * c] += t[0]; O[4 * c + 1] += t[1]; O[4 * c + 2] += t[2]; O[4 * c + 3] += t[3]; }
    SBAR(); WFA(0); WFA(1); SBAR();
    float l = from_upper(O[8]);
    const float amax = (((fabsf(O[0]) + fabsf(O[1])) + (fabsf(O[2]) + fabsf(O[3]))) + ((fabsf(O[4]) + fabsf(O[5])) + (fabsf(O[6]) + fabsf(O[7])))) + ((fabsf(O[8]) + fabsf(O[9])) + (fabsf(O[10]) + fabsf(O[11])));
    const bool good = (l >= 0x1p-30f) && (l <= 0x1p100f) && (amax <= 0x1p126f);
    if (__builtin_expect(!__all(good), 0)) {
      const u32x4 q1 = q1B; const u32x2 q2 = h ? (u32x2){0u, 0u} : q2B;
      float m = -INFINITY;
#pragma unroll 1
      for (int t = 0; t < 64; t++) {
        f32x16 s = mfma32(LD16(WS_KV + t * TILE_B + ok1), q1, Z); s = mfma32k8(LD8(WS_KV + t * TILE_B + ok2), q2, s);
#pragma unroll
        for (int i = 0; i < 16; i++) m = fmaxf(m, s[i]);
      }
      m = half_swap_max(m);
      O = Z;
#pragma unroll 1
      for (int t = 0; t < 64; t++) {
        f32x16 s = mfma32(LD16(WS_KV + t * TILE_B + ok1), q1, Z); s = mfma32k8(LD8(WS_KV + t * TILE_B + ok2), q2, s);
#pragma unroll
        for (int i = 0; i < 16; i++) s[i] = ex2(s[i] - m);
        O = mfma32(LD16(WS_KV + t * TILE_B + ov), (u32x4){pk(s[0], s[1]), pk(s[2], s[3]), pk(s[4], s[5]), pk(s[6], s[7])}, O);
        O = mfma32(LD16(WS_KV + t * TILE_B + ov + 2 * VB), (u32x4){pk(s[8], s[9]), pk(s[10], s[11]), pk(s[12], s[13]), pk(s[14], s[15])}, O);
      }
      l = from_upper(O[8]);
    }
    const float inv = __builtin_amdgcn_rcpf(l) * (1.0f - gtb);
    LAS char* tb = lp + w2 * CH_B + 12288 + w3 * 1536;
    const int uq = (g & 1) * 5 + h;
    LAS char* t0 = tb + (uq >> 1) * 256 + (uq & 1) * 8 + l15 * 16; LAS char* t2 = h ? tb + 1280 + (g & 1) * 8 + l15 * 16 : t0 + 512;
#pragma unroll
    for (int c = 0; c < 3; c++) *(LAS u32x2*)(c < 2 ? t0 + c * 256 : t2) = (u32x2){nz(pk(O[4 * c] * inv, O[4 * c + 1] * inv)), nz(pk(O[4 * c + 2] * inv, O[4 * c + 3] * inv))};
    const unsigned ob = WS_OB + (rt * 8 + w3 * 2 + w2) * OT_B + hp * 1280;
    const u32x4 y0 = ldsr16(tb + ok1), y1 = ldsr16(tb + 1024 + l15 * 16);
    ST16S(y0, ok1, ob); ST16S(y1, lane < 16 ? ok1 : 0x7FFF0000u, ob + 1024);
    SBAR();
  }
  auto of_missing = [&]() { u32x4 mm = of[0][0];
#pragma unroll
    for (int j = 1; j < 10; j++) mm = __builtin_elementwise_max(mm, of[j / 5][j % 5]);
    return missing(mm); };
  if (__builtin_expect(of_missing(), 0)) {
#pragma unroll 1
    for (unsigned spin = 0; spin < SPIN_CAP; ++spin) {
      asm volatile("" ::: "memory");
      OFL(0); OFL(1);
      if (!of_missing()) break;
      __builtin_amdgcn_s_sleep(1);
    }
  }
  f32x4 c[2][5];
#define MM(u, i, first) do { _Pragma("unroll") for (int t = 0; t < 5; t++) c[u][t] = mfma16(wf[i][t], of[u][i], (first) ? (f32x4){0.f, 0.f, 0.f, 0.f} : c[u][t]); SBAR(); } while (0)
#define OBL(u, i) of[u][i] = LD16S(ok1, WS_OB + ofr + (u) * OT_B + (i) * 1024)
#define MMR(u, i, first, j, ob_) do { const LAS char* wb_ = lp + (ob_) + wlane; _Pragma("unroll") for (int t = 0; t < 5; t++) { \
      c[u][t] = mfma16(wf[i][t], of[u][i], (first) ? (f32x4){0.f, 0.f, 0.f, 0.f} : c[u][t]); wf[j][t] = trd2(wb_ + t * 8, wb_ + 1280 + t * 8); SBAR(); } } while (0)
#define OA_(j) (L_WO + (kq * 5 + (j)) * 5120)
#define OB_(j) (kq * 5 + (j) < 17 ? L_WO + (kq * 5 + (j)) * 5120 : 145408 + (kq * 5 + (j) - 17) * 5120)
  MMR(0, 0, true, 2, OA_(2)); MMR(1, 0, true, 3, OA_(3)); MMR(0, 1, false, 4, OA_(4));
  BAR_L();
  SBAR();
  { LAS char* d = lp + L_WO + w * 5120 + lane * 16;
#pragma unroll
    for (int n = 0; n < 15; n++) { const int u = n < 5 ? 1 : (n >= 10), i = n < 5 ? 1 : 2, t = n % 5;
      if (n < 10) *(LAS u32x4*)(d + (n / 5) * 40960 + (n % 5) * 1024) = wbr[n];
      if (n == 10) *(LAS u32x4*)(lp + L_WO + 16 * 5120 + w5 * 1024 + lane * 16) = wbr[10];
      if (n == 11) OBL(0, 0); if (n == 12) OBL(1, 0); if (n == 13) OBL(0, 1); if (n == 14) OBL(1, 1);
      c[u][t] = mfma16(wf[i][t], of[u][i], c[u][t]); SBAR(); }
    asm volatile("" : "+v"(c[1][0])); }
  BAR_L();
  SBAR();
  OBL(0, 2); OBL(1, 2); SBAR();
  MMR(0, 3, false, 0, OB_(0)); OBL(0, 3); SBAR(); MMR(0, 4, false, 1, OB_(1)); OBL(0, 4); SBAR(); MMR(1, 3, false, 2, OB_(2)); OBL(1, 3); SBAR(); MMR(1, 4, false, 3, OB_(3)); OBL(1, 4);
  WFB(4);
  ARRIVE_NW(2);
  SBAR();
  LAS char* xw = lp + kq * 21504 + (tp * 32 + l15) * 336 + g * 80;
#pragma unroll
  for (int n = 0; n < 10; n++) {
    const int u = n < 6 ? n & 1 : (n >= 8), i = n < 6 ? n >> 1 : 3 + (n & 1);
    if (__builtin_expect(missing(of[u][i]), 0)) {
#pragma unroll 1
      for (unsigned spin = 0; spin < SPIN_CAP; ++spin) {
        asm volatile("" ::: "memory");
#pragma unroll
        for (int j = n; j < 10; j++) { const int uu = j < 6 ? j & 1 : (j >= 8), ii = j < 6 ? j >> 1 : 3 + (j & 1); OBL(uu, ii); }
        if (!missing(of[u][i])) break;
        __builtin_amdgcn_s_sleep(1);
      }
    }
    MM(u, i, false);
    if (n == 6) cpk = PEEK(2);
    if (n == 8) { AWAIT(2, cpk); *(LAS f32x4*)(xw) = c[0][0]; *(LAS f32x4*)(xw + 16) = c[0][1]; *(LAS f32x4*)(xw + 32) = c[0][2]; SBAR(); }
    if (n == 9) { *(LAS f32x4*)(xw + 48) = c[0][3]; *(LAS f32x4*)(xw + 64) = c[0][4]; SBAR(); }
  }
#pragma unroll
  for (int t = 0; t < 5; t++) *(LAS f32x4*)(xw + 16 * 336 + t * 16) = c[1][t];
  BAR_L();
  { const __amdgpu_buffer_rsrc_t orr = __builtin_amdgcn_make_buffer_rsrc(out, 0, 2048 * DM * 2, 0x00020000);
#pragma unroll
    for (int j = 0; j < 3; j++) { const int n = tid + 512 * j, nn = n < 1280 ? n : 0, row = (nn * 3277) >> 16, cq = nn - row * 20;
      const LAS f32x4* p = (const LAS f32x4*)(lp + row * 336 + cq * 16);
      const f32x4 z = (p[0] + p[1344]) + (p[2688] + p[4032]);
      __builtin_amdgcn_raw_buffer_store_b64((u32x2){pk(z[0], z[1]), pk(z[2], z[3])}, orr, n < 1280 ? ((rt * 128 + role * 64 + row) * DM + C0 + cq * 4) * 2 : 0x7FFF0000u, 0, 0); }
  }
}

__global__ __launch_bounds__(512, 2) void fused(const u16* __restrict__ x, const u16* __restrict__ wqkv, const u16* __restrict__ wphi, const u16* __restrict__ woA,
                                                const u16* __restrict__ wq, const u16* __restrict__ wk, const u16* __restrict__ wv, const u16* __restrict__ woB,
                                                const u16* __restrict__ gatew, u16* __restrict__ out, char* ws) {
  __shared__ __attribute__((aligned(16))) char lds[L_TOTAL];
  if ((blockIdx.x >> 6) & 1) body<1>(x, wqkv, wphi, woA, wq, wv, woB, gatew, out, ws, (LAS char*)lds);
  else body<0>(x, wqkv, wphi, woA, wq, wk, woB, gatew, out, ws, (LAS char*)lds);
}
}

extern "C" void kernel_launch(void* const* d_in, const int* in_sizes, int n_in, void* d_out, int out_size, void* d_ws, size_t ws_size, hipStream_t stream) {
  using namespace gm;
  fused<<<256, 512, 0, stream>>>((const u16*)d_in[0], (const u16*)d_in[1], (const u16*)d_in[2], (const u16*)d_in[3], (const u16*)d_in[5], (const u16*)d_in[6],
                                 (const u16*)d_in[7], (const u16*)d_in[8], (const u16*)d_in[9], (u16*)d_out, (char*)d_ws);
}
```
